```python
import torch

def _softmax(x, axis=-1):
    return torch.softmax(x, dim=axis)
def _gelu(x):
    return torch.nn.functional.gelu(x, approximate="tanh")
def _silu(x):
    return torch.nn.functional.silu(x)
def _sigmoid(x):
    return torch.sigmoid(x)
def _layernorm(x, axis=-1, eps=1e-5):
    m = x.mean(dim=axis, keepdim=True)
    v = x.var(dim=axis, keepdim=True, unbiased=False)
    return (x - m) / torch.sqrt(v + eps)
def _scatter_add(tgt, idx, src, axis=0):
    return tgt.clone().index_add_(axis, idx, src)
def _segment_sum(src, seg, n):
    out = torch.zeros((n,) + src.shape[1:], dtype=src.dtype)
    return out.index_add_(0, seg, src)
def _pad(x, pw):
    flat = tuple(v for pair in reversed(pw) for v in pair)
    return torch.nn.functional.pad(x, flat)

kernel_name = 'interleave_layers__spectral_gfnet__mixer_channel_mlp__prompt'

def setup_inputs():

    _w = (lambda a: (lambda t: t.to(torch.bfloat16) if t.is_floating_point() else t)(torch.as_tensor(a)))
    def _A_setup():
        B, S, D = 1, 2048, 640
        Df = D // 2 + 1
        return {
            "x": _w((lambda *s: torch.randn(*s, dtype=torch.bfloat16) * 0.05)(B, S, D)),
            "w_r": _w((lambda *s: torch.randn(*s, dtype=torch.bfloat16) * 0.8)(S, Df) * 0.02),
            "w_i": _w((lambda *s: torch.randn(*s, dtype=torch.bfloat16) * 0.8)(S, Df) * 0.02),
        }
    _A_in = _A_setup()

    def _B_setup():
        B, S, D, F = 1, 2048, 640, 1728
        return {
            "x": _w((lambda *s: torch.randn(*s, dtype=torch.bfloat16) * 0.05)(B, S, D)),
            "w1": _w((lambda *s: torch.randn(*s, dtype=torch.bfloat16) * 0.8)(D, F) * (D ** -0.5)),
            "b1": _w((lambda *s: torch.randn(*s, dtype=torch.bfloat16) * 0.4)(F)),
            "w2": _w((lambda *s: torch.randn(*s, dtype=torch.bfloat16) * 0.05)(F, D) * (F ** -0.5)),
            "b2": _w((lambda *s: torch.randn(*s, dtype=torch.bfloat16) * 0.05)(D)),
        }
    _B_in = _B_setup()

    return {
        "A_x": _A_in["x"],
        "A_w_r": _A_in["w_r"],
        "A_w_i": _A_in["w_i"],
        "B_x": _B_in["x"],
        "B_w1": _B_in["w1"],
        "B_b1": _B_in["b1"],
        "B_w2": _B_in["w2"],
        "B_b2": _B_in["b2"],
    }

def reference(A_x, A_w_r, A_w_i, B_x, B_w1, B_b1, B_w2, B_b2):
    def _A_ref(x, w_r, w_i):
        B, S, D = x.shape
        X = (lambda x, axes=(-2,-1): torch.fft.rfft2(x.float(), dim=axes))(x, axes=(-2, -1))
        W = w_r + 1j * w_i
        X = X * W
        y = (lambda x, s, axes=(-2,-1): torch.fft.irfft2(x, s=s, dim=axes).to(torch.bfloat16))(X, (S, D), axes=(-2, -1))
        return y

    def _B_ref(x, w1, b1, w2, b2):
        y = _layernorm(x, axis=-1)
        h = _gelu(torch.einsum("bsd,df->bsf", y, w1) + b1)
        return torch.einsum("bsf,fd->bsd", h, w2) + b2

    x = A_x
    x = x + _A_ref(_layernorm(x, axis=-1), A_w_r, A_w_i)
    x = x + _B_ref(_layernorm(x, axis=-1), B_w1, B_b1, B_w2, B_b2)
    x = x + _A_ref(_layernorm(x, axis=-1), A_w_r, A_w_i)
    x = x + _B_ref(_layernorm(x, axis=-1), B_w1, B_b1, B_w2, B_b2)
    return x
```

```cpp
#include <hip/hip_runtime.h>
#include <hip/hip_bf16.h>
#include <stdint.h>
#include <type_traits>

typedef unsigned short u16;
typedef unsigned int u32;
typedef unsigned long long u64;
typedef short s16x4 __attribute__((ext_vector_type(4)));
typedef __bf16 bf16x8 __attribute__((ext_vector_type(8)));
typedef __bf16 bf16x2 __attribute__((ext_vector_type(2)));
typedef float f32x4 __attribute__((ext_vector_type(4)));
typedef float f32x2 __attribute__((ext_vector_type(2)));
typedef u32 u32x4 __attribute__((ext_vector_type(4)));
typedef u32 u32x2 __attribute__((ext_vector_type(2)));
typedef __amdgpu_buffer_rsrc_t rsrc_t;
#define LAS __attribute__((address_space(3)))
#define DI __device__ __forceinline__

constexpr int S_ = 2048, D_ = 640, F_ = 1728, DF_ = 321;
constexpr float EPS = 1e-5f;
constexpr float SS_SCALE = 1048576.0f;

constexpr size_t XT_BYTES = (size_t)D_ * S_ * 2;
constexpr size_t OFF_XT0 = 0;
constexpr size_t OFF_XT1 = OFF_XT0 + XT_BYTES;
constexpr size_t OFF_XT2 = OFF_XT1 + XT_BYTES;
constexpr size_t OFF_XT3 = OFF_XT2 + XT_BYTES;
constexpr size_t OFF_WT = OFF_XT3 + XT_BYTES;
constexpr size_t OFF_W1F = OFF_WT + (size_t)320 * 2048 * 4;
constexpr size_t W1F_BYTES = (size_t)148 * 20 * 1024;
constexpr size_t OFF_W2F = OFF_W1F + W1F_BYTES;
constexpr size_t W2F_BYTES = (size_t)40 * 54 * 1024;
constexpr size_t OFF_W2O = OFF_W2F + W2F_BYTES;
constexpr size_t OFF_H = OFF_W2O + W2F_BYTES;
constexpr size_t H_BYTES = (size_t)128 * 54 * 1024;
constexpr size_t OFF_H2 = OFF_H + H_BYTES;
constexpr size_t OFF_X3 = OFF_H2 + H_BYTES;
constexpr size_t OFF_SS0 = OFF_X3 + (size_t)128 * 40 * 512;
constexpr size_t OFF_SS2 = OFF_SS0 + 2048 * 8;
constexpr size_t OFF_B2P = OFF_SS2 + 2048 * 8;
constexpr size_t OFF_XF1 = OFF_B2P + 4096;
constexpr size_t OFF_XF3 = OFF_XF1 + XT_BYTES;
constexpr size_t WS_END = OFF_XF3 + XT_BYTES;

#define STAMP(k, i)
#define CSTAMP(k, i)
#define END_STAMP(k)

DI f32x4 mfma16(bf16x8 a, bf16x8 b, f32x4 c) { return __builtin_amdgcn_mfma_f32_16x16x32_bf16(a, b, c, 0, 0, 0); }
DI u32 pk2(float a, float b) { f32x2 v = {a, b}; const bf16x2 r = __builtin_convertvector(v, bf16x2); return __builtin_bit_cast(u32, r); }
DI bf16x8 pk8(float a0, float a1, float a2, float a3, float a4, float a5, float a6, float a7) {
    u32x4 r = {pk2(a0, a1), pk2(a2, a3), pk2(a4, a5), pk2(a6, a7)};
    return __builtin_bit_cast(bf16x8, r);
}
DI bf16x8 pk8(const float (&a)[8]) { return pk8(a[0], a[1], a[2], a[3], a[4], a[5], a[6], a[7]); }
DI bf16x8 asbf(u32x4 r) { return __builtin_bit_cast(bf16x8, r); }
DI bf16x8 neg8(bf16x8 a) { u32x4 r = __builtin_bit_cast(u32x4, a); r ^= 0x80008000u; return __builtin_bit_cast(bf16x8, r); }
DI float bflo(u32 v) { return __builtin_bit_cast(float, v << 16); }
DI float bfhi(u32 v) { return __builtin_bit_cast(float, v & 0xffff0000u); }
DI float cosr(float rev) { return __builtin_amdgcn_cosf(rev); }
DI float sinr(float rev) { return __builtin_amdgcn_sinf(rev); }
DI int slot32(int q, int j) { return j < 4 ? 4 * q + j : 16 + 4 * q + (j - 4); }
DI s16x4 tr_read(const char* p) { return __builtin_amdgcn_ds_read_tr16_b64_v4i16((LAS s16x4*)p); }
DI bf16x8 cat8(s16x4 lo, s16x4 hi) { return __builtin_bit_cast(bf16x8, __builtin_shufflevector(lo, hi, 0, 1, 2, 3, 4, 5, 6, 7)); }
DI int tok_of_p(int p) { return 32 * (p & 63) + (p >> 6); }
DI rsrc_t mk_rsrc(const void* p, size_t bytes) { return __builtin_amdgcn_make_buffer_rsrc((void*)p, 0, (int)bytes, 0x00020000); }
DI u32x4 bload16(rsrc_t r, int voff, int soff) { return __builtin_amdgcn_raw_buffer_load_b128(r, voff, soff, 0); }
DI float ss_to_float(u64 v) { return (float)(u32)(v >> 32) * 4294967296.0f + (float)(u32)v; }
DI u64 ss_from_float(float s) { return (u64)(fminf(s * SS_SCALE, 1.0e18f) + 0.5f); }
DI void pin(float& x) { asm volatile("" : "+v"(x)); }
DI void pin(bf16x8& x) { asm volatile("" : "+v"(x)); }

struct F32c { bf16x8 c[2], sp[2]; };
DI void f32c_init(F32c& k, int lane) {
    const float m = (float)(lane & 15), q4 = (float)(4 * (lane >> 4));
    float c[4], s[4];
#pragma unroll
    for (int j = 0; j < 4; ++j) { const float a = __builtin_amdgcn_fractf(m * (q4 + (float)j) * (1.0f / 32.0f)); c[j] = cosr(a); s[j] = sinr(a); }
    const u32 sg = (lane & 1) ? 0x80008000u : 0u;
    const u32 c0 = pk2(c[0], c[1]), c1 = pk2(c[2], c[3]), s0 = pk2(s[0], s[1]), s1 = pk2(s[2], s[3]);
    const u32x4 cv = {c0, c1, c0 ^ sg, c1 ^ sg}, sv = {s0, s1, s0 ^ sg, s1 ^ sg};
    k.c[0] = asbf(cv); k.sp[0] = asbf(sv);
    k.c[1] = asbf(cv ^ 0x80000000u); k.sp[1] = asbf(sv ^ 0x80000000u);
}

struct RowC { bf16x8 c[2], sp[2], sm[2], c20, s20m; f32x4 twr[2], twi[2]; int off0, off1; };
constexpr int ROWC_LDS = 10 * 1024;
DI void rowc_build(char* tab, int w, int lane) {
    const int n = lane & 15, q = lane >> 4;
    const float nf = (float)n;
    u32x4* t = (u32x4*)tab + lane;
    if (w == 0) {
        F32c f; f32c_init(f, lane);
        t[0] = __builtin_bit_cast(u32x4, f.c[0]); t[64] = __builtin_bit_cast(u32x4, f.c[1]);
        t[128] = __builtin_bit_cast(u32x4, f.sp[0]); t[192] = __builtin_bit_cast(u32x4, f.sp[1]);
    } else if (w <= 2) {
        float c[8];
#pragma unroll
        for (int j = 0; j < 8; ++j) {
            const int d1 = 8 * q + j;
            const float a = __builtin_amdgcn_fractf((float)d1 * nf * 0.05f);
            const float v = w == 1 ? cosr(a) : -sinr(a);
            c[j] = (d1 < 20 && n <= 10) ? v : 0.f;
        }
        t[(3 + w) * 64] = __builtin_bit_cast(u32x4, pk8(c));
    } else if (w <= 4) {
        const float sc = 0.05590169943749474f;
        f32x4 cr, ci;
#pragma unroll
        for (int i = 0; i < 4; ++i) {
            const float a = (float)(16 * (w - 3) + 4 * q + i) * nf * (1.0f / 640.0f);
            cr[i] = cosr(a) * sc; ci[i] = -sinr(a) * sc;
        }
        t[(6 + w - 3) * 64] = __builtin_bit_cast(u32x4, cr); t[(8 + w - 3) * 64] = __builtin_bit_cast(u32x4, ci);
    }
}
DI void rowc_load(RowC& k, const char* tab, int lane) {
    const u32x4* tb = (const u32x4*)tab + lane;
    k.c[0] = asbf(tb[0]); k.c[1] = asbf(tb[64]); k.sp[0] = asbf(tb[128]); k.sp[1] = asbf(tb[192]);
    k.c20 = asbf(tb[256]); k.s20m = asbf(tb[320]);
    k.twr[0] = __builtin_bit_cast(f32x4, tb[384]); k.twr[1] = __builtin_bit_cast(f32x4, tb[448]);
    k.twi[0] = __builtin_bit_cast(f32x4, tb[512]); k.twi[1] = __builtin_bit_cast(f32x4, tb[576]);
    k.sm[0] = neg8(k.sp[0]); k.sm[1] = neg8(k.sp[1]);
    const int q = lane >> 4, qq = (lane & 15) >> 2, pp = lane & 3;
    const int r0 = min(8 * q + qq, 19), r1 = min(8 * q + 4 + qq, 19);
    k.off0 = r0 * 64 + pp * 8; k.off1 = r1 * 64 + pp * 8;
}
DI float wave_sum63(float v) {
    v += __builtin_bit_cast(float, __builtin_amdgcn_update_dpp(0, __builtin_bit_cast(int, v), 0x111, 0xf, 0xf, true));
    v += __builtin_bit_cast(float, __builtin_amdgcn_update_dpp(0, __builtin_bit_cast(int, v), 0x112, 0xf, 0xf, true));
    v += __builtin_bit_cast(float, __builtin_amdgcn_update_dpp(0, __builtin_bit_cast(int, v), 0x114, 0xf, 0xf, true));
    v += __builtin_bit_cast(float, __builtin_amdgcn_update_dpp(0, __builtin_bit_cast(int, v), 0x118, 0xf, 0xf, true));
    v += __builtin_bit_cast(float, __builtin_amdgcn_update_dpp(0, __builtin_bit_cast(int, v), 0x142, 0xa, 0xf, true));
    v += __builtin_bit_cast(float, __builtin_amdgcn_update_dpp(0, __builtin_bit_cast(int, v), 0x143, 0xc, 0xf, true));
    return v;
}
DI void rowfft(const RowC& k, const char* row, float (&A)[8], float (&B)[8], int lane) {
    f32x4 z = {0.f, 0.f, 0.f, 0.f};
    f32x4 yr[2], yi[2];
#pragma unroll
    for (int mt = 0; mt < 2; ++mt) {
        const bf16x8 a = cat8(tr_read(row + k.off0 + mt * 32), tr_read(row + k.off1 + mt * 32));
        yr[mt] = mfma16(a, k.c20, z); yi[mt] = mfma16(a, k.s20m, z);
    }
    float tr[8], ti[8];
#pragma unroll
    for (int i = 0; i < 8; ++i) {
        const float a = yr[i >> 2][i & 3], b = yi[i >> 2][i & 3], cr = k.twr[i >> 2][i & 3], ci = k.twi[i >> 2][i & 3];
        tr[i] = a * cr - b * ci; ti[i] = a * ci + b * cr;
    }
    const bf16x8 Tr = pk8(tr), Ti = pk8(ti);
    f32x4 ore[2], oim[2];
#pragma unroll
    for (int t = 0; t < 2; ++t) {
        ore[t] = mfma16(k.c[t], Tr, z); ore[t] = mfma16(k.sp[t], Ti, ore[t]);
        oim[t] = mfma16(k.sm[t], Tr, z); oim[t] = mfma16(k.c[t], Ti, oim[t]);
    }
#pragma unroll
    for (int r = 0; r < 4; ++r) { A[2 * r] = ore[0][r]; A[2 * r + 1] = oim[0][r]; B[2 * r] = ore[1][r]; B[2 * r + 1] = oim[1][r]; }
    if (lane == 0) { const float h = 0.7071067811865476f; const float dc = A[0] * h; A[0] = B[0] * h; A[1] = dc; }
}
DI int chunk_c0(int n, int q) { return (n == 0 ? 608 : n == 10 ? 576 : 64 * (n - 1)) + 8 * q; }

constexpr int ROWST = 1344;
constexpr int PREP_TAB = 32 * ROWST;
constexpr int PREP_LDS = PREP_TAB + ROWC_LDS;

struct PrepArgs { const u16 *x, *wr, *wi, *w1, *w2, *b2; char* ws; };

DI void task_x(const PrepArgs& a, int b, char* smem) {
    const int tid = threadIdx.x, lane = tid & 63, w = tid >> 6;
    const int p0 = 16 * ((b & 7) * 16 + (b >> 3));
    u32x4 v[3];
#pragma unroll
    for (int i = 0; i < 3; ++i) {
        const int id = min(tid + 512 * i, 1279), r = id / 80, ch = id % 80;
        v[i] = *(const u32x4*)(a.x + (size_t)tok_of_p(p0 + r) * D_ + ch * 8);
    }
    __builtin_amdgcn_sched_barrier(0);
    rowc_build(smem + PREP_TAB, w, lane);
    __builtin_amdgcn_sched_barrier(0);
    STAMP(0, 1);
    if (tid < 16) ((u64*)(a.ws + OFF_SS2))[p0 + tid] = 0ull;
#pragma unroll
    for (int i = 0; i < 3; ++i) {
        const int id = min(tid + 512 * i, 1279), r = id / 80, ch = id % 80;
        *(u32x4*)(smem + r * ROWST + ch * 16) = v[i];
    }
    STAMP(0, 2);
    __syncthreads();
    STAMP(0, 3);
    RowC k; rowc_load(k, smem + PREP_TAB, lane);
    const int n = lane & 15, q = lane >> 4, c0 = chunk_c0(n, q);
    float A[2][8], B[2][8];
#pragma unroll
    for (int i = 0; i < 2; ++i) rowfft(k, smem + (2 * w + i) * ROWST, A[i], B[i], lane);
#pragma unroll
    for (int i = 0; i < 2; ++i) {
        const int r = 2 * w + i;
        float ss = 0.f;
        if (n <= 10) {
#pragma unroll
            for (int e = 0; e < 8; ++e) ss += (lane == 0 && e == 1) ? 0.f : A[i][e] * A[i][e];
            *(bf16x8*)(smem + r * ROWST + c0 * 2) = pk8(A[i]);
        }
        if (n >= 1 && n <= 9) {
#pragma unroll
            for (int e = 0; e < 8; ++e) ss += B[i][e] * B[i][e];
            *(bf16x8*)(smem + r * ROWST + c0 * 2 + 64) = pk8(B[i]);
        }
        ss = wave_sum63(ss);
        if (lane == 63) ((u64*)(a.ws + OFF_SS0))[p0 + r] = ss_from_float(ss);
    }
    STAMP(0, 4);
    __syncthreads();
    STAMP(0, 5);
    u16* xt = (u16*)(a.ws + OFF_XT0);
    const int g = lane >> 4, i16 = lane & 15, qq = i16 >> 2, pp = i16 & 3;
#pragma unroll
    for (int i = 0; i < 3; ++i) {
        const int cbk = w + 8 * i;
        if (cbk < 20) {
            const int cb = 32 * cbk + 16 * (g & 1), ph = g >> 1;
            const char* base = smem + (8 * ph + qq) * ROWST + (cb + 4 * pp) * 2;
            const bf16x8 o = cat8(tr_read(base), tr_read(base + 4 * ROWST));
            *(bf16x8*)(xt + (size_t)(cb + i16) * S_ + p0 + 8 * ph) = o;
        }
    }
}

DI void task_W(const PrepArgs& a, int b) {
    const int lane = threadIdx.x & 63, wv = threadIdx.x >> 6, wt = b * 2 + (wv >> 2), kq = wv & 3;
    const int k1 = wt / 5, cg = wt % 5, kp = 1 + 64 * cg + lane;
    const int r = kp % 20, t = kp / 20;
    int j; bool mir = false;
    if (kp == 320) j = 304;
    else if (r == 0) j = 304 + t;
    else if (r < 10) j = 32 * (r - 1) + t;
    else if (r == 10) j = 288 + t;
    else { j = 32 * (19 - r) + 31 - t; mir = true; }
    const rsrc_t rr = mk_rsrc(a.wr, (size_t)S_ * DF_ * 2), ri = mk_rsrc(a.wi, (size_t)S_ * DF_ * 2);
    const int st = (mir ? -64 : 64) * DF_ * 2, off0 = ((mir ? 2048 - k1 : k1) * DF_ + kp) * 2 + 8 * kq * st;
    u32 re[8], im[8];
#pragma unroll
    for (int k2 = 0; k2 < 8; ++k2) {
        int off = off0 + k2 * st;
        if (k2 == 0 && mir && k1 == 0 && kq == 0) off = kp * 2;
        re[k2] = __builtin_amdgcn_raw_buffer_load_b16(rr, off, 0, 0); im[k2] = __builtin_amdgcn_raw_buffer_load_b16(ri, off, 0, 0);
    }
    const u32 sg = mir ? 0x80000000u : 0u;
    u32x4* dst = (u32x4*)(a.ws + OFF_WT + ((size_t)j * 64 + k1) * 128 + kq * 32);
#pragma unroll
    for (int i = 0; i < 2; ++i) {
        u32x4 o;
#pragma unroll
        for (int e = 0; e < 4; ++e) o[e] = (re[4 * i + e] | (im[4 * i + e] << 16)) ^ sg;
        dst[i] = o;
    }
}

DI void task_w1(const PrepArgs& a, int b, char* smem) {
    const int tid = threadIdx.x, lane = tid & 63, w = tid >> 6;
    char* raw = smem;
    char* tr = smem + 640 * 32;
    char* out = raw;
    u32x4 v[3];
#pragma unroll
    for (int i = 0; i < 3; ++i) {
        const int id = min(tid + 512 * i, 1279), d = id >> 1, h = id & 1;
        v[i] = *(const u32x4*)(a.w1 + (size_t)d * F_ + 16 * b + 8 * h);
    }
    __builtin_amdgcn_sched_barrier(0);
    rowc_build(smem + PREP_TAB, w, lane);
    __builtin_amdgcn_sched_barrier(0);
#pragma unroll
    for (int i = 0; i < 3; ++i) *(u32x4*)(raw + min(tid + 512 * i, 1279) * 16) = v[i];
    __syncthreads();
    const int g = lane >> 4, i16 = lane & 15, qq = i16 >> 2, pp = i16 & 3;
#pragma unroll
    for (int i = 0; i < 5; ++i) {
        const int d0 = 16 * (w + 8 * i) + 4 * g;
        const s16x4 t = tr_read(raw + (d0 + qq) * 32 + pp * 8);
        *(s16x4*)(tr + i16 * ROWST + d0 * 2) = t;
    }
    RowC k; rowc_load(k, smem + PREP_TAB, lane);
    __syncthreads();
    const int n = lane & 15, q = lane >> 4, c0 = chunk_c0(n, q);
    float A[2][8], B[2][8];
#pragma unroll
    for (int i = 0; i < 2; ++i) rowfft(k, tr + (2 * w + i) * ROWST, A[i], B[i], lane);
#pragma unroll
    for (int i = 0; i < 2; ++i) {
        const int f = 2 * w + i;
        if (lane == 0) A[i][1] = 0.f;
        if (n <= 10) *(bf16x8*)(out + ((c0 >> 5) * 64 + q * 16 + f) * 16) = pk8(A[i]);
        if (n >= 1 && n <= 9) *(bf16x8*)(out + (((c0 >> 5) + 1) * 64 + q * 16 + f) * 16) = pk8(B[i]);
    }
    __syncthreads();
    u32x4* dst = (u32x4*)(a.ws + OFF_W1F + (size_t)b * 20 * 1024);
#pragma unroll
    for (int i = 0; i < 3; ++i) { const int id = tid + 512 * i; if (id < 1280) dst[id] = *(const u32x4*)(out + id * 16); }
}

DI void w2_frags(const char* img, char* dstbase, int b, int lane, int w) {
    const int g = lane >> 4, i16 = lane & 15, qq = i16 >> 2, pp = i16 & 3;
#pragma unroll
    for (int i = 0; i < 5; ++i) {
        const int cu = w + 8 * i;
        const char* base = img + (4 * g + qq) * ROWST + (16 * cu + 4 * pp) * 2;
        const bf16x8 v = cat8(tr_read(base), tr_read(base + 16 * ROWST));
        *(bf16x8*)(dstbase + ((size_t)(cu * 54 + b) * 64 + lane) * 16) = v;
    }
}
DI void task_w2(const PrepArgs& a, int b, char* smem, bool transform) {
    const int tid = threadIdx.x, lane = tid & 63, w = tid >> 6;
    u32x4 v[5];
#pragma unroll
    for (int i = 0; i < 5; ++i) {
        const int id = tid + 512 * i, r = id / 80, ch = id % 80;
        v[i] = *(const u32x4*)(a.w2 + (size_t)(32 * b + r) * D_ + ch * 8);
    }
    if (!transform) {
#pragma unroll
        for (int i = 0; i < 5; ++i) { const int id = tid + 512 * i, r = id / 80, ch = id % 80; *(u32x4*)(smem + r * ROWST + ch * 16) = v[i]; }
        __syncthreads();
        w2_frags(smem, a.ws + OFF_W2O, b, lane, w);
        return;
    }
    __builtin_amdgcn_sched_barrier(0);
    rowc_build(smem + PREP_TAB, w, lane);
    __builtin_amdgcn_sched_barrier(0);
#pragma unroll
    for (int i = 0; i < 5; ++i) { const int id = tid + 512 * i, r = id / 80, ch = id % 80; *(u32x4*)(smem + r * ROWST + ch * 16) = v[i]; }
    __syncthreads();
    RowC k; rowc_load(k, smem + PREP_TAB, lane);
    const int n = lane & 15, q = lane >> 4, c0 = chunk_c0(n, q);
#pragma unroll 1
    for (int ii = 0; ii < 2; ++ii) {
        float A[2][8], B[2][8];
#pragma unroll
        for (int i = 0; i < 2; ++i) rowfft(k, smem + (4 * w + 2 * ii + i) * ROWST, A[i], B[i], lane);
#pragma unroll
        for (int i = 0; i < 2; ++i) {
            const int r = 4 * w + 2 * ii + i;
            if (n <= 10) *(bf16x8*)(smem + r * ROWST + c0 * 2) = pk8(A[i]);
            if (n >= 1 && n <= 9) *(bf16x8*)(smem + r * ROWST + c0 * 2 + 64) = pk8(B[i]);
        }
    }
    __syncthreads();
    w2_frags(smem, a.ws + OFF_W2F, b, lane, w);
}

DI int kcol_of(int jc) { return jc < 288 ? (jc >> 5) + 1 + 20 * (jc & 31) : jc < 304 ? 10 + 20 * (jc - 288) : 20 * (jc - 304); }
DI void task_Q(const PrepArgs& a, int u) {
    const int tid = threadIdx.x;
#pragma unroll
    for (int i = 0; i < 3; ++i) {
        const int id = tid + 512 * i;
        if (id < 1280) {
            const int ks = id >> 6, l = id & 63, d = 16 * u + (l & 15), q = l >> 4;
            float v[8];
#pragma unroll
            for (int j = 0; j < 8; j += 2) {
                const int jc = 16 * ks + 4 * q + (j >> 1), kk = kcol_of(jc);
                const float ang = (float)((kk * d) % 640) * (1.0f / 640.0f);
                float c = cosr(ang) * 0.05590169943749474f, s = -sinr(ang) * 0.05590169943749474f;
                if (jc == 304) { c = ((d & 1) ? -1.f : 1.f) * 0.03952847075210474f; s = 0.03952847075210474f; }
                v[j] = c; v[j + 1] = s;
            }
            *(bf16x8*)(a.ws + OFF_W1F + ((size_t)(108 + u) * 20 * 64 + id) * 16) = pk8(v);
        }
    }
}
DI void task_b(const PrepArgs& a, char* smem) {
    const int tid = threadIdx.x, lane = tid & 63;
    if (tid < 80) *(u32x4*)(smem + tid * 16) = *(const u32x4*)(a.b2 + tid * 8);
    rowc_build(smem + PREP_TAB, tid >> 6, lane);
    __syncthreads();
    RowC k; rowc_load(k, smem + PREP_TAB, lane);
    float A[8], B[8];
    rowfft(k, smem, A, B, lane);
    if (tid < 64) {
        const int n = lane & 15, q = lane >> 4, c0 = chunk_c0(n, q);
        float* o = (float*)(a.ws + OFF_B2P);
        if (n <= 10) for (int e = 0; e < 8; ++e) o[c0 + e] = A[e];
        if (n >= 1 && n <= 9) for (int e = 0; e < 8; ++e) o[c0 + 32 + e] = B[e];
    }
}

constexpr int NB_K0 = 128 + 108 + 160;
__global__ __launch_bounds__(512, 4) void k0_prep(PrepArgs a) {
    extern __shared__ __attribute__((aligned(16))) char smem[];
    const int b = blockIdx.x;
    STAMP(0, 0);
    if (b < 128) task_x(a, b, smem);
    else if (b < 236) task_w1(a, b - 128, smem);
    else task_W(a, b - 236);
    END_STAMP(0);
}

struct ColArgs { PrepArgs p; const u16* xin; u16* xout; char* xf; const u64* ss; const u32* wt; int layer; int pad; };
DI int img128(int row, int ch) { return row * 128 + ((ch ^ ((row >> 1) & 7)) << 4); }
constexpr int NB_COL1 = 160 + 54 + 1, NB_COL2 = 160 + 54 + 40;

DI void col_task(const ColArgs& a, int b, char* smem) {
    const int tid = threadIdx.x & 255, lane = tid & 63, w = tid >> 6, half = threadIdx.x >> 8;
    const int j = 2 * b + half;
    char* Ure = smem + half * 24576; char* Uim = Ure + 4096; char* Ere = Ure + 8192; char* Eim = Ure + 12288;
    char* Rre = Ure + 16384; char* Rim = Ure + 20480;
    const int n = lane & 15, q = lane >> 4;
    const u16* xr = a.xin + (size_t)(2 * j) * S_; const u16* xi = xr + S_;
    const u32x4 vr = *(const u32x4*)(xr + tid * 8), vi = *(const u32x4*)(xi + tid * 8);
    u32x4 sv[4];
#pragma unroll
    for (int i = 0; i < 4; ++i) sv[i] = *(const u32x4*)(a.ss + tid * 8 + 2 * i);
    u32x4 wf[2];
#pragma unroll
    for (int t = 0; t < 2; ++t) wf[t] = *(const u32x4*)(a.wt + ((size_t)j * 64 + 16 * w + n) * 32 + 16 * t + 4 * q);
    __builtin_amdgcn_sched_barrier(0);
    F32c f; f32c_init(f, lane);
    bf16x8 c32[2] = {f.c[0], f.c[1]}, s32p[2] = {f.sp[0], f.sp[1]}, s32m[2];
    pin(c32[0]); pin(c32[1]); pin(s32p[0]); pin(s32p[1]);
    bf16x8 c64[2], s64p[2], s64m[2];
    const float k1f = (float)(16 * w + n);
    {
        float c[8], s[8];
#pragma unroll
        for (int e = 0; e < 8; ++e) {
            const float ang = __builtin_amdgcn_fractf((float)(8 * q + e) * k1f * (1.0f / 64.0f));
            c[e] = cosr(ang); s[e] = sinr(ang);
        }
        c64[0] = pk8(c); s64p[0] = pk8(s);
        const u32 sg = (lane & 1) ? 0x80008000u : 0u;
        c64[1] = asbf(__builtin_bit_cast(u32x4, c64[0]) ^ sg); s64p[1] = asbf(__builtin_bit_cast(u32x4, s64p[0]) ^ sg);
        pin(c64[0]); pin(c64[1]); pin(s64p[0]); pin(s64p[1]);
    }
    float twr[8], twi[8], ivr[8], ivi[8];
#pragma unroll
    for (int i = 0; i < 8; ++i) {
        const float af = (float)(16 * (i >> 2) + 4 * q + (i & 3)) * k1f * (1.0f / 2048.0f);
        twr[i] = cosr(af); twi[i] = -sinr(af);
        const float ai = (float)(16 * w + 4 * q + (i & 3)) * (float)(16 * (i >> 2) + n) * (1.0f / 2048.0f);
        ivr[i] = cosr(ai) * (1.0f / 2048.0f); ivi[i] = sinr(ai) * (1.0f / 2048.0f);
        pin(twr[i]); pin(twi[i]); pin(ivr[i]); pin(ivi[i]);
    }
    __builtin_amdgcn_sched_barrier(0);
    STAMP(a.layer ? 4 : 1, 1);
    s64m[0] = neg8(s64p[0]); s64m[1] = neg8(s64p[1]);
    {
        float r[8];
#pragma unroll
        for (int i = 0; i < 8; ++i) {
            const float s = (float)sv[i >> 1][2 * (i & 1) + 1] * 4294967296.0f + (float)sv[i >> 1][2 * (i & 1)];
            r[i] = __builtin_amdgcn_rsqf(s * (1.0f / (SS_SCALE * 640.0f)) + EPS);
        }
        const bf16x8 ur = pk8(bflo(vr[0]) * r[0], bfhi(vr[0]) * r[1], bflo(vr[1]) * r[2], bfhi(vr[1]) * r[3],
                              bflo(vr[2]) * r[4], bfhi(vr[2]) * r[5], bflo(vr[3]) * r[6], bfhi(vr[3]) * r[7]);
        bf16x8 ui = pk8(bflo(vi[0]) * r[0], bfhi(vi[0]) * r[1], bflo(vi[1]) * r[2], bfhi(vi[1]) * r[3],
                        bflo(vi[2]) * r[4], bfhi(vi[2]) * r[5], bflo(vi[3]) * r[6], bfhi(vi[3]) * r[7]);
        if (j == 304) ui = asbf(u32x4{0u, 0u, 0u, 0u});
        const int o = img128(tid >> 3, tid & 7);
        *(bf16x8*)(Ure + o) = ur; *(bf16x8*)(Uim + o) = ui;
        *(u32x4*)(Rre + o) = vr; *(u32x4*)(Rim + o) = vi;
    }
    __syncthreads();
    STAMP(a.layer ? 4 : 1, 2);
    const f32x4 z = {0.f, 0.f, 0.f, 0.f};
    f32x4 dr[2], di[2];
#pragma unroll
    for (int mt = 0; mt < 2; ++mt) {
        dr[mt] = z; di[mt] = z;
#pragma unroll
        for (int ks = 0; ks < 2; ++ks) {
            const int o = img128(16 * mt + n, 4 * ks + q);
            const bf16x8 ar = *(const bf16x8*)(Ure + o), ai = *(const bf16x8*)(Uim + o);
            dr[mt] = mfma16(ar, c64[ks], dr[mt]); dr[mt] = mfma16(ai, s64p[ks], dr[mt]);
            di[mt] = mfma16(ar, s64m[ks], di[mt]); di[mt] = mfma16(ai, c64[ks], di[mt]);
        }
    }
    float tr[8], ti[8];
#pragma unroll
    for (int i = 0; i < 8; ++i) {
        const float x = dr[i >> 2][i & 3], y = di[i >> 2][i & 3];
        tr[i] = x * twr[i] - y * twi[i]; ti[i] = x * twi[i] + y * twr[i];
    }
    bf16x8 Tr = pk8(tr), Ti = pk8(ti);
    s32m[0] = neg8(s32p[0]); s32m[1] = neg8(s32p[1]);
    f32x4 orr[2], oi[2];
#pragma unroll
    for (int t = 0; t < 2; ++t) {
        orr[t] = mfma16(c32[t], Tr, z); orr[t] = mfma16(s32p[t], Ti, orr[t]);
        oi[t] = mfma16(s32m[t], Tr, z); oi[t] = mfma16(c32[t], Ti, oi[t]);
    }
#pragma unroll
    for (int i = 0; i < 8; ++i) {
        const float x = orr[i >> 2][i & 3], y = oi[i >> 2][i & 3];
        const u32 wv = wf[i >> 2][i & 3];
        const float wre = bflo(wv), wim = bfhi(wv);
        tr[i] = x * wre - y * wim; ti[i] = x * wim + y * wre;
    }
    Tr = pk8(tr); Ti = pk8(ti);
#pragma unroll
    for (int nt = 0; nt < 2; ++nt) {
        f32x4 zr = mfma16(Tr, c32[nt], z); zr = mfma16(Ti, s32m[nt], zr);
        f32x4 zi = mfma16(Tr, s32p[nt], z); zi = mfma16(Ti, c32[nt], zi);
        float er[4], ei[4];
#pragma unroll
        for (int r = 0; r < 4; ++r) {
            const float cr = ivr[4 * nt + r], ci = ivi[4 * nt + r];
            er[r] = zr[r] * cr - zi[r] * ci; ei[r] = zr[r] * ci + zi[r] * cr;
        }
        const int o = img128(16 * nt + n, 2 * w + (q >> 1)) + (q & 1) * 8;
        u32x2 pr = {pk2(er[0], er[1]), pk2(er[2], er[3])}, pi = {pk2(ei[0], ei[1]), pk2(ei[2], ei[3])};
        *(u32x2*)(Ere + o) = pr; *(u32x2*)(Eim + o) = pi;
    }
    __syncthreads();
    STAMP(a.layer ? 4 : 1, 3);
    u16* yr_ = a.xout + (size_t)(2 * j) * S_; u16* yi_ = yr_ + S_;
    char* O = smem + 49152 + half * 8192;
#pragma unroll
    for (int nt = 0; nt < 2; ++nt) {
        f32x4 yr = z, yi = z;
#pragma unroll
        for (int ks = 0; ks < 2; ++ks) {
            const int o = img128(16 * nt + n, 4 * ks + q);
            const bf16x8 br = *(const bf16x8*)(Ere + o), bi = *(const bf16x8*)(Eim + o);
            yr = mfma16(c64[ks], br, yr); yr = mfma16(s64m[ks], bi, yr);
            yi = mfma16(s64p[ks], br, yi); yi = mfma16(c64[ks], bi, yi);
        }
        if (j == 304) yi = z;
        const int p = (16 * nt + n) * 64 + 16 * w + 4 * q;
        u32x2 rr[2], ri[2];
        { const int o = img128(16 * nt + n, 2 * w + (q >> 1)) + (q & 1) * 8; rr[nt] = *(const u32x2*)(Rre + o); ri[nt] = *(const u32x2*)(Rim + o); }
        const float fr[4] = {bflo(rr[nt][0]) + yr[0], bfhi(rr[nt][0]) + yr[1], bflo(rr[nt][1]) + yr[2], bfhi(rr[nt][1]) + yr[3]};
        const float fi[4] = {bflo(ri[nt][0]) + yi[0], bfhi(ri[nt][0]) + yi[1], bflo(ri[nt][1]) + yi[2], bfhi(ri[nt][1]) + yi[3]};
        if (a.layer == 0) {
            u32x2 orv = {pk2(fr[0], fr[1]), pk2(fr[2], fr[3])}, oiv = {pk2(fi[0], fi[1]), pk2(fi[2], fi[3])};
            *(u32x2*)(yr_ + p) = orv; *(u32x2*)(yi_ + p) = oiv;
        }
        u32x4 ov = {pk2(fr[0], fi[0]), pk2(fr[1], fi[1]), pk2(fr[2], fi[2]), pk2(fr[3], fi[3])};
        *(u32x4*)(O + p * 4) = ov;
    }
    __syncthreads();
    {
        const int c0 = 4 * b;
        char* dst = a.xf + ((c0 >> 5) * 64 + ((c0 & 31) >> 3) * 16) * 16 + (c0 & 4) * 2;
        const u32* O0 = (const u32*)(smem + 49152); const u32* O1 = O0 + 2048;
#pragma unroll
        for (int i = 0; i < 4; ++i) {
            const int p = threadIdx.x + 512 * i;
            u32x2 v = {O0[p], O1[p]};
            *(u32x2*)(dst + (p >> 4) * 20480 + (p & 15) * 16) = v;
        }
    }
    STAMP(a.layer ? 4 : 1, 4);
}

__global__ __launch_bounds__(512, 2) void k_col(ColArgs a) {
    extern __shared__ __attribute__((aligned(16))) char smem[];
    const int b = blockIdx.x;
    STAMP(a.layer ? 4 : 1, 0);
    if (b < 160) col_task(a, b, smem);
    else if (a.layer == 0) {
        if (b < 160 + 54) task_w2(a.p, b - 160, smem, true);
        else task_b(a.p, smem);
    } else {
        if (b < 160 + 54) task_w2(a.p, b - 160, smem, false);
        else task_Q(a.p, b - 214);
    }
    END_STAMP(a.layer ? 4 : 1);
}

struct G1Args { const char* xf; const char* w1f; const u16* b1; char* h; char* x3; };

DI float gelu_tanh(float x) {
    const float c1 = -2.302208198f, c2 = -0.1029432397f;
    const float t = x * (c1 + c2 * x * x);
    return x * __builtin_amdgcn_rcpf(1.0f + __builtin_amdgcn_exp2f(t));
}

DI f32x2 gelu_tanh2(f32x2 x) {
    const f32x2 c1 = {-2.302208198f, -2.302208198f}, c2 = {-0.1029432397f, -0.1029432397f}, one = {1.0f, 1.0f};
    const f32x2 t = x * (x * x * c2 + c1);
    f32x2 e = {__builtin_amdgcn_exp2f(t[0]), __builtin_amdgcn_exp2f(t[1])};
    e += one;
    const f32x2 r = {__builtin_amdgcn_rcpf(e[0]), __builtin_amdgcn_rcpf(e[1])};
    return x * r;
}
template <int N> DI void wait_vm() { asm volatile("s_waitcnt vmcnt(%0)" ::"n"(N) : "memory"); }
DI void dma16(u32x4 srd, int voff, int soff, u32 ldsaddr) {
    asm volatile("s_mov_b32 m0, %0\n\ts_nop 0\n\tbuffer_load_dwordx4 %1, %2, %3 offen lds" ::"s"(ldsaddr), "v"(voff), "s"(srd), "s"(soff) : "memory", "m0");
}
template <int LIM> DI void dma16_lim(u32x4 srd, int voff, int soff, u32 ldsaddr, int w) {
    asm volatile("s_cmp_lt_i32 %4, %5\n\ts_cbranch_scc0 .Lskip%=\n\ts_mov_b32 m0, %0\n\ts_nop 0\n\tbuffer_load_dwordx4 %1, %2, %3 offen lds\n.Lskip%=:"
                 ::"s"(ldsaddr), "v"(voff), "s"(srd), "s"(soff), "s"(w), "n"(LIM) : "memory", "m0", "scc");
}
DI u32x4 mk_srd(const void* p, size_t bytes) {
    const u64 a = (u64)p;
    u32x4 r = {(u32)a, (u32)(a >> 32) & 0xffffu, (u32)bytes, 0x00020000u};
    return r;
}

#define G1_P 4
#define G1_PW 5
template <int I, int N, class F> DI void static_for(F&& f) {
    if constexpr (I < N) { f(std::integral_constant<int, I>{}); static_for<I + 1, N>(f); }
}
constexpr int g1_younger(int ssync, int t, int P, int PW, int nlo) {
    int cnt = 0; bool seen = false;
    for (int i = 0; i < (P > PW ? P : PW); ++i) {
        if (i < PW) { if (seen) cnt += nlo; if (i == t) seen = true; }
        if (i < P && seen) cnt += 2;
    }
    for (int s = 0; s < ssync; ++s) {
        if (s + PW < 10) { if (seen) cnt += nlo; if (s + PW == t) seen = true; }
        if (s + P < 10 && seen) cnt += 2;
    }
    return cnt;
}
template <int NF, bool LAST, int KG>
DI void g1_body(const G1Args& a, char* smem, int w, int lane, int pt, int u0, int nu, int rot) {
    constexpr int STG = NF * 2048, NST = LAST ? 8 : 10, P = G1_P, PW = G1_PW;
    constexpr int WP = 2 * NF, WR = (WP + 7) / 8, NLO = WP / 8, KID = LAST ? 5 : 2;
    const int pq = w & 3, g = lane >> 4;
    u32x2 bv[NF];
#pragma unroll
    for (int i = 0; i < NF; ++i) bv[i] = *(const u32x2*)(a.b1 + min(16 * (u0 + i), F_ - 16) + 4 * g);
    const u32x4 srdW = mk_srd(a.w1f, W1F_BYTES);
    const u32 lds0 = (u32)(size_t)(LAS char*)smem;
    const rsrc_t rx = mk_rsrc(a.xf, XT_BYTES);
    const int xs = (8 * pt + 2 * pq) * 20480 + KG * 1024, vo = lane * 16;
    auto sr = [&](int st) { const int t = st + rot; return t >= 10 ? t - 10 : t; };
    auto issuew = [&](int st) {
        const u32 base = lds0 + (st % NST) * STG;
        const int so = sr(st) * 2048;
#pragma unroll
        for (int i = 0; i < WR; ++i) {
            const int pc = w + 8 * i;
            if (8 * i + 7 < WP) dma16(srdW, vo, (u0 + (pc >> 1)) * 20480 + so + (pc & 1) * 1024, base + pc * 1024);
            else dma16_lim<(WP & 7)>(srdW, vo, (u0 + (pc >> 1)) * 20480 + so + (pc & 1) * 1024, base + pc * 1024, w);
        }
    };
    const u32 dcm = (KG == 1 && g == 0) ? 0x0000ffffu : 0xffffffffu;
    bf16x8 xb[P][2];
#pragma unroll
    for (int st = 0; st < (P > PW ? P : PW); ++st) {
        if (st < PW) issuew(st);
        __builtin_amdgcn_sched_barrier(0);
        if (st < P) {
#pragma unroll
            for (int pu = 0; pu < 2; ++pu) xb[st][pu] = asbf(bload16(rx, vo, xs + pu * 20480 + sr(st) * 2048));
        }
        __builtin_amdgcn_sched_barrier(0);
    }
    f32x4 acc[NF][2];
#pragma unroll
    for (int i = 0; i < NF; ++i) { acc[i][0] = f32x4{0, 0, 0, 0}; acc[i][1] = f32x4{0, 0, 0, 0}; }
    f32x4 gr[2] = {f32x4{0, 0, 0, 0}, f32x4{0, 0, 0, 0}};
    bf16x8 wa[2][NF];
    auto reads = [&](int st) {
        const char* base = smem + (st % NST) * STG + KG * 1024 + lane * 16;
#pragma unroll
        for (int i = 0; i < NF; ++i) wa[st & 1][i] = *(const bf16x8*)(base + i * 2048);
    };
    wait_vm<g1_younger(0, 1, P, PW, NLO)>();
    __builtin_amdgcn_s_barrier();
    STAMP(KID, 1);
    CSTAMP(KID, 10);
    reads(0);
    auto dmaw = [&](int st, int i) __attribute__((always_inline)) {
        const int pc = w + 8 * i;
        const u32 dst = lds0 + (st % NST) * STG + pc * 1024;
        const int so = (u0 + (pc >> 1)) * 20480 + sr(st) * 2048 + (pc & 1) * 1024;
        if (8 * i + 7 < WP) dma16(srdW, vo, so, dst); else dma16_lim<(WP & 7)>(srdW, vo, so, dst, w);
    };
    static_for<0, 10>([&](auto ST) __attribute__((always_inline)) {
        constexpr int st = decltype(ST)::value;
        if constexpr (st == 1 || st == 4 || st == 7) {
            asm volatile("s_waitcnt lgkmcnt(0)" ::: "memory");
            wait_vm<g1_younger(st, st == 7 ? 9 : st + 3, P, PW, NLO)>();
            __builtin_amdgcn_s_barrier();
        }
        bf16x8 nx[2];
        const char* rbase = smem + ((st + 1) % NST) * STG + KG * 1024 + lane * 16;
        constexpr int NV = WR + 2, V0 = KG == 0 ? 0 : NF - NV;
        static_for<0, NF>([&](auto I) __attribute__((always_inline)) {
            constexpr int i = decltype(I)::value;
            __builtin_amdgcn_sched_barrier(0);
            acc[i][0] = mfma16(wa[st & 1][i], xb[st % P][0], acc[i][0]);
            if constexpr (st + 1 < 10) wa[(st + 1) & 1][i] = *(const bf16x8*)(rbase + i * 2048);
            acc[i][1] = mfma16(wa[st & 1][i], xb[st % P][1], acc[i][1]);
            if constexpr (i < 2) {
                u32x4 xv = __builtin_bit_cast(u32x4, xb[st % P][i]);
                xv[0] &= sr(st) == 9 ? dcm : 0xffffffffu;
                gr[i] = mfma16(asbf(xv), asbf(xv), gr[i]);
            }
            __builtin_amdgcn_sched_barrier(0);
            constexpr int v = i - V0;
            if constexpr (v >= 0 && v < WR) { if constexpr (st + PW < 10) dmaw(st + PW, v); }
            else if constexpr (v >= WR && v < NV) { if constexpr (st + P < 10) nx[v - WR] = asbf(bload16(rx, vo, xs + (v - WR) * 20480 + sr(st + P) * 2048)); }
        });
        __builtin_amdgcn_sched_barrier(0);
        if constexpr (st + P < 10) { xb[st % P][0] = nx[0]; xb[st % P][1] = nx[1]; }
        if (st == 4) STAMP(KID, 2);
    });
    STAMP(KID, 3);
    CSTAMP(KID, 11);
    asm volatile("s_waitcnt lgkmcnt(0)" ::: "memory");
    __builtin_amdgcn_s_barrier();
    float* sx = (float*)(smem + 8 * NF * 512);
#pragma unroll
    for (int i = 0; i < NF; ++i) {
        const f32x4 t = acc[i][1 - KG];
        u32x2 pv = {pk2(t[0], t[1]), pk2(t[2], t[3])};
        *(u32x2*)(smem + ((w * NF + i) * 64 + lane) * 8) = pv;
    }
    float st2[2];
    {
        const int n = lane & 15;
#pragma unroll
        for (int pu = 0; pu < 2; ++pu) {
            const float d01 = (n & 1) ? gr[pu][1] : gr[pu][0], d23 = (n & 1) ? gr[pu][3] : gr[pu][2];
            float d = (n & 2) ? d23 : d01;
            d = (g == (n >> 2)) ? d : 0.f;
            d += __shfl_xor(d, 16); d += __shfl_xor(d, 32);
            st2[pu] = d;
        }
    }
    if (lane < 16) sx[w * 16 + lane] = st2[1 - KG];
    __syncthreads();
    STAMP(KID, 5);
    CSTAMP(KID, 12);
    const float v = (st2[KG] + sx[(w ^ 4) * 16 + (lane & 15)]) * (1.0f / 640.0f);
    const float r1 = __builtin_amdgcn_rsqf(v + EPS);
    const float rr = r1 * __builtin_amdgcn_rsqf(v * r1 * r1 + EPS);
    const int punit = 8 * pt + 2 * pq + KG;
    u32x2 ov[NF], pv[NF];
#pragma unroll
    for (int i = 0; i < NF; ++i) pv[i] = *(const u32x2*)(smem + (((w ^ 4) * NF + i) * 64 + lane) * 8);
#pragma unroll
    for (int i = 0; i < NF; ++i) {
        const f32x2 o0 = {acc[i][KG][0] + bflo(pv[i][0]), acc[i][KG][1] + bfhi(pv[i][0])}, o1 = {acc[i][KG][2] + bflo(pv[i][1]), acc[i][KG][3] + bfhi(pv[i][1])};
        const f32x2 rr2 = {rr, rr};
        const f32x2 b0 = {bflo(bv[i][0]), bfhi(bv[i][0])}, b1 = {bflo(bv[i][1]), bfhi(bv[i][1])};
        f32x2 h0 = gelu_tanh2(o0 * rr2 + b0), h1 = gelu_tanh2(o1 * rr2 + b1);
        if (LAST) {
            const bool lin = u0 + i >= 108;
            h0[0] = lin ? o0[0] : h0[0]; h0[1] = lin ? o0[1] : h0[1]; h1[0] = lin ? o1[0] : h1[0]; h1[1] = lin ? o1[1] : h1[1];
        }
        ov[i] = u32x2{pk2(h0[0], h0[1]), pk2(h1[0], h1[1])};
    }
    char* hb = a.h + ((size_t)punit * 54 * 64 + lane) * 16;
    auto st1 = [&](int i) __attribute__((always_inline)) {
        const int u = u0 + i;
        if (i < nu) {
            if (!LAST || u < 108) *(u32x2*)(hb + (u >> 1) * 1024 + (u & 1) * 8) = ov[i];
            else *(u32x2*)(a.x3 + ((size_t)(punit * 40 + (u - 108)) * 64 + lane) * 8) = ov[i];
        }
    };
    auto st2p = [&](int i) __attribute__((always_inline)) {
        const int u = u0 + i;
        if (i + 1 < nu && (!LAST || u + 1 < 108)) { u32x4 q4 = {ov[i][0], ov[i][1], ov[i + 1][0], ov[i + 1][1]}; *(u32x4*)(hb + (u >> 1) * 1024) = q4; }
        else { st1(i); st1(i + 1); }
    };
    if (u0 & 1) {
        st1(0);
#pragma unroll
        for (int i = 1; i + 1 < NF; i += 2) st2p(i);
        if ((NF & 1) == 0) st1(NF - 1);
    } else {
#pragma unroll
        for (int i = 0; i + 1 < NF; i += 2) st2p(i);
        if (NF & 1) st1(NF - 1);
    }
    STAMP(KID, 4);
}

template <int NF, bool LAST>
__global__ __launch_bounds__(512, 2) void k_g1(G1Args a) {
    extern __shared__ __attribute__((aligned(16))) char smem[];
    const int lane = threadIdx.x & 63;
    const int w = __builtin_amdgcn_readfirstlane(threadIdx.x >> 6);
    const int x = blockIdx.x & 7, ib = blockIdx.x >> 3;
    const int pt = 4 * (x >> 1) + (ib & 3), ft = 8 * (x & 1) + (ib >> 2);
    int u0;
    if (LAST) u0 = ft < 4 ? 10 * ft : 40 + 9 * (ft - 4); else u0 = ft < 12 ? 7 * ft : 84 + 6 * (ft - 12);
    const int nu = LAST ? (ft < 4 ? 10 : 9) : (ft < 12 ? 7 : 6);
    STAMP(LAST ? 5 : 2, 0);
    const int rot = 0;
    if (w < 4) g1_body<NF, LAST, 0>(a, smem, w, lane, pt, u0, nu, rot);
    else g1_body<NF, LAST, 1>(a, smem, w, lane, pt, u0, nu, rot);
    END_STAMP(LAST ? 5 : 2);
}

struct G2Args { const char* h; const char* w2; const u16* xin; u16* xout; const float* b2p; const u16* b2; const char* x3; u16* out; u64* ss; };

#define G2_PD 4
template <bool LAST>
__global__ __launch_bounds__(256, 1) void k_g2(G2Args a) {
    extern __shared__ __attribute__((aligned(16))) char smem[];
    constexpr int PD = G2_PD, KID = LAST ? 6 : 3;
    const int tid = threadIdx.x, lane = tid & 63;
    const int w = __builtin_amdgcn_readfirstlane(tid >> 6);
    const int x = blockIdx.x & 7, ib = blockIdx.x >> 3;
    const int pt = 8 * (x >> 1) + (ib & 7), ct = 4 * (x & 1) + (ib >> 3);
    const int kb = w < 2 ? 14 * w : 28 + 13 * (w - 2), nk = w < 2 ? 14 : 13;
    const int n = lane & 15, q = lane >> 4;
    STAMP(KID, 0);
    u32x2 xv[5]; u32x2 bv[5]; float bb[5];
#pragma unroll
    for (int cu = 0; cu < 5; ++cu) {
        if (!LAST) {
            const int c = 80 * ct + 16 * cu + n;
            xv[cu] = *(const u32x2*)(a.xin + (size_t)c * S_ + 64 * pt + 16 * w + 4 * q);
            bb[cu] = a.b2p[c];
        } else {
            xv[cu] = *(const u32x2*)(a.x3 + ((size_t)((4 * pt + w) * 40 + 5 * ct + cu) * 64 + lane) * 8);
            bv[cu] = *(const u32x2*)(a.b2 + 80 * ct + 16 * cu + 4 * q);
        }
    }
    const rsrc_t rh = mk_rsrc(a.h, H_BYTES), rw = mk_rsrc(a.w2, W2F_BYTES);
    const int hs = (4 * pt * 54 + kb) * 1024, wsb = (5 * ct * 54 + kb) * 1024, vo = lane * 16;
    bf16x8 ha[PD][4], wb[PD][5];
    const int rot = 0;
    auto kof = [&](int i) { if (i >= 13) return nk - 1; const int t = i + rot; return t >= 13 ? t - 13 : t; };
    auto load = [&](int i) {
        const int k = kof(i);
#pragma unroll
        for (int pu = 0; pu < 4; ++pu) ha[i % PD][pu] = asbf(bload16(rh, vo, hs + (pu * 54 + k) * 1024));
#pragma unroll
        for (int cu = 0; cu < 5; ++cu) wb[i % PD][cu] = asbf(bload16(rw, vo, wsb + (cu * 54 + k) * 1024));
    };
#pragma unroll
    for (int i = 0; i < PD; ++i) { __builtin_amdgcn_sched_barrier(0); load(i); }
    __builtin_amdgcn_sched_barrier(0);
    STAMP(KID, 5);
    f32x4 acc[4][5];
#pragma unroll
    for (int pu = 0; pu < 4; ++pu)
#pragma unroll
        for (int cu = 0; cu < 5; ++cu) acc[pu][cu] = f32x4{0, 0, 0, 0};
#pragma unroll
    for (int i = 0; i < 14; ++i) {
        __builtin_amdgcn_sched_barrier(0);
        if (i < 13) {
            bf16x8 na[4], nb[5];
            if (i + PD < 14) {
                const int k = kof(i + PD);
#pragma unroll
                for (int pu = 0; pu < 4; ++pu) na[pu] = asbf(bload16(rh, vo, hs + (pu * 54 + k) * 1024));
#pragma unroll
                for (int cu = 0; cu < 5; ++cu) nb[cu] = asbf(bload16(rw, vo, wsb + (cu * 54 + k) * 1024));
            }
#pragma unroll
            for (int pu = 0; pu < 4; ++pu)
#pragma unroll
                for (int cu = 0; cu < 5; ++cu)
                    acc[pu][cu] = LAST ? mfma16(wb[i % PD][cu], ha[i % PD][pu], acc[pu][cu]) : mfma16(ha[i % PD][pu], wb[i % PD][cu], acc[pu][cu]);
            if (i + PD < 14) {
#pragma unroll
                for (int t = 0; t < 9; ++t) {
                    __builtin_amdgcn_sched_group_barrier(0x008, 2, 0);
                    __builtin_amdgcn_sched_group_barrier(0x020, 1, 0);
                }
            }
            __builtin_amdgcn_sched_barrier(0);
            if (i + PD < 14) {
#pragma unroll
                for (int pu = 0; pu < 4; ++pu) ha[i % PD][pu] = na[pu];
#pragma unroll
                for (int cu = 0; cu < 5; ++cu) wb[i % PD][cu] = nb[cu];
            }
        } else if (nk == 14) {
#pragma unroll
            for (int pu = 0; pu < 4; ++pu)
#pragma unroll
                for (int cu = 0; cu < 5; ++cu)
                    acc[pu][cu] = LAST ? mfma16(wb[i % PD][cu], ha[i % PD][pu], acc[pu][cu]) : mfma16(ha[i % PD][pu], wb[i % PD][cu], acc[pu][cu]);
        }
        if (i == 0) STAMP(KID, 1);
        if (i == 7) STAMP(KID, 2);
    }
    STAMP(KID, 3);
#pragma unroll
    for (int pu = 0; pu < 4; ++pu) {
        if (pu != w) {
#pragma unroll
            for (int cu = 0; cu < 5; ++cu) *(f32x4*)(smem + (((w * 4 + pu) * 5 + cu) * 64 + lane) * 16) = acc[pu][cu];
        }
    }
    __syncthreads();
    f32x4 o[5];
#pragma unroll
    for (int cu = 0; cu < 5; ++cu) {
        o[cu] = w == 0 ? acc[0][cu] : w == 1 ? acc[1][cu] : w == 2 ? acc[2][cu] : acc[3][cu];
#pragma unroll
        for (int s = 1; s < 4; ++s) o[cu] += *(const f32x4*)(smem + (((((w + s) & 3) * 4 + w) * 5 + cu) * 64 + lane) * 16);
    }
    STAMP(KID, 4);
    if (!LAST) {
        float ss[4] = {0.f, 0.f, 0.f, 0.f};
        const int p = 64 * pt + 16 * w + 4 * q;
#pragma unroll
        for (int cu = 0; cu < 5; ++cu) {
            const int c = 80 * ct + 16 * cu + n;
            const u32 o0 = pk2(bflo(xv[cu][0]) + o[cu][0] + bb[cu], bfhi(xv[cu][0]) + o[cu][1] + bb[cu]);
            const u32 o1 = pk2(bflo(xv[cu][1]) + o[cu][2] + bb[cu], bfhi(xv[cu][1]) + o[cu][3] + bb[cu]);
            u32x2 ov = {o0, o1};
            *(u32x2*)(a.xout + (size_t)c * S_ + p) = ov;
            const float m = c == 609 ? 0.f : 1.f;
            ss[0] += m * bflo(o0) * bflo(o0); ss[1] += m * bfhi(o0) * bfhi(o0);
            ss[2] += m * bflo(o1) * bflo(o1); ss[3] += m * bfhi(o1) * bfhi(o1);
        }
#pragma unroll
        for (int r = 0; r < 4; ++r) {
#pragma unroll
            for (int m = 8; m >= 1; m >>= 1) ss[r] += __shfl_xor(ss[r], m);
        }
        if (n < 4) {
            const float sv = n == 0 ? ss[0] : n == 1 ? ss[1] : n == 2 ? ss[2] : ss[3];
            atomicAdd((unsigned long long*)(a.ss + p + n), ss_from_float(sv));
        }
    } else {
        const int p = 16 * (4 * pt + w) + n;
        u16* orow = a.out + (size_t)tok_of_p(p) * D_;
#pragma unroll
        for (int cu = 0; cu < 5; ++cu) {
            const int d = 80 * ct + 16 * cu + 4 * q;
            u32x2 ov = {pk2(bflo(xv[cu][0]) + o[cu][0] + bflo(bv[cu][0]), bfhi(xv[cu][0]) + o[cu][1] + bfhi(bv[cu][0])),
                        pk2(bflo(xv[cu][1]) + o[cu][2] + bflo(bv[cu][1]), bfhi(xv[cu][1]) + o[cu][3] + bfhi(bv[cu][1]))};
            *(u32x2*)(orow + d) = ov;
        }
    }
    END_STAMP(KID);
}

extern "C" void kernel_launch(void* const* d_in, const int* in_sizes, int n_in, void* d_out, int out_size, void* d_ws,
                              size_t ws_size, hipStream_t stream) {
    char* ws = (char*)d_ws;
    constexpr int G1_LDS = 10 * 7 * 2048, G1L_LDS = 8 * 10 * 2048, G2_LDS = 16 * 5 * 1024, COL_LDS = 65536;
    (void)hipFuncSetAttribute((const void*)k_g1<7, false>, hipFuncAttributeMaxDynamicSharedMemorySize, G1_LDS);
    (void)hipFuncSetAttribute((const void*)k_g1<10, true>, hipFuncAttributeMaxDynamicSharedMemorySize, G1L_LDS);
    (void)hipFuncSetAttribute((const void*)k_g2<false>, hipFuncAttributeMaxDynamicSharedMemorySize, G2_LDS);
    (void)hipFuncSetAttribute((const void*)k_g2<true>, hipFuncAttributeMaxDynamicSharedMemorySize, G2_LDS);

    PrepArgs pa{(const u16*)d_in[0], (const u16*)d_in[1], (const u16*)d_in[2], (const u16*)d_in[4], (const u16*)d_in[6], (const u16*)d_in[7], ws};
    k0_prep<<<NB_K0, 512, PREP_LDS, stream>>>(pa);

    ColArgs c1{pa, (const u16*)(ws + OFF_XT0), (u16*)(ws + OFF_XT1), ws + OFF_XF1, (const u64*)(ws + OFF_SS0), (const u32*)(ws + OFF_WT), 0, 0};
    k_col<<<NB_COL1, 512, COL_LDS, stream>>>(c1);
    G1Args g1{ws + OFF_XF1, ws + OFF_W1F, (const u16*)d_in[5], ws + OFF_H, ws + OFF_X3};
    k_g1<7, false><<<256, 512, G1_LDS, stream>>>(g1);
    G2Args g2{ws + OFF_H, ws + OFF_W2F, (const u16*)(ws + OFF_XT1), (u16*)(ws + OFF_XT2), (const float*)(ws + OFF_B2P),
              (const u16*)d_in[7], ws + OFF_X3, (u16*)d_out, (u64*)(ws + OFF_SS2)};
    k_g2<false><<<256, 256, G2_LDS, stream>>>(g2);

    ColArgs c2{pa, (const u16*)(ws + OFF_XT2), (u16*)(ws + OFF_XT3), ws + OFF_XF3, (const u64*)(ws + OFF_SS2), (const u32*)(ws + OFF_WT), 1, 0};
    k_col<<<NB_COL2, 512, COL_LDS, stream>>>(c2);
    G1Args g1b{ws + OFF_XF3, ws + OFF_W1F, (const u16*)d_in[5], ws + OFF_H2, ws + OFF_X3};
    k_g1<10, true><<<256, 512, G1L_LDS, stream>>>(g1b);
    G2Args g2b{ws + OFF_H2, ws + OFF_W2O, (const u16*)(ws + OFF_XT3), (u16*)(ws + OFF_XT2), (const float*)(ws + OFF_B2P),
               (const u16*)d_in[7], ws + OFF_X3, (u16*)d_out, (u64*)(ws + OFF_SS2)};
    k_g2<true><<<256, 256, G2_LDS, stream>>>(g2b);
}
```

```cpp
#include <hip/hip_runtime.h>
#include <hip/hip_bf16.h>
#include <stdint.h>
#include <type_traits>

typedef unsigned short u16;
typedef unsigned int u32;
typedef unsigned long long u64;
typedef short s16x4 __attribute__((ext_vector_type(4)));
typedef __bf16 bf16x8 __attribute__((ext_vector_type(8)));
typedef __bf16 bf16x2 __attribute__((ext_vector_type(2)));
typedef float f32x4 __attribute__((ext_vector_type(4)));
typedef float f32x2 __attribute__((ext_vector_type(2)));
typedef u32 u32x4 __attribute__((ext_vector_type(4)));
typedef u32 u32x2 __attribute__((ext_vector_type(2)));
typedef __amdgpu_buffer_rsrc_t rsrc_t;
#define LAS __attribute__((address_space(3)))
#define DI __device__ __forceinline__

constexpr int S_ = 2048, D_ = 640, F_ = 1728, DF_ = 321;
constexpr float EPS = 1e-5f;
constexpr float SS_SCALE = 1048576.0f;

constexpr size_t XT_BYTES = (size_t)D_ * S_ * 2;
constexpr size_t OFF_XT0 = 0;
constexpr size_t OFF_XT1 = OFF_XT0 + XT_BYTES;
constexpr size_t OFF_XT2 = OFF_XT1 + XT_BYTES;
constexpr size_t OFF_XT3 = OFF_XT2 + XT_BYTES;
constexpr size_t OFF_WT = OFF_XT3 + XT_BYTES;
constexpr size_t OFF_W1F = OFF_WT + (size_t)320 * 2048 * 4;
constexpr size_t W1F_BYTES = (size_t)148 * 20 * 1024;
constexpr size_t OFF_W2F = OFF_W1F + W1F_BYTES;
constexpr size_t W2F_BYTES = (size_t)40 * 54 * 1024;
constexpr size_t OFF_W2O = OFF_W2F + W2F_BYTES;
constexpr size_t OFF_H = OFF_W2O + W2F_BYTES;
constexpr size_t H_BYTES = (size_t)128 * 54 * 1024;
constexpr size_t OFF_H2 = OFF_H + H_BYTES;
constexpr size_t OFF_X3 = OFF_H2 + H_BYTES;
constexpr size_t OFF_SS0 = OFF_X3 + (size_t)128 * 40 * 512;
constexpr size_t OFF_SS2 = OFF_SS0 + 2048 * 8;
constexpr size_t OFF_B2P = OFF_SS2 + 2048 * 8;
constexpr size_t OFF_XF1 = OFF_B2P + 4096;
constexpr size_t OFF_XF3 = OFF_XF1 + XT_BYTES;
constexpr size_t X3_BYTES = (size_t)128 * 40 * 512;
constexpr size_t OFF_CNT = OFF_XF3 + XT_BYTES;
constexpr size_t WS_END = OFF_CNT + 16384;

#define STAMP(k, i)
#define CSTAMP(k, i)
#define END_STAMP(k)

DI f32x4 mfma16(bf16x8 a, bf16x8 b, f32x4 c) { return __builtin_amdgcn_mfma_f32_16x16x32_bf16(a, b, c, 0, 0, 0); }
DI u32 pk2(float a, float b) { f32x2 v = {a, b}; const bf16x2 r = __builtin_convertvector(v, bf16x2); return __builtin_bit_cast(u32, r); }
DI bf16x8 pk8(float a0, float a1, float a2, float a3, float a4, float a5, float a6, float a7) {
    u32x4 r = {pk2(a0, a1), pk2(a2, a3), pk2(a4, a5), pk2(a6, a7)};
    return __builtin_bit_cast(bf16x8, r);
}
DI bf16x8 pk8(const float (&a)[8]) { return pk8(a[0], a[1], a[2], a[3], a[4], a[5], a[6], a[7]); }
DI bf16x8 asbf(u32x4 r) { return __builtin_bit_cast(bf16x8, r); }
DI bf16x8 neg8(bf16x8 a) { u32x4 r = __builtin_bit_cast(u32x4, a); r ^= 0x80008000u; return __builtin_bit_cast(bf16x8, r); }
DI float bflo(u32 v) { return __builtin_bit_cast(float, v << 16); }
DI float bfhi(u32 v) { return __builtin_bit_cast(float, v & 0xffff0000u); }
DI float cosr(float rev) { return __builtin_amdgcn_cosf(rev); }
DI float sinr(float rev) { return __builtin_amdgcn_sinf(rev); }
DI int slot32(int q, int j) { return j < 4 ? 4 * q + j : 16 + 4 * q + (j - 4); }
DI s16x4 tr_read(const char* p) { return __builtin_amdgcn_ds_read_tr16_b64_v4i16((LAS s16x4*)p); }
DI bf16x8 cat8(s16x4 lo, s16x4 hi) { return __builtin_bit_cast(bf16x8, __builtin_shufflevector(lo, hi, 0, 1, 2, 3, 4, 5, 6, 7)); }
DI int tok_of_p(int p) { return 32 * (p & 63) + (p >> 6); }
DI rsrc_t mk_rsrc(const void* p, size_t bytes) { return __builtin_amdgcn_make_buffer_rsrc((void*)p, 0, (int)bytes, 0x00020000); }
DI u32x4 bload16(rsrc_t r, int voff, int soff) { return __builtin_amdgcn_raw_buffer_load_b128(r, voff, soff, 0); }
DI float ss_to_float(u64 v) { return (float)(u32)(v >> 32) * 4294967296.0f + (float)(u32)v; }
DI u64 ss_from_float(float s) { return (u64)(fminf(s * SS_SCALE, 1.0e18f) + 0.5f); }
DI void pin(float& x) { asm volatile("" : "+v"(x)); }
DI void pin(bf16x8& x) { asm volatile("" : "+v"(x)); }

struct F32c { bf16x8 c[2], sp[2]; };
DI void f32c_init(F32c& k, int lane) {
    const float m = (float)(lane & 15), q4 = (float)(4 * (lane >> 4));
    float c[4], s[4];
#pragma unroll
    for (int j = 0; j < 4; ++j) { const float a = __builtin_amdgcn_fractf(m * (q4 + (float)j) * (1.0f / 32.0f)); c[j] = cosr(a); s[j] = sinr(a); }
    const u32 sg = (lane & 1) ? 0x80008000u : 0u;
    const u32 c0 = pk2(c[0], c[1]), c1 = pk2(c[2], c[3]), s0 = pk2(s[0], s[1]), s1 = pk2(s[2], s[3]);
    const u32x4 cv = {c0, c1, c0 ^ sg, c1 ^ sg}, sv = {s0, s1, s0 ^ sg, s1 ^ sg};
    k.c[0] = asbf(cv); k.sp[0] = asbf(sv);
    k.c[1] = asbf(cv ^ 0x80000000u); k.sp[1] = asbf(sv ^ 0x80000000u);
}

struct RowC { bf16x8 c[2], sp[2], sm[2], c20, s20m; f32x4 twr[2], twi[2]; int off0, off1; };
constexpr int ROWC_LDS = 10 * 1024;
DI void rowc_build(char* tab, int w, int lane) {
    const int n = lane & 15, q = lane >> 4;
    const float nf = (float)n;
    u32x4* t = (u32x4*)tab + lane;
    if (w == 0) {
        F32c f; f32c_init(f, lane);
        t[0] = __builtin_bit_cast(u32x4, f.c[0]); t[64] = __builtin_bit_cast(u32x4, f.c[1]);
        t[128] = __builtin_bit_cast(u32x4, f.sp[0]); t[192] = __builtin_bit_cast(u32x4, f.sp[1]);
    } else if (w <= 2) {
        float c[8];
#pragma unroll
        for (int j = 0; j < 8; ++j) {
            const int d1 = 8 * q + j;
            const float a = __builtin_amdgcn_fractf((float)d1 * nf * 0.05f);
            const float v = w == 1 ? cosr(a) : -sinr(a);
            c[j] = (d1 < 20 && n <= 10) ? v : 0.f;
        }
        t[(3 + w) * 64] = __builtin_bit_cast(u32x4, pk8(c));
    } else if (w <= 4) {
        const float sc = 0.05590169943749474f;
        f32x4 cr, ci;
#pragma unroll
        for (int i = 0; i < 4; ++i) {
            const float a = (float)(16 * (w - 3) + 4 * q + i) * nf * (1.0f / 640.0f);
            cr[i] = cosr(a) * sc; ci[i] = -sinr(a) * sc;
        }
        t[(6 + w - 3) * 64] = __builtin_bit_cast(u32x4, cr); t[(8 + w - 3) * 64] = __builtin_bit_cast(u32x4, ci);
    }
}
DI void rowc_load(RowC& k, const char* tab, int lane) {
    const u32x4* tb = (const u32x4*)tab + lane;
    k.c[0] = asbf(tb[0]); k.c[1] = asbf(tb[64]); k.sp[0] = asbf(tb[128]); k.sp[1] = asbf(tb[192]);
    k.c20 = asbf(tb[256]); k.s20m = asbf(tb[320]);
    k.twr[0] = __builtin_bit_cast(f32x4, tb[384]); k.twr[1] = __builtin_bit_cast(f32x4, tb[448]);
    k.twi[0] = __builtin_bit_cast(f32x4, tb[512]); k.twi[1] = __builtin_bit_cast(f32x4, tb[576]);
    k.sm[0] = neg8(k.sp[0]); k.sm[1] = neg8(k.sp[1]);
    const int q = lane >> 4, qq = (lane & 15) >> 2, pp = lane & 3;
    const int r0 = min(8 * q + qq, 19), r1 = min(8 * q + 4 + qq, 19);
    k.off0 = r0 * 64 + pp * 8; k.off1 = r1 * 64 + pp * 8;
}
DI float wave_sum63(float v) {
    v += __builtin_bit_cast(float, __builtin_amdgcn_update_dpp(0, __builtin_bit_cast(int, v), 0x111, 0xf, 0xf, true));
    v += __builtin_bit_cast(float, __builtin_amdgcn_update_dpp(0, __builtin_bit_cast(int, v), 0x112, 0xf, 0xf, true));
    v += __builtin_bit_cast(float, __builtin_amdgcn_update_dpp(0, __builtin_bit_cast(int, v), 0x114, 0xf, 0xf, true));
    v += __builtin_bit_cast(float, __builtin_amdgcn_update_dpp(0, __builtin_bit_cast(int, v), 0x118, 0xf, 0xf, true));
    v += __builtin_bit_cast(float, __builtin_amdgcn_update_dpp(0, __builtin_bit_cast(int, v), 0x142, 0xa, 0xf, true));
    v += __builtin_bit_cast(float, __builtin_amdgcn_update_dpp(0, __builtin_bit_cast(int, v), 0x143, 0xc, 0xf, true));
    return v;
}
DI void rowfft(const RowC& k, const char* row, float (&A)[8], float (&B)[8], int lane) {
    f32x4 z = {0.f, 0.f, 0.f, 0.f};
    f32x4 yr[2], yi[2];
#pragma unroll
    for (int mt = 0; mt < 2; ++mt) {
        const bf16x8 a = cat8(tr_read(row + k.off0 + mt * 32), tr_read(row + k.off1 + mt * 32));
        yr[mt] = mfma16(a, k.c20, z); yi[mt] = mfma16(a, k.s20m, z);
    }
    float tr[8], ti[8];
#pragma unroll
    for (int i = 0; i < 8; ++i) {
        const float a = yr[i >> 2][i & 3], b = yi[i >> 2][i & 3], cr = k.twr[i >> 2][i & 3], ci = k.twi[i >> 2][i & 3];
        tr[i] = a * cr - b * ci; ti[i] = a * ci + b * cr;
    }
    const bf16x8 Tr = pk8(tr), Ti = pk8(ti);
    f32x4 ore[2], oim[2];
#pragma unroll
    for (int t = 0; t < 2; ++t) {
        ore[t] = mfma16(k.c[t], Tr, z); ore[t] = mfma16(k.sp[t], Ti, ore[t]);
        oim[t] = mfma16(k.sm[t], Tr, z); oim[t] = mfma16(k.c[t], Ti, oim[t]);
    }
#pragma unroll
    for (int r = 0; r < 4; ++r) { A[2 * r] = ore[0][r]; A[2 * r + 1] = oim[0][r]; B[2 * r] = ore[1][r]; B[2 * r + 1] = oim[1][r]; }
    if (lane == 0) { const float h = 0.7071067811865476f; const float dc = A[0] * h; A[0] = B[0] * h; A[1] = dc; }
}
DI int chunk_c0(int n, int q) { return (n == 0 ? 608 : n == 10 ? 576 : 64 * (n - 1)) + 8 * q; }

constexpr int ROWST = 1344;
constexpr int PREP_TAB = 32 * ROWST;
constexpr int PREP_LDS = PREP_TAB + ROWC_LDS;

struct PrepArgs { const u16 *x, *wr, *wi, *w1, *w2, *b2; char* ws; };

DI void task_x(const PrepArgs& a, int b, char* smem) {
    const int tid = threadIdx.x, lane = tid & 63, w = tid >> 6;
    const int p0 = 16 * ((b & 7) * 16 + (b >> 3));
    u32x4 v[3];
#pragma unroll
    for (int i = 0; i < 3; ++i) {
        const int id = min(tid + 512 * i, 1279), r = id / 80, ch = id % 80;
        v[i] = *(const u32x4*)(a.x + (size_t)tok_of_p(p0 + r) * D_ + ch * 8);
    }
    __builtin_amdgcn_sched_barrier(0);
    rowc_build(smem + PREP_TAB, w, lane);
    __builtin_amdgcn_sched_barrier(0);
    STAMP(0, 1);
    if (tid < 16) ((u64*)(a.ws + OFF_SS2))[p0 + tid] = 0ull;
    if (b < 8) ((u32*)(a.ws + OFF_CNT))[b * 512 + tid] = 0u;
#pragma unroll
    for (int i = 0; i < 3; ++i) {
        const int id = min(tid + 512 * i, 1279), r = id / 80, ch = id % 80;
        *(u32x4*)(smem + r * ROWST + ch * 16) = v[i];
    }
    STAMP(0, 2);
    __syncthreads();
    STAMP(0, 3);
    RowC k; rowc_load(k, smem + PREP_TAB, lane);
    const int n = lane & 15, q = lane >> 4, c0 = chunk_c0(n, q);
    float A[2][8], B[2][8];
#pragma unroll
    for (int i = 0; i < 2; ++i) rowfft(k, smem + (2 * w + i) * ROWST, A[i], B[i], lane);
#pragma unroll
    for (int i = 0; i < 2; ++i) {
        const int r = 2 * w + i;
        float ss = 0.f;
        if (n <= 10) {
#pragma unroll
            for (int e = 0; e < 8; ++e) ss += (lane == 0 && e == 1) ? 0.f : A[i][e] * A[i][e];
            *(bf16x8*)(smem + r * ROWST + c0 * 2) = pk8(A[i]);
        }
        if (n >= 1 && n <= 9) {
#pragma unroll
            for (int e = 0; e < 8; ++e) ss += B[i][e] * B[i][e];
            *(bf16x8*)(smem + r * ROWST + c0 * 2 + 64) = pk8(B[i]);
        }
        ss = wave_sum63(ss);
        if (lane == 63) ((u64*)(a.ws + OFF_SS0))[p0 + r] = ss_from_float(ss);
    }
    STAMP(0, 4);
    __syncthreads();
    STAMP(0, 5);
    u16* xt = (u16*)(a.ws + OFF_XT0);
    const int g = lane >> 4, i16 = lane & 15, qq = i16 >> 2, pp = i16 & 3;
#pragma unroll
    for (int i = 0; i < 3; ++i) {
        const int cbk = w + 8 * i;
        if (cbk < 20) {
            const int cb = 32 * cbk + 16 * (g & 1), ph = g >> 1;
            const char* base = smem + (8 * ph + qq) * ROWST + (cb + 4 * pp) * 2;
            const bf16x8 o = cat8(tr_read(base), tr_read(base + 4 * ROWST));
            *(bf16x8*)(xt + (size_t)(cb + i16) * S_ + p0 + 8 * ph) = o;
        }
    }
}

DI void task_W(const PrepArgs& a, int b) {
    const int lane = threadIdx.x & 63, wv = threadIdx.x >> 6, wt = b * 2 + (wv >> 2), kq = wv & 3;
    const int k1 = wt / 5, cg = wt % 5, kp = 1 + 64 * cg + lane;
    const int r = kp % 20, t = kp / 20;
    int j; bool mir = false;
    if (kp == 320) j = 304;
    else if (r == 0) j = 304 + t;
    else if (r < 10) j = 32 * (r - 1) + t;
    else if (r == 10) j = 288 + t;
    else { j = 32 * (19 - r) + 31 - t; mir = true; }
    const rsrc_t rr = mk_rsrc(a.wr, (size_t)S_ * DF_ * 2), ri = mk_rsrc(a.wi, (size_t)S_ * DF_ * 2);
    const int st = (mir ? -64 : 64) * DF_ * 2, off0 = ((mir ? 2048 - k1 : k1) * DF_ + kp) * 2 + 8 * kq * st;
    u32 re[8], im[8];
#pragma unroll
    for (int k2 = 0; k2 < 8; ++k2) {
        int off = off0 + k2 * st;
        if (k2 == 0 && mir && k1 == 0 && kq == 0) off = kp * 2;
        re[k2] = __builtin_amdgcn_raw_buffer_load_b16(rr, off, 0, 0); im[k2] = __builtin_amdgcn_raw_buffer_load_b16(ri, off, 0, 0);
    }
    const u32 sg = mir ? 0x80000000u : 0u;
    u32x4* dst = (u32x4*)(a.ws + OFF_WT + ((size_t)j * 64 + k1) * 128 + kq * 32);
#pragma unroll
    for (int i = 0; i < 2; ++i) {
        u32x4 o;
#pragma unroll
        for (int e = 0; e < 4; ++e) o[e] = (re[4 * i + e] | (im[4 * i + e] << 16)) ^ sg;
        dst[i] = o;
    }
}

DI void task_w1(const PrepArgs& a, int b, char* smem) {
    const int tid = threadIdx.x, lane = tid & 63, w = tid >> 6;
    char* raw = smem;
    char* tr = smem + 640 * 32;
    char* out = raw;
    u32x4 v[3];
#pragma unroll
    for (int i = 0; i < 3; ++i) {
        const int id = min(tid + 512 * i, 1279), d = id >> 1, h = id & 1;
        v[i] = *(const u32x4*)(a.w1 + (size_t)d * F_ + 16 * b + 8 * h);
    }
    __builtin_amdgcn_sched_barrier(0);
    rowc_build(smem + PREP_TAB, w, lane);
    __builtin_amdgcn_sched_barrier(0);
#pragma unroll
    for (int i = 0; i < 3; ++i) *(u32x4*)(raw + min(tid + 512 * i, 1279) * 16) = v[i];
    __syncthreads();
    const int g = lane >> 4, i16 = lane & 15, qq = i16 >> 2, pp = i16 & 3;
#pragma unroll
    for (int i = 0; i < 5; ++i) {
        const int d0 = 16 * (w + 8 * i) + 4 * g;
        const s16x4 t = tr_read(raw + (d0 + qq) * 32 + pp * 8);
        *(s16x4*)(tr + i16 * ROWST + d0 * 2) = t;
    }
    RowC k; rowc_load(k, smem + PREP_TAB, lane);
    __syncthreads();
    const int n = lane & 15, q = lane >> 4, c0 = chunk_c0(n, q);
    float A[2][8], B[2][8];
#pragma unroll
    for (int i = 0; i < 2; ++i) rowfft(k, tr + (2 * w + i) * ROWST, A[i], B[i], lane);
#pragma unroll
    for (int i = 0; i < 2; ++i) {
        const int f = 2 * w + i;
        if (lane == 0) A[i][1] = 0.f;
        if (n <= 10) *(bf16x8*)(out + ((c0 >> 5) * 64 + q * 16 + f) * 16) = pk8(A[i]);
        if (n >= 1 && n <= 9) *(bf16x8*)(out + (((c0 >> 5) + 1) * 64 + q * 16 + f) * 16) = pk8(B[i]);
    }
    __syncthreads();
    u32x4* dst = (u32x4*)(a.ws + OFF_W1F + (size_t)b * 20 * 1024);
#pragma unroll
    for (int i = 0; i < 3; ++i) { const int id = tid + 512 * i; if (id < 1280) dst[id] = *(const u32x4*)(out + id * 16); }
}

DI void w2_frags(const char* img, char* dstbase, int b, int lane, int w) {
    const int g = lane >> 4, i16 = lane & 15, qq = i16 >> 2, pp = i16 & 3;
#pragma unroll
    for (int i = 0; i < 5; ++i) {
        const int cu = w + 8 * i;
        const char* base = img + (4 * g + qq) * ROWST + (16 * cu + 4 * pp) * 2;
        const bf16x8 v = cat8(tr_read(base), tr_read(base + 16 * ROWST));
        *(bf16x8*)(dstbase + ((size_t)(cu * 54 + b) * 64 + lane) * 16) = v;
    }
}
DI void task_w2(const PrepArgs& a, int b, char* smem, bool transform) {
    const int tid = threadIdx.x, lane = tid & 63, w = tid >> 6;
    u32x4 v[5];
#pragma unroll
    for (int i = 0; i < 5; ++i) {
        const int id = tid + 512 * i, r = id / 80, ch = id % 80;
        v[i] = *(const u32x4*)(a.w2 + (size_t)(32 * b + r) * D_ + ch * 8);
    }
    if (!transform) {
#pragma unroll
        for (int i = 0; i < 5; ++i) { const int id = tid + 512 * i, r = id / 80, ch = id % 80; *(u32x4*)(smem + r * ROWST + ch * 16) = v[i]; }
        __syncthreads();
        w2_frags(smem, a.ws + OFF_W2O, b, lane, w);
        return;
    }
    __builtin_amdgcn_sched_barrier(0);
    rowc_build(smem + PREP_TAB, w, lane);
    __builtin_amdgcn_sched_barrier(0);
#pragma unroll
    for (int i = 0; i < 5; ++i) { const int id = tid + 512 * i, r = id / 80, ch = id % 80; *(u32x4*)(smem + r * ROWST + ch * 16) = v[i]; }
    __syncthreads();
    RowC k; rowc_load(k, smem + PREP_TAB, lane);
    const int n = lane & 15, q = lane >> 4, c0 = chunk_c0(n, q);
#pragma unroll 1
    for (int ii = 0; ii < 2; ++ii) {
        float A[2][8], B[2][8];
#pragma unroll
        for (int i = 0; i < 2; ++i) rowfft(k, smem + (4 * w + 2 * ii + i) * ROWST, A[i], B[i], lane);
#pragma unroll
        for (int i = 0; i < 2; ++i) {
            const int r = 4 * w + 2 * ii + i;
            if (n <= 10) *(bf16x8*)(smem + r * ROWST + c0 * 2) = pk8(A[i]);
            if (n >= 1 && n <= 9) *(bf16x8*)(smem + r * ROWST + c0 * 2 + 64) = pk8(B[i]);
        }
    }
    __syncthreads();
    w2_frags(smem, a.ws + OFF_W2F, b, lane, w);
}

DI int kcol_of(int jc) { return jc < 288 ? (jc >> 5) + 1 + 20 * (jc & 31) : jc < 304 ? 10 + 20 * (jc - 288) : 20 * (jc - 304); }
DI void task_Q(const PrepArgs& a, int u) {
    const int tid = threadIdx.x;
#pragma unroll
    for (int i = 0; i < 3; ++i) {
        const int id = tid + 512 * i;
        if (id < 1280) {
            const int ks = id >> 6, l = id & 63, d = 16 * u + (l & 15), q = l >> 4;
            float v[8];
#pragma unroll
            for (int j = 0; j < 8; j += 2) {
                const int jc = 16 * ks + 4 * q + (j >> 1), kk = kcol_of(jc);
                const float ang = (float)((kk * d) % 640) * (1.0f / 640.0f);
                float c = cosr(ang) * 0.05590169943749474f, s = -sinr(ang) * 0.05590169943749474f;
                if (jc == 304) { c = ((d & 1) ? -1.f : 1.f) * 0.03952847075210474f; s = 0.03952847075210474f; }
                v[j] = c; v[j + 1] = s;
            }
            *(bf16x8*)(a.ws + OFF_W1F + ((size_t)(108 + u) * 20 * 64 + id) * 16) = pk8(v);
        }
    }
}
DI void task_b(const PrepArgs& a, char* smem) {
    const int tid = threadIdx.x, lane = tid & 63;
    if (tid < 80) *(u32x4*)(smem + tid * 16) = *(const u32x4*)(a.b2 + tid * 8);
    rowc_build(smem + PREP_TAB, tid >> 6, lane);
    __syncthreads();
    RowC k; rowc_load(k, smem + PREP_TAB, lane);
    float A[8], B[8];
    rowfft(k, smem, A, B, lane);
    if (tid < 64) {
        const int n = lane & 15, q = lane >> 4, c0 = chunk_c0(n, q);
        float* o = (float*)(a.ws + OFF_B2P);
        if (n <= 10) for (int e = 0; e < 8; ++e) o[c0 + e] = A[e];
        if (n >= 1 && n <= 9) for (int e = 0; e < 8; ++e) o[c0 + 32 + e] = B[e];
    }
}

constexpr int NB_K0 = 128 + 160;
__global__ __launch_bounds__(512, 4) void k0_prep(PrepArgs a) {
    extern __shared__ __attribute__((aligned(16))) char smem[];
    const int b = blockIdx.x;
    STAMP(0, 0);
    if (b < 128) task_x(a, b, smem);
    else task_W(a, b - 128);
    END_STAMP(0);
}

struct ColArgs { PrepArgs p; const u16* xin; u16* xout; char* xf; const u64* ss; const u32* wt; int layer; int pad; };
DI int img128(int row, int ch) { return row * 128 + ((ch ^ ((row >> 1) & 7)) << 4); }
constexpr int NB_COL1 = 160 + 108 + 54 + 1, NB_COL2 = 160 + 54 + 40;

DI void col_task(const ColArgs& a, int b, char* smem) {
    const int tid = threadIdx.x & 255, lane = tid & 63, w = tid >> 6, half = threadIdx.x >> 8;
    const int j = 2 * b + half;
    char* Ure = smem + half * 24576; char* Uim = Ure + 4096; char* Ere = Ure + 8192; char* Eim = Ure + 12288;
    char* Rre = Ure + 16384; char* Rim = Ure + 20480;
    const int n = lane & 15, q = lane >> 4;
    const u16* xr = a.xin + (size_t)(2 * j) * S_; const u16* xi = xr + S_;
    const u32x4 vr = *(const u32x4*)(xr + tid * 8), vi = *(const u32x4*)(xi + tid * 8);
    u32x4 sv[4];
#pragma unroll
    for (int i = 0; i < 4; ++i) sv[i] = *(const u32x4*)(a.ss + tid * 8 + 2 * i);
    u32x4 wf[2];
#pragma unroll
    for (int t = 0; t < 2; ++t) wf[t] = *(const u32x4*)(a.wt + ((size_t)j * 64 + 16 * w + n) * 32 + 16 * t + 4 * q);
    __builtin_amdgcn_sched_barrier(0);
    F32c f; f32c_init(f, lane);
    bf16x8 c32[2] = {f.c[0], f.c[1]}, s32p[2] = {f.sp[0], f.sp[1]}, s32m[2];
    pin(c32[0]); pin(c32[1]); pin(s32p[0]); pin(s32p[1]);
    bf16x8 c64[2], s64p[2], s64m[2];
    const float k1f = (float)(16 * w + n);
    {
        float c[8], s[8];
#pragma unroll
        for (int e = 0; e < 8; ++e) {
            const float ang = __builtin_amdgcn_fractf((float)(8 * q + e) * k1f * (1.0f / 64.0f));
            c[e] = cosr(ang); s[e] = sinr(ang);
        }
        c64[0] = pk8(c); s64p[0] = pk8(s);
        const u32 sg = (lane & 1) ? 0x80008000u : 0u;
        c64[1] = asbf(__builtin_bit_cast(u32x4, c64[0]) ^ sg); s64p[1] = asbf(__builtin_bit_cast(u32x4, s64p[0]) ^ sg);
        pin(c64[0]); pin(c64[1]); pin(s64p[0]); pin(s64p[1]);
    }
    float twr[8], twi[8], ivr[8], ivi[8];
#pragma unroll
    for (int i = 0; i < 8; ++i) {
        const float af = (float)(16 * (i >> 2) + 4 * q + (i & 3)) * k1f * (1.0f / 2048.0f);
        twr[i] = cosr(af); twi[i] = -sinr(af);
        const float ai = (float)(16 * w + 4 * q + (i & 3)) * (float)(16 * (i >> 2) + n) * (1.0f / 2048.0f);
        ivr[i] = cosr(ai) * (1.0f / 2048.0f); ivi[i] = sinr(ai) * (1.0f / 2048.0f);
        pin(twr[i]); pin(twi[i]); pin(ivr[i]); pin(ivi[i]);
    }
    __builtin_amdgcn_sched_barrier(0);
    STAMP(a.layer ? 4 : 1, 1);
    s64m[0] = neg8(s64p[0]); s64m[1] = neg8(s64p[1]);
    {
        float r[8];
#pragma unroll
        for (int i = 0; i < 8; ++i) {
            const float s = (float)sv[i >> 1][2 * (i & 1) + 1] * 4294967296.0f + (float)sv[i >> 1][2 * (i & 1)];
            r[i] = __builtin_amdgcn_rsqf(s * (1.0f / (SS_SCALE * 640.0f)) + EPS);
        }
        const bf16x8 ur = pk8(bflo(vr[0]) * r[0], bfhi(vr[0]) * r[1], bflo(vr[1]) * r[2], bfhi(vr[1]) * r[3],
                              bflo(vr[2]) * r[4], bfhi(vr[2]) * r[5], bflo(vr[3]) * r[6], bfhi(vr[3]) * r[7]);
        bf16x8 ui = pk8(bflo(vi[0]) * r[0], bfhi(vi[0]) * r[1], bflo(vi[1]) * r[2], bfhi(vi[1]) * r[3],
                        bflo(vi[2]) * r[4], bfhi(vi[2]) * r[5], bflo(vi[3]) * r[6], bfhi(vi[3]) * r[7]);
        if (j == 304) ui = asbf(u32x4{0u, 0u, 0u, 0u});
        const int o = img128(tid >> 3, tid & 7);
        *(bf16x8*)(Ure + o) = ur; *(bf16x8*)(Uim + o) = ui;
        *(u32x4*)(Rre + o) = vr; *(u32x4*)(Rim + o) = vi;
    }
    __syncthreads();
    STAMP(a.layer ? 4 : 1, 2);
    const f32x4 z = {0.f, 0.f, 0.f, 0.f};
    f32x4 dr[2], di[2];
#pragma unroll
    for (int mt = 0; mt < 2; ++mt) {
        dr[mt] = z; di[mt] = z;
#pragma unroll
        for (int ks = 0; ks < 2; ++ks) {
            const int o = img128(16 * mt + n, 4 * ks + q);
            const bf16x8 ar = *(const bf16x8*)(Ure + o), ai = *(const bf16x8*)(Uim + o);
            dr[mt] = mfma16(ar, c64[ks], dr[mt]); dr[mt] = mfma16(ai, s64p[ks], dr[mt]);
            di[mt] = mfma16(ar, s64m[ks], di[mt]); di[mt] = mfma16(ai, c64[ks], di[mt]);
        }
    }
    float tr[8], ti[8];
#pragma unroll
    for (int i = 0; i < 8; ++i) {
        const float x = dr[i >> 2][i & 3], y = di[i >> 2][i & 3];
        tr[i] = x * twr[i] - y * twi[i]; ti[i] = x * twi[i] + y * twr[i];
    }
    bf16x8 Tr = pk8(tr), Ti = pk8(ti);
    s32m[0] = neg8(s32p[0]); s32m[1] = neg8(s32p[1]);
    f32x4 orr[2], oi[2];
#pragma unroll
    for (int t = 0; t < 2; ++t) {
        orr[t] = mfma16(c32[t], Tr, z); orr[t] = mfma16(s32p[t], Ti, orr[t]);
        oi[t] = mfma16(s32m[t], Tr, z); oi[t] = mfma16(c32[t], Ti, oi[t]);
    }
#pragma unroll
    for (int i = 0; i < 8; ++i) {
        const float x = orr[i >> 2][i & 3], y = oi[i >> 2][i & 3];
        const u32 wv = wf[i >> 2][i & 3];
        const float wre = bflo(wv), wim = bfhi(wv);
        tr[i] = x * wre - y * wim; ti[i] = x * wim + y * wre;
    }
    Tr = pk8(tr); Ti = pk8(ti);
#pragma unroll
    for (int nt = 0; nt < 2; ++nt) {
        f32x4 zr = mfma16(Tr, c32[nt], z); zr = mfma16(Ti, s32m[nt], zr);
        f32x4 zi = mfma16(Tr, s32p[nt], z); zi = mfma16(Ti, c32[nt], zi);
        float er[4], ei[4];
#pragma unroll
        for (int r = 0; r < 4; ++r) {
            const float cr = ivr[4 * nt + r], ci = ivi[4 * nt + r];
            er[r] = zr[r] * cr - zi[r] * ci; ei[r] = zr[r] * ci + zi[r] * cr;
        }
        const int o = img128(16 * nt + n, 2 * w + (q >> 1)) + (q & 1) * 8;
        u32x2 pr = {pk2(er[0], er[1]), pk2(er[2], er[3])}, pi = {pk2(ei[0], ei[1]), pk2(ei[2], ei[3])};
        *(u32x2*)(Ere + o) = pr; *(u32x2*)(Eim + o) = pi;
    }
    __syncthreads();
    STAMP(a.layer ? 4 : 1, 3);
    u16* yr_ = a.xout + (size_t)(2 * j) * S_; u16* yi_ = yr_ + S_;
    char* O = smem + 49152 + half * 8192;
#pragma unroll
    for (int nt = 0; nt < 2; ++nt) {
        f32x4 yr = z, yi = z;
#pragma unroll
        for (int ks = 0; ks < 2; ++ks) {
            const int o = img128(16 * nt + n, 4 * ks + q);
            const bf16x8 br = *(const bf16x8*)(Ere + o), bi = *(const bf16x8*)(Eim + o);
            yr = mfma16(c64[ks], br, yr); yr = mfma16(s64m[ks], bi, yr);
            yi = mfma16(s64p[ks], br, yi); yi = mfma16(c64[ks], bi, yi);
        }
        if (j == 304) yi = z;
        const int p = (16 * nt + n) * 64 + 16 * w + 4 * q;
        u32x2 rr[2], ri[2];
        { const int o = img128(16 * nt + n, 2 * w + (q >> 1)) + (q & 1) * 8; rr[nt] = *(const u32x2*)(Rre + o); ri[nt] = *(const u32x2*)(Rim + o); }
        const float fr[4] = {bflo(rr[nt][0]) + yr[0], bfhi(rr[nt][0]) + yr[1], bflo(rr[nt][1]) + yr[2], bfhi(rr[nt][1]) + yr[3]};
        const float fi[4] = {bflo(ri[nt][0]) + yi[0], bfhi(ri[nt][0]) + yi[1], bflo(ri[nt][1]) + yi[2], bfhi(ri[nt][1]) + yi[3]};
        if (a.layer == 0) {
            u32x2 orv = {pk2(fr[0], fr[1]), pk2(fr[2], fr[3])}, oiv = {pk2(fi[0], fi[1]), pk2(fi[2], fi[3])};
            *(u32x2*)(yr_ + p) = orv; *(u32x2*)(yi_ + p) = oiv;
        }
        u32x4 ov = {pk2(fr[0], fi[0]), pk2(fr[1], fi[1]), pk2(fr[2], fi[2]), pk2(fr[3], fi[3])};
        *(u32x4*)(O + p * 4) = ov;
    }
    __syncthreads();
    {
        const int c0 = 4 * b;
        char* dst = a.xf + ((c0 >> 5) * 64 + ((c0 & 31) >> 3) * 16) * 16 + (c0 & 4) * 2;
        const u32* O0 = (const u32*)(smem + 49152); const u32* O1 = O0 + 2048;
#pragma unroll
        for (int i = 0; i < 4; ++i) {
            const int p = threadIdx.x + 512 * i;
            u32x2 v = {O0[p], O1[p]};
            *(u32x2*)(dst + (p >> 4) * 20480 + (p & 15) * 16) = v;
        }
    }
    STAMP(a.layer ? 4 : 1, 4);
}

__global__ __launch_bounds__(512, 4) void k_col(ColArgs a) {
    extern __shared__ __attribute__((aligned(16))) char smem[];
    const int b = blockIdx.x;
    STAMP(a.layer ? 4 : 1, 0);
    if (b < 160) col_task(a, b, smem);
    else if (a.layer == 0) {
        if (b < 160 + 108) task_w1(a.p, b - 160, smem);
        else if (b < 160 + 108 + 54) task_w2(a.p, b - 268, smem, true);
        else task_b(a.p, smem);
    } else {
        if (b < 160 + 54) task_w2(a.p, b - 160, smem, false);
        else task_Q(a.p, b - 214);
    }
    END_STAMP(a.layer ? 4 : 1);
}

struct MlpArgs {
    const char* xf;
    const char* w1f; const u16* b1;
    char* h; char* x3;
    const char* w2;
    const u16* xin; u16* xout; const float* b2p; const u16* b2; u16* out; u64* ss;
    u32* cnt;
};

DI f32x2 gelu_tanh2(f32x2 x) {
    const float k1 = -2.302208198f, k2 = -0.1029432397f;
    const f32x2 c1 = {k1, k1}, c2 = {k2, k2}, one = {1.0f, 1.0f};
    const f32x2 t = x * (x * x * c2 + c1);
    f32x2 e = {__builtin_amdgcn_exp2f(t[0]), __builtin_amdgcn_exp2f(t[1])};
    e += one;
    const f32x2 r = {__builtin_amdgcn_rcpf(e[0]), __builtin_amdgcn_rcpf(e[1])};
    return x * r;
}
template <int N> DI void wait_vm() { asm volatile("s_waitcnt vmcnt(%0)" ::"n"(N) : "memory"); }
DI void wait_lgkm() { asm volatile("s_waitcnt lgkmcnt(0)" ::: "memory"); }
DI void barrier() { __builtin_amdgcn_s_barrier(); }
DI void dma16(u32x4 srd, int voff, int soff, u32 ldsaddr) {
    asm volatile("s_mov_b32 m0, %0\n\ts_nop 0\n\tbuffer_load_dwordx4 %1, %2, %3 offen lds" ::"s"(ldsaddr), "v"(voff), "s"(srd), "s"(soff) : "memory", "m0");
}
template <int LIM> DI void dma16_lim(u32x4 srd, int voff, int soff, u32 ldsaddr, int w) {
    asm volatile("s_cmp_lt_i32 %4, %5\n\ts_cbranch_scc0 .Lskip%=\n\ts_mov_b32 m0, %0\n\ts_nop 0\n\tbuffer_load_dwordx4 %1, %2, %3 offen lds\n.Lskip%=:"
                 ::"s"(ldsaddr), "v"(voff), "s"(srd), "s"(soff), "s"(w), "n"(LIM) : "memory", "m0", "scc");
}
DI u32x4 mk_srd(const void* p, size_t bytes) {
    const u64 a = (u64)p;
    u32x4 r = {(u32)a, (u32)(a >> 32) & 0xffffu, (u32)bytes, 0x00020000u};
    return r;
}
DI float row_sum15(float v) {
    v += __builtin_bit_cast(float, __builtin_amdgcn_update_dpp(0, __builtin_bit_cast(int, v), 0x111, 0xf, 0xf, true));
    v += __builtin_bit_cast(float, __builtin_amdgcn_update_dpp(0, __builtin_bit_cast(int, v), 0x112, 0xf, 0xf, true));
    v += __builtin_bit_cast(float, __builtin_amdgcn_update_dpp(0, __builtin_bit_cast(int, v), 0x114, 0xf, 0xf, true));
    v += __builtin_bit_cast(float, __builtin_amdgcn_update_dpp(0, __builtin_bit_cast(int, v), 0x118, 0xf, 0xf, true));
    return v;
}
template <int I, int N, class F> DI void static_for(F&& f) {
    if constexpr (I < N) { f(std::integral_constant<int, I>{}); static_for<I + 1, N>(f); }
}

constexpr int G1_P = 4, G1_PW = 5;
#define G2_PH_ 4
constexpr int G2_PH = G2_PH_;
constexpr int SC1 = 16;
constexpr int W2STG = 20480;
DI u32 w2slot(int t) { return 163840 - ((t & 7) + 1) * W2STG; }

constexpr int g1_younger(int ssync, int t, int nlo) {
    int cnt = 0; bool seen = false;
    for (int i = 0; i < G1_PW; ++i) {
        if (seen) cnt += nlo;
        if (i == t) seen = true;
        if (i < G1_P && seen) cnt += 2;
    }
    for (int s = 0; s < ssync; ++s) {
        if (s + G1_PW < 10) { if (seen) cnt += nlo; if (s + G1_PW == t) seen = true; }
        if (s + G1_P < 10 && seen) cnt += 2;
    }
    return cnt;
}

template <int I> DI void w2_issue1(u32x4 srd, u32 lds0, int w, int vo, int ct, int t) {
    const int pc = w + 8 * I, q = pc / 5, cu = pc - 5 * q;
    const int so = ((5 * ct + cu) * 54 + (q < 2 ? 14 * q : 28 + 13 * (q - 2)) + t) * 1024;
    if (I < 2) dma16(srd, vo, so, lds0 + w2slot(t) + pc * 1024); else dma16_lim<4>(srd, vo, so, lds0 + w2slot(t) + pc * 1024, w);
}
DI void w2_issue(u32x4 srd, u32 lds0, int w, int vo, int ct, int t) {
    w2_issue1<0>(srd, lds0, w, vo, ct, t); w2_issue1<1>(srd, lds0, w, vo, ct, t); w2_issue1<2>(srd, lds0, w, vo, ct, t);
}

template <int NF, bool LAST, int KG>
DI void g1_phase(const MlpArgs& a, char* smem, int w, int lane, int pt, int u0, int nu, int ct2) {
    constexpr int STG = NF * 2048, NST = LAST ? 8 : 10, P = G1_P, PW = G1_PW;
    constexpr int WP = 2 * NF, WR = (WP + 7) / 8, NLO = WP / 8, KID = LAST ? 5 : 2;
    auto wslot = [](int st) constexpr { return LAST ? (st % NST) * STG : 163840 - (st + 1) * STG; };
    const int pq = w & 3, g = lane >> 4;
    const u32x4 srdW = mk_srd(a.w1f, W1F_BYTES);
    const u32 lds0 = (u32)(size_t)(LAS char*)smem;
    const rsrc_t rx = mk_rsrc(a.xf, XT_BYTES);
    const int xs = (8 * pt + 2 * pq) * 20480 + KG * 1024, vo = lane * 16;
    auto dmaw = [&](int st, int i) __attribute__((always_inline)) {
        const int pc = w + 8 * i;
        const u32 dst = lds0 + wslot(st) + pc * 1024;
        const int so = (u0 + (pc >> 1)) * 20480 + st * 2048 + (pc & 1) * 1024;
        if (8 * i + 7 < WP) dma16(srdW, vo, so, dst); else dma16_lim<(WP & 7)>(srdW, vo, so, dst, w);
    };
    bf16x8 xb[P][2];
#pragma unroll
    for (int st = 0; st < PW; ++st) {
#pragma unroll
        for (int i = 0; i < WR; ++i) dmaw(st, i);
        __builtin_amdgcn_sched_barrier(0);
        if (st < P) {
#pragma unroll
            for (int pu = 0; pu < 2; ++pu) xb[st][pu] = asbf(bload16(rx, vo, xs + pu * 20480 + st * 2048));
        }
        __builtin_amdgcn_sched_barrier(0);
    }
    f32x4 acc[NF][2];
#pragma unroll
    for (int i = 0; i < NF; ++i) { acc[i][0] = f32x4{0, 0, 0, 0}; acc[i][1] = f32x4{0, 0, 0, 0}; }
    f32x4 gr[2] = {f32x4{0, 0, 0, 0}, f32x4{0, 0, 0, 0}};
    bf16x8 wa[2][NF];
    wait_vm<g1_younger(0, 1, NLO)>();
    barrier();
    STAMP(KID, 1);
#pragma unroll
    for (int i = 0; i < NF; ++i) wa[0][i] = *(const bf16x8*)(smem + wslot(0) + KG * 1024 + lane * 16 + i * 2048);
    const u32x4 srd2 = mk_srd(a.w2, W2F_BYTES);
    static_for<0, 10>([&](auto ST) __attribute__((always_inline)) {
        constexpr int st = decltype(ST)::value;
        if constexpr (st == 1 || st == 4 || st == 7) {
            wait_lgkm();
            wait_vm<g1_younger(st, st == 7 ? 9 : st + 3, NLO)>();
            barrier();
        }
        bf16x8 nx[2];
        const char* rbase = smem + wslot(st + 1) + KG * 1024 + lane * 16;
        constexpr int NV = WR + 2, V0 = KG == 0 ? 0 : NF - NV;
        static_for<0, NF>([&](auto I) __attribute__((always_inline)) {
            constexpr int i = decltype(I)::value;
            __builtin_amdgcn_sched_barrier(0);
            acc[i][0] = mfma16(wa[st & 1][i], xb[st % P][0], acc[i][0]);
            if constexpr (st + 1 < 10) wa[(st + 1) & 1][i] = *(const bf16x8*)(rbase + i * 2048);
            acc[i][1] = mfma16(wa[st & 1][i], xb[st % P][1], acc[i][1]);
            if constexpr (i < 2) {
                u32x4 xv = __builtin_bit_cast(u32x4, xb[st % P][i]);
                if constexpr (st == 9 && KG == 1) { if (g == 0) xv[0] &= 0x0000ffffu; }
                gr[i] = mfma16(asbf(xv), asbf(xv), gr[i]);
            }
            __builtin_amdgcn_sched_barrier(0);
            constexpr int v = i - V0;
            if constexpr (v >= 0 && v < WR) { if constexpr (st + PW < 10) dmaw(st + PW, v); }
            else if constexpr (v >= WR && v < NV) { if constexpr (st + P < 10) nx[v - WR] = asbf(bload16(rx, vo, xs + (v - WR) * 20480 + (st + P) * 2048)); }
            if constexpr (st >= 6) {
                constexpr int sl = i - (KG == 0 ? 0 : NF - 4), e = 4 * (st - 6) + sl;
                if constexpr (sl >= 0 && sl < 4 && e < 15) {
                    constexpr int t2 = LAST ? (e / 3 + 3) % 5 : e / 3;
                    w2_issue1<e % 3>(srd2, lds0, w, vo, ct2, t2);
                }
            }
        });
        __builtin_amdgcn_sched_barrier(0);
        if constexpr (st + P < 10) { xb[st % P][0] = nx[0]; xb[st % P][1] = nx[1]; }
        if (st == 4) STAMP(KID, 2);
    });
    STAMP(KID, 3);
    u32x2 bv[NF];
#pragma unroll
    for (int i = 0; i < NF; ++i) bv[i] = *(const u32x2*)(a.b1 + min(16 * (u0 + i), F_ - 16) + 4 * g);
    __builtin_amdgcn_sched_barrier(0);
    wait_lgkm();
    barrier();
    float* sx = (float*)(smem + 8 * NF * 512);
#pragma unroll
    for (int i = 0; i < NF; ++i) {
        const f32x4 t = acc[i][1 - KG];
        u32x2 pv = {pk2(t[0], t[1]), pk2(t[2], t[3])};
        *(u32x2*)(smem + ((w * NF + i) * 64 + lane) * 8) = pv;
    }
    float st2[2];
    {
        const int n = lane & 15;
#pragma unroll
        for (int pu = 0; pu < 2; ++pu) {
            const float d01 = (n & 1) ? gr[pu][1] : gr[pu][0], d23 = (n & 1) ? gr[pu][3] : gr[pu][2];
            float d = (n & 2) ? d23 : d01;
            d = (g == (n >> 2)) ? d : 0.f;
            d += __shfl_xor(d, 16); d += __shfl_xor(d, 32);
            st2[pu] = d;
        }
    }
    if (lane < 16) sx[w * 16 + lane] = st2[1 - KG];
    wait_lgkm();
    barrier();
    STAMP(KID, 5);
    const float v = (st2[KG] + sx[(w ^ 4) * 16 + (lane & 15)]) * (1.0f / 640.0f);
    const float r1 = __builtin_amdgcn_rsqf(v + EPS);
    const float rr = r1 * __builtin_amdgcn_rsqf(v * r1 * r1 + EPS);
    const int punit = 8 * pt + 2 * pq + KG;
    u32x2 ov[NF], pv[NF];
#pragma unroll
    for (int i = 0; i < NF; ++i) pv[i] = *(const u32x2*)(smem + (((w ^ 4) * NF + i) * 64 + lane) * 8);
    static_for<0, NF>([&](auto I) __attribute__((always_inline)) {
        constexpr int i = decltype(I)::value;
        if constexpr (!LAST && i == 0) {
            __builtin_amdgcn_sched_barrier(0);
            w2_issue(srd2, lds0, w, vo, ct2, 5);
            __builtin_amdgcn_sched_barrier(0);
        }
        const f32x2 o0 = {acc[i][KG][0] + bflo(pv[i][0]), acc[i][KG][1] + bfhi(pv[i][0])}, o1 = {acc[i][KG][2] + bflo(pv[i][1]), acc[i][KG][3] + bfhi(pv[i][1])};
        const f32x2 rr2 = {rr, rr};
        const f32x2 b0 = {bflo(bv[i][0]), bfhi(bv[i][0])}, b1 = {bflo(bv[i][1]), bfhi(bv[i][1])};
        f32x2 h0 = gelu_tanh2(o0 * rr2 + b0), h1 = gelu_tanh2(o1 * rr2 + b1);
        if (LAST) {
            const bool lin = u0 + i >= 108;
            h0[0] = lin ? o0[0] : h0[0]; h0[1] = lin ? o0[1] : h0[1]; h1[0] = lin ? o1[0] : h1[0]; h1[1] = lin ? o1[1] : h1[1];
        }
        ov[i] = u32x2{pk2(h0[0], h0[1]), pk2(h1[0], h1[1])};
    });
    __builtin_amdgcn_sched_barrier(0);
    const rsrc_t rh = mk_rsrc(a.h, H_BYTES), r3 = mk_rsrc(a.x3, X3_BYTES);
    const int hb = punit * 54 * 1024;
    auto st1 = [&](int i) __attribute__((always_inline)) {
        const int u = u0 + i;
        if (i < nu) {
            if (!LAST || u < 108) __builtin_amdgcn_raw_buffer_store_b64(ov[i], rh, lane * 16 + (u & 1) * 8, hb + (u >> 1) * 1024, SC1);
            else __builtin_amdgcn_raw_buffer_store_b64(ov[i], r3, lane * 8, (punit * 40 + (u - 108)) * 512, SC1);
        }
    };
    auto st2p = [&](int i) __attribute__((always_inline)) {
        const int u = u0 + i;
        if (i + 1 < nu && (!LAST || u + 1 < 108)) {
            u32x4 q4 = {ov[i][0], ov[i][1], ov[i + 1][0], ov[i + 1][1]};
            __builtin_amdgcn_raw_buffer_store_b128(q4, rh, lane * 16, hb + (u >> 1) * 1024, SC1);
        } else { st1(i); st1(i + 1); }
    };
    if (u0 & 1) {
        st1(0);
#pragma unroll
        for (int i = 1; i + 1 < NF; i += 2) st2p(i);
        if ((NF & 1) == 0) st1(NF - 1);
    } else {
#pragma unroll
        for (int i = 0; i + 1 < NF; i += 2) st2p(i);
        if (NF & 1) st1(NF - 1);
    }
    STAMP(KID, 4);
}

template <bool LAST>
DI void g2_phase(const MlpArgs& a, char* smem, int w, int lane, int pt1, int pt, int ct) {
    constexpr int PH = G2_PH, KID = LAST ? 6 : 3;
    typedef __attribute__((address_space(1))) u32 gu32;
    const int pg = w & 1, kq = w >> 1, n = lane & 15, q = lane >> 4;
    const int kb = kq < 2 ? 14 * kq : 28 + 13 * (kq - 2), nk = kq < 2 ? 14 : 13;
    const u32x4 srd2 = mk_srd(a.w2, W2F_BYTES);
    const u32 lds0 = (u32)(size_t)(LAS char*)smem;
    const rsrc_t rh = mk_rsrc(a.h, H_BYTES), rw = mk_rsrc(a.w2, W2F_BYTES);
    const int vo = lane * 16, hs = ((4 * pt + 2 * pg) * 54 + kb) * 1024;
    bf16x8 wr[6][5];
    asm volatile("" ::: "memory");
    __builtin_amdgcn_sched_barrier(0);
#pragma unroll
    for (int t = 0; t < 3; ++t)
#pragma unroll
        for (int i = 0; i < 5; ++i) wr[t][i] = asbf(bload16(rw, vo, ((5 * ct + i) * 54 + kb + 8 + t) * 1024));
    __builtin_amdgcn_sched_barrier(0);
    wait_lgkm();
    wait_vm<15>();
    barrier();
    if (threadIdx.x == 0) __hip_atomic_fetch_add((gu32*)(a.cnt + 64 * pt1), 1u, __ATOMIC_RELAXED, __HIP_MEMORY_SCOPE_AGENT);
    STAMP(LAST ? 5 : 2, 15);
#pragma unroll
    for (int t = LAST ? 5 : 6; t < 8; ++t) w2_issue(srd2, lds0, w, vo, ct, t);
    __builtin_amdgcn_sched_barrier(0);
#pragma unroll
    for (int t = 3; t < 6; ++t)
#pragma unroll
        for (int i = 0; i < 5; ++i) wr[t][i] = asbf(bload16(rw, vo, ((5 * ct + i) * 54 + kb + 8 + t) * 1024));
    __builtin_amdgcn_sched_barrier(0);
    if (w == 0) {
        gu32* c = (gu32*)(a.cnt + 64 * (pt >> 1));
        gu32* tmo = (gu32*)(a.cnt + 64 * 16);
        for (u32 spins = 0;; ++spins) {
            if ((u32)__builtin_amdgcn_readfirstlane(__hip_atomic_load(c, __ATOMIC_RELAXED, __HIP_MEMORY_SCOPE_AGENT)) >= 16u) break;
            __builtin_amdgcn_s_sleep(1);
            if (spins > (1u << 18)) { if (lane == 0) __hip_atomic_store(tmo, 1u, __ATOMIC_RELAXED, __HIP_MEMORY_SCOPE_AGENT); break; }
        }
    }
    wait_vm<0>();
    barrier();
    STAMP(KID, 5);
    bf16x8 hb[PH][2];
    auto hload = [&](int t, int pu) __attribute__((always_inline)) {
        const int k = t < 13 ? t : nk - 1;
        return asbf(__builtin_amdgcn_raw_buffer_load_b128(rh, vo, hs + (pu * 54 + k) * 1024, SC1));
    };
#pragma unroll
    for (int t = 0; t < PH; ++t) { hb[t][0] = hload(t, 0); hb[t][1] = hload(t, 1); __builtin_amdgcn_sched_barrier(0); }
    u32x2 xv[3], bv[3]; float bb[3];
    f32x4 acc[2][5];
#pragma unroll
    for (int i = 0; i < 5; ++i) { acc[0][i] = f32x4{0, 0, 0, 0}; acc[1][i] = f32x4{0, 0, 0, 0}; }
    bf16x8 wa[2][5];
    const char* wbase = smem + kq * 5120 + lane * 16;
#pragma unroll
    for (int i = 0; i < 5; ++i) wa[0][i] = *(const bf16x8*)(wbase + w2slot(0) + i * 1024);
    static_for<0, 14>([&](auto T) __attribute__((always_inline)) {
        constexpr int t = decltype(T)::value;
        if constexpr (t == 8) {
            __builtin_amdgcn_sched_barrier(0);
#pragma unroll
            for (int j = 0; j < 3; ++j) {
                const int tt = min(kq + 4 * j, 9), pu = 2 * pg + tt / 5, cu = tt % 5;
                if (!LAST) {
                    const int c = 80 * ct + 16 * cu + n;
                    xv[j] = *(const u32x2*)(a.xin + (size_t)c * S_ + 64 * pt + 16 * pu + 4 * q);
                    bb[j] = a.b2p[c];
                } else {
                    xv[j] = __builtin_amdgcn_raw_buffer_load_b64(mk_rsrc(a.x3, X3_BYTES), lane * 8, ((4 * pt + pu) * 40 + 5 * ct + cu) * 512, SC1);
                    bv[j] = *(const u32x2*)(a.b2 + 80 * ct + 16 * cu + 4 * q);
                }
            }
        }
        bf16x8 nh[2];
        auto step = [&]() __attribute__((always_inline)) {
            static_for<0, 5>([&](auto I) __attribute__((always_inline)) {
                constexpr int i = decltype(I)::value;
                __builtin_amdgcn_sched_barrier(0);
                bf16x8 wv;
                if constexpr (t < 8) wv = wa[t & 1][i]; else wv = wr[t - 8][i];
                acc[0][i] = LAST ? mfma16(wv, hb[t % PH][0], acc[0][i]) : mfma16(hb[t % PH][0], wv, acc[0][i]);
                if constexpr (t + 1 < 8) wa[(t + 1) & 1][i] = *(const bf16x8*)(wbase + w2slot(t + 1) + i * 1024);
                acc[1][i] = LAST ? mfma16(wv, hb[t % PH][1], acc[1][i]) : mfma16(hb[t % PH][1], wv, acc[1][i]);
                __builtin_amdgcn_sched_barrier(0);
                if constexpr (t + PH < 14 && (i == 1 || i == 3)) nh[i >> 1] = hload(t + PH, i >> 1);
            });
        };
        if constexpr (t < 13) step(); else { if (nk == 14) step(); }
        __builtin_amdgcn_sched_barrier(0);
        if constexpr (t + PH < 14) { hb[t % PH][0] = nh[0]; hb[t % PH][1] = nh[1]; }
        if (t == 0) STAMP(KID, 1);
        if (t == 7) STAMP(KID, 2);
    });
    STAMP(KID, 3);
    wait_lgkm();
    barrier();
#pragma unroll
    for (int t = 0; t < 10; ++t)
        if ((t & 3) != kq) *(f32x4*)(smem + ((w * 10 + t) * 64 + lane) * 16) = acc[t / 5][t % 5];
    wait_lgkm();
    barrier();
    float ssp[2][4];
#pragma unroll
    for (int r = 0; r < 4; ++r) { ssp[0][r] = 0.f; ssp[1][r] = 0.f; }
    auto finish = [&](auto KQ) __attribute__((always_inline)) {
        constexpr int kqc = decltype(KQ)::value;
        static_for<0, 3>([&](auto J) __attribute__((always_inline)) {
            constexpr int j = decltype(J)::value, t = kqc + 4 * j;
            if constexpr (t < 10) {
                constexpr int pl = t / 5, cu = t % 5;
                f32x4 o = acc[pl][cu];
#pragma unroll
                for (int s = 1; s < 4; ++s) o += *(const f32x4*)(smem + (((pg + 2 * ((kqc + s) & 3)) * 10 + t) * 64 + lane) * 16);
                const int pu = 2 * pg + pl;
                if (!LAST) {
                    const int c = 80 * ct + 16 * cu + n, p = 64 * pt + 16 * pu + 4 * q;
                    const u32 o0 = pk2(bflo(xv[j][0]) + o[0] + bb[j], bfhi(xv[j][0]) + o[1] + bb[j]);
                    const u32 o1 = pk2(bflo(xv[j][1]) + o[2] + bb[j], bfhi(xv[j][1]) + o[3] + bb[j]);
                    u32x2 ov = {o0, o1};
                    *(u32x2*)(a.xout + (size_t)c * S_ + p) = ov;
                    const float m = c == 609 ? 0.f : 1.f;
                    ssp[pl][0] += m * bflo(o0) * bflo(o0); ssp[pl][1] += m * bfhi(o0) * bfhi(o0);
                    ssp[pl][2] += m * bflo(o1) * bflo(o1); ssp[pl][3] += m * bfhi(o1) * bfhi(o1);
                } else {
                    const int p = 16 * (4 * pt + pu) + n, d = 80 * ct + 16 * cu + 4 * q;
                    u32x2 ov = {pk2(bflo(xv[j][0]) + o[0] + bflo(bv[j][0]), bfhi(xv[j][0]) + o[1] + bfhi(bv[j][0])),
                                pk2(bflo(xv[j][1]) + o[2] + bflo(bv[j][1]), bfhi(xv[j][1]) + o[3] + bfhi(bv[j][1]))};
                    *(u32x2*)(a.out + (size_t)tok_of_p(p) * D_ + d) = ov;
                }
            }
        });
    };
    if (kq == 0) finish(std::integral_constant<int, 0>{});
    else if (kq == 1) finish(std::integral_constant<int, 1>{});
    else if (kq == 2) finish(std::integral_constant<int, 2>{});
    else finish(std::integral_constant<int, 3>{});
    if (!LAST) {
        float* sp = (float*)(smem + 81920);
#pragma unroll
        for (int pl = 0; pl < 2; ++pl)
#pragma unroll
            for (int r = 0; r < 4; ++r) {
                const float v = row_sum15(ssp[pl][r]);
                if (n == 15) sp[w * 32 + 16 * pl + 4 * q + r] = v;
            }
        wait_lgkm();
        barrier();
        if (w == 0) {
            const int g2 = lane >> 5, r = lane & 31;
            const float v = (sp[g2 * 32 + r] + sp[(g2 + 2) * 32 + r]) + (sp[(g2 + 4) * 32 + r] + sp[(g2 + 6) * 32 + r]);
            atomicAdd((unsigned long long*)(a.ss + 64 * pt + lane), ss_from_float(v));
        }
    }
}

template <bool LAST>
__global__ __launch_bounds__(512, 2) void k_mlp(MlpArgs a) {
    extern __shared__ __attribute__((aligned(16))) char smem[];
    constexpr int NF = LAST ? 10 : 7;
    const int lane = threadIdx.x & 63;
    const int w = __builtin_amdgcn_readfirstlane(threadIdx.x >> 6);
    const int x = blockIdx.x & 7, ib = blockIdx.x >> 3;
    const int pt = 4 * (x >> 1) + (ib & 3), ft = 8 * (x & 1) + (ib >> 2);
    const int pt2 = 8 * (x >> 1) + (ib & 7), ct2 = 4 * (x & 1) + (ib >> 3);
    int u0;
    if (LAST) u0 = ft < 4 ? 10 * ft : 40 + 9 * (ft - 4); else u0 = ft < 12 ? 7 * ft : 84 + 6 * (ft - 12);
    const int nu = LAST ? (ft < 4 ? 10 : 9) : (ft < 12 ? 7 : 6);
    STAMP(LAST ? 5 : 2, 0);
    STAMP(LAST ? 6 : 3, 0);
    if (w < 4) g1_phase<NF, LAST, 0>(a, smem, w, lane, pt, u0, nu, ct2);
    else g1_phase<NF, LAST, 1>(a, smem, w, lane, pt, u0, nu, ct2);
    g2_phase<LAST>(a, smem, w, lane, pt, pt2, ct2);
    END_STAMP(LAST ? 6 : 3);
}

extern "C" void kernel_launch(void* const* d_in, const int* in_sizes, int n_in, void* d_out, int out_size, void* d_ws,
                              size_t ws_size, hipStream_t stream) {
    char* ws = (char*)d_ws;
    constexpr int MLP_LDS = 163840, COL_LDS = 65536;
    (void)hipFuncSetAttribute((const void*)k_mlp<false>, hipFuncAttributeMaxDynamicSharedMemorySize, MLP_LDS);
    (void)hipFuncSetAttribute((const void*)k_mlp<true>, hipFuncAttributeMaxDynamicSharedMemorySize, MLP_LDS);

    PrepArgs pa{(const u16*)d_in[0], (const u16*)d_in[1], (const u16*)d_in[2], (const u16*)d_in[4], (const u16*)d_in[6], (const u16*)d_in[7], ws};
    k0_prep<<<NB_K0, 512, PREP_LDS, stream>>>(pa);

    ColArgs c1{pa, (const u16*)(ws + OFF_XT0), (u16*)(ws + OFF_XT1), ws + OFF_XF1, (const u64*)(ws + OFF_SS0), (const u32*)(ws + OFF_WT), 0, 0};
    k_col<<<NB_COL1, 512, COL_LDS, stream>>>(c1);
    MlpArgs m1{ws + OFF_XF1, ws + OFF_W1F, (const u16*)d_in[5], ws + OFF_H, ws + OFF_X3, ws + OFF_W2F, (const u16*)(ws + OFF_XT1), (u16*)(ws + OFF_XT2),
               (const float*)(ws + OFF_B2P), (const u16*)d_in[7], (u16*)d_out, (u64*)(ws + OFF_SS2), (u32*)(ws + OFF_CNT)};
    k_mlp<false><<<256, 512, MLP_LDS, stream>>>(m1);

    ColArgs c2{pa, (const u16*)(ws + OFF_XT2), (u16*)(ws + OFF_XT3), ws + OFF_XF3, (const u64*)(ws + OFF_SS2), (const u32*)(ws + OFF_WT), 1, 0};
    k_col<<<NB_COL2, 512, COL_LDS, stream>>>(c2);
    MlpArgs m2{ws + OFF_XF3, ws + OFF_W1F, (const u16*)d_in[5], ws + OFF_H2, ws + OFF_X3, ws + OFF_W2O, (const u16*)(ws + OFF_XT1), (u16*)(ws + OFF_XT2),
               (const float*)(ws + OFF_B2P), (const u16*)d_in[7], (u16*)d_out, (u64*)(ws + OFF_SS2), (u32*)(ws + OFF_CNT + 8192)};
    k_mlp<true><<<256, 512, MLP_LDS, stream>>>(m2);
}
```

```cpp
#include <hip/hip_runtime.h>
#include <hip/hip_bf16.h>
#include <stdint.h>
#include <type_traits>

typedef unsigned short u16;
typedef unsigned int u32;
typedef unsigned long long u64;
typedef short s16x4 __attribute__((ext_vector_type(4)));
typedef __bf16 bf16x8 __attribute__((ext_vector_type(8)));
typedef __bf16 bf16x2 __attribute__((ext_vector_type(2)));
typedef float f32x4 __attribute__((ext_vector_type(4)));
typedef float f32x2 __attribute__((ext_vector_type(2)));
typedef u32 u32x4 __attribute__((ext_vector_type(4)));
typedef u32 u32x2 __attribute__((ext_vector_type(2)));
typedef __amdgpu_buffer_rsrc_t rsrc_t;
#define LAS __attribute__((address_space(3)))
#define DI __device__ __forceinline__

constexpr int S_ = 2048, D_ = 640, F_ = 1728, DF_ = 321;
constexpr float EPS = 1e-5f;
constexpr float SS_SCALE = 1048576.0f;

constexpr size_t XT_BYTES = (size_t)D_ * S_ * 2;
constexpr size_t OFF_XT0 = 0;
constexpr size_t OFF_XT1 = OFF_XT0 + XT_BYTES;
constexpr size_t OFF_XT2 = OFF_XT1 + XT_BYTES;
constexpr size_t OFF_XT3 = OFF_XT2 + XT_BYTES;
constexpr size_t OFF_WT = OFF_XT3 + XT_BYTES;
constexpr size_t OFF_W1F = OFF_WT + (size_t)320 * 2048 * 4;
constexpr size_t W1F_BYTES = (size_t)148 * 20 * 1024;
constexpr size_t OFF_W2F = OFF_W1F + W1F_BYTES;
constexpr size_t W2F_BYTES = (size_t)40 * 54 * 1024;
constexpr size_t OFF_W2O = OFF_W2F + W2F_BYTES;
constexpr size_t OFF_H = OFF_W2O + W2F_BYTES;
constexpr size_t H_BYTES = (size_t)128 * 54 * 1024;
constexpr size_t OFF_H2 = OFF_H + H_BYTES;
constexpr size_t OFF_X3 = OFF_H2 + H_BYTES;
constexpr size_t OFF_SS0 = OFF_X3 + (size_t)128 * 40 * 512;
constexpr size_t OFF_SS2 = OFF_SS0 + 2048 * 8;
constexpr size_t OFF_B2P = OFF_SS2 + 2048 * 8;
constexpr size_t OFF_XF1 = OFF_B2P + 4096;
constexpr size_t OFF_XF3 = OFF_XF1 + XT_BYTES;
constexpr size_t X3_BYTES = (size_t)128 * 40 * 512;
constexpr size_t OFF_CNT = OFF_XF3 + XT_BYTES;
constexpr size_t WS_END = OFF_CNT + 65536;

#define STAMP(k, i)
#define CSTAMP(k, i)
#define END_STAMP(k)

DI f32x4 mfma16(bf16x8 a, bf16x8 b, f32x4 c) { return __builtin_amdgcn_mfma_f32_16x16x32_bf16(a, b, c, 0, 0, 0); }
DI u32 pk2(float a, float b) { f32x2 v = {a, b}; const bf16x2 r = __builtin_convertvector(v, bf16x2); return __builtin_bit_cast(u32, r); }
DI bf16x8 pk8(float a0, float a1, float a2, float a3, float a4, float a5, float a6, float a7) {
    u32x4 r = {pk2(a0, a1), pk2(a2, a3), pk2(a4, a5), pk2(a6, a7)};
    return __builtin_bit_cast(bf16x8, r);
}
DI bf16x8 pk8(const float (&a)[8]) { return pk8(a[0], a[1], a[2], a[3], a[4], a[5], a[6], a[7]); }
DI bf16x8 asbf(u32x4 r) { return __builtin_bit_cast(bf16x8, r); }
DI bf16x8 neg8(bf16x8 a) { u32x4 r = __builtin_bit_cast(u32x4, a); r ^= 0x80008000u; return __builtin_bit_cast(bf16x8, r); }
DI float bflo(u32 v) { return __builtin_bit_cast(float, v << 16); }
DI float bfhi(u32 v) { return __builtin_bit_cast(float, v & 0xffff0000u); }
DI float cosr(float rev) { return __builtin_amdgcn_cosf(rev); }
DI float sinr(float rev) { return __builtin_amdgcn_sinf(rev); }
DI int slot32(int q, int j) { return j < 4 ? 4 * q + j : 16 + 4 * q + (j - 4); }
DI s16x4 tr_read(const char* p) { return __builtin_amdgcn_ds_read_tr16_b64_v4i16((LAS s16x4*)p); }
DI bf16x8 cat8(s16x4 lo, s16x4 hi) { return __builtin_bit_cast(bf16x8, __builtin_shufflevector(lo, hi, 0, 1, 2, 3, 4, 5, 6, 7)); }
DI int tok_of_p(int p) { return 32 * (p & 63) + (p >> 6); }
DI rsrc_t mk_rsrc(const void* p, size_t bytes) { return __builtin_amdgcn_make_buffer_rsrc((void*)p, 0, (int)bytes, 0x00020000); }
DI u32x4 bload16(rsrc_t r, int voff, int soff) { return __builtin_amdgcn_raw_buffer_load_b128(r, voff, soff, 0); }
DI float ss_to_float(u64 v) { return (float)(u32)(v >> 32) * 4294967296.0f + (float)(u32)v; }
DI u64 ss_from_float(float s) { return (u64)(fminf(s * SS_SCALE, 1.0e18f) + 0.5f); }
DI void pin(float& x) { asm volatile("" : "+v"(x)); }
DI void pin(bf16x8& x) { asm volatile("" : "+v"(x)); }

struct F32c { bf16x8 c[2], sp[2]; };
DI void f32c_init(F32c& k, int lane) {
    const float m = (float)(lane & 15), q4 = (float)(4 * (lane >> 4));
    float c[4], s[4];
#pragma unroll
    for (int j = 0; j < 4; ++j) { const float a = __builtin_amdgcn_fractf(m * (q4 + (float)j) * (1.0f / 32.0f)); c[j] = cosr(a); s[j] = sinr(a); }
    const u32 sg = (lane & 1) ? 0x80008000u : 0u;
    const u32 c0 = pk2(c[0], c[1]), c1 = pk2(c[2], c[3]), s0 = pk2(s[0], s[1]), s1 = pk2(s[2], s[3]);
    const u32x4 cv = {c0, c1, c0 ^ sg, c1 ^ sg}, sv = {s0, s1, s0 ^ sg, s1 ^ sg};
    k.c[0] = asbf(cv); k.sp[0] = asbf(sv);
    k.c[1] = asbf(cv ^ 0x80000000u); k.sp[1] = asbf(sv ^ 0x80000000u);
}

struct RowC { bf16x8 c[2], sp[2], sm[2], c20, s20m; f32x4 twr[2], twi[2]; int off0, off1; };
constexpr int ROWC_LDS = 10 * 1024;
DI void rowc_build(char* tab, int w, int lane) {
    const int n = lane & 15, q = lane >> 4;
    const float nf = (float)n;
    u32x4* t = (u32x4*)tab + lane;
    if (w == 0) {
        F32c f; f32c_init(f, lane);
        t[0] = __builtin_bit_cast(u32x4, f.c[0]); t[64] = __builtin_bit_cast(u32x4, f.c[1]);
        t[128] = __builtin_bit_cast(u32x4, f.sp[0]); t[192] = __builtin_bit_cast(u32x4, f.sp[1]);
    } else if (w <= 2) {
        float c[8];
#pragma unroll
        for (int j = 0; j < 8; ++j) {
            const int d1 = 8 * q + j;
            const float a = __builtin_amdgcn_fractf((float)d1 * nf * 0.05f);
            const float v = w == 1 ? cosr(a) : -sinr(a);
            c[j] = (d1 < 20 && n <= 10) ? v : 0.f;
        }
        t[(3 + w) * 64] = __builtin_bit_cast(u32x4, pk8(c));
    } else if (w <= 4) {
        const float sc = 0.05590169943749474f;
        f32x4 cr, ci;
#pragma unroll
        for (int i = 0; i < 4; ++i) {
            const float a = (float)(16 * (w - 3) + 4 * q + i) * nf * (1.0f / 640.0f);
            cr[i] = cosr(a) * sc; ci[i] = -sinr(a) * sc;
        }
        t[(6 + w - 3) * 64] = __builtin_bit_cast(u32x4, cr); t[(8 + w - 3) * 64] = __builtin_bit_cast(u32x4, ci);
    }
}
DI void rowc_load(RowC& k, const char* tab, int lane) {
    const u32x4* tb = (const u32x4*)tab + lane;
    k.c[0] = asbf(tb[0]); k.c[1] = asbf(tb[64]); k.sp[0] = asbf(tb[128]); k.sp[1] = asbf(tb[192]);
    k.c20 = asbf(tb[256]); k.s20m = asbf(tb[320]);
    k.twr[0] = __builtin_bit_cast(f32x4, tb[384]); k.twr[1] = __builtin_bit_cast(f32x4, tb[448]);
    k.twi[0] = __builtin_bit_cast(f32x4, tb[512]); k.twi[1] = __builtin_bit_cast(f32x4, tb[576]);
    k.sm[0] = neg8(k.sp[0]); k.sm[1] = neg8(k.sp[1]);
    const int q = lane >> 4, qq = (lane & 15) >> 2, pp = lane & 3;
    const int r0 = min(8 * q + qq, 19), r1 = min(8 * q + 4 + qq, 19);
    k.off0 = r0 * 64 + pp * 8; k.off1 = r1 * 64 + pp * 8;
}
DI float wave_sum63(float v) {
    v += __builtin_bit_cast(float, __builtin_amdgcn_update_dpp(0, __builtin_bit_cast(int, v), 0x111, 0xf, 0xf, true));
    v += __builtin_bit_cast(float, __builtin_amdgcn_update_dpp(0, __builtin_bit_cast(int, v), 0x112, 0xf, 0xf, true));
    v += __builtin_bit_cast(float, __builtin_amdgcn_update_dpp(0, __builtin_bit_cast(int, v), 0x114, 0xf, 0xf, true));
    v += __builtin_bit_cast(float, __builtin_amdgcn_update_dpp(0, __builtin_bit_cast(int, v), 0x118, 0xf, 0xf, true));
    v += __builtin_bit_cast(float, __builtin_amdgcn_update_dpp(0, __builtin_bit_cast(int, v), 0x142, 0xa, 0xf, true));
    v += __builtin_bit_cast(float, __builtin_amdgcn_update_dpp(0, __builtin_bit_cast(int, v), 0x143, 0xc, 0xf, true));
    return v;
}
DI void rowfft(const RowC& k, const char* row, float (&A)[8], float (&B)[8], int lane) {
    f32x4 z = {0.f, 0.f, 0.f, 0.f};
    f32x4 yr[2], yi[2];
#pragma unroll
    for (int mt = 0; mt < 2; ++mt) {
        const bf16x8 a = cat8(tr_read(row + k.off0 + mt * 32), tr_read(row + k.off1 + mt * 32));
        yr[mt] = mfma16(a, k.c20, z); yi[mt] = mfma16(a, k.s20m, z);
    }
    float tr[8], ti[8];
#pragma unroll
    for (int i = 0; i < 8; ++i) {
        const float a = yr[i >> 2][i & 3], b = yi[i >> 2][i & 3], cr = k.twr[i >> 2][i & 3], ci = k.twi[i >> 2][i & 3];
        tr[i] = a * cr - b * ci; ti[i] = a * ci + b * cr;
    }
    const bf16x8 Tr = pk8(tr), Ti = pk8(ti);
    f32x4 ore[2], oim[2];
#pragma unroll
    for (int t = 0; t < 2; ++t) {
        ore[t] = mfma16(k.c[t], Tr, z); ore[t] = mfma16(k.sp[t], Ti, ore[t]);
        oim[t] = mfma16(k.sm[t], Tr, z); oim[t] = mfma16(k.c[t], Ti, oim[t]);
    }
#pragma unroll
    for (int r = 0; r < 4; ++r) { A[2 * r] = ore[0][r]; A[2 * r + 1] = oim[0][r]; B[2 * r] = ore[1][r]; B[2 * r + 1] = oim[1][r]; }
    if (lane == 0) { const float h = 0.7071067811865476f; const float dc = A[0] * h; A[0] = B[0] * h; A[1] = dc; }
}
DI int sw_off(int c) { const int ch = c >> 3; return ((ch ^ ((ch >> 3) & 7)) << 4) + (c & 7) * 2; }
DI int chunk_c0(int n, int q) { return (n == 0 ? 608 : n == 10 ? 576 : 64 * (n - 1)) + 8 * q; }

constexpr int ROWST = 1344;
constexpr int PREP_TAB = 640 * 32 + 16 * ROWST;
constexpr int PREP_LDS = PREP_TAB + ROWC_LDS;

struct PrepArgs { const u16 *x, *wr, *wi, *w1, *w2, *b2; char* ws; };

DI void task_x(const PrepArgs& a, int b, char* smem) {
    const int tid = threadIdx.x, lane = tid & 63, w = tid >> 6;
    const int p0 = 16 * ((b & 7) * 16 + (b >> 3));
    u32x4 v[3];
#pragma unroll
    for (int i = 0; i < 3; ++i) {
        const int id = min(tid + 512 * i, 1279), r = id / 80, ch = id % 80;
        v[i] = *(const u32x4*)(a.x + (size_t)tok_of_p(p0 + r) * D_ + ch * 8);
    }
    __builtin_amdgcn_sched_barrier(0);
    rowc_build(smem + PREP_TAB, w, lane);
    __builtin_amdgcn_sched_barrier(0);
    STAMP(0, 1);
    if (tid < 16) ((u64*)(a.ws + OFF_SS2))[p0 + tid] = 0ull;
    if (b < 32) ((u32*)(a.ws + OFF_CNT))[b * 512 + tid] = 0u;
#pragma unroll
    for (int i = 0; i < 3; ++i) {
        const int id = min(tid + 512 * i, 1279), r = id / 80, ch = id % 80;
        *(u32x4*)(smem + r * ROWST + ch * 16) = v[i];
    }
    STAMP(0, 2);
    __syncthreads();
    STAMP(0, 3);
    RowC k; rowc_load(k, smem + PREP_TAB, lane);
    const int n = lane & 15, q = lane >> 4, c0 = chunk_c0(n, q);
    float A[2][8], B[2][8];
#pragma unroll
    for (int i = 0; i < 2; ++i) rowfft(k, smem + (2 * w + i) * ROWST, A[i], B[i], lane);
#pragma unroll
    for (int i = 0; i < 2; ++i) {
        const int r = 2 * w + i;
        float ss = 0.f;
        if (n <= 10) {
#pragma unroll
            for (int e = 0; e < 8; ++e) ss += (lane == 0 && e == 1) ? 0.f : A[i][e] * A[i][e];
            *(bf16x8*)(smem + r * ROWST + sw_off(c0)) = pk8(A[i]);
        }
        if (n >= 1 && n <= 9) {
#pragma unroll
            for (int e = 0; e < 8; ++e) ss += B[i][e] * B[i][e];
            *(bf16x8*)(smem + r * ROWST + sw_off(c0 + 32)) = pk8(B[i]);
        }
        ss = wave_sum63(ss);
        if (lane == 63) ((u64*)(a.ws + OFF_SS0))[p0 + r] = ss_from_float(ss);
    }
    STAMP(0, 4);
    __syncthreads();
    STAMP(0, 5);
    u16* xt = (u16*)(a.ws + OFF_XT0);
    const int g = lane >> 4, i16 = lane & 15, qq = i16 >> 2, pp = i16 & 3;
#pragma unroll
    for (int i = 0; i < 3; ++i) {
        const int cbk = w + 8 * i;
        if (cbk < 20) {
            const int cb = 32 * cbk + 16 * (g & 1), ph = g >> 1;
            const char* base = smem + (8 * ph + qq) * ROWST + sw_off(cb + 4 * pp);
            const bf16x8 o = cat8(tr_read(base), tr_read(base + 4 * ROWST));
            *(bf16x8*)(xt + (size_t)(cb + i16) * S_ + p0 + 8 * ph) = o;
        }
    }
}

DI void task_W(const PrepArgs& a, int b) {
    const int lane = threadIdx.x & 63, wv = threadIdx.x >> 6, wt = b * 2 + (wv >> 2), kq = wv & 3;
    const int k1 = wt / 5, cg = wt % 5, kp = 1 + 64 * cg + lane;
    const int r = kp % 20, t = kp / 20;
    int j; bool mir = false;
    if (kp == 320) j = 304;
    else if (r == 0) j = 304 + t;
    else if (r < 10) j = 32 * (r - 1) + t;
    else if (r == 10) j = 288 + t;
    else { j = 32 * (19 - r) + 31 - t; mir = true; }
    const rsrc_t rr = mk_rsrc(a.wr, (size_t)S_ * DF_ * 2), ri = mk_rsrc(a.wi, (size_t)S_ * DF_ * 2);
    const int st = (mir ? -64 : 64) * DF_ * 2, off0 = ((mir ? 2048 - k1 : k1) * DF_ + kp) * 2 + 8 * kq * st;
    u32 re[8], im[8];
#pragma unroll
    for (int k2 = 0; k2 < 8; ++k2) {
        int off = off0 + k2 * st;
        if (k2 == 0 && mir && k1 == 0 && kq == 0) off = kp * 2;
        re[k2] = __builtin_amdgcn_raw_buffer_load_b16(rr, off, 0, 0); im[k2] = __builtin_amdgcn_raw_buffer_load_b16(ri, off, 0, 0);
    }
    const u32 sg = mir ? 0x80000000u : 0u;
    u32x4* dst = (u32x4*)(a.ws + OFF_WT + ((size_t)j * 64 + k1) * 128 + kq * 32);
#pragma unroll
    for (int i = 0; i < 2; ++i) {
        u32x4 o;
#pragma unroll
        for (int e = 0; e < 4; ++e) o[e] = (re[4 * i + e] | (im[4 * i + e] << 16)) ^ sg;
        dst[i] = o;
    }
}

DI void task_w1(const PrepArgs& a, int b, char* smem) {
    const int tid = threadIdx.x, lane = tid & 63, w = tid >> 6;
    char* raw = smem;
    char* tr = smem + 640 * 32;
    char* out = raw;
    u32x4 v[3];
#pragma unroll
    for (int i = 0; i < 3; ++i) {
        const int id = min(tid + 512 * i, 1279), d = id >> 1, h = id & 1;
        v[i] = *(const u32x4*)(a.w1 + (size_t)d * F_ + 16 * b + 8 * h);
    }
    __builtin_amdgcn_sched_barrier(0);
    rowc_build(smem + PREP_TAB, w, lane);
    __builtin_amdgcn_sched_barrier(0);
#pragma unroll
    for (int i = 0; i < 3; ++i) *(u32x4*)(raw + min(tid + 512 * i, 1279) * 16) = v[i];
    __syncthreads();
    const int g = lane >> 4, i16 = lane & 15, qq = i16 >> 2, pp = i16 & 3;
#pragma unroll
    for (int i = 0; i < 5; ++i) {
        const int d0 = 16 * (w + 8 * i) + 4 * g;
        const s16x4 t = tr_read(raw + (d0 + qq) * 32 + pp * 8);
        *(s16x4*)(tr + i16 * ROWST + d0 * 2) = t;
    }
    RowC k; rowc_load(k, smem + PREP_TAB, lane);
    __syncthreads();
    const int n = lane & 15, q = lane >> 4, c0 = chunk_c0(n, q);
    float A[2][8], B[2][8];
#pragma unroll
    for (int i = 0; i < 2; ++i) rowfft(k, tr + (2 * w + i) * ROWST, A[i], B[i], lane);
#pragma unroll
    for (int i = 0; i < 2; ++i) {
        const int f = 2 * w + i;
        if (lane == 0) A[i][1] = 0.f;
        if (n <= 10) *(bf16x8*)(out + ((c0 >> 5) * 64 + q * 16 + f) * 16) = pk8(A[i]);
        if (n >= 1 && n <= 9) *(bf16x8*)(out + (((c0 >> 5) + 1) * 64 + q * 16 + f) * 16) = pk8(B[i]);
    }
    __syncthreads();
    u32x4* dst = (u32x4*)(a.ws + OFF_W1F + (size_t)b * 20 * 1024);
#pragma unroll
    for (int i = 0; i < 3; ++i) { const int id = tid + 512 * i; if (id < 1280) dst[id] = *(const u32x4*)(out + id * 16); }
}

template <bool SW> DI void w2_frags(const char* img, char* dstbase, int b, int lane, int w) {
    const int g = lane >> 4, i16 = lane & 15, qq = i16 >> 2, pp = i16 & 3;
#pragma unroll
    for (int i = 0; i < 5; ++i) {
        const int cu = w + 8 * i, c = 16 * cu + 4 * pp;
        const s16x4 v = tr_read(img + (4 * g + qq) * ROWST + (SW ? sw_off(c) : c * 2));
        *(s16x4*)(dstbase + ((size_t)(cu * 54 + (b >> 1)) * 64 + lane) * 16 + (b & 1) * 8) = v;
    }
}
DI void task_w2(const PrepArgs& a, int b, char* smem) {
    const int tid = threadIdx.x, lane = tid & 63, w = tid >> 6;
    u32x4 v[3];
#pragma unroll
    for (int i = 0; i < 3; ++i) {
        const int id = min(tid + 512 * i, 1279), r = id / 80, ch = id % 80;
        v[i] = *(const u32x4*)(a.w2 + (size_t)(16 * b + r) * D_ + ch * 8);
    }
    __builtin_amdgcn_sched_barrier(0);
    rowc_build(smem + PREP_TAB, w, lane);
    __builtin_amdgcn_sched_barrier(0);
#pragma unroll
    for (int i = 0; i < 3; ++i) { const int id = min(tid + 512 * i, 1279), r = id / 80, ch = id % 80; *(u32x4*)(smem + r * ROWST + ch * 16) = v[i]; }
    __syncthreads();
    RowC k; rowc_load(k, smem + PREP_TAB, lane);
    w2_frags<false>(smem, a.ws + OFF_W2O, b, lane, w);
    const int n = lane & 15, q = lane >> 4, c0 = chunk_c0(n, q);
    float A[2][8], B[2][8];
#pragma unroll
    for (int i = 0; i < 2; ++i) rowfft(k, smem + (2 * w + i) * ROWST, A[i], B[i], lane);
    __syncthreads();
#pragma unroll
    for (int i = 0; i < 2; ++i) {
        const int r = 2 * w + i;
        if (n <= 10) *(bf16x8*)(smem + r * ROWST + sw_off(c0)) = pk8(A[i]);
        if (n >= 1 && n <= 9) *(bf16x8*)(smem + r * ROWST + sw_off(c0 + 32)) = pk8(B[i]);
    }
    __syncthreads();
    w2_frags<true>(smem, a.ws + OFF_W2F, b, lane, w);
}

DI int kcol_of(int jc) { return jc < 288 ? (jc >> 5) + 1 + 20 * (jc & 31) : jc < 304 ? 10 + 20 * (jc - 288) : 20 * (jc - 304); }
DI void task_Q(const PrepArgs& a, int u) {
    const int tid = threadIdx.x;
#pragma unroll
    for (int ii = 0; ii < 3; ++ii) {
        const int i = tid + 512 * ii;
        if (i < 1280) {
            const int ks = i >> 6, l = i & 63, d = 16 * u + (l & 15), q = l >> 4;
            float v[8];
#pragma unroll
            for (int j = 0; j < 8; j += 2) {
                const int jc = 16 * ks + 4 * q + (j >> 1), kk = kcol_of(jc);
                const float ang = (float)((kk * d) % 640) * (1.0f / 640.0f);
                float c = cosr(ang) * 0.05590169943749474f, s = -sinr(ang) * 0.05590169943749474f;
                if (jc == 304) { c = ((d & 1) ? -1.f : 1.f) * 0.03952847075210474f; s = 0.03952847075210474f; }
                v[j] = c; v[j + 1] = s;
            }
            *(bf16x8*)(a.ws + OFF_W1F + ((size_t)(108 + u) * 20 * 64 + i) * 16) = pk8(v);
        }
    }
}
DI void task_b(const PrepArgs& a, char* smem) {
    const int tid = threadIdx.x, lane = tid & 63;
    if (tid < 80) *(u32x4*)(smem + tid * 16) = *(const u32x4*)(a.b2 + tid * 8);
    rowc_build(smem + PREP_TAB, tid >> 6, lane);
    __syncthreads();
    RowC k; rowc_load(k, smem + PREP_TAB, lane);
    float A[8], B[8];
    rowfft(k, smem, A, B, lane);
    if (tid < 64) {
        const int n = lane & 15, q = lane >> 4, c0 = chunk_c0(n, q);
        float* o = (float*)(a.ws + OFF_B2P);
        if (n <= 10) for (int e = 0; e < 8; ++e) o[c0 + e] = A[e];
        if (n >= 1 && n <= 9) for (int e = 0; e < 8; ++e) o[c0 + 32 + e] = B[e];
    }
}

constexpr int NB_K0 = 128 + 108 + 108 + 160 + 1 + 40;
__global__ __launch_bounds__(512, 4) void k0_prep(PrepArgs a) {
    extern __shared__ __attribute__((aligned(16))) char smem[];
    const int b = blockIdx.x;
    STAMP(0, 0);
    if (b < 128) task_x(a, b, smem);
    else if (b < 236) task_w1(a, b - 128, smem);
    else if (b < 344) task_w2(a, b - 236, smem);
    else if (b < 504) task_W(a, b - 344);
    else if (b == 504) task_b(a, smem);
    else task_Q(a, b - 505);
    END_STAMP(0);
}

template <int N> DI void wait_vm() { asm volatile("s_waitcnt vmcnt(%0)" ::"n"(N) : "memory"); }
DI void wait_lgkm() { asm volatile("s_waitcnt lgkmcnt(0)" ::: "memory"); }
DI void barrier() { __builtin_amdgcn_s_barrier(); }
DI void dma16(u32x4 srd, int voff, int soff, u32 ldsaddr) {
    asm volatile("s_mov_b32 m0, %0\n\ts_nop 0\n\tbuffer_load_dwordx4 %1, %2, %3 offen lds" ::"s"(ldsaddr), "v"(voff), "s"(srd), "s"(soff) : "memory", "m0");
}
template <int LIM> DI void dma16_lim(u32x4 srd, int voff, int soff, u32 ldsaddr, int w) {
    asm volatile("s_cmp_lt_i32 %4, %5\n\ts_cbranch_scc0 .Lskip%=\n\ts_mov_b32 m0, %0\n\ts_nop 0\n\tbuffer_load_dwordx4 %1, %2, %3 offen lds\n.Lskip%=:"
                 ::"s"(ldsaddr), "v"(voff), "s"(srd), "s"(soff), "s"(w), "n"(LIM) : "memory", "m0", "scc");
}
DI u32x4 mk_srd(const void* p, size_t bytes) {
    const u64 a = (u64)p;
    u32x4 r = {(u32)a, (u32)(a >> 32) & 0xffffu, (u32)bytes, 0x00020000u};
    return r;
}
DI float row_sum15(float v) {
    v += __builtin_bit_cast(float, __builtin_amdgcn_update_dpp(0, __builtin_bit_cast(int, v), 0x111, 0xf, 0xf, true));
    v += __builtin_bit_cast(float, __builtin_amdgcn_update_dpp(0, __builtin_bit_cast(int, v), 0x112, 0xf, 0xf, true));
    v += __builtin_bit_cast(float, __builtin_amdgcn_update_dpp(0, __builtin_bit_cast(int, v), 0x114, 0xf, 0xf, true));
    v += __builtin_bit_cast(float, __builtin_amdgcn_update_dpp(0, __builtin_bit_cast(int, v), 0x118, 0xf, 0xf, true));
    return v;
}
template <int I, int N, class F> DI void static_for(F&& f) {
    if constexpr (I < N) { f(std::integral_constant<int, I>{}); static_for<I + 1, N>(f); }
}

constexpr int G1_P = 4, G1_PW = 5;
constexpr int SC1 = 16;
typedef __attribute__((address_space(1))) u32 gu32;
DI u32 ld_agent(const u32* p) { return __hip_atomic_load((gu32*)p, __ATOMIC_RELAXED, __HIP_MEMORY_SCOPE_AGENT); }
DI void add_agent(u32* p) { __hip_atomic_fetch_add((gu32*)p, 1u, __ATOMIC_RELAXED, __HIP_MEMORY_SCOPE_AGENT); }
DI void signal_all(char* ws, int which) {
    if (threadIdx.x == 0) {
        u32* c = (u32*)(ws + OFF_CNT + 16384 + which * 8192) + (blockIdx.x & 7) * 32;
#pragma unroll
        for (int r = 0; r < 8; ++r) add_agent(c + r * 256);
    }
}
DI void poll_all(char* ws, int which, u32 per_shard, int w, int lane) {
    if (w == 0) {
        const u32* c = (const u32*)(ws + OFF_CNT + 16384 + which * 8192) + ((blockIdx.x >> 3) & 7) * 256 + (lane & 7) * 32;
        for (u32 spins = 0;; ++spins) {
            if (__all(ld_agent(c) >= per_shard)) break;
            __builtin_amdgcn_s_sleep(1);
            if (spins > (1u << 18)) { if (lane == 0) __hip_atomic_store((gu32*)(ws + OFF_CNT + 49152), 1u, __ATOMIC_RELAXED, __HIP_MEMORY_SCOPE_AGENT); break; }
        }
    }
}

struct MainArgs { PrepArgs p; const u16* b1; u16* out; };
DI int img128(int row, int ch) { return row * 128 + ((ch ^ ((row >> 1) & 7)) << 4); }
template <bool LAST> DI void w1_prefetch(char* ws, char* smem, int w, int lane, int u0);

template <int L>
DI void col_task(char* ws, int b, char* smem, int u0) {
    const int tid = threadIdx.x & 255, lane = tid & 63, w = tid >> 6, half = threadIdx.x >> 8;
    const int j = 2 * b + half;
    char* Ure = smem + half * 24576; char* Uim = Ure + 4096; char* Ere = Ure + 8192; char* Eim = Ure + 12288;
    char* Rre = Ure + 16384; char* Rim = Ure + 20480;
    const int n = lane & 15, q = lane >> 4;
    const int wv = __builtin_amdgcn_readfirstlane(threadIdx.x >> 6);
    const rsrc_t rx = mk_rsrc(ws + (L == 0 ? OFF_XT0 : OFF_XT2), XT_BYTES), rs = mk_rsrc(ws + (L == 0 ? OFF_SS0 : OFF_SS2), 2048 * 8);
    u32x4 vr, vi, sv[4];
    auto load_in = [&]() __attribute__((always_inline)) {
        vr = __builtin_amdgcn_raw_buffer_load_b128(rx, (2 * j * S_ + tid * 8) * 2, 0, L == 0 ? 0 : SC1);
        vi = __builtin_amdgcn_raw_buffer_load_b128(rx, ((2 * j + 1) * S_ + tid * 8) * 2, 0, L == 0 ? 0 : SC1);
#pragma unroll
        for (int i = 0; i < 4; ++i) sv[i] = __builtin_amdgcn_raw_buffer_load_b128(rs, tid * 64 + 16 * i, 0, L == 0 ? 0 : SC1);
    };
    if (L == 0) load_in();
    u32x4 wf[2];
#pragma unroll
    for (int t = 0; t < 2; ++t) wf[t] = *(const u32x4*)((const u32*)(ws + OFF_WT) + ((size_t)j * 64 + 16 * w + n) * 32 + 16 * t + 4 * q);
    __builtin_amdgcn_sched_barrier(0);
    F32c f; f32c_init(f, lane);
    bf16x8 c32[2] = {f.c[0], f.c[1]}, s32p[2] = {f.sp[0], f.sp[1]}, s32m[2];
    pin(c32[0]); pin(c32[1]); pin(s32p[0]); pin(s32p[1]);
    bf16x8 c64[2], s64p[2], s64m[2];
    const float k1f = (float)(16 * w + n);
    {
        float c[8], s[8];
#pragma unroll
        for (int e = 0; e < 8; ++e) {
            const float ang = __builtin_amdgcn_fractf((float)(8 * q + e) * k1f * (1.0f / 64.0f));
            c[e] = cosr(ang); s[e] = sinr(ang);
        }
        c64[0] = pk8(c); s64p[0] = pk8(s);
        const u32 sg = (lane & 1) ? 0x80008000u : 0u;
        c64[1] = asbf(__builtin_bit_cast(u32x4, c64[0]) ^ sg); s64p[1] = asbf(__builtin_bit_cast(u32x4, s64p[0]) ^ sg);
        pin(c64[0]); pin(c64[1]); pin(s64p[0]); pin(s64p[1]);
    }
    float twr[8], twi[8], ivr[8], ivi[8];
#pragma unroll
    for (int i = 0; i < 8; ++i) {
        const float af = (float)(16 * (i >> 2) + 4 * q + (i & 3)) * k1f * (1.0f / 2048.0f);
        twr[i] = cosr(af); twi[i] = -sinr(af);
        const float ai = (float)(16 * w + 4 * q + (i & 3)) * (float)(16 * (i >> 2) + n) * (1.0f / 2048.0f);
        ivr[i] = cosr(ai) * (1.0f / 2048.0f); ivi[i] = sinr(ai) * (1.0f / 2048.0f);
        pin(twr[i]); pin(twi[i]); pin(ivr[i]); pin(ivi[i]);
    }
    __builtin_amdgcn_sched_barrier(0);
    STAMP(L ? 4 : 1, 1);
    if (L == 1) {
        poll_all(ws, 1, 32u, wv, lane);
        barrier();
        STAMP(4, 5);
        load_in();
        __builtin_amdgcn_sched_barrier(0);
    }
    s64m[0] = neg8(s64p[0]); s64m[1] = neg8(s64p[1]);
    {
        float r[8];
#pragma unroll
        for (int i = 0; i < 8; ++i) {
            const float s = (float)sv[i >> 1][2 * (i & 1) + 1] * 4294967296.0f + (float)sv[i >> 1][2 * (i & 1)];
            r[i] = __builtin_amdgcn_rsqf(s * (1.0f / (SS_SCALE * 640.0f)) + EPS);
        }
        const bf16x8 ur = pk8(bflo(vr[0]) * r[0], bfhi(vr[0]) * r[1], bflo(vr[1]) * r[2], bfhi(vr[1]) * r[3],
                              bflo(vr[2]) * r[4], bfhi(vr[2]) * r[5], bflo(vr[3]) * r[6], bfhi(vr[3]) * r[7]);
        bf16x8 ui = pk8(bflo(vi[0]) * r[0], bfhi(vi[0]) * r[1], bflo(vi[1]) * r[2], bfhi(vi[1]) * r[3],
                        bflo(vi[2]) * r[4], bfhi(vi[2]) * r[5], bflo(vi[3]) * r[6], bfhi(vi[3]) * r[7]);
        if (j == 304) ui = asbf(u32x4{0u, 0u, 0u, 0u});
        const int o = img128(tid >> 3, tid & 7);
        *(bf16x8*)(Ure + o) = ur; *(bf16x8*)(Uim + o) = ui;
        *(u32x4*)(Rre + o) = vr; *(u32x4*)(Rim + o) = vi;
    }
    __syncthreads();
    STAMP(L ? 4 : 1, 2);
    const f32x4 z = {0.f, 0.f, 0.f, 0.f};
    f32x4 dr[2], di[2];
#pragma unroll
    for (int mt = 0; mt < 2; ++mt) {
        dr[mt] = z; di[mt] = z;
#pragma unroll
        for (int ks = 0; ks < 2; ++ks) {
            const int o = img128(16 * mt + n, 4 * ks + q);
            const bf16x8 ar = *(const bf16x8*)(Ure + o), ai = *(const bf16x8*)(Uim + o);
            dr[mt] = mfma16(ar, c64[ks], dr[mt]); dr[mt] = mfma16(ai, s64p[ks], dr[mt]);
            di[mt] = mfma16(ar, s64m[ks], di[mt]); di[mt] = mfma16(ai, c64[ks], di[mt]);
        }
    }
    float tr[8], ti[8];
#pragma unroll
    for (int i = 0; i < 8; ++i) {
        const float x = dr[i >> 2][i & 3], y = di[i >> 2][i & 3];
        tr[i] = x * twr[i] - y * twi[i]; ti[i] = x * twi[i] + y * twr[i];
    }
    bf16x8 Tr = pk8(tr), Ti = pk8(ti);
    s32m[0] = neg8(s32p[0]); s32m[1] = neg8(s32p[1]);
    f32x4 orr[2], oi[2];
#pragma unroll
    for (int t = 0; t < 2; ++t) {
        orr[t] = mfma16(c32[t], Tr, z); orr[t] = mfma16(s32p[t], Ti, orr[t]);
        oi[t] = mfma16(s32m[t], Tr, z); oi[t] = mfma16(c32[t], Ti, oi[t]);
    }
#pragma unroll
    for (int i = 0; i < 8; ++i) {
        const float x = orr[i >> 2][i & 3], y = oi[i >> 2][i & 3];
        const u32 wv = wf[i >> 2][i & 3];
        const float wre = bflo(wv), wim = bfhi(wv);
        tr[i] = x * wre - y * wim; ti[i] = x * wim + y * wre;
    }
    Tr = pk8(tr); Ti = pk8(ti);
#pragma unroll
    for (int nt = 0; nt < 2; ++nt) {
        f32x4 zr = mfma16(Tr, c32[nt], z); zr = mfma16(Ti, s32m[nt], zr);
        f32x4 zi = mfma16(Tr, s32p[nt], z); zi = mfma16(Ti, c32[nt], zi);
        float er[4], ei[4];
#pragma unroll
        for (int r = 0; r < 4; ++r) {
            const float cr = ivr[4 * nt + r], ci = ivi[4 * nt + r];
            er[r] = zr[r] * cr - zi[r] * ci; ei[r] = zr[r] * ci + zi[r] * cr;
        }
        const int o = img128(16 * nt + n, 2 * w + (q >> 1)) + (q & 1) * 8;
        u32x2 pr = {pk2(er[0], er[1]), pk2(er[2], er[3])}, pi = {pk2(ei[0], ei[1]), pk2(ei[2], ei[3])};
        *(u32x2*)(Ere + o) = pr; *(u32x2*)(Eim + o) = pi;
    }
    __syncthreads();
    STAMP(L ? 4 : 1, 3);
    const rsrc_t ro = mk_rsrc(ws + OFF_XT1, XT_BYTES), rf = mk_rsrc(ws + (L == 0 ? OFF_XF1 : OFF_XF3), XT_BYTES);
    char* O = Ure;
#pragma unroll
    for (int nt = 0; nt < 2; ++nt) {
        f32x4 yr = z, yi = z;
#pragma unroll
        for (int ks = 0; ks < 2; ++ks) {
            const int o = img128(16 * nt + n, 4 * ks + q);
            const bf16x8 br = *(const bf16x8*)(Ere + o), bi = *(const bf16x8*)(Eim + o);
            yr = mfma16(c64[ks], br, yr); yr = mfma16(s64m[ks], bi, yr);
            yi = mfma16(s64p[ks], br, yi); yi = mfma16(c64[ks], bi, yi);
        }
        if (j == 304) yi = z;
        const int p = (16 * nt + n) * 64 + 16 * w + 4 * q;
        u32x2 rr[2], ri[2];
        { const int o = img128(16 * nt + n, 2 * w + (q >> 1)) + (q & 1) * 8; rr[nt] = *(const u32x2*)(Rre + o); ri[nt] = *(const u32x2*)(Rim + o); }
        const float fr[4] = {bflo(rr[nt][0]) + yr[0], bfhi(rr[nt][0]) + yr[1], bflo(rr[nt][1]) + yr[2], bfhi(rr[nt][1]) + yr[3]};
        const float fi[4] = {bflo(ri[nt][0]) + yi[0], bfhi(ri[nt][0]) + yi[1], bflo(ri[nt][1]) + yi[2], bfhi(ri[nt][1]) + yi[3]};
        if (L == 0) {
            u32x2 orv = {pk2(fr[0], fr[1]), pk2(fr[2], fr[3])}, oiv = {pk2(fi[0], fi[1]), pk2(fi[2], fi[3])};
            __builtin_amdgcn_raw_buffer_store_b64(orv, ro, (2 * j * S_ + p) * 2, 0, SC1);
            __builtin_amdgcn_raw_buffer_store_b64(oiv, ro, ((2 * j + 1) * S_ + p) * 2, 0, SC1);
        }
        u32x4 ov = {pk2(fr[0], fi[0]), pk2(fr[1], fi[1]), pk2(fr[2], fi[2]), pk2(fr[3], fi[3])};
        *(u32x4*)(O + p * 4) = ov;
    }
    __syncthreads();
    {
        const int c0 = 4 * b, dst = ((c0 >> 5) * 64 + ((c0 & 31) >> 3) * 16) * 16 + (c0 & 4) * 2;
        const u32* O0 = (const u32*)smem; const u32* O1 = (const u32*)(smem + 24576);
#pragma unroll
        for (int i = 0; i < 4; ++i) {
            const int p = threadIdx.x + 512 * i;
            u32x2 v = {O0[p], O1[p]};
            __builtin_amdgcn_raw_buffer_store_b64(v, rf, (p >> 4) * 20480 + (p & 15) * 16, dst, SC1);
        }
    }
    STAMP(L ? 4 : 1, 4);
    asm volatile("" ::: "memory");
    __builtin_amdgcn_sched_barrier(0);
    w1_prefetch<L == 1>(ws, smem, wv, lane, u0);
    wait_lgkm();
    wait_vm<(L == 1 ? 10 : 5)>();
    barrier();
    signal_all(ws, L == 0 ? 0 : 2);
    STAMP(L ? 4 : 1, 15);
}

DI f32x2 gelu_tanh2(f32x2 x) {
    const float k1 = -2.302208198f, k2 = -0.1029432397f;
    const f32x2 c1 = {k1, k1}, c2 = {k2, k2}, one = {1.0f, 1.0f};
    const f32x2 t = x * (x * x * c2 + c1);
    f32x2 e = {__builtin_amdgcn_exp2f(t[0]), __builtin_amdgcn_exp2f(t[1])};
    e += one;
    const f32x2 r = {__builtin_amdgcn_rcpf(e[0]), __builtin_amdgcn_rcpf(e[1])};
    return x * r;
}
constexpr int G2_PH = 4;
constexpr int W2STG = 20480;
DI u32 w2slot(int t) { return 163840 - ((t & 7) + 1) * W2STG; }
template <bool LAST> constexpr int w1slot(int st) { return LAST ? 163840 - (st % 8 + 1) * 20480 : 163840 - (st + 1) * 14336; }

template <bool LAST, int I> DI void w1_piece(u32x4 srd, u32 lds0, int w, int vo, int u0, int st) {
    constexpr int WP = LAST ? 20 : 14;
    const int pc = w + 8 * I;
    const u32 dst = lds0 + w1slot<LAST>(st) + pc * 1024;
    const int so = (u0 + (pc >> 1)) * 20480 + st * 2048 + (pc & 1) * 1024;
    if (8 * I + 7 < WP) dma16(srd, vo, so, dst); else dma16_lim<(WP & 7)>(srd, vo, so, dst, w);
}
template <bool LAST> DI void w1_prefetch(char* ws, char* smem, int w, int lane, int u0) {
    const u32x4 srd = mk_srd(ws + OFF_W1F, W1F_BYTES);
    const u32 lds0 = (u32)(size_t)(LAS char*)smem;
#pragma unroll
    for (int st = 0; st < G1_PW; ++st) {
        w1_piece<LAST, 0>(srd, lds0, w, lane * 16, u0, st);
        w1_piece<LAST, 1>(srd, lds0, w, lane * 16, u0, st);
        if (LAST) w1_piece<LAST, 2>(srd, lds0, w, lane * 16, u0, st);
    }
}

constexpr int g1_younger(int ssync, int t, int nlo) {
    int cnt = 0; bool seen = false;
    for (int s = 0; s < ssync; ++s) {
        if (s + G1_PW < 10) { if (seen) cnt += nlo; if (s + G1_PW == t) seen = true; }
        if (s + G1_P < 10 && seen) cnt += 2;
    }
    return cnt;
}

template <int I> DI void w2_issue1(u32x4 srd, u32 lds0, int w, int vo, int ct, int t) {
    const int pc = w + 8 * I, q = pc / 5, cu = pc - 5 * q;
    const int so = ((5 * ct + cu) * 54 + (q < 2 ? 14 * q : 28 + 13 * (q - 2)) + t) * 1024;
    if (I < 2) dma16(srd, vo, so, lds0 + w2slot(t) + pc * 1024); else dma16_lim<4>(srd, vo, so, lds0 + w2slot(t) + pc * 1024, w);
}
DI void w2_issue(u32x4 srd, u32 lds0, int w, int vo, int ct, int t) {
    w2_issue1<0>(srd, lds0, w, vo, ct, t); w2_issue1<1>(srd, lds0, w, vo, ct, t); w2_issue1<2>(srd, lds0, w, vo, ct, t);
}

template <int NF, bool LAST, int KG>
DI void g1_phase(const MainArgs& a, char* smem, int w, int lane, int pt, int u0, int nu, int ct2) {
    constexpr int P = G1_P, PW = G1_PW;
    constexpr int WP = 2 * NF, WR = (WP + 7) / 8, NLO = WP / 8, KID = LAST ? 5 : 2;
    char* ws = a.p.ws;
    const int pq = w & 3, g = lane >> 4;
    const u32x4 srdW = mk_srd(ws + OFF_W1F, W1F_BYTES), srd2 = mk_srd(ws + (LAST ? OFF_W2O : OFF_W2F), W2F_BYTES);
    const u32 lds0 = (u32)(size_t)(LAS char*)smem;
    const rsrc_t rx = mk_rsrc(ws + (LAST ? OFF_XF3 : OFF_XF1), XT_BYTES);
    const int xs = (8 * pt + 2 * pq) * 20480 + KG * 1024, vo = lane * 16;
    auto xload = [&](int st, int pu) __attribute__((always_inline)) { return asbf(__builtin_amdgcn_raw_buffer_load_b128(rx, vo, xs + pu * 20480 + st * 2048, SC1)); };
    bf16x8 xb[P][2];
#pragma unroll
    for (int st = 0; st < P; ++st) { xb[st][0] = xload(st, 0); xb[st][1] = xload(st, 1); __builtin_amdgcn_sched_barrier(0); }
    f32x4 acc[NF][2];
#pragma unroll
    for (int i = 0; i < NF; ++i) { acc[i][0] = f32x4{0, 0, 0, 0}; acc[i][1] = f32x4{0, 0, 0, 0}; }
    f32x4 gr[2] = {f32x4{0, 0, 0, 0}, f32x4{0, 0, 0, 0}};
    bf16x8 wa[2][NF];
#pragma unroll
    for (int i = 0; i < NF; ++i) wa[0][i] = *(const bf16x8*)(smem + w1slot<LAST>(0) + KG * 1024 + lane * 16 + i * 2048);
    static_for<0, 10>([&](auto ST) __attribute__((always_inline)) {
        constexpr int st = decltype(ST)::value;
        if constexpr (LAST && st == 2) { wait_lgkm(); barrier(); }
        if constexpr (st == 4 || st == 7) {
            wait_lgkm();
            wait_vm<g1_younger(st, st == 7 ? 9 : 7, NLO)>();
            barrier();
        }
        bf16x8 nx[2];
        const char* rbase = smem + w1slot<LAST>(st + 1) + KG * 1024 + lane * 16;
        constexpr int NV = WR + 2, V0 = KG == 0 ? 0 : NF - NV;
        static_for<0, NF>([&](auto I) __attribute__((always_inline)) {
            constexpr int i = decltype(I)::value;
            __builtin_amdgcn_sched_barrier(0);
            acc[i][0] = mfma16(wa[st & 1][i], xb[st % P][0], acc[i][0]);
            if constexpr (st + 1 < 10) wa[(st + 1) & 1][i] = *(const bf16x8*)(rbase + i * 2048);
            acc[i][1] = mfma16(wa[st & 1][i], xb[st % P][1], acc[i][1]);
            if constexpr (i < 2) {
                u32x4 xv = __builtin_bit_cast(u32x4, xb[st % P][i]);
                if constexpr (st == 9 && KG == 1) { if (g == 0) xv[0] &= 0x0000ffffu; }
                gr[i] = mfma16(asbf(xv), asbf(xv), gr[i]);
            }
            __builtin_amdgcn_sched_barrier(0);
            constexpr int v = i - V0;
            if constexpr (v >= 0 && v < WR) { if constexpr (st + PW < 10) w1_piece<LAST, v>(srdW, lds0, w, vo, u0, st + PW); }
            else if constexpr (v >= WR && v < NV) { if constexpr (st + P < 10) nx[v - WR] = xload(st + P, v - WR); }
            if constexpr (st >= 6) {
                constexpr int sl = i - (KG == 0 ? 0 : NF - 4), e = 4 * (st - 6) + sl;
                if constexpr (sl >= 0 && sl < 4 && e < (LAST ? 9 : 15)) w2_issue1<e % 3>(srd2, lds0, w, vo, ct2, (LAST ? 2 : 0) + e / 3);
            }
        });
        __builtin_amdgcn_sched_barrier(0);
        if constexpr (st + P < 10) { xb[st % P][0] = nx[0]; xb[st % P][1] = nx[1]; }
        if (st == 4) STAMP(KID, 2);
    });
    STAMP(KID, 3);
    u32x2 bv[NF];
#pragma unroll
    for (int i = 0; i < NF; ++i) bv[i] = *(const u32x2*)(a.b1 + min(16 * (u0 + i), F_ - 16) + 4 * g);
    __builtin_amdgcn_sched_barrier(0);
    wait_lgkm();
    barrier();
    float* sx = (float*)(smem + 8 * NF * 512);
#pragma unroll
    for (int i = 0; i < NF; ++i) {
        const f32x4 t = acc[i][1 - KG];
        u32x2 pv = {pk2(t[0], t[1]), pk2(t[2], t[3])};
        *(u32x2*)(smem + ((w * NF + i) * 64 + lane) * 8) = pv;
    }
    float st2[2];
    {
        const int n = lane & 15;
#pragma unroll
        for (int pu = 0; pu < 2; ++pu) {
            const float d01 = (n & 1) ? gr[pu][1] : gr[pu][0], d23 = (n & 1) ? gr[pu][3] : gr[pu][2];
            float d = (n & 2) ? d23 : d01;
            d = (g == (n >> 2)) ? d : 0.f;
            d += __shfl_xor(d, 16); d += __shfl_xor(d, 32);
            st2[pu] = d;
        }
    }
    if (lane < 16) sx[w * 16 + lane] = st2[1 - KG];
    wait_lgkm();
    barrier();
    STAMP(KID, 5);
    const float v = (st2[KG] + sx[(w ^ 4) * 16 + (lane & 15)]) * (1.0f / 640.0f);
    const float r1 = __builtin_amdgcn_rsqf(v + EPS);
    const float rr = r1 * __builtin_amdgcn_rsqf(v * r1 * r1 + EPS);
    const int punit = 8 * pt + 2 * pq + KG;
    u32x2 ov[NF], pv[NF];
#pragma unroll
    for (int i = 0; i < NF; ++i) pv[i] = *(const u32x2*)(smem + (((w ^ 4) * NF + i) * 64 + lane) * 8);
    static_for<0, NF>([&](auto I) __attribute__((always_inline)) {
        constexpr int i = decltype(I)::value;
        if constexpr (LAST ? i < 2 : i == 0) {
            __builtin_amdgcn_sched_barrier(0);
            w2_issue(srd2, lds0, w, vo, ct2, LAST ? i : 5);
            __builtin_amdgcn_sched_barrier(0);
        }
        const f32x2 o0 = {acc[i][KG][0] + bflo(pv[i][0]), acc[i][KG][1] + bfhi(pv[i][0])}, o1 = {acc[i][KG][2] + bflo(pv[i][1]), acc[i][KG][3] + bfhi(pv[i][1])};
        const f32x2 rr2 = {rr, rr};
        const f32x2 b0 = {bflo(bv[i][0]), bfhi(bv[i][0])}, b1 = {bflo(bv[i][1]), bfhi(bv[i][1])};
        f32x2 h0 = gelu_tanh2(o0 * rr2 + b0), h1 = gelu_tanh2(o1 * rr2 + b1);
        if (LAST) {
            const bool lin = u0 + i >= 108;
            h0[0] = lin ? o0[0] : h0[0]; h0[1] = lin ? o0[1] : h0[1]; h1[0] = lin ? o1[0] : h1[0]; h1[1] = lin ? o1[1] : h1[1];
        }
        ov[i] = u32x2{pk2(h0[0], h0[1]), pk2(h1[0], h1[1])};
    });
    __builtin_amdgcn_sched_barrier(0);
    const rsrc_t rh = mk_rsrc(ws + (LAST ? OFF_H2 : OFF_H), H_BYTES), r3 = mk_rsrc(ws + OFF_X3, X3_BYTES);
    const int hb = punit * 54 * 1024;
    auto st1 = [&](int i) __attribute__((always_inline)) {
        const int u = u0 + i;
        if (i < nu) {
            if (!LAST || u < 108) __builtin_amdgcn_raw_buffer_store_b64(ov[i], rh, lane * 16 + (u & 1) * 8, hb + (u >> 1) * 1024, SC1);
            else __builtin_amdgcn_raw_buffer_store_b64(ov[i], r3, lane * 8, (punit * 40 + (u - 108)) * 512, SC1);
        }
    };
    auto st2p = [&](int i) __attribute__((always_inline)) {
        const int u = u0 + i;
        if (i + 1 < nu && (!LAST || u + 1 < 108)) {
            u32x4 q4 = {ov[i][0], ov[i][1], ov[i + 1][0], ov[i + 1][1]};
            __builtin_amdgcn_raw_buffer_store_b128(q4, rh, lane * 16, hb + (u >> 1) * 1024, SC1);
        } else { st1(i); st1(i + 1); }
    };
    if (u0 & 1) {
        st1(0);
#pragma unroll
        for (int i = 1; i + 1 < NF; i += 2) st2p(i);
        if ((NF & 1) == 0) st1(NF - 1);
    } else {
#pragma unroll
        for (int i = 0; i + 1 < NF; i += 2) st2p(i);
        if (NF & 1) st1(NF - 1);
    }
    STAMP(KID, 4);
}

template <bool LAST>
DI void g2_phase(const MainArgs& a, char* smem, int w, int lane, int pt1, int pt, int ct) {
    constexpr int PH = G2_PH, KID = LAST ? 6 : 3;
    char* ws = a.p.ws;
    u32* cnt = (u32*)(ws + OFF_CNT + (LAST ? 8192 : 0));
    const int pg = w & 1, kq = w >> 1, n = lane & 15, q = lane >> 4;
    const int kb = kq < 2 ? 14 * kq : 28 + 13 * (kq - 2), nk = kq < 2 ? 14 : 13;
    const u32x4 srd2 = mk_srd(ws + (LAST ? OFF_W2O : OFF_W2F), W2F_BYTES);
    const u32 lds0 = (u32)(size_t)(LAS char*)smem;
    const rsrc_t rh = mk_rsrc(ws + (LAST ? OFF_H2 : OFF_H), H_BYTES), rw = mk_rsrc(ws + (LAST ? OFF_W2O : OFF_W2F), W2F_BYTES);
    const int vo = lane * 16, hs = ((4 * pt + 2 * pg) * 54 + kb) * 1024;
    bf16x8 wr[6][5];
    asm volatile("" ::: "memory");
    __builtin_amdgcn_sched_barrier(0);
#pragma unroll
    for (int t = 0; t < 3; ++t)
#pragma unroll
        for (int i = 0; i < 5; ++i) wr[t][i] = asbf(bload16(rw, vo, ((5 * ct + i) * 54 + kb + 8 + t) * 1024));
    __builtin_amdgcn_sched_barrier(0);
    wait_lgkm();
    wait_vm<15>();
    barrier();
    if (threadIdx.x == 0) add_agent(cnt + 64 * pt1);
    STAMP(LAST ? 5 : 2, 15);
#pragma unroll
    for (int t = LAST ? 5 : 6; t < 8; ++t) w2_issue(srd2, lds0, w, vo, ct, t);
    __builtin_amdgcn_sched_barrier(0);
#pragma unroll
    for (int t = 3; t < 6; ++t)
#pragma unroll
        for (int i = 0; i < 5; ++i) wr[t][i] = asbf(bload16(rw, vo, ((5 * ct + i) * 54 + kb + 8 + t) * 1024));
    __builtin_amdgcn_sched_barrier(0);
    if (w == 0) {
        for (u32 spins = 0;; ++spins) {
            if ((u32)__builtin_amdgcn_readfirstlane(ld_agent(cnt + 64 * (pt >> 1))) >= 16u) break;
            __builtin_amdgcn_s_sleep(1);
            if (spins > (1u << 18)) { if (lane == 0) __hip_atomic_store((gu32*)(ws + OFF_CNT + 49152), 1u, __ATOMIC_RELAXED, __HIP_MEMORY_SCOPE_AGENT); break; }
        }
    }
    wait_vm<0>();
    barrier();
    STAMP(KID, 5);
    bf16x8 hb[PH][2];
    auto hload = [&](int t, int pu) __attribute__((always_inline)) {
        const int k = t < 13 ? t : nk - 1;
        return asbf(__builtin_amdgcn_raw_buffer_load_b128(rh, vo, hs + (pu * 54 + k) * 1024, SC1));
    };
#pragma unroll
    for (int t = 0; t < PH; ++t) { hb[t][0] = hload(t, 0); hb[t][1] = hload(t, 1); __builtin_amdgcn_sched_barrier(0); }
    u32x2 xv[3], bv[3]; float bb[3];
    f32x4 acc[2][5];
#pragma unroll
    for (int i = 0; i < 5; ++i) { acc[0][i] = f32x4{0, 0, 0, 0}; acc[1][i] = f32x4{0, 0, 0, 0}; }
    bf16x8 wa[2][5];
    const char* wbase = smem + kq * 5120 + lane * 16;
#pragma unroll
    for (int i = 0; i < 5; ++i) wa[0][i] = *(const bf16x8*)(wbase + w2slot(0) + i * 1024);
    static_for<0, 14>([&](auto T) __attribute__((always_inline)) {
        constexpr int t = decltype(T)::value;
        if constexpr (t == 8) {
            __builtin_amdgcn_sched_barrier(0);
#pragma unroll
            for (int j = 0; j < 3; ++j) {
                const int tt = min(kq + 4 * j, 9), pu = 2 * pg + tt / 5, cu = tt % 5;
                if (!LAST) {
                    const int c = 80 * ct + 16 * cu + n;
                    xv[j] = __builtin_amdgcn_raw_buffer_load_b64(mk_rsrc(ws + OFF_XT1, XT_BYTES), (c * S_ + 64 * pt + 16 * pu + 4 * q) * 2, 0, SC1);
                    bb[j] = ((const float*)(ws + OFF_B2P))[c];
                } else {
                    xv[j] = __builtin_amdgcn_raw_buffer_load_b64(mk_rsrc(ws + OFF_X3, X3_BYTES), lane * 8, ((4 * pt + pu) * 40 + 5 * ct + cu) * 512, SC1);
                    bv[j] = *(const u32x2*)(a.p.b2 + 80 * ct + 16 * cu + 4 * q);
                }
            }
        }
        bf16x8 nh[2];
        auto step = [&]() __attribute__((always_inline)) {
            static_for<0, 5>([&](auto I) __attribute__((always_inline)) {
                constexpr int i = decltype(I)::value;
                __builtin_amdgcn_sched_barrier(0);
                bf16x8 wv;
                if constexpr (t < 8) wv = wa[t & 1][i]; else wv = wr[t - 8][i];
                acc[0][i] = LAST ? mfma16(wv, hb[t % PH][0], acc[0][i]) : mfma16(hb[t % PH][0], wv, acc[0][i]);
                if constexpr (t + 1 < 8) wa[(t + 1) & 1][i] = *(const bf16x8*)(wbase + w2slot(t + 1) + i * 1024);
                acc[1][i] = LAST ? mfma16(wv, hb[t % PH][1], acc[1][i]) : mfma16(hb[t % PH][1], wv, acc[1][i]);
                __builtin_amdgcn_sched_barrier(0);
                if constexpr (t + PH < 14 && (i == 1 || i == 3)) nh[i >> 1] = hload(t + PH, i >> 1);
            });
        };
        if constexpr (t < 13) step(); else { if (nk == 14) step(); }
        __builtin_amdgcn_sched_barrier(0);
        if constexpr (t + PH < 14) { hb[t % PH][0] = nh[0]; hb[t % PH][1] = nh[1]; }
        if (t == 0) STAMP(KID, 1);
        if (t == 7) STAMP(KID, 2);
    });
    STAMP(KID, 3);
    wait_lgkm();
    barrier();
#pragma unroll
    for (int t = 0; t < 10; ++t)
        if ((t & 3) != kq) *(f32x4*)(smem + ((w * 10 + t) * 64 + lane) * 16) = acc[t / 5][t % 5];
    wait_lgkm();
    barrier();
    float ssp[2][4];
#pragma unroll
    for (int r = 0; r < 4; ++r) { ssp[0][r] = 0.f; ssp[1][r] = 0.f; }
    const rsrc_t ro = mk_rsrc(ws + OFF_XT2, XT_BYTES);
    auto finish = [&](auto KQ) __attribute__((always_inline)) {
        constexpr int kqc = decltype(KQ)::value;
        static_for<0, 3>([&](auto J) __attribute__((always_inline)) {
            constexpr int j = decltype(J)::value, t = kqc + 4 * j;
            if constexpr (t < 10) {
                constexpr int pl = t / 5, cu = t % 5;
                f32x4 o = acc[pl][cu];
#pragma unroll
                for (int s = 1; s < 4; ++s) o += *(const f32x4*)(smem + (((pg + 2 * ((kqc + s) & 3)) * 10 + t) * 64 + lane) * 16);
                const int pu = 2 * pg + pl;
                if (!LAST) {
                    const int c = 80 * ct + 16 * cu + n, p = 64 * pt + 16 * pu + 4 * q;
                    const u32 o0 = pk2(bflo(xv[j][0]) + o[0] + bb[j], bfhi(xv[j][0]) + o[1] + bb[j]);
                    const u32 o1 = pk2(bflo(xv[j][1]) + o[2] + bb[j], bfhi(xv[j][1]) + o[3] + bb[j]);
                    u32x2 ov = {o0, o1};
                    __builtin_amdgcn_raw_buffer_store_b64(ov, ro, (c * S_ + p) * 2, 0, SC1);
                    const float m = c == 609 ? 0.f : 1.f;
                    ssp[pl][0] += m * bflo(o0) * bflo(o0); ssp[pl][1] += m * bfhi(o0) * bfhi(o0);
                    ssp[pl][2] += m * bflo(o1) * bflo(o1); ssp[pl][3] += m * bfhi(o1) * bfhi(o1);
                } else {
                    const int p = 16 * (4 * pt + pu) + n, d = 80 * ct + 16 * cu + 4 * q;
                    u32x2 ov = {pk2(bflo(xv[j][0]) + o[0] + bflo(bv[j][0]), bfhi(xv[j][0]) + o[1] + bfhi(bv[j][0])),
                                pk2(bflo(xv[j][1]) + o[2] + bflo(bv[j][1]), bfhi(xv[j][1]) + o[3] + bfhi(bv[j][1]))};
                    *(u32x2*)(a.out + (size_t)tok_of_p(p) * D_ + d) = ov;
                }
            }
        });
    };
    if (kq == 0) finish(std::integral_constant<int, 0>{});
    else if (kq == 1) finish(std::integral_constant<int, 1>{});
    else if (kq == 2) finish(std::integral_constant<int, 2>{});
    else finish(std::integral_constant<int, 3>{});
    if (!LAST) {
        float* sp = (float*)smem;
#pragma unroll
        for (int pl = 0; pl < 2; ++pl)
#pragma unroll
            for (int r = 0; r < 4; ++r) {
                const float v = row_sum15(ssp[pl][r]);
                if (n == 15) sp[w * 32 + 16 * pl + 4 * q + r] = v;
            }
        wait_lgkm();
        barrier();
        if (w == 0) {
            const int g2 = lane >> 5, r = lane & 31;
            const float v = (sp[g2 * 32 + r] + sp[(g2 + 2) * 32 + r]) + (sp[(g2 + 4) * 32 + r] + sp[(g2 + 6) * 32 + r]);
            __hip_atomic_fetch_add((__attribute__((address_space(1))) u64*)(ws + OFF_SS2) + 64 * pt + lane, ss_from_float(v), __ATOMIC_RELAXED, __HIP_MEMORY_SCOPE_AGENT);
        }
        wait_lgkm();
        wait_vm<0>();
        barrier();
        signal_all(ws, 1);
    }
}

template <bool LAST>
DI void mlp(const MainArgs& a, char* smem, int w, int lane, int u0) {
    constexpr int NF = LAST ? 10 : 7;
    const int x = blockIdx.x & 7, ib = blockIdx.x >> 3;
    const int pt = 4 * (x >> 1) + (ib & 3), ft = 8 * (x & 1) + (ib >> 2);
    const int pt2 = 8 * (x >> 1) + (ib & 7), ct2 = 4 * (x & 1) + (ib >> 3);
    const int nu = LAST ? (ft < 4 ? 10 : 9) : (ft < 12 ? 7 : 6);
    poll_all(a.p.ws, LAST ? 2 : 0, 20u, w, lane);
    wait_vm<0>();
    barrier();
    STAMP(LAST ? 5 : 2, 1);
    if (w < 4) g1_phase<NF, LAST, 0>(a, smem, w, lane, pt, u0, nu, ct2);
    else g1_phase<NF, LAST, 1>(a, smem, w, lane, pt, u0, nu, ct2);
    g2_phase<LAST>(a, smem, w, lane, pt, pt2, ct2);
}

__global__ __launch_bounds__(512, 2) void k_main(MainArgs a) {
    extern __shared__ __attribute__((aligned(16))) char smem[];
    const int lane = threadIdx.x & 63, b = blockIdx.x;
    const int w = __builtin_amdgcn_readfirstlane(threadIdx.x >> 6);
    const int ft = 8 * (b & 1) + (b >> 5);
    const int u0a = ft < 12 ? 7 * ft : 84 + 6 * (ft - 12), u0b = ft < 4 ? 10 * ft : 40 + 9 * (ft - 4);
    if (b < 160) col_task<0>(a.p.ws, b, smem, u0a); else w1_prefetch<false>(a.p.ws, smem, w, lane, u0a);
    mlp<false>(a, smem, w, lane, u0a);
    END_STAMP(3);
    if (b < 160) col_task<1>(a.p.ws, b, smem, u0b); else w1_prefetch<true>(a.p.ws, smem, w, lane, u0b);
    mlp<true>(a, smem, w, lane, u0b);
    END_STAMP(6);
}

extern "C" void kernel_launch(void* const* d_in, const int* in_sizes, int n_in, void* d_out, int out_size, void* d_ws,
                              size_t ws_size, hipStream_t stream) {
    char* ws = (char*)d_ws;
    constexpr int MAIN_LDS = 163840;
    (void)hipFuncSetAttribute((const void*)k_main, hipFuncAttributeMaxDynamicSharedMemorySize, MAIN_LDS);
    PrepArgs pa{(const u16*)d_in[0], (const u16*)d_in[1], (const u16*)d_in[2], (const u16*)d_in[4], (const u16*)d_in[6], (const u16*)d_in[7], ws};
    k0_prep<<<NB_K0, 512, PREP_LDS, stream>>>(pa);
    MainArgs ma{pa, (const u16*)d_in[5], (u16*)d_out};
    k_main<<<256, 512, MAIN_LDS, stream>>>(ma);
}
```

```cpp
#include <hip/hip_runtime.h>
#include <hip/hip_bf16.h>
#include <stdint.h>
#include <type_traits>

typedef unsigned short u16;
typedef unsigned int u32;
typedef unsigned long long u64;
typedef short s16x4 __attribute__((ext_vector_type(4)));
typedef __bf16 bf16x8 __attribute__((ext_vector_type(8)));
typedef __bf16 bf16x2 __attribute__((ext_vector_type(2)));
typedef float f32x4 __attribute__((ext_vector_type(4)));
typedef float f32x2 __attribute__((ext_vector_type(2)));
typedef u32 u32x4 __attribute__((ext_vector_type(4)));
typedef u32 u32x2 __attribute__((ext_vector_type(2)));
typedef __amdgpu_buffer_rsrc_t rsrc_t;
#define LAS __attribute__((address_space(3)))
#define DI __device__ __forceinline__

constexpr int S_ = 2048, D_ = 640, F_ = 1728, DF_ = 321;
constexpr float EPS = 1e-5f;
constexpr int SC1 = 16;
constexpr float SS_SCALE = 1048576.0f;

constexpr size_t XT_BYTES = (size_t)D_ * S_ * 2;
constexpr size_t OFF_XT0 = 0;
constexpr size_t OFF_XT1 = OFF_XT0 + XT_BYTES;
constexpr size_t OFF_XT2 = OFF_XT1 + XT_BYTES;
constexpr size_t OFF_XT3 = OFF_XT2 + XT_BYTES;
constexpr size_t OFF_WT = OFF_XT3 + XT_BYTES;
constexpr size_t OFF_W1F = OFF_WT + (size_t)320 * 2048 * 4;
constexpr size_t W1F_BYTES = (size_t)148 * 20 * 1024;
constexpr size_t OFF_W2F = OFF_W1F + W1F_BYTES;
constexpr size_t W2F_BYTES = (size_t)40 * 54 * 1024;
constexpr size_t OFF_W2O = OFF_W2F + W2F_BYTES;
constexpr size_t OFF_H = OFF_W2O + W2F_BYTES;
constexpr size_t H_BYTES = (size_t)128 * 54 * 1024;
constexpr size_t OFF_H2 = OFF_H + H_BYTES;
constexpr size_t OFF_X3 = OFF_H2 + H_BYTES;
constexpr size_t OFF_SS0 = OFF_X3 + (size_t)128 * 40 * 512;
constexpr size_t OFF_SS2 = OFF_SS0 + 2048 * 8;
constexpr size_t OFF_B2P = OFF_SS2 + 2048 * 8;
constexpr size_t OFF_XF1 = OFF_B2P + 4096;
constexpr size_t OFF_XF3 = OFF_XF1 + XT_BYTES;
constexpr size_t X3_BYTES = (size_t)128 * 40 * 512;
constexpr size_t OFF_CNT = OFF_XF3 + XT_BYTES;
constexpr size_t WS_END = OFF_CNT + 65536;

#define STAMP(k, i)
#define CSTAMP(k, i)
#define END_STAMP(k)

DI f32x4 mfma16(bf16x8 a, bf16x8 b, f32x4 c) { return __builtin_amdgcn_mfma_f32_16x16x32_bf16(a, b, c, 0, 0, 0); }
DI u32 pk2(float a, float b) { f32x2 v = {a, b}; const bf16x2 r = __builtin_convertvector(v, bf16x2); return __builtin_bit_cast(u32, r); }
DI bf16x8 pk8(float a0, float a1, float a2, float a3, float a4, float a5, float a6, float a7) {
    u32x4 r = {pk2(a0, a1), pk2(a2, a3), pk2(a4, a5), pk2(a6, a7)};
    return __builtin_bit_cast(bf16x8, r);
}
DI bf16x8 pk8(const float (&a)[8]) { return pk8(a[0], a[1], a[2], a[3], a[4], a[5], a[6], a[7]); }
DI bf16x8 asbf(u32x4 r) { return __builtin_bit_cast(bf16x8, r); }
DI bf16x8 neg8(bf16x8 a) { u32x4 r = __builtin_bit_cast(u32x4, a); r ^= 0x80008000u; return __builtin_bit_cast(bf16x8, r); }
DI float bflo(u32 v) { return __builtin_bit_cast(float, v << 16); }
DI float bfhi(u32 v) { return __builtin_bit_cast(float, v & 0xffff0000u); }
DI float cosr(float rev) { return __builtin_amdgcn_cosf(rev); }
DI float sinr(float rev) { return __builtin_amdgcn_sinf(rev); }
DI int slot32(int q, int j) { return j < 4 ? 4 * q + j : 16 + 4 * q + (j - 4); }
DI s16x4 tr_read(const char* p) { return __builtin_amdgcn_ds_read_tr16_b64_v4i16((LAS s16x4*)p); }
DI bf16x8 cat8(s16x4 lo, s16x4 hi) { return __builtin_bit_cast(bf16x8, __builtin_shufflevector(lo, hi, 0, 1, 2, 3, 4, 5, 6, 7)); }
DI int tok_of_p(int p) { return 32 * (p & 63) + (p >> 6); }
DI rsrc_t mk_rsrc(const void* p, size_t bytes) { return __builtin_amdgcn_make_buffer_rsrc((void*)p, 0, (int)bytes, 0x00020000); }
DI u32x4 bload16(rsrc_t r, int voff, int soff) { return __builtin_amdgcn_raw_buffer_load_b128(r, voff, soff, 0); }
DI float ss_to_float(u64 v) { return (float)(u32)(v >> 32) * 4294967296.0f + (float)(u32)v; }
DI u64 ss_from_float(float s) { return (u64)(fminf(s * SS_SCALE, 1.0e18f) + 0.5f); }
DI void pin(float& x) { asm volatile("" : "+v"(x)); }
DI void pin(bf16x8& x) { asm volatile("" : "+v"(x)); }

struct F32c { bf16x8 c[2], sp[2]; };
DI void f32c_init(F32c& k, int lane) {
    const float m = (float)(lane & 15), q4 = (float)(4 * (lane >> 4));
    float c[4], s[4];
#pragma unroll
    for (int j = 0; j < 4; ++j) { const float a = __builtin_amdgcn_fractf(m * (q4 + (float)j) * (1.0f / 32.0f)); c[j] = cosr(a); s[j] = sinr(a); }
    const u32 sg = (lane & 1) ? 0x80008000u : 0u;
    const u32 c0 = pk2(c[0], c[1]), c1 = pk2(c[2], c[3]), s0 = pk2(s[0], s[1]), s1 = pk2(s[2], s[3]);
    const u32x4 cv = {c0, c1, c0 ^ sg, c1 ^ sg}, sv = {s0, s1, s0 ^ sg, s1 ^ sg};
    k.c[0] = asbf(cv); k.sp[0] = asbf(sv);
    k.c[1] = asbf(cv ^ 0x80000000u); k.sp[1] = asbf(sv ^ 0x80000000u);
}

struct RowC { bf16x8 c[2], sp[2], sm[2], c20, s20m; f32x4 twr[2], twi[2]; int off0, off1; };
constexpr int ROWC_LDS = 10 * 1024;
DI void rowc_build(char* tab, int w, int lane) {
    const int n = lane & 15, q = lane >> 4;
    const float nf = (float)n;
    u32x4* t = (u32x4*)tab + lane;
    if (w == 0) {
        F32c f; f32c_init(f, lane);
        t[0] = __builtin_bit_cast(u32x4, f.c[0]); t[64] = __builtin_bit_cast(u32x4, f.c[1]);
        t[128] = __builtin_bit_cast(u32x4, f.sp[0]); t[192] = __builtin_bit_cast(u32x4, f.sp[1]);
    } else if (w <= 2) {
        float c[8];
#pragma unroll
        for (int j = 0; j < 8; ++j) {
            const int d1 = 8 * q + j;
            const float a = __builtin_amdgcn_fractf((float)d1 * nf * 0.05f);
            const float v = w == 1 ? cosr(a) : -sinr(a);
            c[j] = (d1 < 20 && n <= 10) ? v : 0.f;
        }
        t[(3 + w) * 64] = __builtin_bit_cast(u32x4, pk8(c));
    } else if (w <= 4) {
        const float sc = 0.05590169943749474f;
        f32x4 cr, ci;
#pragma unroll
        for (int i = 0; i < 4; ++i) {
            const float a = (float)(16 * (w - 3) + 4 * q + i) * nf * (1.0f / 640.0f);
            cr[i] = cosr(a) * sc; ci[i] = -sinr(a) * sc;
        }
        t[(6 + w - 3) * 64] = __builtin_bit_cast(u32x4, cr); t[(8 + w - 3) * 64] = __builtin_bit_cast(u32x4, ci);
    }
}
DI void rowc_load(RowC& k, const char* tab, int lane) {
    const u32x4* tb = (const u32x4*)tab + lane;
    k.c[0] = asbf(tb[0]); k.c[1] = asbf(tb[64]); k.sp[0] = asbf(tb[128]); k.sp[1] = asbf(tb[192]);
    k.c20 = asbf(tb[256]); k.s20m = asbf(tb[320]);
    k.twr[0] = __builtin_bit_cast(f32x4, tb[384]); k.twr[1] = __builtin_bit_cast(f32x4, tb[448]);
    k.twi[0] = __builtin_bit_cast(f32x4, tb[512]); k.twi[1] = __builtin_bit_cast(f32x4, tb[576]);
    k.sm[0] = neg8(k.sp[0]); k.sm[1] = neg8(k.sp[1]);
    const int q = lane >> 4, qq = (lane & 15) >> 2, pp = lane & 3;
    const int r0 = min(8 * q + qq, 19), r1 = min(8 * q + 4 + qq, 19);
    k.off0 = r0 * 64 + pp * 8; k.off1 = r1 * 64 + pp * 8;
}
DI float wave_sum63(float v) {
    v += __builtin_bit_cast(float, __builtin_amdgcn_update_dpp(0, __builtin_bit_cast(int, v), 0x111, 0xf, 0xf, true));
    v += __builtin_bit_cast(float, __builtin_amdgcn_update_dpp(0, __builtin_bit_cast(int, v), 0x112, 0xf, 0xf, true));
    v += __builtin_bit_cast(float, __builtin_amdgcn_update_dpp(0, __builtin_bit_cast(int, v), 0x114, 0xf, 0xf, true));
    v += __builtin_bit_cast(float, __builtin_amdgcn_update_dpp(0, __builtin_bit_cast(int, v), 0x118, 0xf, 0xf, true));
    v += __builtin_bit_cast(float, __builtin_amdgcn_update_dpp(0, __builtin_bit_cast(int, v), 0x142, 0xa, 0xf, true));
    v += __builtin_bit_cast(float, __builtin_amdgcn_update_dpp(0, __builtin_bit_cast(int, v), 0x143, 0xc, 0xf, true));
    return v;
}
DI void rowfft(const RowC& k, const char* row, float (&A)[8], float (&B)[8], int lane) {
    f32x4 z = {0.f, 0.f, 0.f, 0.f};
    f32x4 yr[2], yi[2];
#pragma unroll
    for (int mt = 0; mt < 2; ++mt) {
        const bf16x8 a = cat8(tr_read(row + k.off0 + mt * 32), tr_read(row + k.off1 + mt * 32));
        yr[mt] = mfma16(a, k.c20, z); yi[mt] = mfma16(a, k.s20m, z);
    }
    float tr[8], ti[8];
#pragma unroll
    for (int i = 0; i < 8; ++i) {
        const float a = yr[i >> 2][i & 3], b = yi[i >> 2][i & 3], cr = k.twr[i >> 2][i & 3], ci = k.twi[i >> 2][i & 3];
        tr[i] = a * cr - b * ci; ti[i] = a * ci + b * cr;
    }
    const bf16x8 Tr = pk8(tr), Ti = pk8(ti);
    f32x4 ore[2], oim[2];
#pragma unroll
    for (int t = 0; t < 2; ++t) {
        ore[t] = mfma16(k.c[t], Tr, z); ore[t] = mfma16(k.sp[t], Ti, ore[t]);
        oim[t] = mfma16(k.sm[t], Tr, z); oim[t] = mfma16(k.c[t], Ti, oim[t]);
    }
#pragma unroll
    for (int r = 0; r < 4; ++r) { A[2 * r] = ore[0][r]; A[2 * r + 1] = oim[0][r]; B[2 * r] = ore[1][r]; B[2 * r + 1] = oim[1][r]; }
    if (lane == 0) { const float h = 0.7071067811865476f; const float dc = A[0] * h; A[0] = B[0] * h; A[1] = dc; }
}
DI int sw_off(int c) { const int ch = c >> 3; return ((ch ^ ((ch >> 3) & 7)) << 4) + (c & 7) * 2; }
DI int chunk_c0(int n, int q) { return (n == 0 ? 608 : n == 10 ? 576 : 64 * (n - 1)) + 8 * q; }

constexpr int ROWST = 1344;
constexpr int PREP_TAB = 640 * 32 + 16 * ROWST;
constexpr int PREP_LDS = PREP_TAB + ROWC_LDS;

struct PrepArgs { const u16 *x, *wr, *wi, *w1, *w2, *b2; char* ws; };

DI void task_x(const PrepArgs& a, int b, char* smem) {
    const int tid = threadIdx.x, lane = tid & 63, w = tid >> 6;
    const int p0 = 16 * ((b & 7) * 16 + (b >> 3));
    u32x4 v[3];
#pragma unroll
    for (int i = 0; i < 3; ++i) {
        const int id = min(tid + 512 * i, 1279), r = id / 80, ch = id % 80;
        v[i] = *(const u32x4*)(a.x + (size_t)tok_of_p(p0 + r) * D_ + ch * 8);
    }
    __builtin_amdgcn_sched_barrier(0);
    rowc_build(smem + PREP_TAB, w, lane);
    __builtin_amdgcn_sched_barrier(0);
    STAMP(0, 1);
    if (tid < 16) ((u64*)(a.ws + OFF_SS2))[p0 + tid] = 0ull;
    if (b < 32) ((u32*)(a.ws + OFF_CNT))[b * 512 + tid] = 0u;
#pragma unroll
    for (int i = 0; i < 3; ++i) {
        const int id = min(tid + 512 * i, 1279), r = id / 80, ch = id % 80;
        *(u32x4*)(smem + r * ROWST + ch * 16) = v[i];
    }
    STAMP(0, 2);
    __syncthreads();
    STAMP(0, 3);
    RowC k; rowc_load(k, smem + PREP_TAB, lane);
    const int n = lane & 15, q = lane >> 4, c0 = chunk_c0(n, q);
    float A[2][8], B[2][8];
#pragma unroll
    for (int i = 0; i < 2; ++i) rowfft(k, smem + (2 * w + i) * ROWST, A[i], B[i], lane);
#pragma unroll
    for (int i = 0; i < 2; ++i) {
        const int r = 2 * w + i;
        float ss = 0.f;
        if (n <= 10) {
#pragma unroll
            for (int e = 0; e < 8; ++e) ss += (lane == 0 && e == 1) ? 0.f : A[i][e] * A[i][e];
            *(bf16x8*)(smem + r * ROWST + sw_off(c0)) = pk8(A[i]);
        }
        if (n >= 1 && n <= 9) {
#pragma unroll
            for (int e = 0; e < 8; ++e) ss += B[i][e] * B[i][e];
            *(bf16x8*)(smem + r * ROWST + sw_off(c0 + 32)) = pk8(B[i]);
        }
        ss = wave_sum63(ss);
        if (lane == 63) ((u64*)(a.ws + OFF_SS0))[p0 + r] = ss_from_float(ss);
    }
    STAMP(0, 4);
    __syncthreads();
    STAMP(0, 5);
    u16* xt = (u16*)(a.ws + OFF_XT0);
    const int g = lane >> 4, i16 = lane & 15, qq = i16 >> 2, pp = i16 & 3;
#pragma unroll
    for (int i = 0; i < 3; ++i) {
        const int cbk = w + 8 * i;
        if (cbk < 20) {
            const int cb = 32 * cbk + 16 * (g & 1), ph = g >> 1;
            const char* base = smem + (8 * ph + qq) * ROWST + sw_off(cb + 4 * pp);
            const bf16x8 o = cat8(tr_read(base), tr_read(base + 4 * ROWST));
            *(bf16x8*)(xt + (size_t)(cb + i16) * S_ + p0 + 8 * ph) = o;
        }
    }
}

DI void task_W(const PrepArgs& a, int b) {
    const int lane = threadIdx.x & 63, wv = threadIdx.x >> 6, wt = b * 2 + (wv >> 2), kq = wv & 3;
    const int k1 = wt / 5, cg = wt % 5, kp = 1 + 64 * cg + lane;
    const int r = kp % 20, t = kp / 20;
    int j; bool mir = false;
    if (kp == 320) j = 304;
    else if (r == 0) j = 304 + t;
    else if (r < 10) j = 32 * (r - 1) + t;
    else if (r == 10) j = 288 + t;
    else { j = 32 * (19 - r) + 31 - t; mir = true; }
    const rsrc_t rr = mk_rsrc(a.wr, (size_t)S_ * DF_ * 2), ri = mk_rsrc(a.wi, (size_t)S_ * DF_ * 2);
    const int st = (mir ? -64 : 64) * DF_ * 2, off0 = ((mir ? 2048 - k1 : k1) * DF_ + kp) * 2 + 8 * kq * st;
    u32 re[8], im[8];
#pragma unroll
    for (int k2 = 0; k2 < 8; ++k2) {
        int off = off0 + k2 * st;
        if (k2 == 0 && mir && k1 == 0 && kq == 0) off = kp * 2;
        re[k2] = __builtin_amdgcn_raw_buffer_load_b16(rr, off, 0, 0); im[k2] = __builtin_amdgcn_raw_buffer_load_b16(ri, off, 0, 0);
    }
    const u32 sg = mir ? 0x80000000u : 0u;
    u32x4* dst = (u32x4*)(a.ws + OFF_WT + ((size_t)j * 64 + k1) * 128 + kq * 32);
#pragma unroll
    for (int i = 0; i < 2; ++i) {
        u32x4 o;
#pragma unroll
        for (int e = 0; e < 4; ++e) o[e] = (re[4 * i + e] | (im[4 * i + e] << 16)) ^ sg;
        dst[i] = o;
    }
}

DI void task_w1(const PrepArgs& a, int b, char* smem) {
    const int tid = threadIdx.x, lane = tid & 63, w = tid >> 6;
    char* raw = smem;
    char* tr = smem + 640 * 32;
    char* out = raw;
    u32x4 v[3];
#pragma unroll
    for (int i = 0; i < 3; ++i) {
        const int id = min(tid + 512 * i, 1279), d = id >> 1, h = id & 1;
        v[i] = *(const u32x4*)(a.w1 + (size_t)d * F_ + 16 * b + 8 * h);
    }
    __builtin_amdgcn_sched_barrier(0);
    rowc_build(smem + PREP_TAB, w, lane);
    __builtin_amdgcn_sched_barrier(0);
#pragma unroll
    for (int i = 0; i < 3; ++i) *(u32x4*)(raw + min(tid + 512 * i, 1279) * 16) = v[i];
    __syncthreads();
    const int g = lane >> 4, i16 = lane & 15, qq = i16 >> 2, pp = i16 & 3;
#pragma unroll
    for (int i = 0; i < 5; ++i) {
        const int d0 = 16 * (w + 8 * i) + 4 * g;
        const s16x4 t = tr_read(raw + (d0 + qq) * 32 + pp * 8);
        *(s16x4*)(tr + i16 * ROWST + d0 * 2) = t;
    }
    RowC k; rowc_load(k, smem + PREP_TAB, lane);
    __syncthreads();
    const int n = lane & 15, q = lane >> 4, c0 = chunk_c0(n, q);
    float A[2][8], B[2][8];
#pragma unroll
    for (int i = 0; i < 2; ++i) rowfft(k, tr + (2 * w + i) * ROWST, A[i], B[i], lane);
#pragma unroll
    for (int i = 0; i < 2; ++i) {
        const int f = 2 * w + i;
        if (lane == 0) A[i][1] = 0.f;
        if (n <= 10) *(bf16x8*)(out + ((c0 >> 5) * 64 + q * 16 + f) * 16) = pk8(A[i]);
        if (n >= 1 && n <= 9) *(bf16x8*)(out + (((c0 >> 5) + 1) * 64 + q * 16 + f) * 16) = pk8(B[i]);
    }
    __syncthreads();
    u32x4* dst = (u32x4*)(a.ws + OFF_W1F + (size_t)b * 20 * 1024);
#pragma unroll
    for (int i = 0; i < 3; ++i) { const int id = tid + 512 * i; if (id < 1280) dst[id] = *(const u32x4*)(out + id * 16); }
}

template <bool SW> DI void w2_frags(const char* img, char* dstbase, int b, int lane, int w) {
    const int g = lane >> 4, i16 = lane & 15, qq = i16 >> 2, pp = i16 & 3;
    const rsrc_t rd = mk_rsrc(dstbase, W2F_BYTES);
#pragma unroll
    for (int i = 0; i < 5; ++i) {
        const int cu = w + 8 * i, c = 16 * cu + 4 * pp;
        const char* base = img + (4 * g + qq) * ROWST + (SW ? sw_off(c) : c * 2);
        const bf16x8 v = cat8(tr_read(base), tr_read(base + 16 * ROWST));
        __builtin_amdgcn_raw_buffer_store_b128(__builtin_bit_cast(u32x4, v), rd, lane * 16, (cu * 54 + b) * 1024, SC1);
    }
}
constexpr int W2_TAB = 32 * ROWST;
DI void task_w2(const PrepArgs& a, int b, char* smem) {
    const int tid = threadIdx.x, lane = tid & 63, w = tid >> 6;
    u32x4 v[5];
#pragma unroll
    for (int i = 0; i < 5; ++i) {
        const int id = tid + 512 * i, r = id / 80, ch = id % 80;
        v[i] = *(const u32x4*)(a.w2 + (size_t)(32 * b + r) * D_ + ch * 8);
    }
    __builtin_amdgcn_sched_barrier(0);
    rowc_build(smem + W2_TAB, w, lane);
    __builtin_amdgcn_sched_barrier(0);
#pragma unroll
    for (int i = 0; i < 5; ++i) { const int id = tid + 512 * i, r = id / 80, ch = id % 80; *(u32x4*)(smem + r * ROWST + ch * 16) = v[i]; }
    __syncthreads();
    RowC k; rowc_load(k, smem + W2_TAB, lane);
    w2_frags<false>(smem, a.ws + OFF_W2O, b, lane, w);
    const int n = lane & 15, q = lane >> 4, c0 = chunk_c0(n, q);
    u32x4 A8[4], B8[4];
#pragma unroll
    for (int i = 0; i < 4; ++i) {
        float A[8], B[8];
        rowfft(k, smem + (4 * w + i) * ROWST, A, B, lane);
        A8[i] = __builtin_bit_cast(u32x4, pk8(A)); B8[i] = __builtin_bit_cast(u32x4, pk8(B));
    }
    __syncthreads();
#pragma unroll
    for (int i = 0; i < 4; ++i) {
        const int r = 4 * w + i;
        if (n <= 10) *(u32x4*)(smem + r * ROWST + sw_off(c0)) = A8[i];
        if (n >= 1 && n <= 9) *(u32x4*)(smem + r * ROWST + sw_off(c0 + 32)) = B8[i];
    }
    __syncthreads();
    w2_frags<true>(smem, a.ws + OFF_W2F, b, lane, w);
}

DI int kcol_of(int jc) { return jc < 288 ? (jc >> 5) + 1 + 20 * (jc & 31) : jc < 304 ? 10 + 20 * (jc - 288) : 20 * (jc - 304); }
DI void task_Q(const PrepArgs& a, int u) {
    const int tid = threadIdx.x;
#pragma unroll
    for (int ii = 0; ii < 3; ++ii) {
        const int i = tid + 512 * ii;
        if (i < 1280) {
            const int ks = i >> 6, l = i & 63, d = 16 * u + (l & 15), q = l >> 4;
            float v[8];
#pragma unroll
            for (int j = 0; j < 8; j += 2) {
                const int jc = 16 * ks + 4 * q + (j >> 1), kk = kcol_of(jc);
                const float ang = (float)((kk * d) % 640) * (1.0f / 640.0f);
                float c = cosr(ang) * 0.05590169943749474f, s = -sinr(ang) * 0.05590169943749474f;
                if (jc == 304) { c = ((d & 1) ? -1.f : 1.f) * 0.03952847075210474f; s = 0.03952847075210474f; }
                v[j] = c; v[j + 1] = s;
            }
            __builtin_amdgcn_raw_buffer_store_b128(__builtin_bit_cast(u32x4, pk8(v)), mk_rsrc(a.ws + OFF_W1F, W1F_BYTES), i * 16, (108 + u) * 20480, SC1);
        }
    }
}
DI void task_b(const PrepArgs& a, char* smem) {
    const int tid = threadIdx.x, lane = tid & 63;
    if (tid < 80) *(u32x4*)(smem + tid * 16) = *(const u32x4*)(a.b2 + tid * 8);
    rowc_build(smem + PREP_TAB, tid >> 6, lane);
    __syncthreads();
    RowC k; rowc_load(k, smem + PREP_TAB, lane);
    float A[8], B[8];
    rowfft(k, smem, A, B, lane);
    if (tid < 64) {
        const int n = lane & 15, q = lane >> 4, c0 = chunk_c0(n, q);
        float* o = (float*)(a.ws + OFF_B2P);
        if (n <= 10) for (int e = 0; e < 8; ++e) o[c0 + e] = A[e];
        if (n >= 1 && n <= 9) for (int e = 0; e < 8; ++e) o[c0 + 32 + e] = B[e];
    }
}

constexpr int NB_K0 = 128 + 108 + 160 + 1;
__global__ __launch_bounds__(512, 4) void k0_prep(PrepArgs a) {
    extern __shared__ __attribute__((aligned(16))) char smem[];
    const int b = blockIdx.x;
    STAMP(0, 0);
    if (b < 128) task_x(a, b, smem);
    else if (b < 236) task_w1(a, b - 128, smem);
    else if (b < 396) task_W(a, b - 236);
    else task_b(a, smem);
    END_STAMP(0);
}

template <int N> DI void wait_vm() { asm volatile("s_waitcnt vmcnt(%0)" ::"n"(N) : "memory"); }
DI void wait_lgkm() { asm volatile("s_waitcnt lgkmcnt(0)" ::: "memory"); }
DI void barrier() { __builtin_amdgcn_s_barrier(); }
template <bool SC = false> DI void dma16(u32x4 srd, int voff, int soff, u32 ldsaddr) {
    if (SC) asm volatile("s_mov_b32 m0, %0\n\ts_nop 0\n\tbuffer_load_dwordx4 %1, %2, %3 offen sc1 lds" ::"s"(ldsaddr), "v"(voff), "s"(srd), "s"(soff) : "memory", "m0");
    else asm volatile("s_mov_b32 m0, %0\n\ts_nop 0\n\tbuffer_load_dwordx4 %1, %2, %3 offen lds" ::"s"(ldsaddr), "v"(voff), "s"(srd), "s"(soff) : "memory", "m0");
}
template <int LIM, bool SC = false> DI void dma16_lim(u32x4 srd, int voff, int soff, u32 ldsaddr, int w) {
    if (SC) asm volatile("s_cmp_lt_i32 %4, %5\n\ts_cbranch_scc0 .Lskip%=\n\ts_mov_b32 m0, %0\n\ts_nop 0\n\tbuffer_load_dwordx4 %1, %2, %3 offen sc1 lds\n.Lskip%=:"
                 ::"s"(ldsaddr), "v"(voff), "s"(srd), "s"(soff), "s"(w), "n"(LIM) : "memory", "m0", "scc");
    else asm volatile("s_cmp_lt_i32 %4, %5\n\ts_cbranch_scc0 .Lskip%=\n\ts_mov_b32 m0, %0\n\ts_nop 0\n\tbuffer_load_dwordx4 %1, %2, %3 offen lds\n.Lskip%=:"
                 ::"s"(ldsaddr), "v"(voff), "s"(srd), "s"(soff), "s"(w), "n"(LIM) : "memory", "m0", "scc");
}
DI u32x4 mk_srd(const void* p, size_t bytes) {
    const u64 a = (u64)p;
    u32x4 r = {(u32)a, (u32)(a >> 32) & 0xffffu, (u32)bytes, 0x00020000u};
    return r;
}
DI float row_sum15(float v) {
    v += __builtin_bit_cast(float, __builtin_amdgcn_update_dpp(0, __builtin_bit_cast(int, v), 0x111, 0xf, 0xf, true));
    v += __builtin_bit_cast(float, __builtin_amdgcn_update_dpp(0, __builtin_bit_cast(int, v), 0x112, 0xf, 0xf, true));
    v += __builtin_bit_cast(float, __builtin_amdgcn_update_dpp(0, __builtin_bit_cast(int, v), 0x114, 0xf, 0xf, true));
    v += __builtin_bit_cast(float, __builtin_amdgcn_update_dpp(0, __builtin_bit_cast(int, v), 0x118, 0xf, 0xf, true));
    return v;
}
template <int I, int N, class F> DI void static_for(F&& f) {
    if constexpr (I < N) { f(std::integral_constant<int, I>{}); static_for<I + 1, N>(f); }
}

constexpr int G1_P = 4, G1_PW = 5;
typedef __attribute__((address_space(1))) u32 gu32;
DI u32 ld_agent(const u32* p) { return __hip_atomic_load((gu32*)p, __ATOMIC_RELAXED, __HIP_MEMORY_SCOPE_AGENT); }
DI void add_agent(u32* p) { __hip_atomic_fetch_add((gu32*)p, 1u, __ATOMIC_RELAXED, __HIP_MEMORY_SCOPE_AGENT); }
DI void signal_all(char* ws, int which) {
    if (threadIdx.x == 0) {
        u32* c = (u32*)(ws + OFF_CNT + 16384 + which * 8192) + (blockIdx.x & 7) * 32;
#pragma unroll
        for (int r = 0; r < 8; ++r) add_agent(c + r * 256);
    }
}
#define POLL_SLEEP 8
DI void poll_until(const u32* p, u32 want, char* ws, int lane) {
    for (u32 spins = 0;; ++spins) {
        if (__all(ld_agent(p) >= want)) break;
        __builtin_amdgcn_s_sleep(POLL_SLEEP);
        if (spins > (1u << 18)) { if (lane == 0) __hip_atomic_store((gu32*)(ws + OFF_CNT + 49152), 1u, __ATOMIC_RELAXED, __HIP_MEMORY_SCOPE_AGENT); break; }
    }
}
DI void poll_all(char* ws, int which, u32 per_shard, int w, int lane) {
    if (w == 0) poll_until((const u32*)(ws + OFF_CNT + 16384 + which * 8192) + ((blockIdx.x >> 3) & 7) * 256 + (lane & 7) * 32, per_shard, ws, lane);
}

struct MainArgs { PrepArgs p; const u16* b1; u16* out; };
DI int img128(int row, int ch) { return row * 128 + ((ch ^ ((row >> 1) & 7)) << 4); }
template <bool LAST> DI void w1_prefetch(char* ws, char* smem, int w, int lane, int u0);

template <int L>
DI void col_task(char* ws, int b, char* smem, int u0) {
    const int tid = threadIdx.x & 255, lane = tid & 63, w = tid >> 6, half = threadIdx.x >> 8;
    const int j = 2 * b + half;
    char* Ure = smem + half * 24576; char* Uim = Ure + 4096; char* Ere = Ure + 8192; char* Eim = Ure + 12288;
    char* Rre = Ure + 16384; char* Rim = Ure + 20480;
    const int n = lane & 15, q = lane >> 4;
    const int wv = __builtin_amdgcn_readfirstlane(threadIdx.x >> 6);
    const rsrc_t rx = mk_rsrc(ws + (L == 0 ? OFF_XT0 : OFF_XT2), XT_BYTES), rs = mk_rsrc(ws + (L == 0 ? OFF_SS0 : OFF_SS2), 2048 * 8);
    u32x4 vr, vi, sv[4];
    auto load_in = [&]() __attribute__((always_inline)) {
        vr = __builtin_amdgcn_raw_buffer_load_b128(rx, (2 * j * S_ + tid * 8) * 2, 0, L == 0 ? 0 : SC1);
        vi = __builtin_amdgcn_raw_buffer_load_b128(rx, ((2 * j + 1) * S_ + tid * 8) * 2, 0, L == 0 ? 0 : SC1);
#pragma unroll
        for (int i = 0; i < 4; ++i) sv[i] = __builtin_amdgcn_raw_buffer_load_b128(rs, tid * 64 + 16 * i, 0, L == 0 ? 0 : SC1);
    };
    if (L == 0) load_in();
    u32x4 wf[2];
#pragma unroll
    for (int t = 0; t < 2; ++t) wf[t] = *(const u32x4*)((const u32*)(ws + OFF_WT) + ((size_t)j * 64 + 16 * w + n) * 32 + 16 * t + 4 * q);
    __builtin_amdgcn_sched_barrier(0);
    F32c f; f32c_init(f, lane);
    bf16x8 c32[2] = {f.c[0], f.c[1]}, s32p[2] = {f.sp[0], f.sp[1]}, s32m[2];
    pin(c32[0]); pin(c32[1]); pin(s32p[0]); pin(s32p[1]);
    bf16x8 c64[2], s64p[2], s64m[2];
    const float k1f = (float)(16 * w + n);
    {
        float c[8], s[8];
#pragma unroll
        for (int e = 0; e < 8; ++e) {
            const float ang = __builtin_amdgcn_fractf((float)(8 * q + e) * k1f * (1.0f / 64.0f));
            c[e] = cosr(ang); s[e] = sinr(ang);
        }
        c64[0] = pk8(c); s64p[0] = pk8(s);
        const u32 sg = (lane & 1) ? 0x80008000u : 0u;
        c64[1] = asbf(__builtin_bit_cast(u32x4, c64[0]) ^ sg); s64p[1] = asbf(__builtin_bit_cast(u32x4, s64p[0]) ^ sg);
        pin(c64[0]); pin(c64[1]); pin(s64p[0]); pin(s64p[1]);
    }
    float twr[8], twi[8], ivr[8], ivi[8];
#pragma unroll
    for (int i = 0; i < 8; ++i) {
        const float af = (float)(16 * (i >> 2) + 4 * q + (i & 3)) * k1f * (1.0f / 2048.0f);
        twr[i] = cosr(af); twi[i] = -sinr(af);
        const float ai = (float)(16 * w + 4 * q + (i & 3)) * (float)(16 * (i >> 2) + n) * (1.0f / 2048.0f);
        ivr[i] = cosr(ai) * (1.0f / 2048.0f); ivi[i] = sinr(ai) * (1.0f / 2048.0f);
        pin(twr[i]); pin(twi[i]); pin(ivr[i]); pin(ivi[i]);
    }
    __builtin_amdgcn_sched_barrier(0);
    STAMP(L ? 4 : 1, 1);
    if (L == 1) {
        poll_all(ws, 1, 32u, wv, lane);
        barrier();
        STAMP(4, 5);
        load_in();
        __builtin_amdgcn_sched_barrier(0);
    }
    s64m[0] = neg8(s64p[0]); s64m[1] = neg8(s64p[1]);
    {
        float r[8];
#pragma unroll
        for (int i = 0; i < 8; ++i) {
            const float s = (float)sv[i >> 1][2 * (i & 1) + 1] * 4294967296.0f + (float)sv[i >> 1][2 * (i & 1)];
            r[i] = __builtin_amdgcn_rsqf(s * (1.0f / (SS_SCALE * 640.0f)) + EPS);
        }
        const bf16x8 ur = pk8(bflo(vr[0]) * r[0], bfhi(vr[0]) * r[1], bflo(vr[1]) * r[2], bfhi(vr[1]) * r[3],
                              bflo(vr[2]) * r[4], bfhi(vr[2]) * r[5], bflo(vr[3]) * r[6], bfhi(vr[3]) * r[7]);
        bf16x8 ui = pk8(bflo(vi[0]) * r[0], bfhi(vi[0]) * r[1], bflo(vi[1]) * r[2], bfhi(vi[1]) * r[3],
                        bflo(vi[2]) * r[4], bfhi(vi[2]) * r[5], bflo(vi[3]) * r[6], bfhi(vi[3]) * r[7]);
        if (j == 304) ui = asbf(u32x4{0u, 0u, 0u, 0u});
        const int o = img128(tid >> 3, tid & 7);
        *(bf16x8*)(Ure + o) = ur; *(bf16x8*)(Uim + o) = ui;
        *(u32x4*)(Rre + o) = vr; *(u32x4*)(Rim + o) = vi;
    }
    __syncthreads();
    STAMP(L ? 4 : 1, 2);
    const f32x4 z = {0.f, 0.f, 0.f, 0.f};
    f32x4 dr[2], di[2];
#pragma unroll
    for (int mt = 0; mt < 2; ++mt) {
        dr[mt] = z; di[mt] = z;
#pragma unroll
        for (int ks = 0; ks < 2; ++ks) {
            const int o = img128(16 * mt + n, 4 * ks + q);
            const bf16x8 ar = *(const bf16x8*)(Ure + o), ai = *(const bf16x8*)(Uim + o);
            dr[mt] = mfma16(ar, c64[ks], dr[mt]); dr[mt] = mfma16(ai, s64p[ks], dr[mt]);
            di[mt] = mfma16(ar, s64m[ks], di[mt]); di[mt] = mfma16(ai, c64[ks], di[mt]);
        }
    }
    float tr[8], ti[8];
#pragma unroll
    for (int i = 0; i < 8; ++i) {
        const float x = dr[i >> 2][i & 3], y = di[i >> 2][i & 3];
        tr[i] = x * twr[i] - y * twi[i]; ti[i] = x * twi[i] + y * twr[i];
    }
    bf16x8 Tr = pk8(tr), Ti = pk8(ti);
    s32m[0] = neg8(s32p[0]); s32m[1] = neg8(s32p[1]);
    f32x4 orr[2], oi[2];
#pragma unroll
    for (int t = 0; t < 2; ++t) {
        orr[t] = mfma16(c32[t], Tr, z); orr[t] = mfma16(s32p[t], Ti, orr[t]);
        oi[t] = mfma16(s32m[t], Tr, z); oi[t] = mfma16(c32[t], Ti, oi[t]);
    }
#pragma unroll
    for (int i = 0; i < 8; ++i) {
        const float x = orr[i >> 2][i & 3], y = oi[i >> 2][i & 3];
        const u32 wv = wf[i >> 2][i & 3];
        const float wre = bflo(wv), wim = bfhi(wv);
        tr[i] = x * wre - y * wim; ti[i] = x * wim + y * wre;
    }
    Tr = pk8(tr); Ti = pk8(ti);
#pragma unroll
    for (int nt = 0; nt < 2; ++nt) {
        f32x4 zr = mfma16(Tr, c32[nt], z); zr = mfma16(Ti, s32m[nt], zr);
        f32x4 zi = mfma16(Tr, s32p[nt], z); zi = mfma16(Ti, c32[nt], zi);
        float er[4], ei[4];
#pragma unroll
        for (int r = 0; r < 4; ++r) {
            const float cr = ivr[4 * nt + r], ci = ivi[4 * nt + r];
            er[r] = zr[r] * cr - zi[r] * ci; ei[r] = zr[r] * ci + zi[r] * cr;
        }
        const int o = img128(16 * nt + n, 2 * w + (q >> 1)) + (q & 1) * 8;
        u32x2 pr = {pk2(er[0], er[1]), pk2(er[2], er[3])}, pi = {pk2(ei[0], ei[1]), pk2(ei[2], ei[3])};
        *(u32x2*)(Ere + o) = pr; *(u32x2*)(Eim + o) = pi;
    }
    __syncthreads();
    STAMP(L ? 4 : 1, 3);
    const rsrc_t rf = mk_rsrc(ws + (L == 0 ? OFF_XF1 : OFF_XF3), XT_BYTES);
    char* O = Ure;
#pragma unroll
    for (int nt = 0; nt < 2; ++nt) {
        f32x4 yr = z, yi = z;
#pragma unroll
        for (int ks = 0; ks < 2; ++ks) {
            const int o = img128(16 * nt + n, 4 * ks + q);
            const bf16x8 br = *(const bf16x8*)(Ere + o), bi = *(const bf16x8*)(Eim + o);
            yr = mfma16(c64[ks], br, yr); yr = mfma16(s64m[ks], bi, yr);
            yi = mfma16(s64p[ks], br, yi); yi = mfma16(c64[ks], bi, yi);
        }
        if (j == 304) yi = z;
        const int p = (16 * nt + n) * 64 + 16 * w + 4 * q;
        u32x2 rr[2], ri[2];
        { const int o = img128(16 * nt + n, 2 * w + (q >> 1)) + (q & 1) * 8; rr[nt] = *(const u32x2*)(Rre + o); ri[nt] = *(const u32x2*)(Rim + o); }
        const float fr[4] = {bflo(rr[nt][0]) + yr[0], bfhi(rr[nt][0]) + yr[1], bflo(rr[nt][1]) + yr[2], bfhi(rr[nt][1]) + yr[3]};
        const float fi[4] = {bflo(ri[nt][0]) + yi[0], bfhi(ri[nt][0]) + yi[1], bflo(ri[nt][1]) + yi[2], bfhi(ri[nt][1]) + yi[3]};
        u32x4 ov = {pk2(fr[0], fi[0]), pk2(fr[1], fi[1]), pk2(fr[2], fi[2]), pk2(fr[3], fi[3])};
        *(u32x4*)(O + p * 4) = ov;
    }
    __syncthreads();
    {
        const int c0 = 4 * b, dst = ((c0 >> 5) * 64 + ((c0 & 31) >> 3) * 16) * 16 + (c0 & 4) * 2;
        const u32* O0 = (const u32*)smem; const u32* O1 = (const u32*)(smem + 24576);
#pragma unroll
        for (int i = 0; i < 4; ++i) {
            const int p = threadIdx.x + 512 * i;
            u32x2 v = {O0[p], O1[p]};
            __builtin_amdgcn_raw_buffer_store_b64(v, rf, (p >> 4) * 20480 + (p & 15) * 16, dst, SC1);
        }
    }
    STAMP(L ? 4 : 1, 4);
    wait_lgkm();
    wait_vm<0>();
    barrier();
    signal_all(ws, L == 0 ? 0 : 2);
    w1_prefetch<L == 1>(ws, smem, wv, lane, u0);
    STAMP(L ? 4 : 1, 15);
}

DI f32x2 gelu_tanh2(f32x2 x) {
    const float k1 = -2.302208198f, k2 = -0.1029432397f;
    const f32x2 c1 = {k1, k1}, c2 = {k2, k2}, one = {1.0f, 1.0f};
    const f32x2 t = x * (x * x * c2 + c1);
    f32x2 e = {__builtin_amdgcn_exp2f(t[0]), __builtin_amdgcn_exp2f(t[1])};
    e += one;
    const f32x2 r = {__builtin_amdgcn_rcpf(e[0]), __builtin_amdgcn_rcpf(e[1])};
    return x * r;
}
constexpr int G2_PH = 4;
constexpr int W2STG = 20480;
DI u32 w2slot(int t) { return 163840 - ((t & 7) + 1) * W2STG; }
template <bool LAST> constexpr int w1slot(int st) { return LAST ? 163840 - (st % 8 + 1) * 20480 : 163840 - (st + 1) * 14336; }

template <bool LAST, int I> DI void w1_piece(u32x4 srd, u32 lds0, int w, int vo, int u0, int st) {
    constexpr int WP = LAST ? 20 : 14;
    const int pc = w + 8 * I;
    const u32 dst = lds0 + w1slot<LAST>(st) + pc * 1024;
    const int so = (u0 + (pc >> 1)) * 20480 + st * 2048 + (pc & 1) * 1024;
    if (8 * I + 7 < WP) dma16<LAST>(srd, vo, so, dst); else dma16_lim<(WP & 7), LAST>(srd, vo, so, dst, w);
}
template <bool LAST> DI void w1_prefetch(char* ws, char* smem, int w, int lane, int u0) {
    const u32x4 srd = mk_srd(ws + OFF_W1F, W1F_BYTES);
    const u32 lds0 = (u32)(size_t)(LAS char*)smem;
#pragma unroll
    for (int st = 0; st < G1_PW; ++st) {
        w1_piece<LAST, 0>(srd, lds0, w, lane * 16, u0, st);
        w1_piece<LAST, 1>(srd, lds0, w, lane * 16, u0, st);
        if (LAST) w1_piece<LAST, 2>(srd, lds0, w, lane * 16, u0, st);
    }
}

constexpr int g1_younger(int ssync, int t, int nlo) {
    int cnt = 0; bool seen = false;
    for (int s = 0; s < ssync; ++s) {
        if (s + G1_PW < 10) { if (seen) cnt += nlo; if (s + G1_PW == t) seen = true; }
        if (s + G1_P < 10 && seen) cnt += 2;
    }
    return cnt;
}

template <int I> DI void w2_issue1(u32x4 srd, u32 lds0, int w, int vo, int ct, int t) {
    const int pc = w + 8 * I, q = pc / 5, cu = pc - 5 * q;
    const int so = ((5 * ct + cu) * 54 + (q < 2 ? 14 * q : 28 + 13 * (q - 2)) + t) * 1024;
    if (I < 2) dma16<true>(srd, vo, so, lds0 + w2slot(t) + pc * 1024); else dma16_lim<4, true>(srd, vo, so, lds0 + w2slot(t) + pc * 1024, w);
}
DI void w2_issue(u32x4 srd, u32 lds0, int w, int vo, int ct, int t) {
    w2_issue1<0>(srd, lds0, w, vo, ct, t); w2_issue1<1>(srd, lds0, w, vo, ct, t); w2_issue1<2>(srd, lds0, w, vo, ct, t);
}

template <int NF, bool LAST, int KG>
DI void g1_phase(const MainArgs& a, char* smem, int w, int lane, int pt, int u0, int nu, int ct2) {
    constexpr int P = G1_P, PW = G1_PW;
    constexpr int WP = 2 * NF, WR = (WP + 7) / 8, NLO = WP / 8, KID = LAST ? 5 : 2;
    char* ws = a.p.ws;
    const int pq = w & 3, g = lane >> 4;
    const u32x4 srdW = mk_srd(ws + OFF_W1F, W1F_BYTES), srd2 = mk_srd(ws + (LAST ? OFF_W2O : OFF_W2F), W2F_BYTES);
    const u32 lds0 = (u32)(size_t)(LAS char*)smem;
    const rsrc_t rx = mk_rsrc(ws + (LAST ? OFF_XF3 : OFF_XF1), XT_BYTES);
    const int xs = (8 * pt + 2 * pq) * 20480 + KG * 1024, vo = lane * 16;
    auto xload = [&](int st, int pu) __attribute__((always_inline)) { return asbf(__builtin_amdgcn_raw_buffer_load_b128(rx, vo, xs + pu * 20480 + st * 2048, SC1)); };
    bf16x8 xb[P][2];
#pragma unroll
    for (int st = 0; st < P; ++st) { xb[st][0] = xload(st, 0); xb[st][1] = xload(st, 1); __builtin_amdgcn_sched_barrier(0); }
    f32x4 acc[NF][2];
#pragma unroll
    for (int i = 0; i < NF; ++i) { acc[i][0] = f32x4{0, 0, 0, 0}; acc[i][1] = f32x4{0, 0, 0, 0}; }
    f32x4 gr[2] = {f32x4{0, 0, 0, 0}, f32x4{0, 0, 0, 0}};
    bf16x8 wa[2][NF];
#pragma unroll
    for (int i = 0; i < NF; ++i) wa[0][i] = *(const bf16x8*)(smem + w1slot<LAST>(0) + KG * 1024 + lane * 16 + i * 2048);
    static_for<0, 10>([&](auto ST) __attribute__((always_inline)) {
        constexpr int st = decltype(ST)::value;
        if constexpr (LAST && st == 2) { wait_lgkm(); barrier(); }
        if constexpr (st == 4 || st == 7) {
            wait_lgkm();
            wait_vm<g1_younger(st, st == 7 ? 9 : 7, NLO)>();
            barrier();
        }
        bf16x8 nx[2];
        const char* rbase = smem + w1slot<LAST>(st + 1) + KG * 1024 + lane * 16;
        constexpr int NV = WR + 2, V0 = KG == 0 ? 0 : NF - NV;
        static_for<0, NF>([&](auto I) __attribute__((always_inline)) {
            constexpr int i = decltype(I)::value;
            __builtin_amdgcn_sched_barrier(0);
            acc[i][0] = mfma16(wa[st & 1][i], xb[st % P][0], acc[i][0]);
            if constexpr (st + 1 < 10) wa[(st + 1) & 1][i] = *(const bf16x8*)(rbase + i * 2048);
            acc[i][1] = mfma16(wa[st & 1][i], xb[st % P][1], acc[i][1]);
            if constexpr (i < 2) {
                u32x4 xv = __builtin_bit_cast(u32x4, xb[st % P][i]);
                if constexpr (st == 9 && KG == 1) { if (g == 0) xv[0] &= 0x0000ffffu; }
                gr[i] = mfma16(asbf(xv), asbf(xv), gr[i]);
            }
            __builtin_amdgcn_sched_barrier(0);
            constexpr int v = i - V0;
            if constexpr (v >= 0 && v < WR) { if constexpr (st + PW < 10) w1_piece<LAST, v>(srdW, lds0, w, vo, u0, st + PW); }
            else if constexpr (v >= WR && v < NV) { if constexpr (st + P < 10) nx[v - WR] = xload(st + P, v - WR); }
            if constexpr (st >= 6) {
                constexpr int sl = i - (KG == 0 ? 0 : NF - 4), e = 4 * (st - 6) + sl;
                if constexpr (sl >= 0 && sl < 4 && e < (LAST ? 9 : 15)) w2_issue1<e % 3>(srd2, lds0, w, vo, ct2, (LAST ? 2 : 0) + e / 3);
            }
        });
        __builtin_amdgcn_sched_barrier(0);
        if constexpr (st + P < 10) { xb[st % P][0] = nx[0]; xb[st % P][1] = nx[1]; }
        if (st == 4) STAMP(KID, 2);
    });
    STAMP(KID, 3);
    u32x2 bv[NF];
#pragma unroll
    for (int i = 0; i < NF; ++i) bv[i] = *(const u32x2*)(a.b1 + min(16 * (u0 + i), F_ - 16) + 4 * g);
    __builtin_amdgcn_sched_barrier(0);
    wait_lgkm();
    barrier();
    float* sx = (float*)(smem + 8 * NF * 512);
#pragma unroll
    for (int i = 0; i < NF; ++i) {
        const f32x4 t = acc[i][1 - KG];
        u32x2 pv = {pk2(t[0], t[1]), pk2(t[2], t[3])};
        *(u32x2*)(smem + ((w * NF + i) * 64 + lane) * 8) = pv;
    }
    float st2[2];
    {
        const int n = lane & 15;
#pragma unroll
        for (int pu = 0; pu < 2; ++pu) {
            const float d01 = (n & 1) ? gr[pu][1] : gr[pu][0], d23 = (n & 1) ? gr[pu][3] : gr[pu][2];
            float d = (n & 2) ? d23 : d01;
            d = (g == (n >> 2)) ? d : 0.f;
            d += __shfl_xor(d, 16); d += __shfl_xor(d, 32);
            st2[pu] = d;
        }
    }
    if (lane < 16) sx[w * 16 + lane] = st2[1 - KG];
    wait_lgkm();
    barrier();
    STAMP(KID, 5);
    const float v = (st2[KG] + sx[(w ^ 4) * 16 + (lane & 15)]) * (1.0f / 640.0f);
    const float r1 = __builtin_amdgcn_rsqf(v + EPS);
    const float rr = r1 * __builtin_amdgcn_rsqf(v * r1 * r1 + EPS);
    const int punit = 8 * pt + 2 * pq + KG;
    u32x2 ov[NF], pv[NF];
#pragma unroll
    for (int i = 0; i < NF; ++i) pv[i] = *(const u32x2*)(smem + (((w ^ 4) * NF + i) * 64 + lane) * 8);
    static_for<0, NF>([&](auto I) __attribute__((always_inline)) {
        constexpr int i = decltype(I)::value;
        if constexpr (LAST ? i < 2 : i == 0) {
            __builtin_amdgcn_sched_barrier(0);
            w2_issue(srd2, lds0, w, vo, ct2, LAST ? i : 5);
            __builtin_amdgcn_sched_barrier(0);
        }
        const f32x2 o0 = {acc[i][KG][0] + bflo(pv[i][0]), acc[i][KG][1] + bfhi(pv[i][0])}, o1 = {acc[i][KG][2] + bflo(pv[i][1]), acc[i][KG][3] + bfhi(pv[i][1])};
        const f32x2 rr2 = {rr, rr};
        const f32x2 b0 = {bflo(bv[i][0]), bfhi(bv[i][0])}, b1 = {bflo(bv[i][1]), bfhi(bv[i][1])};
        f32x2 h0 = gelu_tanh2(o0 * rr2 + b0), h1 = gelu_tanh2(o1 * rr2 + b1);
        if (LAST) {
            const bool lin = u0 + i >= 108;
            h0[0] = lin ? o0[0] : h0[0]; h0[1] = lin ? o0[1] : h0[1]; h1[0] = lin ? o1[0] : h1[0]; h1[1] = lin ? o1[1] : h1[1];
        }
        ov[i] = u32x2{pk2(h0[0], h0[1]), pk2(h1[0], h1[1])};
    });
    __builtin_amdgcn_sched_barrier(0);
    const rsrc_t rh = mk_rsrc(ws + (LAST ? OFF_H2 : OFF_H), H_BYTES), r3 = mk_rsrc(ws + OFF_X3, X3_BYTES);
    const int hb = punit * 54 * 1024;
    auto st1 = [&](int i) __attribute__((always_inline)) {
        const int u = u0 + i;
        if (i < nu) {
            if (!LAST || u < 108) __builtin_amdgcn_raw_buffer_store_b64(ov[i], rh, lane * 16 + (u & 1) * 8, hb + (u >> 1) * 1024, SC1);
            else __builtin_amdgcn_raw_buffer_store_b64(ov[i], r3, lane * 8, (punit * 40 + (u - 108)) * 512, SC1);
        }
    };
    auto st2p = [&](int i) __attribute__((always_inline)) {
        const int u = u0 + i;
        if (i + 1 < nu && (!LAST || u + 1 < 108)) {
            u32x4 q4 = {ov[i][0], ov[i][1], ov[i + 1][0], ov[i + 1][1]};
            __builtin_amdgcn_raw_buffer_store_b128(q4, rh, lane * 16, hb + (u >> 1) * 1024, SC1);
        } else { st1(i); st1(i + 1); }
    };
    if (u0 & 1) {
        st1(0);
#pragma unroll
        for (int i = 1; i + 1 < NF; i += 2) st2p(i);
        if ((NF & 1) == 0) st1(NF - 1);
    } else {
#pragma unroll
        for (int i = 0; i + 1 < NF; i += 2) st2p(i);
        if (NF & 1) st1(NF - 1);
    }
    STAMP(KID, 4);
}

template <bool LAST>
DI void g2_phase(const MainArgs& a, char* smem, int w, int lane, int pt1, int pt, int ct) {
    constexpr int PH = G2_PH, KID = LAST ? 6 : 3;
    char* ws = a.p.ws;
    u32* cnt = (u32*)(ws + OFF_CNT + (LAST ? 8192 : 0));
    const int pg = w & 1, kq = w >> 1, n = lane & 15, q = lane >> 4;
    const int kb = kq < 2 ? 14 * kq : 28 + 13 * (kq - 2), nk = kq < 2 ? 14 : 13;
    const u32x4 srd2 = mk_srd(ws + (LAST ? OFF_W2O : OFF_W2F), W2F_BYTES);
    const u32 lds0 = (u32)(size_t)(LAS char*)smem;
    const rsrc_t rh = mk_rsrc(ws + (LAST ? OFF_H2 : OFF_H), H_BYTES), rw = mk_rsrc(ws + (LAST ? OFF_W2O : OFF_W2F), W2F_BYTES);
    const int vo = lane * 16, hs = ((4 * pt + 2 * pg) * 54 + kb) * 1024;
    bf16x8 wr[6][5];
    asm volatile("" ::: "memory");
    __builtin_amdgcn_sched_barrier(0);
#pragma unroll
    for (int t = 0; t < 3; ++t)
#pragma unroll
        for (int i = 0; i < 5; ++i) wr[t][i] = asbf(__builtin_amdgcn_raw_buffer_load_b128(rw, vo, ((5 * ct + i) * 54 + kb + 8 + t) * 1024, SC1));
    __builtin_amdgcn_sched_barrier(0);
    wait_lgkm();
    wait_vm<15>();
    barrier();
    if (threadIdx.x == 0) add_agent(cnt + 64 * pt1);
    STAMP(LAST ? 5 : 2, 15);
#pragma unroll
    for (int t = LAST ? 5 : 6; t < 8; ++t) w2_issue(srd2, lds0, w, vo, ct, t);
    __builtin_amdgcn_sched_barrier(0);
#pragma unroll
    for (int t = 3; t < 6; ++t)
#pragma unroll
        for (int i = 0; i < 5; ++i) wr[t][i] = asbf(__builtin_amdgcn_raw_buffer_load_b128(rw, vo, ((5 * ct + i) * 54 + kb + 8 + t) * 1024, SC1));
    __builtin_amdgcn_sched_barrier(0);
    if (w == 0) poll_until(cnt + 64 * (pt >> 1), 16u, ws, lane);
    wait_vm<0>();
    barrier();
    STAMP(KID, 5);
    bf16x8 hb[PH][2];
    auto hload = [&](int t, int pu) __attribute__((always_inline)) {
        const int k = t < 13 ? t : nk - 1;
        return asbf(__builtin_amdgcn_raw_buffer_load_b128(rh, vo, hs + (pu * 54 + k) * 1024, SC1));
    };
#pragma unroll
    for (int t = 0; t < PH; ++t) { hb[t][0] = hload(t, 0); hb[t][1] = hload(t, 1); __builtin_amdgcn_sched_barrier(0); }
    u32x2 xv[3], bv[3]; float bb[3];
    f32x4 acc[2][5];
#pragma unroll
    for (int i = 0; i < 5; ++i) { acc[0][i] = f32x4{0, 0, 0, 0}; acc[1][i] = f32x4{0, 0, 0, 0}; }
    bf16x8 wa[2][5];
    const char* wbase = smem + kq * 5120 + lane * 16;
#pragma unroll
    for (int i = 0; i < 5; ++i) wa[0][i] = *(const bf16x8*)(wbase + w2slot(0) + i * 1024);
    static_for<0, 14>([&](auto T) __attribute__((always_inline)) {
        constexpr int t = decltype(T)::value;
        if constexpr (t == 10) {
            __builtin_amdgcn_sched_barrier(0);
#pragma unroll
            for (int j = 0; j < 3; ++j) {
                const int tt = min(kq + 4 * j, 9), pu = 2 * pg + tt / 5, cu = tt % 5;
                if (!LAST) {
                    const int c = 80 * ct + 16 * cu + n;
                    const int off = (((4 * pt + pu) * 20 + (c >> 5)) * 64 + ((c & 31) >> 3) * 16 + 4 * q) * 16 + (c & 7) * 2;
                    const rsrc_t r1 = mk_rsrc(ws + OFF_XF1, XT_BYTES);
                    const u32 e0 = __builtin_amdgcn_raw_buffer_load_b16(r1, off, 0, SC1), e1 = __builtin_amdgcn_raw_buffer_load_b16(r1, off + 16, 0, SC1);
                    const u32 e2 = __builtin_amdgcn_raw_buffer_load_b16(r1, off + 32, 0, SC1), e3 = __builtin_amdgcn_raw_buffer_load_b16(r1, off + 48, 0, SC1);
                    xv[j] = u32x2{e0 | (e1 << 16), e2 | (e3 << 16)};
                    bb[j] = ((const float*)(ws + OFF_B2P))[c];
                } else {
                    xv[j] = __builtin_amdgcn_raw_buffer_load_b64(mk_rsrc(ws + OFF_X3, X3_BYTES), lane * 8, ((4 * pt + pu) * 40 + 5 * ct + cu) * 512, SC1);
                    bv[j] = *(const u32x2*)(a.p.b2 + 80 * ct + 16 * cu + 4 * q);
                }
            }
        }
        bf16x8 nh[2];
        auto step = [&]() __attribute__((always_inline)) {
            static_for<0, 5>([&](auto I) __attribute__((always_inline)) {
                constexpr int i = decltype(I)::value;
                __builtin_amdgcn_sched_barrier(0);
                bf16x8 wv;
                if constexpr (t < 8) wv = wa[t & 1][i]; else wv = wr[t - 8][i];
                acc[0][i] = LAST ? mfma16(wv, hb[t % PH][0], acc[0][i]) : mfma16(hb[t % PH][0], wv, acc[0][i]);
                if constexpr (t + 1 < 8) wa[(t + 1) & 1][i] = *(const bf16x8*)(wbase + w2slot(t + 1) + i * 1024);
                acc[1][i] = LAST ? mfma16(wv, hb[t % PH][1], acc[1][i]) : mfma16(hb[t % PH][1], wv, acc[1][i]);
                __builtin_amdgcn_sched_barrier(0);
                if constexpr (t + PH < 14 && (i == 1 || i == 3)) nh[i >> 1] = hload(t + PH, i >> 1);
            });
        };
        if constexpr (t < 13) step(); else { if (nk == 14) step(); }
        __builtin_amdgcn_sched_barrier(0);
        if constexpr (t + PH < 14) { hb[t % PH][0] = nh[0]; hb[t % PH][1] = nh[1]; }
        if (t == 0) STAMP(KID, 1);
        if (t == 7) STAMP(KID, 2);
    });
    STAMP(KID, 3);
    wait_lgkm();
    barrier();
#pragma unroll
    for (int t = 0; t < 10; ++t)
        if ((t & 3) != kq) *(f32x4*)(smem + ((w * 10 + t) * 64 + lane) * 16) = acc[t / 5][t % 5];
    wait_lgkm();
    barrier();
    float ssp[2][4];
#pragma unroll
    for (int r = 0; r < 4; ++r) { ssp[0][r] = 0.f; ssp[1][r] = 0.f; }
    const rsrc_t ro = mk_rsrc(ws + OFF_XT2, XT_BYTES);
    auto finish = [&](auto KQ) __attribute__((always_inline)) {
        constexpr int kqc = decltype(KQ)::value;
        static_for<0, 3>([&](auto J) __attribute__((always_inline)) {
            constexpr int j = decltype(J)::value, t = kqc + 4 * j;
            if constexpr (t < 10) {
                constexpr int pl = t / 5, cu = t % 5;
                f32x4 o = acc[pl][cu];
#pragma unroll
                for (int s = 1; s < 4; ++s) o += *(const f32x4*)(smem + (((pg + 2 * ((kqc + s) & 3)) * 10 + t) * 64 + lane) * 16);
                const int pu = 2 * pg + pl;
                if (!LAST) {
                    const int c = 80 * ct + 16 * cu + n, p = 64 * pt + 16 * pu + 4 * q;
                    const u32 o0 = pk2(bflo(xv[j][0]) + o[0] + bb[j], bfhi(xv[j][0]) + o[1] + bb[j]);
                    const u32 o1 = pk2(bflo(xv[j][1]) + o[2] + bb[j], bfhi(xv[j][1]) + o[3] + bb[j]);
                    u32x2 ov = {o0, o1};
                    __builtin_amdgcn_raw_buffer_store_b64(ov, ro, (c * S_ + p) * 2, 0, SC1);
                    const float m = c == 609 ? 0.f : 1.f;
                    ssp[pl][0] += m * bflo(o0) * bflo(o0); ssp[pl][1] += m * bfhi(o0) * bfhi(o0);
                    ssp[pl][2] += m * bflo(o1) * bflo(o1); ssp[pl][3] += m * bfhi(o1) * bfhi(o1);
                } else {
                    const int p = 16 * (4 * pt + pu) + n, d = 80 * ct + 16 * cu + 4 * q;
                    u32x2 ov = {pk2(bflo(xv[j][0]) + o[0] + bflo(bv[j][0]), bfhi(xv[j][0]) + o[1] + bfhi(bv[j][0])),
                                pk2(bflo(xv[j][1]) + o[2] + bflo(bv[j][1]), bfhi(xv[j][1]) + o[3] + bfhi(bv[j][1]))};
                    *(u32x2*)(a.out + (size_t)tok_of_p(p) * D_ + d) = ov;
                }
            }
        });
    };
    if (kq == 0) finish(std::integral_constant<int, 0>{});
    else if (kq == 1) finish(std::integral_constant<int, 1>{});
    else if (kq == 2) finish(std::integral_constant<int, 2>{});
    else finish(std::integral_constant<int, 3>{});
    if (!LAST) {
        float* sp = (float*)smem;
#pragma unroll
        for (int pl = 0; pl < 2; ++pl)
#pragma unroll
            for (int r = 0; r < 4; ++r) {
                const float v = row_sum15(ssp[pl][r]);
                if (n == 15) sp[w * 32 + 16 * pl + 4 * q + r] = v;
            }
        wait_lgkm();
        barrier();
        if (w == 0) {
            const int g2 = lane >> 5, r = lane & 31;
            const float v = (sp[g2 * 32 + r] + sp[(g2 + 2) * 32 + r]) + (sp[(g2 + 4) * 32 + r] + sp[(g2 + 6) * 32 + r]);
            __hip_atomic_fetch_add((__attribute__((address_space(1))) u64*)(ws + OFF_SS2) + 64 * pt + lane, ss_from_float(v), __ATOMIC_RELAXED, __HIP_MEMORY_SCOPE_AGENT);
        }
        wait_lgkm();
        wait_vm<0>();
        barrier();
        signal_all(ws, 1);
    }
}

template <bool LAST>
DI void mlp(const MainArgs& a, char* smem, int w, int lane, int u0) {
    constexpr int NF = LAST ? 10 : 7;
    const int x = blockIdx.x & 7, ib = blockIdx.x >> 3;
    const int pt = 4 * (x >> 1) + (ib & 3), ft = 8 * (x & 1) + (ib >> 2);
    const int pt2 = 8 * (x >> 1) + (ib & 7), ct2 = 4 * (x & 1) + (ib >> 3);
    const int nu = LAST ? (ft < 4 ? 10 : 9) : (ft < 12 ? 7 : 6);
    if (LAST) poll_all(a.p.ws, 2, 20u, w, lane);
    else if (w == 0) {
        const int sh = lane & 7, wh = (lane >> 3) & 1;
        poll_until((const u32*)(a.p.ws + OFF_CNT + 16384 + (wh ? 3 : 0) * 8192) + ((blockIdx.x >> 3) & 7) * 256 + sh * 32, wh ? (sh < 6 ? 12u : 11u) : 20u, a.p.ws, lane);
    }
    wait_vm<0>();
    barrier();
    STAMP(LAST ? 5 : 2, 1);
    if (w < 4) g1_phase<NF, LAST, 0>(a, smem, w, lane, pt, u0, nu, ct2);
    else g1_phase<NF, LAST, 1>(a, smem, w, lane, pt, u0, nu, ct2);
    g2_phase<LAST>(a, smem, w, lane, pt, pt2, ct2);
}

__global__ __launch_bounds__(512, 2) void k_main(MainArgs a) {
    extern __shared__ __attribute__((aligned(16))) char smem[];
    const int lane = threadIdx.x & 63, b = blockIdx.x;
    const int w = __builtin_amdgcn_readfirstlane(threadIdx.x >> 6);
    const int ft = 8 * (b & 1) + (b >> 5);
    const int u0a = ft < 12 ? 7 * ft : 84 + 6 * (ft - 12), u0b = ft < 4 ? 10 * ft : 40 + 9 * (ft - 4);
    if (b < 160) col_task<0>(a.p.ws, b, smem, u0a);
    else {
        w1_prefetch<false>(a.p.ws, smem, w, lane, u0a);
        if (b < 254) {
            if (b < 214) task_w2(a.p, b - 160, smem); else task_Q(a.p, b - 214);
            wait_lgkm();
            wait_vm<0>();
            barrier();
            signal_all(a.p.ws, 3);
        }
    }
    mlp<false>(a, smem, w, lane, u0a);
    END_STAMP(3);
    if (b < 160) col_task<1>(a.p.ws, b, smem, u0b); else w1_prefetch<true>(a.p.ws, smem, w, lane, u0b);
    mlp<true>(a, smem, w, lane, u0b);
    END_STAMP(6);
}

extern "C" void kernel_launch(void* const* d_in, const int* in_sizes, int n_in, void* d_out, int out_size, void* d_ws,
                              size_t ws_size, hipStream_t stream) {
    char* ws = (char*)d_ws;
    constexpr int MAIN_LDS = 163840;
    (void)hipFuncSetAttribute((const void*)k_main, hipFuncAttributeMaxDynamicSharedMemorySize, MAIN_LDS);
    PrepArgs pa{(const u16*)d_in[0], (const u16*)d_in[1], (const u16*)d_in[2], (const u16*)d_in[4], (const u16*)d_in[6], (const u16*)d_in[7], ws};
    k0_prep<<<NB_K0, 512, PREP_LDS, stream>>>(pa);
    MainArgs ma{pa, (const u16*)d_in[5], (u16*)d_out};
    k_main<<<256, 512, MAIN_LDS, stream>>>(ma);
}
```

```cpp
#include <hip/hip_runtime.h>
#include <hip/hip_bf16.h>
#include <stdint.h>
#include <type_traits>

typedef unsigned short u16;
typedef unsigned int u32;
typedef unsigned long long u64;
typedef short s16x4 __attribute__((ext_vector_type(4)));
typedef __bf16 bf16x8 __attribute__((ext_vector_type(8)));
typedef __bf16 bf16x2 __attribute__((ext_vector_type(2)));
typedef float f32x4 __attribute__((ext_vector_type(4)));
typedef float f32x2 __attribute__((ext_vector_type(2)));
typedef u32 u32x4 __attribute__((ext_vector_type(4)));
typedef u32 u32x2 __attribute__((ext_vector_type(2)));
typedef __amdgpu_buffer_rsrc_t rsrc_t;
#define LAS __attribute__((address_space(3)))
#define DI __device__ __forceinline__

constexpr int S_ = 2048, D_ = 640, F_ = 1728, DF_ = 321;
constexpr float EPS = 1e-5f;
constexpr int SC1 = 16;
constexpr float SS_SCALE = 1048576.0f;

constexpr size_t XT_BYTES = (size_t)D_ * S_ * 2;
constexpr size_t OFF_XT0 = 0;
constexpr size_t OFF_XT1 = OFF_XT0 + XT_BYTES;
constexpr size_t OFF_XT2 = OFF_XT1 + XT_BYTES;
constexpr size_t OFF_XT3 = OFF_XT2 + XT_BYTES;
constexpr size_t OFF_WT = OFF_XT3 + XT_BYTES;
constexpr size_t OFF_W1F = OFF_WT + (size_t)320 * 2048 * 4;
constexpr size_t W1F_BYTES = (size_t)148 * 20 * 1024;
constexpr size_t OFF_W2F = OFF_W1F + W1F_BYTES;
constexpr size_t W2F_BYTES = (size_t)40 * 54 * 1024;
constexpr size_t OFF_W2O = OFF_W2F + W2F_BYTES;
constexpr size_t OFF_H = OFF_W2O + W2F_BYTES;
constexpr size_t H_BYTES = (size_t)128 * 54 * 1024;
constexpr size_t OFF_H2 = OFF_H + H_BYTES;
constexpr size_t OFF_X3 = OFF_H2 + H_BYTES;
constexpr size_t OFF_SS0 = OFF_X3 + (size_t)128 * 40 * 512;
constexpr size_t OFF_SS2 = OFF_SS0 + 2048 * 8;
constexpr size_t OFF_B2P = OFF_SS2 + 2048 * 8;
constexpr size_t OFF_XF1 = OFF_B2P + 4096;
constexpr size_t OFF_XF3 = OFF_XF1 + XT_BYTES;
constexpr size_t X3_BYTES = (size_t)128 * 40 * 512;
constexpr size_t OFF_CNT = OFF_XF3 + XT_BYTES;
constexpr size_t WS_END = OFF_CNT + 65536;

#define STAMP(k, i)
#define CSTAMP(k, i)
#define END_STAMP(k)

DI f32x4 mfma16(bf16x8 a, bf16x8 b, f32x4 c) { return __builtin_amdgcn_mfma_f32_16x16x32_bf16(a, b, c, 0, 0, 0); }
DI u32 pk2(float a, float b) { f32x2 v = {a, b}; const bf16x2 r = __builtin_convertvector(v, bf16x2); return __builtin_bit_cast(u32, r); }
DI bf16x8 pk8(float a0, float a1, float a2, float a3, float a4, float a5, float a6, float a7) {
    u32x4 r = {pk2(a0, a1), pk2(a2, a3), pk2(a4, a5), pk2(a6, a7)};
    return __builtin_bit_cast(bf16x8, r);
}
DI bf16x8 pk8(const float (&a)[8]) { return pk8(a[0], a[1], a[2], a[3], a[4], a[5], a[6], a[7]); }
DI bf16x8 asbf(u32x4 r) { return __builtin_bit_cast(bf16x8, r); }
DI bf16x8 neg8(bf16x8 a) { u32x4 r = __builtin_bit_cast(u32x4, a); r ^= 0x80008000u; return __builtin_bit_cast(bf16x8, r); }
DI float bflo(u32 v) { return __builtin_bit_cast(float, v << 16); }
DI float bfhi(u32 v) { return __builtin_bit_cast(float, v & 0xffff0000u); }
DI float cosr(float rev) { return __builtin_amdgcn_cosf(rev); }
DI float sinr(float rev) { return __builtin_amdgcn_sinf(rev); }
DI int slot32(int q, int j) { return j < 4 ? 4 * q + j : 16 + 4 * q + (j - 4); }
DI s16x4 tr_read(const char* p) { return __builtin_amdgcn_ds_read_tr16_b64_v4i16((LAS s16x4*)p); }
DI bf16x8 cat8(s16x4 lo, s16x4 hi) { return __builtin_bit_cast(bf16x8, __builtin_shufflevector(lo, hi, 0, 1, 2, 3, 4, 5, 6, 7)); }
DI int tok_of_p(int p) { return 32 * (p & 63) + (p >> 6); }
DI rsrc_t mk_rsrc(const void* p, size_t bytes) { return __builtin_amdgcn_make_buffer_rsrc((void*)p, 0, (int)bytes, 0x00020000); }
DI u32x4 bload16(rsrc_t r, int voff, int soff) { return __builtin_amdgcn_raw_buffer_load_b128(r, voff, soff, 0); }
DI float ss_to_float(u64 v) { return (float)(u32)(v >> 32) * 4294967296.0f + (float)(u32)v; }
DI u64 ss_from_float(float s) { return (u64)(fminf(s * SS_SCALE, 1.0e18f) + 0.5f); }
DI void pin(float& x) { asm volatile("" : "+v"(x)); }
DI void pin(bf16x8& x) { asm volatile("" : "+v"(x)); }

struct F32c { bf16x8 c[2], sp[2]; };
DI void f32c_init(F32c& k, int lane) {
    const float m = (float)(lane & 15), q4 = (float)(4 * (lane >> 4));
    float c[4], s[4];
#pragma unroll
    for (int j = 0; j < 4; ++j) { const float a = __builtin_amdgcn_fractf(m * (q4 + (float)j) * (1.0f / 32.0f)); c[j] = cosr(a); s[j] = sinr(a); }
    const u32 sg = (lane & 1) ? 0x80008000u : 0u;
    const u32 c0 = pk2(c[0], c[1]), c1 = pk2(c[2], c[3]), s0 = pk2(s[0], s[1]), s1 = pk2(s[2], s[3]);
    const u32x4 cv = {c0, c1, c0 ^ sg, c1 ^ sg}, sv = {s0, s1, s0 ^ sg, s1 ^ sg};
    k.c[0] = asbf(cv); k.sp[0] = asbf(sv);
    k.c[1] = asbf(cv ^ 0x80000000u); k.sp[1] = asbf(sv ^ 0x80000000u);
}

struct RowC { bf16x8 c[2], sp[2], sm[2], c20, s20m; f32x4 twr[2], twi[2]; int off0, off1; };
constexpr int ROWC_LDS = 10 * 1024;
DI void rowc_build(char* tab, int w, int lane) {
    const int n = lane & 15, q = lane >> 4;
    const float nf = (float)n;
    u32x4* t = (u32x4*)tab + lane;
    if (w == 0) {
        F32c f; f32c_init(f, lane);
        t[0] = __builtin_bit_cast(u32x4, f.c[0]); t[64] = __builtin_bit_cast(u32x4, f.c[1]);
        t[128] = __builtin_bit_cast(u32x4, f.sp[0]); t[192] = __builtin_bit_cast(u32x4, f.sp[1]);
    } else if (w <= 2) {
        float c[8];
#pragma unroll
        for (int j = 0; j < 8; ++j) {
            const int d1 = 8 * q + j;
            const float a = __builtin_amdgcn_fractf((float)d1 * nf * 0.05f);
            const float v = w == 1 ? cosr(a) : -sinr(a);
            c[j] = (d1 < 20 && n <= 10) ? v : 0.f;
        }
        t[(3 + w) * 64] = __builtin_bit_cast(u32x4, pk8(c));
    } else if (w <= 4) {
        const float sc = 0.05590169943749474f;
        f32x4 cr, ci;
#pragma unroll
        for (int i = 0; i < 4; ++i) {
            const float a = (float)(16 * (w - 3) + 4 * q + i) * nf * (1.0f / 640.0f);
            cr[i] = cosr(a) * sc; ci[i] = -sinr(a) * sc;
        }
        t[(6 + w - 3) * 64] = __builtin_bit_cast(u32x4, cr); t[(8 + w - 3) * 64] = __builtin_bit_cast(u32x4, ci);
    }
}
DI void rowc_load(RowC& k, const char* tab, int lane) {
    const u32x4* tb = (const u32x4*)tab + lane;
    k.c[0] = asbf(tb[0]); k.c[1] = asbf(tb[64]); k.sp[0] = asbf(tb[128]); k.sp[1] = asbf(tb[192]);
    k.c20 = asbf(tb[256]); k.s20m = asbf(tb[320]);
    k.twr[0] = __builtin_bit_cast(f32x4, tb[384]); k.twr[1] = __builtin_bit_cast(f32x4, tb[448]);
    k.twi[0] = __builtin_bit_cast(f32x4, tb[512]); k.twi[1] = __builtin_bit_cast(f32x4, tb[576]);
    k.sm[0] = neg8(k.sp[0]); k.sm[1] = neg8(k.sp[1]);
    const int q = lane >> 4, qq = (lane & 15) >> 2, pp = lane & 3;
    const int r0 = min(8 * q + qq, 19), r1 = min(8 * q + 4 + qq, 19);
    k.off0 = r0 * 64 + pp * 8; k.off1 = r1 * 64 + pp * 8;
}
DI float wave_sum63(float v) {
    v += __builtin_bit_cast(float, __builtin_amdgcn_update_dpp(0, __builtin_bit_cast(int, v), 0x111, 0xf, 0xf, true));
    v += __builtin_bit_cast(float, __builtin_amdgcn_update_dpp(0, __builtin_bit_cast(int, v), 0x112, 0xf, 0xf, true));
    v += __builtin_bit_cast(float, __builtin_amdgcn_update_dpp(0, __builtin_bit_cast(int, v), 0x114, 0xf, 0xf, true));
    v += __builtin_bit_cast(float, __builtin_amdgcn_update_dpp(0, __builtin_bit_cast(int, v), 0x118, 0xf, 0xf, true));
    v += __builtin_bit_cast(float, __builtin_amdgcn_update_dpp(0, __builtin_bit_cast(int, v), 0x142, 0xa, 0xf, true));
    v += __builtin_bit_cast(float, __builtin_amdgcn_update_dpp(0, __builtin_bit_cast(int, v), 0x143, 0xc, 0xf, true));
    return v;
}
DI void rowfft(const RowC& k, const char* row, float (&A)[8], float (&B)[8], int lane) {
    f32x4 z = {0.f, 0.f, 0.f, 0.f};
    f32x4 yr[2], yi[2];
#pragma unroll
    for (int mt = 0; mt < 2; ++mt) {
        const bf16x8 a = cat8(tr_read(row + k.off0 + mt * 32), tr_read(row + k.off1 + mt * 32));
        yr[mt] = mfma16(a, k.c20, z); yi[mt] = mfma16(a, k.s20m, z);
    }
    float tr[8], ti[8];
#pragma unroll
    for (int i = 0; i < 8; ++i) {
        const float a = yr[i >> 2][i & 3], b = yi[i >> 2][i & 3], cr = k.twr[i >> 2][i & 3], ci = k.twi[i >> 2][i & 3];
        tr[i] = a * cr - b * ci; ti[i] = a * ci + b * cr;
    }
    const bf16x8 Tr = pk8(tr), Ti = pk8(ti);
    f32x4 ore[2], oim[2];
#pragma unroll
    for (int t = 0; t < 2; ++t) {
        ore[t] = mfma16(k.c[t], Tr, z); ore[t] = mfma16(k.sp[t], Ti, ore[t]);
        oim[t] = mfma16(k.sm[t], Tr, z); oim[t] = mfma16(k.c[t], Ti, oim[t]);
    }
#pragma unroll
    for (int r = 0; r < 4; ++r) { A[2 * r] = ore[0][r]; A[2 * r + 1] = oim[0][r]; B[2 * r] = ore[1][r]; B[2 * r + 1] = oim[1][r]; }
    if (lane == 0) { const float h = 0.7071067811865476f; const float dc = A[0] * h; A[0] = B[0] * h; A[1] = dc; }
}
DI int sw_off(int c) { const int ch = c >> 3; return ((ch ^ ((ch >> 3) & 7)) << 4) + (c & 7) * 2; }
DI int chunk_c0(int n, int q) { return (n == 0 ? 608 : n == 10 ? 576 : 64 * (n - 1)) + 8 * q; }

constexpr int ROWST = 1344;
constexpr int PREP_TAB = 640 * 32 + 16 * ROWST;
constexpr int PREP_LDS = PREP_TAB + ROWC_LDS;

struct PrepArgs { const u16 *x, *wr, *wi, *w1, *w2, *b2; char* ws; };

DI void task_x(const PrepArgs& a, int b, char* smem) {
    const int tid = threadIdx.x, lane = tid & 63, w = tid >> 6;
    const int p0 = 16 * ((b & 7) * 16 + (b >> 3));
    u32x4 v[3];
#pragma unroll
    for (int i = 0; i < 3; ++i) {
        const int id = min(tid + 512 * i, 1279), r = id / 80, ch = id % 80;
        v[i] = *(const u32x4*)(a.x + (size_t)tok_of_p(p0 + r) * D_ + ch * 8);
    }
    __builtin_amdgcn_sched_barrier(0);
    rowc_build(smem + PREP_TAB, w, lane);
    __builtin_amdgcn_sched_barrier(0);
    STAMP(0, 1);
    if (tid < 16) ((u64*)(a.ws + OFF_SS2))[p0 + tid] = 0ull;
    if (b < 32) ((u32*)(a.ws + OFF_CNT))[b * 512 + tid] = 0u;
#pragma unroll
    for (int i = 0; i < 3; ++i) {
        const int id = min(tid + 512 * i, 1279), r = id / 80, ch = id % 80;
        *(u32x4*)(smem + r * ROWST + ch * 16) = v[i];
    }
    STAMP(0, 2);
    __syncthreads();
    STAMP(0, 3);
    RowC k; rowc_load(k, smem + PREP_TAB, lane);
    const int n = lane & 15, q = lane >> 4, c0 = chunk_c0(n, q);
    float A[2][8], B[2][8];
#pragma unroll
    for (int i = 0; i < 2; ++i) rowfft(k, smem + (2 * w + i) * ROWST, A[i], B[i], lane);
#pragma unroll
    for (int i = 0; i < 2; ++i) {
        const int r = 2 * w + i;
        float ss = 0.f;
        if (n <= 10) {
#pragma unroll
            for (int e = 0; e < 8; ++e) ss += (lane == 0 && e == 1) ? 0.f : A[i][e] * A[i][e];
            *(bf16x8*)(smem + r * ROWST + sw_off(c0)) = pk8(A[i]);
        }
        if (n >= 1 && n <= 9) {
#pragma unroll
            for (int e = 0; e < 8; ++e) ss += B[i][e] * B[i][e];
            *(bf16x8*)(smem + r * ROWST + sw_off(c0 + 32)) = pk8(B[i]);
        }
        ss = wave_sum63(ss);
        if (lane == 63) ((u64*)(a.ws + OFF_SS0))[p0 + r] = ss_from_float(ss);
    }
    STAMP(0, 4);
    __syncthreads();
    STAMP(0, 5);
    u16* xt = (u16*)(a.ws + OFF_XT0);
    const int g = lane >> 4, i16 = lane & 15, qq = i16 >> 2, pp = i16 & 3;
#pragma unroll
    for (int i = 0; i < 3; ++i) {
        const int cbk = w + 8 * i;
        if (cbk < 20) {
            const int cb = 32 * cbk + 16 * (g & 1), ph = g >> 1;
            const char* base = smem + (8 * ph + qq) * ROWST + sw_off(cb + 4 * pp);
            const bf16x8 o = cat8(tr_read(base), tr_read(base + 4 * ROWST));
            *(bf16x8*)(xt + (size_t)(cb + i16) * S_ + p0 + 8 * ph) = o;
        }
    }
}

DI void task_W(const PrepArgs& a, int b) {
    const int lane = threadIdx.x & 63, wv = threadIdx.x >> 6, wt = b * 2 + (wv >> 2), kq = wv & 3;
    const int k1 = wt / 5, cg = wt % 5, kp = 1 + 64 * cg + lane;
    const int r = kp % 20, t = kp / 20;
    int j; bool mir = false;
    if (kp == 320) j = 304;
    else if (r == 0) j = 304 + t;
    else if (r < 10) j = 32 * (r - 1) + t;
    else if (r == 10) j = 288 + t;
    else { j = 32 * (19 - r) + 31 - t; mir = true; }
    const rsrc_t rr = mk_rsrc(a.wr, (size_t)S_ * DF_ * 2), ri = mk_rsrc(a.wi, (size_t)S_ * DF_ * 2);
    const int st = (mir ? -64 : 64) * DF_ * 2, off0 = ((mir ? 2048 - k1 : k1) * DF_ + kp) * 2 + 8 * kq * st;
    u32 re[8], im[8];
#pragma unroll
    for (int k2 = 0; k2 < 8; ++k2) {
        int off = off0 + k2 * st;
        if (k2 == 0 && mir && k1 == 0 && kq == 0) off = kp * 2;
        re[k2] = __builtin_amdgcn_raw_buffer_load_b16(rr, off, 0, 0); im[k2] = __builtin_amdgcn_raw_buffer_load_b16(ri, off, 0, 0);
    }
    const u32 sg = mir ? 0x80000000u : 0u;
    u32x4* dst = (u32x4*)(a.ws + OFF_WT + ((size_t)j * 64 + k1) * 128 + kq * 32);
#pragma unroll
    for (int i = 0; i < 2; ++i) {
        u32x4 o;
#pragma unroll
        for (int e = 0; e < 4; ++e) o[e] = (re[4 * i + e] | (im[4 * i + e] << 16)) ^ sg;
        dst[i] = o;
    }
}

DI void task_w1(const PrepArgs& a, int b, char* smem) {
    const int tid = threadIdx.x, lane = tid & 63, w = tid >> 6;
    char* raw = smem;
    char* tr = smem + 640 * 32;
    char* out = raw;
    u32x4 v[3];
#pragma unroll
    for (int i = 0; i < 3; ++i) {
        const int id = min(tid + 512 * i, 1279), d = id >> 1, h = id & 1;
        v[i] = *(const u32x4*)(a.w1 + (size_t)d * F_ + 16 * b + 8 * h);
    }
    __builtin_amdgcn_sched_barrier(0);
    rowc_build(smem + PREP_TAB, w, lane);
    __builtin_amdgcn_sched_barrier(0);
#pragma unroll
    for (int i = 0; i < 3; ++i) *(u32x4*)(raw + min(tid + 512 * i, 1279) * 16) = v[i];
    __syncthreads();
    const int g = lane >> 4, i16 = lane & 15, qq = i16 >> 2, pp = i16 & 3;
#pragma unroll
    for (int i = 0; i < 5; ++i) {
        const int d0 = 16 * (w + 8 * i) + 4 * g;
        const s16x4 t = tr_read(raw + (d0 + qq) * 32 + pp * 8);
        *(s16x4*)(tr + i16 * ROWST + d0 * 2) = t;
    }
    RowC k; rowc_load(k, smem + PREP_TAB, lane);
    __syncthreads();
    const int n = lane & 15, q = lane >> 4, c0 = chunk_c0(n, q);
    float A[2][8], B[2][8];
#pragma unroll
    for (int i = 0; i < 2; ++i) rowfft(k, tr + (2 * w + i) * ROWST, A[i], B[i], lane);
#pragma unroll
    for (int i = 0; i < 2; ++i) {
        const int f = 2 * w + i;
        if (lane == 0) A[i][1] = 0.f;
        if (n <= 10) *(bf16x8*)(out + ((c0 >> 5) * 64 + q * 16 + f) * 16) = pk8(A[i]);
        if (n >= 1 && n <= 9) *(bf16x8*)(out + (((c0 >> 5) + 1) * 64 + q * 16 + f) * 16) = pk8(B[i]);
    }
    __syncthreads();
    u32x4* dst = (u32x4*)(a.ws + OFF_W1F + (size_t)b * 20 * 1024);
#pragma unroll
    for (int i = 0; i < 3; ++i) { const int id = tid + 512 * i; if (id < 1280) dst[id] = *(const u32x4*)(out + id * 16); }
}

template <bool SW> DI void w2_frags(const char* img, char* dstbase, int b, int lane, int w) {
    const int g = lane >> 4, i16 = lane & 15, qq = i16 >> 2, pp = i16 & 3;
    const rsrc_t rd = mk_rsrc(dstbase, W2F_BYTES);
#pragma unroll
    for (int i = 0; i < 5; ++i) {
        const int cu = w + 8 * i, c = 16 * cu + 4 * pp;
        const char* base = img + (4 * g + qq) * ROWST + (SW ? sw_off(c) : c * 2);
        const bf16x8 v = cat8(tr_read(base), tr_read(base + 16 * ROWST));
        __builtin_amdgcn_raw_buffer_store_b128(__builtin_bit_cast(u32x4, v), rd, lane * 16, (cu * 54 + b) * 1024, SC1);
    }
}
constexpr int W2_TAB = 32 * ROWST;
DI void task_w2(const PrepArgs& a, int b, char* smem) {
    const int tid = threadIdx.x, lane = tid & 63, w = tid >> 6;
    u32x4 v[5];
#pragma unroll
    for (int i = 0; i < 5; ++i) {
        const int id = tid + 512 * i, r = id / 80, ch = id % 80;
        v[i] = *(const u32x4*)(a.w2 + (size_t)(32 * b + r) * D_ + ch * 8);
    }
    __builtin_amdgcn_sched_barrier(0);
    rowc_build(smem + W2_TAB, w, lane);
    __builtin_amdgcn_sched_barrier(0);
#pragma unroll
    for (int i = 0; i < 5; ++i) { const int id = tid + 512 * i, r = id / 80, ch = id % 80; *(u32x4*)(smem + r * ROWST + ch * 16) = v[i]; }
    __syncthreads();
    RowC k; rowc_load(k, smem + W2_TAB, lane);
    w2_frags<false>(smem, a.ws + OFF_W2O, b, lane, w);
    const int n = lane & 15, q = lane >> 4, c0 = chunk_c0(n, q);
    u32x4 A8[4], B8[4];
#pragma unroll
    for (int i = 0; i < 4; ++i) {
        float A[8], B[8];
        rowfft(k, smem + (4 * w + i) * ROWST, A, B, lane);
        A8[i] = __builtin_bit_cast(u32x4, pk8(A)); B8[i] = __builtin_bit_cast(u32x4, pk8(B));
    }
    __syncthreads();
#pragma unroll
    for (int i = 0; i < 4; ++i) {
        const int r = 4 * w + i;
        if (n <= 10) *(u32x4*)(smem + r * ROWST + sw_off(c0)) = A8[i];
        if (n >= 1 && n <= 9) *(u32x4*)(smem + r * ROWST + sw_off(c0 + 32)) = B8[i];
    }
    __syncthreads();
    w2_frags<true>(smem, a.ws + OFF_W2F, b, lane, w);
}

DI int kcol_of(int jc) { return jc < 288 ? (jc >> 5) + 1 + 20 * (jc & 31) : jc < 304 ? 10 + 20 * (jc - 288) : 20 * (jc - 304); }
DI void task_Q(const PrepArgs& a, int u) {
    const int tid = threadIdx.x;
#pragma unroll
    for (int ii = 0; ii < 3; ++ii) {
        const int i = tid + 512 * ii;
        if (i < 1280) {
            const int ks = i >> 6, l = i & 63, d = 16 * u + (l & 15), q = l >> 4;
            float v[8];
#pragma unroll
            for (int j = 0; j < 8; j += 2) {
                const int jc = 16 * ks + 4 * q + (j >> 1), kk = kcol_of(jc);
                const float ang = (float)((kk * d) % 640) * (1.0f / 640.0f);
                float c = cosr(ang) * 0.05590169943749474f, s = -sinr(ang) * 0.05590169943749474f;
                if (jc == 304) { c = ((d & 1) ? -1.f : 1.f) * 0.03952847075210474f; s = 0.03952847075210474f; }
                v[j] = c; v[j + 1] = s;
            }
            __builtin_amdgcn_raw_buffer_store_b128(__builtin_bit_cast(u32x4, pk8(v)), mk_rsrc(a.ws + OFF_W1F, W1F_BYTES), i * 16, (108 + u) * 20480, SC1);
        }
    }
}
DI void task_b(const PrepArgs& a, char* smem) {
    const int tid = threadIdx.x, lane = tid & 63;
    if (tid < 80) *(u32x4*)(smem + tid * 16) = *(const u32x4*)(a.b2 + tid * 8);
    rowc_build(smem + PREP_TAB, tid >> 6, lane);
    __syncthreads();
    RowC k; rowc_load(k, smem + PREP_TAB, lane);
    float A[8], B[8];
    rowfft(k, smem, A, B, lane);
    if (tid < 64) {
        const int n = lane & 15, q = lane >> 4, c0 = chunk_c0(n, q);
        float* o = (float*)(a.ws + OFF_B2P);
        if (n <= 10) for (int e = 0; e < 8; ++e) o[c0 + e] = A[e];
        if (n >= 1 && n <= 9) for (int e = 0; e < 8; ++e) o[c0 + 32 + e] = B[e];
    }
}

constexpr int NB_K0 = 128 + 108 + 160 + 1;
constexpr int MAIN_CODE_PREFETCH = 65536;
DI void prefetch_main_code(int slice, int lane) {
    u32 lo, hi;
    asm volatile("s_getpc_b64 s[40:41]\n\ts_add_u32 s40, s40, _Z6k_main8MainArgs@rel32@lo+4\n\ts_addc_u32 s41, s41, _Z6k_main8MainArgs@rel32@hi+12\n\t"
                 "s_mov_b32 %0, s40\n\ts_mov_b32 %1, s41" : "=s"(lo), "=s"(hi) :: "s40", "s41", "scc");
    const u64 base = ((u64)hi << 32) | lo;
    const char* p = (const char*)base + slice * 4096 + lane * 16;
    u32x4 t0, t1, t2, t3;
    asm volatile("global_load_dwordx4 %0, %4, off\n\tglobal_load_dwordx4 %1, %4, off offset:1024\n\tglobal_load_dwordx4 %2, %4, off offset:2048\n\t"
                 "global_load_dwordx4 %3, %4, off offset:3072\n\ts_waitcnt vmcnt(0)"
                 : "=&v"(t0), "=&v"(t1), "=&v"(t2), "=&v"(t3) : "v"(p) : "memory");
}
__global__ __launch_bounds__(512, 4) void k0_prep(PrepArgs a) {
    extern __shared__ __attribute__((aligned(16))) char smem[];
    const int b = blockIdx.x;
    STAMP(0, 0);
    if (b >= 236 && b < 236 + 8 * (MAIN_CODE_PREFETCH / 4096) && (threadIdx.x >> 6) == 7) prefetch_main_code((b - 236) >> 3, threadIdx.x & 63);
    if (b < 128) task_x(a, b, smem);
    else if (b < 236) task_w1(a, b - 128, smem);
    else if (b < 396) task_W(a, b - 236);
    else task_b(a, smem);
    END_STAMP(0);
}

template <int N> DI void wait_vm() { asm volatile("s_waitcnt vmcnt(%0)" ::"n"(N) : "memory"); }
DI void wait_lgkm() { asm volatile("s_waitcnt lgkmcnt(0)" ::: "memory"); }
DI void barrier() { __builtin_amdgcn_s_barrier(); }
template <bool SC = false> DI void dma16(u32x4 srd, int voff, int soff, u32 ldsaddr) {
    if (SC) asm volatile("s_mov_b32 m0, %0\n\ts_nop 0\n\tbuffer_load_dwordx4 %1, %2, %3 offen sc1 lds" ::"s"(ldsaddr), "v"(voff), "s"(srd), "s"(soff) : "memory", "m0");
    else asm volatile("s_mov_b32 m0, %0\n\ts_nop 0\n\tbuffer_load_dwordx4 %1, %2, %3 offen lds" ::"s"(ldsaddr), "v"(voff), "s"(srd), "s"(soff) : "memory", "m0");
}
template <int LIM, bool SC = false> DI void dma16_lim(u32x4 srd, int voff, int soff, u32 ldsaddr, int w) {
    if (SC) asm volatile("s_cmp_lt_i32 %4, %5\n\ts_cbranch_scc0 .Lskip%=\n\ts_mov_b32 m0, %0\n\ts_nop 0\n\tbuffer_load_dwordx4 %1, %2, %3 offen sc1 lds\n.Lskip%=:"
                 ::"s"(ldsaddr), "v"(voff), "s"(srd), "s"(soff), "s"(w), "n"(LIM) : "memory", "m0", "scc");
    else asm volatile("s_cmp_lt_i32 %4, %5\n\ts_cbranch_scc0 .Lskip%=\n\ts_mov_b32 m0, %0\n\ts_nop 0\n\tbuffer_load_dwordx4 %1, %2, %3 offen lds\n.Lskip%=:"
                 ::"s"(ldsaddr), "v"(voff), "s"(srd), "s"(soff), "s"(w), "n"(LIM) : "memory", "m0", "scc");
}
DI u32x4 mk_srd(const void* p, size_t bytes) {
    const u64 a = (u64)p;
    u32x4 r = {(u32)a, (u32)(a >> 32) & 0xffffu, (u32)bytes, 0x00020000u};
    return r;
}
DI float row_sum15(float v) {
    v += __builtin_bit_cast(float, __builtin_amdgcn_update_dpp(0, __builtin_bit_cast(int, v), 0x111, 0xf, 0xf, true));
    v += __builtin_bit_cast(float, __builtin_amdgcn_update_dpp(0, __builtin_bit_cast(int, v), 0x112, 0xf, 0xf, true));
    v += __builtin_bit_cast(float, __builtin_amdgcn_update_dpp(0, __builtin_bit_cast(int, v), 0x114, 0xf, 0xf, true));
    v += __builtin_bit_cast(float, __builtin_amdgcn_update_dpp(0, __builtin_bit_cast(int, v), 0x118, 0xf, 0xf, true));
    return v;
}
template <int I, int N, class F> DI void static_for(F&& f) {
    if constexpr (I < N) { f(std::integral_constant<int, I>{}); static_for<I + 1, N>(f); }
}

constexpr int G1_P = 4, G1_PW = 5;
typedef __attribute__((address_space(1))) u32 gu32;
DI u32 ld_agent(const u32* p) { return __hip_atomic_load((gu32*)p, __ATOMIC_RELAXED, __HIP_MEMORY_SCOPE_AGENT); }
DI void add_agent(u32* p) { __hip_atomic_fetch_add((gu32*)p, 1u, __ATOMIC_RELAXED, __HIP_MEMORY_SCOPE_AGENT); }
DI void signal_all(char* ws, int which) {
    if (threadIdx.x == 0) {
        u32* c = (u32*)(ws + OFF_CNT + 16384 + which * 8192) + (blockIdx.x & 7) * 32;
#pragma unroll
        for (int r = 0; r < 8; ++r) add_agent(c + r * 256);
    }
}
#define POLL_SLEEP 8
DI void poll_until(const u32* p, u32 want, char* ws, int lane) {
    for (u32 spins = 0;; ++spins) {
        if (__all(ld_agent(p) >= want)) break;
        __builtin_amdgcn_s_sleep(POLL_SLEEP);
        if (spins > (1u << 18)) { if (lane == 0) __hip_atomic_store((gu32*)(ws + OFF_CNT + 49152), 1u, __ATOMIC_RELAXED, __HIP_MEMORY_SCOPE_AGENT); break; }
    }
}
DI void poll_all(char* ws, int which, u32 per_shard, int w, int lane) {
    if (w == 0) poll_until((const u32*)(ws + OFF_CNT + 16384 + which * 8192) + ((blockIdx.x >> 3) & 7) * 256 + (lane & 7) * 32, per_shard, ws, lane);
}

struct MainArgs { PrepArgs p; const u16* b1; u16* out; };
DI int img128(int row, int ch) { return row * 128 + ((ch ^ ((row >> 1) & 7)) << 4); }
template <bool LAST> DI void w1_prefetch(char* ws, char* smem, int w, int lane, int u0);

template <int L>
DI void col_task(char* ws, int b, char* smem, int u0) {
    const int tid = threadIdx.x & 255, lane = tid & 63, w = tid >> 6, half = threadIdx.x >> 8;
    const int j = 2 * b + half;
    char* Ure = smem + half * 24576; char* Uim = Ure + 4096; char* Ere = Ure + 8192; char* Eim = Ure + 12288;
    char* Rre = Ure + 16384; char* Rim = Ure + 20480;
    const int n = lane & 15, q = lane >> 4;
    const int wv = __builtin_amdgcn_readfirstlane(threadIdx.x >> 6);
    const rsrc_t rx = mk_rsrc(ws + (L == 0 ? OFF_XT0 : OFF_XT2), XT_BYTES), rs = mk_rsrc(ws + (L == 0 ? OFF_SS0 : OFF_SS2), 2048 * 8);
    u32x4 vr, vi, sv[4];
    auto load_in = [&]() __attribute__((always_inline)) {
        vr = __builtin_amdgcn_raw_buffer_load_b128(rx, (2 * j * S_ + tid * 8) * 2, 0, L == 0 ? 0 : SC1);
        vi = __builtin_amdgcn_raw_buffer_load_b128(rx, ((2 * j + 1) * S_ + tid * 8) * 2, 0, L == 0 ? 0 : SC1);
#pragma unroll
        for (int i = 0; i < 4; ++i) sv[i] = __builtin_amdgcn_raw_buffer_load_b128(rs, tid * 64 + 16 * i, 0, L == 0 ? 0 : SC1);
    };
    if (L == 0) load_in();
    u32x4 wf[2];
#pragma unroll
    for (int t = 0; t < 2; ++t) wf[t] = *(const u32x4*)((const u32*)(ws + OFF_WT) + ((size_t)j * 64 + 16 * w + n) * 32 + 16 * t + 4 * q);
    __builtin_amdgcn_sched_barrier(0);
    F32c f; f32c_init(f, lane);
    bf16x8 c32[2] = {f.c[0], f.c[1]}, s32p[2] = {f.sp[0], f.sp[1]}, s32m[2];
    pin(c32[0]); pin(c32[1]); pin(s32p[0]); pin(s32p[1]);
    bf16x8 c64[2], s64p[2], s64m[2];
    const float k1f = (float)(16 * w + n);
    {
        float c[8], s[8];
#pragma unroll
        for (int e = 0; e < 8; ++e) {
            const float ang = __builtin_amdgcn_fractf((float)(8 * q + e) * k1f * (1.0f / 64.0f));
            c[e] = cosr(ang); s[e] = sinr(ang);
        }
        c64[0] = pk8(c); s64p[0] = pk8(s);
        const u32 sg = (lane & 1) ? 0x80008000u : 0u;
        c64[1] = asbf(__builtin_bit_cast(u32x4, c64[0]) ^ sg); s64p[1] = asbf(__builtin_bit_cast(u32x4, s64p[0]) ^ sg);
        pin(c64[0]); pin(c64[1]); pin(s64p[0]); pin(s64p[1]);
    }
    float twr[8], twi[8], ivr[8], ivi[8];
#pragma unroll
    for (int i = 0; i < 8; ++i) {
        const float af = (float)(16 * (i >> 2) + 4 * q + (i & 3)) * k1f * (1.0f / 2048.0f);
        twr[i] = cosr(af); twi[i] = -sinr(af);
        const float ai = (float)(16 * w + 4 * q + (i & 3)) * (float)(16 * (i >> 2) + n) * (1.0f / 2048.0f);
        ivr[i] = cosr(ai) * (1.0f / 2048.0f); ivi[i] = sinr(ai) * (1.0f / 2048.0f);
        pin(twr[i]); pin(twi[i]); pin(ivr[i]); pin(ivi[i]);
    }
    __builtin_amdgcn_sched_barrier(0);
    STAMP(L ? 4 : 1, 1);
    if (L == 1) {
        poll_all(ws, 1, 32u, wv, lane);
        barrier();
        STAMP(4, 5);
        load_in();
        __builtin_amdgcn_sched_barrier(0);
    }
    s64m[0] = neg8(s64p[0]); s64m[1] = neg8(s64p[1]);
    {
        float r[8];
#pragma unroll
        for (int i = 0; i < 8; ++i) {
            const float s = (float)sv[i >> 1][2 * (i & 1) + 1] * 4294967296.0f + (float)sv[i >> 1][2 * (i & 1)];
            r[i] = __builtin_amdgcn_rsqf(s * (1.0f / (SS_SCALE * 640.0f)) + EPS);
        }
        const bf16x8 ur = pk8(bflo(vr[0]) * r[0], bfhi(vr[0]) * r[1], bflo(vr[1]) * r[2], bfhi(vr[1]) * r[3],
                              bflo(vr[2]) * r[4], bfhi(vr[2]) * r[5], bflo(vr[3]) * r[6], bfhi(vr[3]) * r[7]);
        bf16x8 ui = pk8(bflo(vi[0]) * r[0], bfhi(vi[0]) * r[1], bflo(vi[1]) * r[2], bfhi(vi[1]) * r[3],
                        bflo(vi[2]) * r[4], bfhi(vi[2]) * r[5], bflo(vi[3]) * r[6], bfhi(vi[3]) * r[7]);
        if (j == 304) ui = asbf(u32x4{0u, 0u, 0u, 0u});
        const int o = img128(tid >> 3, tid & 7);
        *(bf16x8*)(Ure + o) = ur; *(bf16x8*)(Uim + o) = ui;
        *(u32x4*)(Rre + o) = vr; *(u32x4*)(Rim + o) = vi;
    }
    __syncthreads();
    STAMP(L ? 4 : 1, 2);
    const f32x4 z = {0.f, 0.f, 0.f, 0.f};
    f32x4 dr[2], di[2];
#pragma unroll
    for (int mt = 0; mt < 2; ++mt) {
        dr[mt] = z; di[mt] = z;
#pragma unroll
        for (int ks = 0; ks < 2; ++ks) {
            const int o = img128(16 * mt + n, 4 * ks + q);
            const bf16x8 ar = *(const bf16x8*)(Ure + o), ai = *(const bf16x8*)(Uim + o);
            dr[mt] = mfma16(ar, c64[ks], dr[mt]); dr[mt] = mfma16(ai, s64p[ks], dr[mt]);
            di[mt] = mfma16(ar, s64m[ks], di[mt]); di[mt] = mfma16(ai, c64[ks], di[mt]);
        }
    }
    float tr[8], ti[8];
#pragma unroll
    for (int i = 0; i < 8; ++i) {
        const float x = dr[i >> 2][i & 3], y = di[i >> 2][i & 3];
        tr[i] = x * twr[i] - y * twi[i]; ti[i] = x * twi[i] + y * twr[i];
    }
    bf16x8 Tr = pk8(tr), Ti = pk8(ti);
    s32m[0] = neg8(s32p[0]); s32m[1] = neg8(s32p[1]);
    f32x4 orr[2], oi[2];
#pragma unroll
    for (int t = 0; t < 2; ++t) {
        orr[t] = mfma16(c32[t], Tr, z); orr[t] = mfma16(s32p[t], Ti, orr[t]);
        oi[t] = mfma16(s32m[t], Tr, z); oi[t] = mfma16(c32[t], Ti, oi[t]);
    }
#pragma unroll
    for (int i = 0; i < 8; ++i) {
        const float x = orr[i >> 2][i & 3], y = oi[i >> 2][i & 3];
        const u32 wv = wf[i >> 2][i & 3];
        const float wre = bflo(wv), wim = bfhi(wv);
        tr[i] = x * wre - y * wim; ti[i] = x * wim + y * wre;
    }
    Tr = pk8(tr); Ti = pk8(ti);
#pragma unroll
    for (int nt = 0; nt < 2; ++nt) {
        f32x4 zr = mfma16(Tr, c32[nt], z); zr = mfma16(Ti, s32m[nt], zr);
        f32x4 zi = mfma16(Tr, s32p[nt], z); zi = mfma16(Ti, c32[nt], zi);
        float er[4], ei[4];
#pragma unroll
        for (int r = 0; r < 4; ++r) {
            const float cr = ivr[4 * nt + r], ci = ivi[4 * nt + r];
            er[r] = zr[r] * cr - zi[r] * ci; ei[r] = zr[r] * ci + zi[r] * cr;
        }
        const int o = img128(16 * nt + n, 2 * w + (q >> 1)) + (q & 1) * 8;
        u32x2 pr = {pk2(er[0], er[1]), pk2(er[2], er[3])}, pi = {pk2(ei[0], ei[1]), pk2(ei[2], ei[3])};
        *(u32x2*)(Ere + o) = pr; *(u32x2*)(Eim + o) = pi;
    }
    __syncthreads();
    STAMP(L ? 4 : 1, 3);
    const rsrc_t rf = mk_rsrc(ws + (L == 0 ? OFF_XF1 : OFF_XF3), XT_BYTES);
    char* O = Ure;
#pragma unroll
    for (int nt = 0; nt < 2; ++nt) {
        f32x4 yr = z, yi = z;
#pragma unroll
        for (int ks = 0; ks < 2; ++ks) {
            const int o = img128(16 * nt + n, 4 * ks + q);
            const bf16x8 br = *(const bf16x8*)(Ere + o), bi = *(const bf16x8*)(Eim + o);
            yr = mfma16(c64[ks], br, yr); yr = mfma16(s64m[ks], bi, yr);
            yi = mfma16(s64p[ks], br, yi); yi = mfma16(c64[ks], bi, yi);
        }
        if (j == 304) yi = z;
        const int p = (16 * nt + n) * 64 + 16 * w + 4 * q;
        u32x2 rr[2], ri[2];
        { const int o = img128(16 * nt + n, 2 * w + (q >> 1)) + (q & 1) * 8; rr[nt] = *(const u32x2*)(Rre + o); ri[nt] = *(const u32x2*)(Rim + o); }
        const float fr[4] = {bflo(rr[nt][0]) + yr[0], bfhi(rr[nt][0]) + yr[1], bflo(rr[nt][1]) + yr[2], bfhi(rr[nt][1]) + yr[3]};
        const float fi[4] = {bflo(ri[nt][0]) + yi[0], bfhi(ri[nt][0]) + yi[1], bflo(ri[nt][1]) + yi[2], bfhi(ri[nt][1]) + yi[3]};
        u32x4 ov = {pk2(fr[0], fi[0]), pk2(fr[1], fi[1]), pk2(fr[2], fi[2]), pk2(fr[3], fi[3])};
        *(u32x4*)(O + p * 4) = ov;
    }
    __syncthreads();
    {
        const int c0 = 4 * b, dst = ((c0 >> 5) * 64 + ((c0 & 31) >> 3) * 16) * 16 + (c0 & 4) * 2;
        const u32* O0 = (const u32*)smem; const u32* O1 = (const u32*)(smem + 24576);
#pragma unroll
        for (int i = 0; i < 4; ++i) {
            const int p = threadIdx.x + 512 * i;
            u32x2 v = {O0[p], O1[p]};
            __builtin_amdgcn_raw_buffer_store_b64(v, rf, (p >> 4) * 20480 + (p & 15) * 16, dst, SC1);
        }
    }
    STAMP(L ? 4 : 1, 4);
    wait_lgkm();
    wait_vm<0>();
    barrier();
    signal_all(ws, L == 0 ? 0 : 2);
    w1_prefetch<L == 1>(ws, smem, wv, lane, u0);
    STAMP(L ? 4 : 1, 15);
}

DI f32x2 gelu_tanh2(f32x2 x) {
    const float k1 = -2.302208198f, k2 = -0.1029432397f;
    const f32x2 c1 = {k1, k1}, c2 = {k2, k2}, one = {1.0f, 1.0f};
    const f32x2 t = x * (x * x * c2 + c1);
    f32x2 e = {__builtin_amdgcn_exp2f(t[0]), __builtin_amdgcn_exp2f(t[1])};
    e += one;
    const f32x2 r = {__builtin_amdgcn_rcpf(e[0]), __builtin_amdgcn_rcpf(e[1])};
    return x * r;
}
constexpr int G2_PH = 4;
constexpr int W2STG = 20480;
DI u32 w2slot(int t) { return 163840 - ((t & 7) + 1) * W2STG; }
template <bool LAST> constexpr int w1slot(int st) { return LAST ? 163840 - (st % 8 + 1) * 20480 : 163840 - (st + 1) * 14336; }

template <bool LAST, int I> DI void w1_piece(u32x4 srd, u32 lds0, int w, int vo, int u0, int st) {
    constexpr int WP = LAST ? 20 : 14;
    const int pc = w + 8 * I;
    const u32 dst = lds0 + w1slot<LAST>(st) + pc * 1024;
    const int so = (u0 + (pc >> 1)) * 20480 + st * 2048 + (pc & 1) * 1024;
    if (8 * I + 7 < WP) dma16<LAST>(srd, vo, so, dst); else dma16_lim<(WP & 7), LAST>(srd, vo, so, dst, w);
}
template <bool LAST> DI void w1_prefetch(char* ws, char* smem, int w, int lane, int u0) {
    const u32x4 srd = mk_srd(ws + OFF_W1F, W1F_BYTES);
    const u32 lds0 = (u32)(size_t)(LAS char*)smem;
#pragma unroll
    for (int st = 0; st < G1_PW; ++st) {
        w1_piece<LAST, 0>(srd, lds0, w, lane * 16, u0, st);
        w1_piece<LAST, 1>(srd, lds0, w, lane * 16, u0, st);
        if (LAST) w1_piece<LAST, 2>(srd, lds0, w, lane * 16, u0, st);
    }
}

constexpr int g1_younger(int ssync, int t, int nlo) {
    int cnt = 0; bool seen = false;
    for (int s = 0; s < ssync; ++s) {
        if (s + G1_PW < 10) { if (seen) cnt += nlo; if (s + G1_PW == t) seen = true; }
        if (s + G1_P < 10 && seen) cnt += 2;
    }
    return cnt;
}

template <int I> DI void w2_issue1(u32x4 srd, u32 lds0, int w, int vo, int ct, int t) {
    const int pc = w + 8 * I, q = pc / 5, cu = pc - 5 * q;
    const int so = ((5 * ct + cu) * 54 + (q < 2 ? 14 * q : 28 + 13 * (q - 2)) + t) * 1024;
    if (I < 2) dma16<true>(srd, vo, so, lds0 + w2slot(t) + pc * 1024); else dma16_lim<4, true>(srd, vo, so, lds0 + w2slot(t) + pc * 1024, w);
}
DI void w2_issue(u32x4 srd, u32 lds0, int w, int vo, int ct, int t) {
    w2_issue1<0>(srd, lds0, w, vo, ct, t); w2_issue1<1>(srd, lds0, w, vo, ct, t); w2_issue1<2>(srd, lds0, w, vo, ct, t);
}

template <int NF, bool LAST, int KG>
DI void g1_phase(const MainArgs& a, char* smem, int w, int lane, int pt, int u0, int nu, int ct2) {
    constexpr int P = G1_P, PW = G1_PW;
    constexpr int WP = 2 * NF, WR = (WP + 7) / 8, NLO = WP / 8, KID = LAST ? 5 : 2;
    char* ws = a.p.ws;
    const int pq = w & 3, g = lane >> 4;
    const u32x4 srdW = mk_srd(ws + OFF_W1F, W1F_BYTES), srd2 = mk_srd(ws + (LAST ? OFF_W2O : OFF_W2F), W2F_BYTES);
    const u32 lds0 = (u32)(size_t)(LAS char*)smem;
    const rsrc_t rx = mk_rsrc(ws + (LAST ? OFF_XF3 : OFF_XF1), XT_BYTES);
    const int xs = (8 * pt + 2 * pq) * 20480 + KG * 1024, vo = lane * 16;
    auto xload = [&](int st, int pu) __attribute__((always_inline)) { return asbf(__builtin_amdgcn_raw_buffer_load_b128(rx, vo, xs + pu * 20480 + st * 2048, SC1)); };
    bf16x8 xb[P][2];
#pragma unroll
    for (int st = 0; st < P; ++st) { xb[st][0] = xload(st, 0); xb[st][1] = xload(st, 1); __builtin_amdgcn_sched_barrier(0); }
    f32x4 acc[NF][2];
#pragma unroll
    for (int i = 0; i < NF; ++i) { acc[i][0] = f32x4{0, 0, 0, 0}; acc[i][1] = f32x4{0, 0, 0, 0}; }
    f32x4 gr[2] = {f32x4{0, 0, 0, 0}, f32x4{0, 0, 0, 0}};
    bf16x8 wa[2][NF];
#pragma unroll
    for (int i = 0; i < NF; ++i) wa[0][i] = *(const bf16x8*)(smem + w1slot<LAST>(0) + KG * 1024 + lane * 16 + i * 2048);
    static_for<0, 10>([&](auto ST) __attribute__((always_inline)) {
        constexpr int st = decltype(ST)::value;
        if constexpr (LAST && st == 2) { wait_lgkm(); barrier(); }
        if constexpr (st == 4 || st == 7) {
            wait_lgkm();
            wait_vm<g1_younger(st, st == 7 ? 9 : 7, NLO)>();
            barrier();
        }
        bf16x8 nx[2];
        const char* rbase = smem + w1slot<LAST>(st + 1) + KG * 1024 + lane * 16;
        constexpr int NV = WR + 2, V0 = KG == 0 ? 0 : NF - NV;
        static_for<0, NF>([&](auto I) __attribute__((always_inline)) {
            constexpr int i = decltype(I)::value;
            __builtin_amdgcn_sched_barrier(0);
            acc[i][0] = mfma16(wa[st & 1][i], xb[st % P][0], acc[i][0]);
            if constexpr (st + 1 < 10) wa[(st + 1) & 1][i] = *(const bf16x8*)(rbase + i * 2048);
            acc[i][1] = mfma16(wa[st & 1][i], xb[st % P][1], acc[i][1]);
            if constexpr (i < 2) {
                u32x4 xv = __builtin_bit_cast(u32x4, xb[st % P][i]);
                if constexpr (st == 9 && KG == 1) { if (g == 0) xv[0] &= 0x0000ffffu; }
                gr[i] = mfma16(asbf(xv), asbf(xv), gr[i]);
            }
            __builtin_amdgcn_sched_barrier(0);
            constexpr int v = i - V0;
            if constexpr (v >= 0 && v < WR) { if constexpr (st + PW < 10) w1_piece<LAST, v>(srdW, lds0, w, vo, u0, st + PW); }
            else if constexpr (v >= WR && v < NV) { if constexpr (st + P < 10) nx[v - WR] = xload(st + P, v - WR); }
            if constexpr (st >= 6) {
                constexpr int sl = i - (KG == 0 ? 0 : NF - 4), e = 4 * (st - 6) + sl;
                if constexpr (sl >= 0 && sl < 4 && e < (LAST ? 9 : 15)) w2_issue1<e % 3>(srd2, lds0, w, vo, ct2, (LAST ? 2 : 0) + e / 3);
            }
        });
        __builtin_amdgcn_sched_barrier(0);
        if constexpr (st + P < 10) { xb[st % P][0] = nx[0]; xb[st % P][1] = nx[1]; }
        if (st == 4) STAMP(KID, 2);
    });
    STAMP(KID, 3);
    u32x2 bv[NF];
#pragma unroll
    for (int i = 0; i < NF; ++i) bv[i] = *(const u32x2*)(a.b1 + min(16 * (u0 + i), F_ - 16) + 4 * g);
    __builtin_amdgcn_sched_barrier(0);
    wait_lgkm();
    barrier();
    float* sx = (float*)(smem + 8 * NF * 512);
#pragma unroll
    for (int i = 0; i < NF; ++i) {
        const f32x4 t = acc[i][1 - KG];
        u32x2 pv = {pk2(t[0], t[1]), pk2(t[2], t[3])};
        *(u32x2*)(smem + ((w * NF + i) * 64 + lane) * 8) = pv;
    }
    float st2[2];
    {
        const int n = lane & 15;
#pragma unroll
        for (int pu = 0; pu < 2; ++pu) {
            const float d01 = (n & 1) ? gr[pu][1] : gr[pu][0], d23 = (n & 1) ? gr[pu][3] : gr[pu][2];
            float d = (n & 2) ? d23 : d01;
            d = (g == (n >> 2)) ? d : 0.f;
            d += __shfl_xor(d, 16); d += __shfl_xor(d, 32);
            st2[pu] = d;
        }
    }
    if (lane < 16) sx[w * 16 + lane] = st2[1 - KG];
    wait_lgkm();
    barrier();
    STAMP(KID, 5);
    const float v = (st2[KG] + sx[(w ^ 4) * 16 + (lane & 15)]) * (1.0f / 640.0f);
    const float r1 = __builtin_amdgcn_rsqf(v + EPS);
    const float rr = r1 * __builtin_amdgcn_rsqf(v * r1 * r1 + EPS);
    const int punit = 8 * pt + 2 * pq + KG;
    u32x2 ov[NF], pv[NF];
#pragma unroll
    for (int i = 0; i < NF; ++i) pv[i] = *(const u32x2*)(smem + (((w ^ 4) * NF + i) * 64 + lane) * 8);
    static_for<0, NF>([&](auto I) __attribute__((always_inline)) {
        constexpr int i = decltype(I)::value;
        if constexpr (LAST ? i < 2 : i == 0) {
            __builtin_amdgcn_sched_barrier(0);
            w2_issue(srd2, lds0, w, vo, ct2, LAST ? i : 5);
            __builtin_amdgcn_sched_barrier(0);
        }
        const f32x2 o0 = {acc[i][KG][0] + bflo(pv[i][0]), acc[i][KG][1] + bfhi(pv[i][0])}, o1 = {acc[i][KG][2] + bflo(pv[i][1]), acc[i][KG][3] + bfhi(pv[i][1])};
        const f32x2 rr2 = {rr, rr};
        const f32x2 b0 = {bflo(bv[i][0]), bfhi(bv[i][0])}, b1 = {bflo(bv[i][1]), bfhi(bv[i][1])};
        f32x2 h0 = gelu_tanh2(o0 * rr2 + b0), h1 = gelu_tanh2(o1 * rr2 + b1);
        if (LAST) {
            const bool lin = u0 + i >= 108;
            h0[0] = lin ? o0[0] : h0[0]; h0[1] = lin ? o0[1] : h0[1]; h1[0] = lin ? o1[0] : h1[0]; h1[1] = lin ? o1[1] : h1[1];
        }
        ov[i] = u32x2{pk2(h0[0], h0[1]), pk2(h1[0], h1[1])};
    });
    __builtin_amdgcn_sched_barrier(0);
    const rsrc_t rh = mk_rsrc(ws + (LAST ? OFF_H2 : OFF_H), H_BYTES), r3 = mk_rsrc(ws + OFF_X3, X3_BYTES);
    const int hb = punit * 54 * 1024;
    auto st1 = [&](int i) __attribute__((always_inline)) {
        const int u = u0 + i;
        if (i < nu) {
            if (!LAST || u < 108) __builtin_amdgcn_raw_buffer_store_b64(ov[i], rh, lane * 16 + (u & 1) * 8, hb + (u >> 1) * 1024, SC1);
            else __builtin_amdgcn_raw_buffer_store_b64(ov[i], r3, lane * 8, (punit * 40 + (u - 108)) * 512, SC1);
        }
    };
    auto st2p = [&](int i) __attribute__((always_inline)) {
        const int u = u0 + i;
        if (i + 1 < nu && (!LAST || u + 1 < 108)) {
            u32x4 q4 = {ov[i][0], ov[i][1], ov[i + 1][0], ov[i + 1][1]};
            __builtin_amdgcn_raw_buffer_store_b128(q4, rh, lane * 16, hb + (u >> 1) * 1024, SC1);
        } else { st1(i); st1(i + 1); }
    };
    if (u0 & 1) {
        st1(0);
#pragma unroll
        for (int i = 1; i + 1 < NF; i += 2) st2p(i);
        if ((NF & 1) == 0) st1(NF - 1);
    } else {
#pragma unroll
        for (int i = 0; i + 1 < NF; i += 2) st2p(i);
        if (NF & 1) st1(NF - 1);
    }
    STAMP(KID, 4);
}

template <bool LAST>
DI void g2_phase(const MainArgs& a, char* smem, int w, int lane, int pt1, int pt, int ct) {
    constexpr int PH = G2_PH, KID = LAST ? 6 : 3;
    char* ws = a.p.ws;
    u32* cnt = (u32*)(ws + OFF_CNT + (LAST ? 8192 : 0));
    const int pg = w & 1, kq = w >> 1, n = lane & 15, q = lane >> 4;
    const int kb = kq < 2 ? 14 * kq : 28 + 13 * (kq - 2), nk = kq < 2 ? 14 : 13;
    const u32x4 srd2 = mk_srd(ws + (LAST ? OFF_W2O : OFF_W2F), W2F_BYTES);
    const u32 lds0 = (u32)(size_t)(LAS char*)smem;
    const rsrc_t rh = mk_rsrc(ws + (LAST ? OFF_H2 : OFF_H), H_BYTES), rw = mk_rsrc(ws + (LAST ? OFF_W2O : OFF_W2F), W2F_BYTES);
    const int vo = lane * 16, hs = ((4 * pt + 2 * pg) * 54 + kb) * 1024;
    bf16x8 wr[6][5];
    asm volatile("" ::: "memory");
    __builtin_amdgcn_sched_barrier(0);
#pragma unroll
    for (int t = 0; t < 3; ++t)
#pragma unroll
        for (int i = 0; i < 5; ++i) wr[t][i] = asbf(__builtin_amdgcn_raw_buffer_load_b128(rw, vo, ((5 * ct + i) * 54 + kb + 8 + t) * 1024, SC1));
    __builtin_amdgcn_sched_barrier(0);
    wait_lgkm();
    wait_vm<15>();
    barrier();
    if (threadIdx.x == 0) add_agent(cnt + 64 * pt1);
    STAMP(LAST ? 5 : 2, 15);
#pragma unroll
    for (int t = LAST ? 5 : 6; t < 8; ++t) w2_issue(srd2, lds0, w, vo, ct, t);
    __builtin_amdgcn_sched_barrier(0);
#pragma unroll
    for (int t = 3; t < 6; ++t)
#pragma unroll
        for (int i = 0; i < 5; ++i) wr[t][i] = asbf(__builtin_amdgcn_raw_buffer_load_b128(rw, vo, ((5 * ct + i) * 54 + kb + 8 + t) * 1024, SC1));
    __builtin_amdgcn_sched_barrier(0);
    if (w == 0) poll_until(cnt + 64 * (pt >> 1), 16u, ws, lane);
    wait_vm<0>();
    barrier();
    STAMP(KID, 5);
    bf16x8 hb[PH][2];
    auto hload = [&](int t, int pu) __attribute__((always_inline)) {
        const int k = t < 13 ? t : nk - 1;
        return asbf(__builtin_amdgcn_raw_buffer_load_b128(rh, vo, hs + (pu * 54 + k) * 1024, SC1));
    };
#pragma unroll
    for (int t = 0; t < PH; ++t) { hb[t][0] = hload(t, 0); hb[t][1] = hload(t, 1); __builtin_amdgcn_sched_barrier(0); }
    u32x2 xv[3], bv[3]; float bb[3];
    f32x4 acc[2][5];
#pragma unroll
    for (int i = 0; i < 5; ++i) { acc[0][i] = f32x4{0, 0, 0, 0}; acc[1][i] = f32x4{0, 0, 0, 0}; }
    bf16x8 wa[2][5];
    const char* wbase = smem + kq * 5120 + lane * 16;
#pragma unroll
    for (int i = 0; i < 5; ++i) wa[0][i] = *(const bf16x8*)(wbase + w2slot(0) + i * 1024);
    static_for<0, 14>([&](auto T) __attribute__((always_inline)) {
        constexpr int t = decltype(T)::value;
        if constexpr (t == 10) {
            __builtin_amdgcn_sched_barrier(0);
#pragma unroll
            for (int j = 0; j < 3; ++j) {
                const int tt = min(kq + 4 * j, 9), pu = 2 * pg + tt / 5, cu = tt % 5;
                if (!LAST) {
                    const int c = 80 * ct + 16 * cu + n;
                    const int off = (((4 * pt + pu) * 20 + (c >> 5)) * 64 + ((c & 31) >> 3) * 16 + 4 * q) * 16 + (c & 7) * 2;
                    const rsrc_t r1 = mk_rsrc(ws + OFF_XF1, XT_BYTES);
                    const u32 e0 = __builtin_amdgcn_raw_buffer_load_b16(r1, off, 0, SC1), e1 = __builtin_amdgcn_raw_buffer_load_b16(r1, off + 16, 0, SC1);
                    const u32 e2 = __builtin_amdgcn_raw_buffer_load_b16(r1, off + 32, 0, SC1), e3 = __builtin_amdgcn_raw_buffer_load_b16(r1, off + 48, 0, SC1);
                    xv[j] = u32x2{e0 | (e1 << 16), e2 | (e3 << 16)};
                    bb[j] = ((const float*)(ws + OFF_B2P))[c];
                } else {
                    xv[j] = __builtin_amdgcn_raw_buffer_load_b64(mk_rsrc(ws + OFF_X3, X3_BYTES), lane * 8, ((4 * pt + pu) * 40 + 5 * ct + cu) * 512, SC1);
                    bv[j] = *(const u32x2*)(a.p.b2 + 80 * ct + 16 * cu + 4 * q);
                }
            }
        }
        bf16x8 nh[2];
        auto step = [&]() __attribute__((always_inline)) {
            static_for<0, 5>([&](auto I) __attribute__((always_inline)) {
                constexpr int i = decltype(I)::value;
                __builtin_amdgcn_sched_barrier(0);
                bf16x8 wv;
                if constexpr (t < 8) wv = wa[t & 1][i]; else wv = wr[t - 8][i];
                acc[0][i] = LAST ? mfma16(wv, hb[t % PH][0], acc[0][i]) : mfma16(hb[t % PH][0], wv, acc[0][i]);
                if constexpr (t + 1 < 8) wa[(t + 1) & 1][i] = *(const bf16x8*)(wbase + w2slot(t + 1) + i * 1024);
                acc[1][i] = LAST ? mfma16(wv, hb[t % PH][1], acc[1][i]) : mfma16(hb[t % PH][1], wv, acc[1][i]);
                __builtin_amdgcn_sched_barrier(0);
                if constexpr (t + PH < 14 && (i == 1 || i == 3)) nh[i >> 1] = hload(t + PH, i >> 1);
            });
        };
        if constexpr (t < 13) step(); else { if (nk == 14) step(); }
        __builtin_amdgcn_sched_barrier(0);
        if constexpr (t + PH < 14) { hb[t % PH][0] = nh[0]; hb[t % PH][1] = nh[1]; }
        if (t == 0) STAMP(KID, 1);
        if (t == 7) STAMP(KID, 2);
    });
    STAMP(KID, 3);
    wait_lgkm();
    barrier();
#pragma unroll
    for (int t = 0; t < 10; ++t)
        if ((t & 3) != kq) *(f32x4*)(smem + ((w * 10 + t) * 64 + lane) * 16) = acc[t / 5][t % 5];
    wait_lgkm();
    barrier();
    float ssp[2][4];
#pragma unroll
    for (int r = 0; r < 4; ++r) { ssp[0][r] = 0.f; ssp[1][r] = 0.f; }
    const rsrc_t ro = mk_rsrc(ws + OFF_XT2, XT_BYTES);
    auto finish = [&](auto KQ) __attribute__((always_inline)) {
        constexpr int kqc = decltype(KQ)::value;
        static_for<0, 3>([&](auto J) __attribute__((always_inline)) {
            constexpr int j = decltype(J)::value, t = kqc + 4 * j;
            if constexpr (t < 10) {
                constexpr int pl = t / 5, cu = t % 5;
                f32x4 o = acc[pl][cu];
#pragma unroll
                for (int s = 1; s < 4; ++s) o += *(const f32x4*)(smem + (((pg + 2 * ((kqc + s) & 3)) * 10 + t) * 64 + lane) * 16);
                const int pu = 2 * pg + pl;
                if (!LAST) {
                    const int c = 80 * ct + 16 * cu + n, p = 64 * pt + 16 * pu + 4 * q;
                    const u32 o0 = pk2(bflo(xv[j][0]) + o[0] + bb[j], bfhi(xv[j][0]) + o[1] + bb[j]);
                    const u32 o1 = pk2(bflo(xv[j][1]) + o[2] + bb[j], bfhi(xv[j][1]) + o[3] + bb[j]);
                    u32x2 ov = {o0, o1};
                    __builtin_amdgcn_raw_buffer_store_b64(ov, ro, (c * S_ + p) * 2, 0, SC1);
                    const float m = c == 609 ? 0.f : 1.f;
                    ssp[pl][0] += m * bflo(o0) * bflo(o0); ssp[pl][1] += m * bfhi(o0) * bfhi(o0);
                    ssp[pl][2] += m * bflo(o1) * bflo(o1); ssp[pl][3] += m * bfhi(o1) * bfhi(o1);
                } else {
                    const int p = 16 * (4 * pt + pu) + n, d = 80 * ct + 16 * cu + 4 * q;
                    u32x2 ov = {pk2(bflo(xv[j][0]) + o[0] + bflo(bv[j][0]), bfhi(xv[j][0]) + o[1] + bfhi(bv[j][0])),
                                pk2(bflo(xv[j][1]) + o[2] + bflo(bv[j][1]), bfhi(xv[j][1]) + o[3] + bfhi(bv[j][1]))};
                    *(u32x2*)(a.out + (size_t)tok_of_p(p) * D_ + d) = ov;
                }
            }
        });
    };
    if (kq == 0) finish(std::integral_constant<int, 0>{});
    else if (kq == 1) finish(std::integral_constant<int, 1>{});
    else if (kq == 2) finish(std::integral_constant<int, 2>{});
    else finish(std::integral_constant<int, 3>{});
    if (!LAST) {
        float* sp = (float*)smem;
#pragma unroll
        for (int pl = 0; pl < 2; ++pl)
#pragma unroll
            for (int r = 0; r < 4; ++r) {
                const float v = row_sum15(ssp[pl][r]);
                if (n == 15) sp[w * 32 + 16 * pl + 4 * q + r] = v;
            }
        wait_lgkm();
        barrier();
        if (w == 0) {
            const int g2 = lane >> 5, r = lane & 31;
            const float v = (sp[g2 * 32 + r] + sp[(g2 + 2) * 32 + r]) + (sp[(g2 + 4) * 32 + r] + sp[(g2 + 6) * 32 + r]);
            __hip_atomic_fetch_add((__attribute__((address_space(1))) u64*)(ws + OFF_SS2) + 64 * pt + lane, ss_from_float(v), __ATOMIC_RELAXED, __HIP_MEMORY_SCOPE_AGENT);
        }
        wait_lgkm();
        wait_vm<0>();
        barrier();
        signal_all(ws, 1);
    }
}

template <bool LAST>
DI void mlp(const MainArgs& a, char* smem, int w, int lane, int u0) {
    constexpr int NF = LAST ? 10 : 7;
    const int x = blockIdx.x & 7, ib = blockIdx.x >> 3;
    const int pt = 4 * (x >> 1) + (ib & 3), ft = 8 * (x & 1) + (ib >> 2);
    const int pt2 = 8 * (x >> 1) + (ib & 7), ct2 = 4 * (x & 1) + (ib >> 3);
    const int nu = LAST ? (ft < 4 ? 10 : 9) : (ft < 12 ? 7 : 6);
    if (LAST) poll_all(a.p.ws, 2, 20u, w, lane);
    else if (w == 0) {
        const int sh = lane & 7, wh = (lane >> 3) & 1;
        poll_until((const u32*)(a.p.ws + OFF_CNT + 16384 + (wh ? 3 : 0) * 8192) + ((blockIdx.x >> 3) & 7) * 256 + sh * 32, wh ? (sh < 6 ? 12u : 11u) : 20u, a.p.ws, lane);
    }
    wait_vm<0>();
    barrier();
    STAMP(LAST ? 5 : 2, 1);
    if (w < 4) g1_phase<NF, LAST, 0>(a, smem, w, lane, pt, u0, nu, ct2);
    else g1_phase<NF, LAST, 1>(a, smem, w, lane, pt, u0, nu, ct2);
    g2_phase<LAST>(a, smem, w, lane, pt, pt2, ct2);
}

__global__ __launch_bounds__(512, 2) void k_main(MainArgs a) {
    extern __shared__ __attribute__((aligned(16))) char smem[];
    const int lane = threadIdx.x & 63, b = blockIdx.x;
    const int w = __builtin_amdgcn_readfirstlane(threadIdx.x >> 6);
    const int ft = 8 * (b & 1) + (b >> 5);
    const int u0a = ft < 12 ? 7 * ft : 84 + 6 * (ft - 12), u0b = ft < 4 ? 10 * ft : 40 + 9 * (ft - 4);
    if (b < 160) col_task<0>(a.p.ws, b, smem, u0a);
    else {
        w1_prefetch<false>(a.p.ws, smem, w, lane, u0a);
        if (b < 254) {
            if (b < 214) task_w2(a.p, b - 160, smem); else task_Q(a.p, b - 214);
            wait_lgkm();
            wait_vm<0>();
            barrier();
            signal_all(a.p.ws, 3);
        }
    }
    mlp<false>(a, smem, w, lane, u0a);
    END_STAMP(3);
    if (b < 160) col_task<1>(a.p.ws, b, smem, u0b); else w1_prefetch<true>(a.p.ws, smem, w, lane, u0b);
    mlp<true>(a, smem, w, lane, u0b);
    END_STAMP(6);
}

extern "C" void kernel_launch(void* const* d_in, const int* in_sizes, int n_in, void* d_out, int out_size, void* d_ws,
                              size_t ws_size, hipStream_t stream) {
    char* ws = (char*)d_ws;
    constexpr int MAIN_LDS = 163840;
    (void)hipFuncSetAttribute((const void*)k_main, hipFuncAttributeMaxDynamicSharedMemorySize, MAIN_LDS);
    PrepArgs pa{(const u16*)d_in[0], (const u16*)d_in[1], (const u16*)d_in[2], (const u16*)d_in[4], (const u16*)d_in[6], (const u16*)d_in[7], ws};
    k0_prep<<<NB_K0, 512, PREP_LDS, stream>>>(pa);
    MainArgs ma{pa, (const u16*)d_in[5], (u16*)d_out};
    k_main<<<256, 512, MAIN_LDS, stream>>>(ma);
}
```

```cpp
#include <hip/hip_runtime.h>
#include <hip/hip_bf16.h>
#include <stdint.h>
#include <type_traits>

typedef unsigned short u16;
typedef unsigned int u32;
typedef unsigned long long u64;
typedef short s16x4 __attribute__((ext_vector_type(4)));
typedef __bf16 bf16x8 __attribute__((ext_vector_type(8)));
typedef __bf16 bf16x2 __attribute__((ext_vector_type(2)));
typedef float f32x4 __attribute__((ext_vector_type(4)));
typedef float f32x2 __attribute__((ext_vector_type(2)));
typedef u32 u32x4 __attribute__((ext_vector_type(4)));
typedef u32 u32x2 __attribute__((ext_vector_type(2)));
typedef __amdgpu_buffer_rsrc_t rsrc_t;
#define LAS __attribute__((address_space(3)))
#define DI __device__ __forceinline__

constexpr int S_ = 2048, D_ = 640, F_ = 1728, DF_ = 321;
constexpr float EPS = 1e-5f;
constexpr int SC1 = 16;
constexpr float SS_SCALE = 1048576.0f;

constexpr size_t XT_BYTES = (size_t)D_ * S_ * 2;
constexpr size_t OFF_XT0 = 0;
constexpr size_t OFF_XT1 = OFF_XT0 + XT_BYTES;
constexpr size_t OFF_XT2 = OFF_XT1 + XT_BYTES;
constexpr size_t OFF_XT3 = OFF_XT2 + XT_BYTES;
constexpr size_t OFF_WT = OFF_XT3 + XT_BYTES;
constexpr size_t OFF_W1F = OFF_WT + (size_t)320 * 2048 * 4;
constexpr size_t W1F_BYTES = (size_t)148 * 20 * 1024;
constexpr size_t OFF_W2F = OFF_W1F + W1F_BYTES;
constexpr size_t W2F_BYTES = (size_t)40 * 54 * 1024;
constexpr size_t OFF_W2O = OFF_W2F + W2F_BYTES;
constexpr size_t OFF_H = OFF_W2O + W2F_BYTES;
constexpr size_t H_BYTES = (size_t)128 * 54 * 1024;
constexpr size_t OFF_H2 = OFF_H;
constexpr size_t OFF_X3 = OFF_H + 2 * H_BYTES;
constexpr size_t OFF_SS0 = OFF_X3 + (size_t)128 * 40 * 512;
constexpr size_t OFF_SS2 = OFF_SS0 + 2048 * 8;
constexpr size_t OFF_B2P = OFF_SS2 + 2048 * 8;
constexpr size_t OFF_XF1 = OFF_B2P + 4096;
constexpr size_t OFF_XF3 = OFF_XF1;
constexpr size_t X3_BYTES = (size_t)128 * 40 * 512;
constexpr size_t OFF_CNT = OFF_XF1 + 2 * XT_BYTES;
constexpr size_t WS_END = OFF_CNT + 65536;

#define STAMP(k, i)
#define CSTAMP(k, i)
#define END_STAMP(k)

DI f32x4 mfma16(bf16x8 a, bf16x8 b, f32x4 c) { return __builtin_amdgcn_mfma_f32_16x16x32_bf16(a, b, c, 0, 0, 0); }
DI u32 pk2(float a, float b) { f32x2 v = {a, b}; const bf16x2 r = __builtin_convertvector(v, bf16x2); return __builtin_bit_cast(u32, r); }
DI bf16x8 pk8(float a0, float a1, float a2, float a3, float a4, float a5, float a6, float a7) {
    u32x4 r = {pk2(a0, a1), pk2(a2, a3), pk2(a4, a5), pk2(a6, a7)};
    return __builtin_bit_cast(bf16x8, r);
}
DI bf16x8 pk8(const float (&a)[8]) { return pk8(a[0], a[1], a[2], a[3], a[4], a[5], a[6], a[7]); }
DI bf16x8 asbf(u32x4 r) { return __builtin_bit_cast(bf16x8, r); }
DI bf16x8 neg8(bf16x8 a) { u32x4 r = __builtin_bit_cast(u32x4, a); r ^= 0x80008000u; return __builtin_bit_cast(bf16x8, r); }
DI float bflo(u32 v) { return __builtin_bit_cast(float, v << 16); }
DI float bfhi(u32 v) { return __builtin_bit_cast(float, v & 0xffff0000u); }
DI float cosr(float rev) { return __builtin_amdgcn_cosf(rev); }
DI float sinr(float rev) { return __builtin_amdgcn_sinf(rev); }
DI int slot32(int q, int j) { return j < 4 ? 4 * q + j : 16 + 4 * q + (j - 4); }
DI s16x4 tr_read(const char* p) { return __builtin_amdgcn_ds_read_tr16_b64_v4i16((LAS s16x4*)p); }
DI bf16x8 cat8(s16x4 lo, s16x4 hi) { return __builtin_bit_cast(bf16x8, __builtin_shufflevector(lo, hi, 0, 1, 2, 3, 4, 5, 6, 7)); }
DI int tok_of_p(int p) { return 32 * (p & 63) + (p >> 6); }
DI rsrc_t mk_rsrc(const void* p, size_t bytes) { return __builtin_amdgcn_make_buffer_rsrc((void*)p, 0, (int)bytes, 0x00020000); }
DI u32x4 bload16(rsrc_t r, int voff, int soff) { return __builtin_amdgcn_raw_buffer_load_b128(r, voff, soff, 0); }
DI float ss_to_float(u64 v) { return (float)(u32)(v >> 32) * 4294967296.0f + (float)(u32)v; }
DI u64 ss_from_float(float s) { return (u64)(fminf(s * SS_SCALE, 1.0e18f) + 0.5f); }
DI void pin(float& x) { asm volatile("" : "+v"(x)); }
DI void pin(bf16x8& x) { asm volatile("" : "+v"(x)); }

struct F32c { bf16x8 c[2], sp[2]; };
DI void f32c_init(F32c& k, int lane) {
    const float m = (float)(lane & 15), q4 = (float)(4 * (lane >> 4));
    float c[4], s[4];
#pragma unroll
    for (int j = 0; j < 4; ++j) { const float a = __builtin_amdgcn_fractf(m * (q4 + (float)j) * (1.0f / 32.0f)); c[j] = cosr(a); s[j] = sinr(a); }
    const u32 sg = (lane & 1) ? 0x80008000u : 0u;
    const u32 c0 = pk2(c[0], c[1]), c1 = pk2(c[2], c[3]), s0 = pk2(s[0], s[1]), s1 = pk2(s[2], s[3]);
    const u32x4 cv = {c0, c1, c0 ^ sg, c1 ^ sg}, sv = {s0, s1, s0 ^ sg, s1 ^ sg};
    k.c[0] = asbf(cv); k.sp[0] = asbf(sv);
    k.c[1] = asbf(cv ^ 0x80000000u); k.sp[1] = asbf(sv ^ 0x80000000u);
}

struct RowC { bf16x8 c[2], sp[2], sm[2], c20, s20m; f32x4 twr[2], twi[2]; int off0, off1; };
constexpr int ROWC_LDS = 10 * 1024;
DI void rowc_build(char* tab, int w, int lane) {
    const int n = lane & 15, q = lane >> 4;
    const float nf = (float)n;
    u32x4* t = (u32x4*)tab + lane;
    if (w == 0) {
        F32c f; f32c_init(f, lane);
        t[0] = __builtin_bit_cast(u32x4, f.c[0]); t[64] = __builtin_bit_cast(u32x4, f.c[1]);
        t[128] = __builtin_bit_cast(u32x4, f.sp[0]); t[192] = __builtin_bit_cast(u32x4, f.sp[1]);
    } else if (w <= 2) {
        float c[8];
#pragma unroll
        for (int j = 0; j < 8; ++j) {
            const int d1 = 8 * q + j;
            const float a = __builtin_amdgcn_fractf((float)d1 * nf * 0.05f);
            const float v = w == 1 ? cosr(a) : -sinr(a);
            c[j] = (d1 < 20 && n <= 10) ? v : 0.f;
        }
        t[(3 + w) * 64] = __builtin_bit_cast(u32x4, pk8(c));
    } else if (w <= 4) {
        const float sc = 0.05590169943749474f;
        f32x4 cr, ci;
#pragma unroll
        for (int i = 0; i < 4; ++i) {
            const float a = (float)(16 * (w - 3) + 4 * q + i) * nf * (1.0f / 640.0f);
            cr[i] = cosr(a) * sc; ci[i] = -sinr(a) * sc;
        }
        t[(6 + w - 3) * 64] = __builtin_bit_cast(u32x4, cr); t[(8 + w - 3) * 64] = __builtin_bit_cast(u32x4, ci);
    }
}
DI void rowc_load(RowC& k, const char* tab, int lane) {
    const u32x4* tb = (const u32x4*)tab + lane;
    k.c[0] = asbf(tb[0]); k.c[1] = asbf(tb[64]); k.sp[0] = asbf(tb[128]); k.sp[1] = asbf(tb[192]);
    k.c20 = asbf(tb[256]); k.s20m = asbf(tb[320]);
    k.twr[0] = __builtin_bit_cast(f32x4, tb[384]); k.twr[1] = __builtin_bit_cast(f32x4, tb[448]);
    k.twi[0] = __builtin_bit_cast(f32x4, tb[512]); k.twi[1] = __builtin_bit_cast(f32x4, tb[576]);
    k.sm[0] = neg8(k.sp[0]); k.sm[1] = neg8(k.sp[1]);
    const int q = lane >> 4, qq = (lane & 15) >> 2, pp = lane & 3;
    const int r0 = min(8 * q + qq, 19), r1 = min(8 * q + 4 + qq, 19);
    k.off0 = r0 * 64 + pp * 8; k.off1 = r1 * 64 + pp * 8;
}
DI float wave_sum63(float v) {
    v += __builtin_bit_cast(float, __builtin_amdgcn_update_dpp(0, __builtin_bit_cast(int, v), 0x111, 0xf, 0xf, true));
    v += __builtin_bit_cast(float, __builtin_amdgcn_update_dpp(0, __builtin_bit_cast(int, v), 0x112, 0xf, 0xf, true));
    v += __builtin_bit_cast(float, __builtin_amdgcn_update_dpp(0, __builtin_bit_cast(int, v), 0x114, 0xf, 0xf, true));
    v += __builtin_bit_cast(float, __builtin_amdgcn_update_dpp(0, __builtin_bit_cast(int, v), 0x118, 0xf, 0xf, true));
    v += __builtin_bit_cast(float, __builtin_amdgcn_update_dpp(0, __builtin_bit_cast(int, v), 0x142, 0xa, 0xf, true));
    v += __builtin_bit_cast(float, __builtin_amdgcn_update_dpp(0, __builtin_bit_cast(int, v), 0x143, 0xc, 0xf, true));
    return v;
}
DI void rowfft(const RowC& k, const char* row, float (&A)[8], float (&B)[8], int lane) {
    f32x4 z = {0.f, 0.f, 0.f, 0.f};
    f32x4 yr[2], yi[2];
#pragma unroll
    for (int mt = 0; mt < 2; ++mt) {
        const bf16x8 a = cat8(tr_read(row + k.off0 + mt * 32), tr_read(row + k.off1 + mt * 32));
        yr[mt] = mfma16(a, k.c20, z); yi[mt] = mfma16(a, k.s20m, z);
    }
    float tr[8], ti[8];
#pragma unroll
    for (int i = 0; i < 8; ++i) {
        const float a = yr[i >> 2][i & 3], b = yi[i >> 2][i & 3], cr = k.twr[i >> 2][i & 3], ci = k.twi[i >> 2][i & 3];
        tr[i] = a * cr - b * ci; ti[i] = a * ci + b * cr;
    }
    const bf16x8 Tr = pk8(tr), Ti = pk8(ti);
    f32x4 ore[2], oim[2];
#pragma unroll
    for (int t = 0; t < 2; ++t) {
        ore[t] = mfma16(k.c[t], Tr, z); ore[t] = mfma16(k.sp[t], Ti, ore[t]);
        oim[t] = mfma16(k.sm[t], Tr, z); oim[t] = mfma16(k.c[t], Ti, oim[t]);
    }
#pragma unroll
    for (int r = 0; r < 4; ++r) { A[2 * r] = ore[0][r]; A[2 * r + 1] = oim[0][r]; B[2 * r] = ore[1][r]; B[2 * r + 1] = oim[1][r]; }
    if (lane == 0) { const float h = 0.7071067811865476f; const float dc = A[0] * h; A[0] = B[0] * h; A[1] = dc; }
}
DI int sw_off(int c) { const int ch = c >> 3; return ((ch ^ ((ch >> 3) & 7)) << 4) + (c & 7) * 2; }
DI int chunk_c0(int n, int q) { return (n == 0 ? 608 : n == 10 ? 576 : 64 * (n - 1)) + 8 * q; }

constexpr int ROWST = 1344;
constexpr int PREP_TAB = 640 * 32 + 16 * ROWST;
constexpr int PREP_LDS = PREP_TAB + ROWC_LDS;

struct PrepArgs { const u16 *x, *wr, *wi, *w1, *w2, *b2; char* ws; };

DI void task_x(const PrepArgs& a, int b, char* smem) {
    const int tid = threadIdx.x, lane = tid & 63, w = tid >> 6;
    const int p0 = 16 * ((b & 7) * 16 + (b >> 3));
    u32x4 v[3];
#pragma unroll
    for (int i = 0; i < 3; ++i) {
        const int id = min(tid + 512 * i, 1279), r = id / 80, ch = id % 80;
        v[i] = *(const u32x4*)(a.x + (size_t)tok_of_p(p0 + r) * D_ + ch * 8);
    }
    __builtin_amdgcn_sched_barrier(0);
    rowc_build(smem + PREP_TAB, w, lane);
    __builtin_amdgcn_sched_barrier(0);
    STAMP(0, 1);
    if (tid < 16) ((u64*)(a.ws + OFF_SS2))[p0 + tid] = 0ull;
    if (b < 32) ((u32*)(a.ws + OFF_CNT))[b * 512 + tid] = 0u;
#pragma unroll
    for (int i = 0; i < 3; ++i) {
        const int id = min(tid + 512 * i, 1279), r = id / 80, ch = id % 80;
        *(u32x4*)(smem + r * ROWST + ch * 16) = v[i];
    }
    STAMP(0, 2);
    __syncthreads();
    STAMP(0, 3);
    RowC k; rowc_load(k, smem + PREP_TAB, lane);
    const int n = lane & 15, q = lane >> 4, c0 = chunk_c0(n, q);
    float A[2][8], B[2][8];
#pragma unroll
    for (int i = 0; i < 2; ++i) rowfft(k, smem + (2 * w + i) * ROWST, A[i], B[i], lane);
#pragma unroll
    for (int i = 0; i < 2; ++i) {
        const int r = 2 * w + i;
        float ss = 0.f;
        if (n <= 10) {
#pragma unroll
            for (int e = 0; e < 8; ++e) ss += (lane == 0 && e == 1) ? 0.f : A[i][e] * A[i][e];
            *(bf16x8*)(smem + r * ROWST + sw_off(c0)) = pk8(A[i]);
        }
        if (n >= 1 && n <= 9) {
#pragma unroll
            for (int e = 0; e < 8; ++e) ss += B[i][e] * B[i][e];
            *(bf16x8*)(smem + r * ROWST + sw_off(c0 + 32)) = pk8(B[i]);
        }
        ss = wave_sum63(ss);
        if (lane == 63) ((u64*)(a.ws + OFF_SS0))[p0 + r] = ss_from_float(ss);
    }
    STAMP(0, 4);
    __syncthreads();
    STAMP(0, 5);
    u16* xt = (u16*)(a.ws + OFF_XT0);
    const int g = lane >> 4, i16 = lane & 15, qq = i16 >> 2, pp = i16 & 3;
#pragma unroll
    for (int i = 0; i < 3; ++i) {
        const int cbk = w + 8 * i;
        if (cbk < 20) {
            const int cb = 32 * cbk + 16 * (g & 1), ph = g >> 1;
            const char* base = smem + (8 * ph + qq) * ROWST + sw_off(cb + 4 * pp);
            const bf16x8 o = cat8(tr_read(base), tr_read(base + 4 * ROWST));
            *(bf16x8*)(xt + (size_t)(cb + i16) * S_ + p0 + 8 * ph) = o;
        }
    }
}

DI void task_W(const PrepArgs& a, int b) {
    const int lane = threadIdx.x & 63, wv = threadIdx.x >> 6, wt = b * 2 + (wv >> 2), kq = wv & 3;
    const int k1 = wt / 5, cg = wt % 5, kp = 1 + 64 * cg + lane;
    const int r = kp % 20, t = kp / 20;
    int j; bool mir = false;
    if (kp == 320) j = 304;
    else if (r == 0) j = 304 + t;
    else if (r < 10) j = 32 * (r - 1) + t;
    else if (r == 10) j = 288 + t;
    else { j = 32 * (19 - r) + 31 - t; mir = true; }
    const rsrc_t rr = mk_rsrc(a.wr, (size_t)S_ * DF_ * 2), ri = mk_rsrc(a.wi, (size_t)S_ * DF_ * 2);
    const int st = (mir ? -64 : 64) * DF_ * 2, off0 = ((mir ? 2048 - k1 : k1) * DF_ + kp) * 2 + 8 * kq * st;
    u32 re[8], im[8];
#pragma unroll
    for (int k2 = 0; k2 < 8; ++k2) {
        int off = off0 + k2 * st;
        if (k2 == 0 && mir && k1 == 0 && kq == 0) off = kp * 2;
        re[k2] = __builtin_amdgcn_raw_buffer_load_b16(rr, off, 0, 0); im[k2] = __builtin_amdgcn_raw_buffer_load_b16(ri, off, 0, 0);
    }
    const u32 sg = mir ? 0x80000000u : 0u;
    u32x4* dst = (u32x4*)(a.ws + OFF_WT + ((size_t)j * 64 + k1) * 128 + kq * 32);
#pragma unroll
    for (int i = 0; i < 2; ++i) {
        u32x4 o;
#pragma unroll
        for (int e = 0; e < 4; ++e) o[e] = (re[4 * i + e] | (im[4 * i + e] << 16)) ^ sg;
        dst[i] = o;
    }
}

DI void task_w1(const PrepArgs& a, int b, char* smem) {
    const int tid = threadIdx.x, lane = tid & 63, w = tid >> 6;
    char* raw = smem;
    char* tr = smem + 640 * 32;
    char* out = raw;
    u32x4 v[3];
#pragma unroll
    for (int i = 0; i < 3; ++i) {
        const int id = min(tid + 512 * i, 1279), d = id >> 1, h = id & 1;
        v[i] = *(const u32x4*)(a.w1 + (size_t)d * F_ + 16 * b + 8 * h);
    }
    __builtin_amdgcn_sched_barrier(0);
    rowc_build(smem + PREP_TAB, w, lane);
    __builtin_amdgcn_sched_barrier(0);
#pragma unroll
    for (int i = 0; i < 3; ++i) *(u32x4*)(raw + min(tid + 512 * i, 1279) * 16) = v[i];
    __syncthreads();
    const int g = lane >> 4, i16 = lane & 15, qq = i16 >> 2, pp = i16 & 3;
#pragma unroll
    for (int i = 0; i < 5; ++i) {
        const int d0 = 16 * (w + 8 * i) + 4 * g;
        const s16x4 t = tr_read(raw + (d0 + qq) * 32 + pp * 8);
        *(s16x4*)(tr + i16 * ROWST + d0 * 2) = t;
    }
    RowC k; rowc_load(k, smem + PREP_TAB, lane);
    __syncthreads();
    const int n = lane & 15, q = lane >> 4, c0 = chunk_c0(n, q);
    float A[2][8], B[2][8];
#pragma unroll
    for (int i = 0; i < 2; ++i) rowfft(k, tr + (2 * w + i) * ROWST, A[i], B[i], lane);
#pragma unroll
    for (int i = 0; i < 2; ++i) {
        const int f = 2 * w + i;
        if (lane == 0) A[i][1] = 0.f;
        if (n <= 10) *(bf16x8*)(out + ((c0 >> 5) * 64 + q * 16 + f) * 16) = pk8(A[i]);
        if (n >= 1 && n <= 9) *(bf16x8*)(out + (((c0 >> 5) + 1) * 64 + q * 16 + f) * 16) = pk8(B[i]);
    }
    __syncthreads();
    u32x4* dst = (u32x4*)(a.ws + OFF_W1F + (size_t)b * 20 * 1024);
#pragma unroll
    for (int i = 0; i < 3; ++i) { const int id = tid + 512 * i; if (id < 1280) dst[id] = *(const u32x4*)(out + id * 16); }
}

template <bool SW> DI void w2_frags(const char* img, char* dstbase, int b, int lane, int w) {
    const int g = lane >> 4, i16 = lane & 15, qq = i16 >> 2, pp = i16 & 3;
    const rsrc_t rd = mk_rsrc(dstbase, W2F_BYTES);
#pragma unroll
    for (int i = 0; i < 5; ++i) {
        const int cu = w + 8 * i, c = 16 * cu + 4 * pp;
        const char* base = img + (4 * g + qq) * ROWST + (SW ? sw_off(c) : c * 2);
        const bf16x8 v = cat8(tr_read(base), tr_read(base + 16 * ROWST));
        __builtin_amdgcn_raw_buffer_store_b128(__builtin_bit_cast(u32x4, v), rd, lane * 16, (cu * 54 + b) * 1024, SC1);
    }
}
constexpr int W2_TAB = 32 * ROWST;
DI void task_w2(const PrepArgs& a, int b, char* smem) {
    const int tid = threadIdx.x, lane = tid & 63, w = tid >> 6;
    u32x4 v[5];
#pragma unroll
    for (int i = 0; i < 5; ++i) {
        const int id = tid + 512 * i, r = id / 80, ch = id % 80;
        v[i] = *(const u32x4*)(a.w2 + (size_t)(32 * b + r) * D_ + ch * 8);
    }
    __builtin_amdgcn_sched_barrier(0);
    rowc_build(smem + W2_TAB, w, lane);
    __builtin_amdgcn_sched_barrier(0);
#pragma unroll
    for (int i = 0; i < 5; ++i) { const int id = tid + 512 * i, r = id / 80, ch = id % 80; *(u32x4*)(smem + r * ROWST + ch * 16) = v[i]; }
    __syncthreads();
    RowC k; rowc_load(k, smem + W2_TAB, lane);
    w2_frags<false>(smem, a.ws + OFF_W2O, b, lane, w);
    const int n = lane & 15, q = lane >> 4, c0 = chunk_c0(n, q);
    u32x4 A8[4], B8[4];
#pragma unroll
    for (int i = 0; i < 4; ++i) {
        float A[8], B[8];
        rowfft(k, smem + (4 * w + i) * ROWST, A, B, lane);
        A8[i] = __builtin_bit_cast(u32x4, pk8(A)); B8[i] = __builtin_bit_cast(u32x4, pk8(B));
    }
    __syncthreads();
#pragma unroll
    for (int i = 0; i < 4; ++i) {
        const int r = 4 * w + i;
        if (n <= 10) *(u32x4*)(smem + r * ROWST + sw_off(c0)) = A8[i];
        if (n >= 1 && n <= 9) *(u32x4*)(smem + r * ROWST + sw_off(c0 + 32)) = B8[i];
    }
    __syncthreads();
    w2_frags<true>(smem, a.ws + OFF_W2F, b, lane, w);
}

DI int kcol_of(int jc) { return jc < 288 ? (jc >> 5) + 1 + 20 * (jc & 31) : jc < 304 ? 10 + 20 * (jc - 288) : 20 * (jc - 304); }
DI void task_Q(const PrepArgs& a, int u) {
    const int tid = threadIdx.x;
#pragma unroll
    for (int ii = 0; ii < 3; ++ii) {
        const int i = tid + 512 * ii;
        if (i < 1280) {
            const int ks = i >> 6, l = i & 63, d = 16 * u + (l & 15), q = l >> 4;
            float v[8];
#pragma unroll
            for (int j = 0; j < 8; j += 2) {
                const int jc = 16 * ks + 4 * q + (j >> 1), kk = kcol_of(jc);
                const float ang = (float)((kk * d) % 640) * (1.0f / 640.0f);
                float c = cosr(ang) * 0.05590169943749474f, s = -sinr(ang) * 0.05590169943749474f;
                if (jc == 304) { c = ((d & 1) ? -1.f : 1.f) * 0.03952847075210474f; s = 0.03952847075210474f; }
                v[j] = c; v[j + 1] = s;
            }
            __builtin_amdgcn_raw_buffer_store_b128(__builtin_bit_cast(u32x4, pk8(v)), mk_rsrc(a.ws + OFF_W1F, W1F_BYTES), i * 16, (108 + u) * 20480, SC1);
        }
    }
}
DI void task_b(const PrepArgs& a, char* smem) {
    const int tid = threadIdx.x, lane = tid & 63;
    if (tid < 80) *(u32x4*)(smem + tid * 16) = *(const u32x4*)(a.b2 + tid * 8);
    rowc_build(smem + PREP_TAB, tid >> 6, lane);
    __syncthreads();
    RowC k; rowc_load(k, smem + PREP_TAB, lane);
    float A[8], B[8];
    rowfft(k, smem, A, B, lane);
    if (tid < 64) {
        const int n = lane & 15, q = lane >> 4, c0 = chunk_c0(n, q);
        float* o = (float*)(a.ws + OFF_B2P);
        if (n <= 10) for (int e = 0; e < 8; ++e) o[c0 + e] = A[e];
        if (n >= 1 && n <= 9) for (int e = 0; e < 8; ++e) o[c0 + 32 + e] = B[e];
    }
}

constexpr int NB_K0 = 128 + 108 + 160 + 1;
constexpr int MAIN_CODE_PREFETCH = 65536;
DI void prefetch_main_code(int slice, int lane) {
    u32 lo, hi;
    asm volatile("s_getpc_b64 s[40:41]\n\ts_add_u32 s40, s40, _Z6k_main8MainArgs@rel32@lo+4\n\ts_addc_u32 s41, s41, _Z6k_main8MainArgs@rel32@hi+12\n\t"
                 "s_mov_b32 %0, s40\n\ts_mov_b32 %1, s41" : "=s"(lo), "=s"(hi) :: "s40", "s41", "scc");
    const u64 base = ((u64)hi << 32) | lo;
    const char* p = (const char*)base + slice * 4096 + lane * 16;
    u32x4 t0, t1, t2, t3;
    asm volatile("global_load_dwordx4 %0, %4, off\n\tglobal_load_dwordx4 %1, %4, off offset:1024\n\tglobal_load_dwordx4 %2, %4, off offset:2048\n\t"
                 "global_load_dwordx4 %3, %4, off offset:3072\n\ts_waitcnt vmcnt(0)"
                 : "=&v"(t0), "=&v"(t1), "=&v"(t2), "=&v"(t3) : "v"(p) : "memory");
}
__global__ __launch_bounds__(512, 4) void k0_prep(PrepArgs a) {
    extern __shared__ __attribute__((aligned(16))) char smem[];
    const int b = blockIdx.x;
    STAMP(0, 0);
    if (b >= 236 && b < 236 + 8 * (MAIN_CODE_PREFETCH / 4096) && (threadIdx.x >> 6) == 7) prefetch_main_code((b - 236) >> 3, threadIdx.x & 63);
    if (b < 128) task_x(a, b, smem);
    else if (b < 236) task_w1(a, b - 128, smem);
    else if (b < 396) task_W(a, b - 236);
    else task_b(a, smem);
    END_STAMP(0);
}

template <int N> DI void wait_vm() { asm volatile("s_waitcnt vmcnt(%0)" ::"n"(N) : "memory"); }
DI void wait_lgkm() { asm volatile("s_waitcnt lgkmcnt(0)" ::: "memory"); }
DI void barrier() { __builtin_amdgcn_s_barrier(); }
template <bool SC = false> DI void dma16(u32x4 srd, int voff, int soff, u32 ldsaddr) {
    if (SC) asm volatile("s_mov_b32 m0, %0\n\ts_nop 0\n\tbuffer_load_dwordx4 %1, %2, %3 offen sc1 lds" ::"s"(ldsaddr), "v"(voff), "s"(srd), "s"(soff) : "memory", "m0");
    else asm volatile("s_mov_b32 m0, %0\n\ts_nop 0\n\tbuffer_load_dwordx4 %1, %2, %3 offen lds" ::"s"(ldsaddr), "v"(voff), "s"(srd), "s"(soff) : "memory", "m0");
}
template <int LIM, bool SC = false> DI void dma16_lim(u32x4 srd, int voff, int soff, u32 ldsaddr, int w) {
    if (SC) asm volatile("s_cmp_lt_i32 %4, %5\n\ts_cbranch_scc0 .Lskip%=\n\ts_mov_b32 m0, %0\n\ts_nop 0\n\tbuffer_load_dwordx4 %1, %2, %3 offen sc1 lds\n.Lskip%=:"
                 ::"s"(ldsaddr), "v"(voff), "s"(srd), "s"(soff), "s"(w), "n"(LIM) : "memory", "m0", "scc");
    else asm volatile("s_cmp_lt_i32 %4, %5\n\ts_cbranch_scc0 .Lskip%=\n\ts_mov_b32 m0, %0\n\ts_nop 0\n\tbuffer_load_dwordx4 %1, %2, %3 offen lds\n.Lskip%=:"
                 ::"s"(ldsaddr), "v"(voff), "s"(srd), "s"(soff), "s"(w), "n"(LIM) : "memory", "m0", "scc");
}
DI u32x4 mk_srd(const void* p, size_t bytes) {
    const u64 a = (u64)p;
    u32x4 r = {(u32)a, (u32)(a >> 32) & 0xffffu, (u32)bytes, 0x00020000u};
    return r;
}
DI float row_sum15(float v) {
    v += __builtin_bit_cast(float, __builtin_amdgcn_update_dpp(0, __builtin_bit_cast(int, v), 0x111, 0xf, 0xf, true));
    v += __builtin_bit_cast(float, __builtin_amdgcn_update_dpp(0, __builtin_bit_cast(int, v), 0x112, 0xf, 0xf, true));
    v += __builtin_bit_cast(float, __builtin_amdgcn_update_dpp(0, __builtin_bit_cast(int, v), 0x114, 0xf, 0xf, true));
    v += __builtin_bit_cast(float, __builtin_amdgcn_update_dpp(0, __builtin_bit_cast(int, v), 0x118, 0xf, 0xf, true));
    return v;
}
template <int I, int N, class F> DI void static_for(F&& f) {
    if constexpr (I < N) { f(std::integral_constant<int, I>{}); static_for<I + 1, N>(f); }
}

constexpr int G1_P = 4, G1_PW = 5;
typedef __attribute__((address_space(1))) u32 gu32;
DI u32 ld_agent(const u32* p) { return __hip_atomic_load((gu32*)p, __ATOMIC_RELAXED, __HIP_MEMORY_SCOPE_AGENT); }
DI void add_agent(u32* p) { __hip_atomic_fetch_add((gu32*)p, 1u, __ATOMIC_RELAXED, __HIP_MEMORY_SCOPE_AGENT); }
DI void signal_all(char* ws, int which) {
    if (threadIdx.x == 0) {
        u32* c = (u32*)(ws + OFF_CNT + 16384 + which * 8192) + (blockIdx.x & 7) * 32;
#pragma unroll
        for (int r = 0; r < 8; ++r) add_agent(c + r * 256);
    }
}
#define POLL_SLEEP 8
DI void poll_until(const u32* p, u32 want, char* ws, int lane) {
    for (u32 spins = 0;; ++spins) {
        if (__all(ld_agent(p) >= want)) break;
        __builtin_amdgcn_s_sleep(POLL_SLEEP);
        if (spins > (1u << 18)) { if (lane == 0) __hip_atomic_store((gu32*)(ws + OFF_CNT + 49152), 1u, __ATOMIC_RELAXED, __HIP_MEMORY_SCOPE_AGENT); break; }
    }
}
DI void poll_all(char* ws, int which, u32 per_shard, int w, int lane) {
    if (w == 0) poll_until((const u32*)(ws + OFF_CNT + 16384 + which * 8192) + ((blockIdx.x >> 3) & 7) * 256 + (lane & 7) * 32, per_shard, ws, lane);
}

struct MainArgs { PrepArgs p; const u16* b1; u16* out; };
DI int img128(int row, int ch) { return row * 128 + ((ch ^ ((row >> 1) & 7)) << 4); }
template <bool LAST> DI void w1_prefetch(char* ws, char* smem, int w, int lane, int u0);

template <int L>
DI void col_task(char* ws, int b, char* smem, int u0) {
    const int tid = threadIdx.x & 255, lane = tid & 63, w = tid >> 6, half = threadIdx.x >> 8;
    const int j = 2 * b + half;
    char* Ure = smem + half * 24576; char* Uim = Ure + 4096; char* Ere = Ure + 8192; char* Eim = Ure + 12288;
    char* Rre = Ure + 16384; char* Rim = Ure + 20480;
    const int n = lane & 15, q = lane >> 4;
    const int wv = __builtin_amdgcn_readfirstlane(threadIdx.x >> 6);
    const rsrc_t rx = mk_rsrc(ws + (L == 0 ? OFF_XT0 : OFF_XT2), XT_BYTES), rs = mk_rsrc(ws + (L == 0 ? OFF_SS0 : OFF_SS2), 2048 * 8);
    u32x4 vr, vi, sv[4];
    auto load_in = [&]() __attribute__((always_inline)) {
        vr = __builtin_amdgcn_raw_buffer_load_b128(rx, (2 * j * S_ + tid * 8) * 2, 0, L == 0 ? 0 : SC1);
        vi = __builtin_amdgcn_raw_buffer_load_b128(rx, ((2 * j + 1) * S_ + tid * 8) * 2, 0, L == 0 ? 0 : SC1);
#pragma unroll
        for (int i = 0; i < 4; ++i) sv[i] = __builtin_amdgcn_raw_buffer_load_b128(rs, tid * 64 + 16 * i, 0, L == 0 ? 0 : SC1);
    };
    if (L == 0) load_in();
    u32x4 wf[2];
#pragma unroll
    for (int t = 0; t < 2; ++t) wf[t] = *(const u32x4*)((const u32*)(ws + OFF_WT) + ((size_t)j * 64 + 16 * w + n) * 32 + 16 * t + 4 * q);
    __builtin_amdgcn_sched_barrier(0);
    F32c f; f32c_init(f, lane);
    bf16x8 c32[2] = {f.c[0], f.c[1]}, s32p[2] = {f.sp[0], f.sp[1]}, s32m[2];
    pin(c32[0]); pin(c32[1]); pin(s32p[0]); pin(s32p[1]);
    bf16x8 c64[2], s64p[2], s64m[2];
    const float k1f = (float)(16 * w + n);
    {
        float c[8], s[8];
#pragma unroll
        for (int e = 0; e < 8; ++e) {
            const float ang = __builtin_amdgcn_fractf((float)(8 * q + e) * k1f * (1.0f / 64.0f));
            c[e] = cosr(ang); s[e] = sinr(ang);
        }
        c64[0] = pk8(c); s64p[0] = pk8(s);
        const u32 sg = (lane & 1) ? 0x80008000u : 0u;
        c64[1] = asbf(__builtin_bit_cast(u32x4, c64[0]) ^ sg); s64p[1] = asbf(__builtin_bit_cast(u32x4, s64p[0]) ^ sg);
        pin(c64[0]); pin(c64[1]); pin(s64p[0]); pin(s64p[1]);
    }
    float twr[8], twi[8], ivr[8], ivi[8];
#pragma unroll
    for (int i = 0; i < 8; ++i) {
        const float af = (float)(16 * (i >> 2) + 4 * q + (i & 3)) * k1f * (1.0f / 2048.0f);
        twr[i] = cosr(af); twi[i] = -sinr(af);
        const float ai = (float)(16 * w + 4 * q + (i & 3)) * (float)(16 * (i >> 2) + n) * (1.0f / 2048.0f);
        ivr[i] = cosr(ai) * (1.0f / 2048.0f); ivi[i] = sinr(ai) * (1.0f / 2048.0f);
        pin(twr[i]); pin(twi[i]); pin(ivr[i]); pin(ivi[i]);
    }
    __builtin_amdgcn_sched_barrier(0);
    STAMP(L ? 4 : 1, 1);
    if (L == 1) {
        poll_all(ws, 1, 32u, wv, lane);
        barrier();
        STAMP(4, 5);
        load_in();
        __builtin_amdgcn_sched_barrier(0);
    }
    s64m[0] = neg8(s64p[0]); s64m[1] = neg8(s64p[1]);
    {
        float r[8];
#pragma unroll
        for (int i = 0; i < 8; ++i) {
            const float s = (float)sv[i >> 1][2 * (i & 1) + 1] * 4294967296.0f + (float)sv[i >> 1][2 * (i & 1)];
            r[i] = __builtin_amdgcn_rsqf(s * (1.0f / (SS_SCALE * 640.0f)) + EPS);
        }
        const bf16x8 ur = pk8(bflo(vr[0]) * r[0], bfhi(vr[0]) * r[1], bflo(vr[1]) * r[2], bfhi(vr[1]) * r[3],
                              bflo(vr[2]) * r[4], bfhi(vr[2]) * r[5], bflo(vr[3]) * r[6], bfhi(vr[3]) * r[7]);
        bf16x8 ui = pk8(bflo(vi[0]) * r[0], bfhi(vi[0]) * r[1], bflo(vi[1]) * r[2], bfhi(vi[1]) * r[3],
                        bflo(vi[2]) * r[4], bfhi(vi[2]) * r[5], bflo(vi[3]) * r[6], bfhi(vi[3]) * r[7]);
        if (j == 304) ui = asbf(u32x4{0u, 0u, 0u, 0u});
        const int o = img128(tid >> 3, tid & 7);
        *(bf16x8*)(Ure + o) = ur; *(bf16x8*)(Uim + o) = ui;
        *(u32x4*)(Rre + o) = vr; *(u32x4*)(Rim + o) = vi;
    }
    __syncthreads();
    STAMP(L ? 4 : 1, 2);
    const f32x4 z = {0.f, 0.f, 0.f, 0.f};
    f32x4 dr[2], di[2];
#pragma unroll
    for (int mt = 0; mt < 2; ++mt) {
        dr[mt] = z; di[mt] = z;
#pragma unroll
        for (int ks = 0; ks < 2; ++ks) {
            const int o = img128(16 * mt + n, 4 * ks + q);
            const bf16x8 ar = *(const bf16x8*)(Ure + o), ai = *(const bf16x8*)(Uim + o);
            dr[mt] = mfma16(ar, c64[ks], dr[mt]); dr[mt] = mfma16(ai, s64p[ks], dr[mt]);
            di[mt] = mfma16(ar, s64m[ks], di[mt]); di[mt] = mfma16(ai, c64[ks], di[mt]);
        }
    }
    float tr[8], ti[8];
#pragma unroll
    for (int i = 0; i < 8; ++i) {
        const float x = dr[i >> 2][i & 3], y = di[i >> 2][i & 3];
        tr[i] = x * twr[i] - y * twi[i]; ti[i] = x * twi[i] + y * twr[i];
    }
    bf16x8 Tr = pk8(tr), Ti = pk8(ti);
    s32m[0] = neg8(s32p[0]); s32m[1] = neg8(s32p[1]);
    f32x4 orr[2], oi[2];
#pragma unroll
    for (int t = 0; t < 2; ++t) {
        orr[t] = mfma16(c32[t], Tr, z); orr[t] = mfma16(s32p[t], Ti, orr[t]);
        oi[t] = mfma16(s32m[t], Tr, z); oi[t] = mfma16(c32[t], Ti, oi[t]);
    }
#pragma unroll
    for (int i = 0; i < 8; ++i) {
        const float x = orr[i >> 2][i & 3], y = oi[i >> 2][i & 3];
        const u32 wv = wf[i >> 2][i & 3];
        const float wre = bflo(wv), wim = bfhi(wv);
        tr[i] = x * wre - y * wim; ti[i] = x * wim + y * wre;
    }
    Tr = pk8(tr); Ti = pk8(ti);
#pragma unroll
    for (int nt = 0; nt < 2; ++nt) {
        f32x4 zr = mfma16(Tr, c32[nt], z); zr = mfma16(Ti, s32m[nt], zr);
        f32x4 zi = mfma16(Tr, s32p[nt], z); zi = mfma16(Ti, c32[nt], zi);
        float er[4], ei[4];
#pragma unroll
        for (int r = 0; r < 4; ++r) {
            const float cr = ivr[4 * nt + r], ci = ivi[4 * nt + r];
            er[r] = zr[r] * cr - zi[r] * ci; ei[r] = zr[r] * ci + zi[r] * cr;
        }
        const int o = img128(16 * nt + n, 2 * w + (q >> 1)) + (q & 1) * 8;
        u32x2 pr = {pk2(er[0], er[1]), pk2(er[2], er[3])}, pi = {pk2(ei[0], ei[1]), pk2(ei[2], ei[3])};
        *(u32x2*)(Ere + o) = pr; *(u32x2*)(Eim + o) = pi;
    }
    __syncthreads();
    STAMP(L ? 4 : 1, 3);
    const rsrc_t rf = mk_rsrc(ws + (L == 0 ? OFF_XF1 : OFF_XF3), XT_BYTES);
    char* O = Ure;
#pragma unroll
    for (int nt = 0; nt < 2; ++nt) {
        f32x4 yr = z, yi = z;
#pragma unroll
        for (int ks = 0; ks < 2; ++ks) {
            const int o = img128(16 * nt + n, 4 * ks + q);
            const bf16x8 br = *(const bf16x8*)(Ere + o), bi = *(const bf16x8*)(Eim + o);
            yr = mfma16(c64[ks], br, yr); yr = mfma16(s64m[ks], bi, yr);
            yi = mfma16(s64p[ks], br, yi); yi = mfma16(c64[ks], bi, yi);
        }
        if (j == 304) yi = z;
        const int p = (16 * nt + n) * 64 + 16 * w + 4 * q;
        u32x2 rr[2], ri[2];
        { const int o = img128(16 * nt + n, 2 * w + (q >> 1)) + (q & 1) * 8; rr[nt] = *(const u32x2*)(Rre + o); ri[nt] = *(const u32x2*)(Rim + o); }
        const float fr[4] = {bflo(rr[nt][0]) + yr[0], bfhi(rr[nt][0]) + yr[1], bflo(rr[nt][1]) + yr[2], bfhi(rr[nt][1]) + yr[3]};
        const float fi[4] = {bflo(ri[nt][0]) + yi[0], bfhi(ri[nt][0]) + yi[1], bflo(ri[nt][1]) + yi[2], bfhi(ri[nt][1]) + yi[3]};
        u32x4 ov = {pk2(fr[0], fi[0]), pk2(fr[1], fi[1]), pk2(fr[2], fi[2]), pk2(fr[3], fi[3])};
        *(u32x4*)(O + p * 4) = ov;
    }
    __syncthreads();
    {
        const int c0 = 4 * b, dst = ((c0 >> 5) * 64 + ((c0 & 31) >> 3) * 16) * 16 + (c0 & 4) * 2;
        const u32* O0 = (const u32*)smem; const u32* O1 = (const u32*)(smem + 24576);
#pragma unroll
        for (int i = 0; i < 4; ++i) {
            const int p = threadIdx.x + 512 * i;
            u32x2 v = {O0[p], O1[p]};
            __builtin_amdgcn_raw_buffer_store_b64(v, rf, (p >> 4) * 20480 + (p & 15) * 16, dst, SC1);
        }
    }
    STAMP(L ? 4 : 1, 4);
    wait_lgkm();
    wait_vm<0>();
    barrier();
    signal_all(ws, L == 0 ? 0 : 2);
    w1_prefetch<L == 1>(ws, smem, wv, lane, u0);
    STAMP(L ? 4 : 1, 15);
}

DI f32x2 gelu_tanh2(f32x2 x) {
    const float k1 = -2.302208198f, k2 = -0.1029432397f;
    const f32x2 c1 = {k1, k1}, c2 = {k2, k2}, one = {1.0f, 1.0f};
    const f32x2 t = x * (x * x * c2 + c1);
    f32x2 e = {__builtin_amdgcn_exp2f(t[0]), __builtin_amdgcn_exp2f(t[1])};
    e += one;
    const f32x2 r = {__builtin_amdgcn_rcpf(e[0]), __builtin_amdgcn_rcpf(e[1])};
    return x * r;
}
constexpr int G2_PH = 4;
constexpr int W2STG = 20480;
DI u32 w2slot(int t) { return 163840 - ((t & 7) + 1) * W2STG; }
template <bool LAST> constexpr int w1slot(int st) { return LAST ? 163840 - (st % 8 + 1) * 20480 : 163840 - (st + 1) * 14336; }

template <bool LAST, int I> DI void w1_piece(u32x4 srd, u32 lds0, int w, int vo, int u0, int st) {
    constexpr int WP = LAST ? 20 : 14;
    const int pc = w + 8 * I;
    const u32 dst = lds0 + w1slot<LAST>(st) + pc * 1024;
    const int so = (u0 + (pc >> 1)) * 20480 + st * 2048 + (pc & 1) * 1024;
    if (8 * I + 7 < WP) dma16<LAST>(srd, vo, so, dst); else dma16_lim<(WP & 7), LAST>(srd, vo, so, dst, w);
}
template <bool LAST> DI void w1_prefetch(char* ws, char* smem, int w, int lane, int u0) {
    const u32x4 srd = mk_srd(ws + OFF_W1F, W1F_BYTES);
    const u32 lds0 = (u32)(size_t)(LAS char*)smem;
#pragma unroll
    for (int st = 0; st < G1_PW; ++st) {
        w1_piece<LAST, 0>(srd, lds0, w, lane * 16, u0, st);
        w1_piece<LAST, 1>(srd, lds0, w, lane * 16, u0, st);
        if (LAST) w1_piece<LAST, 2>(srd, lds0, w, lane * 16, u0, st);
    }
}

constexpr int g1_younger(int ssync, int t, int nlo) {
    int cnt = 0; bool seen = false;
    for (int s = 0; s < ssync; ++s) {
        if (s + G1_PW < 10) { if (seen) cnt += nlo; if (s + G1_PW == t) seen = true; }
        if (s + G1_P < 10 && seen) cnt += 2;
    }
    return cnt;
}

template <int I> DI void w2_issue1(u32x4 srd, u32 lds0, int w, int vo, int ct, int t) {
    const int pc = w + 8 * I, q = pc / 5, cu = pc - 5 * q;
    const int so = ((5 * ct + cu) * 54 + (q < 2 ? 14 * q : 28 + 13 * (q - 2)) + t) * 1024;
    if (I < 2) dma16<true>(srd, vo, so, lds0 + w2slot(t) + pc * 1024); else dma16_lim<4, true>(srd, vo, so, lds0 + w2slot(t) + pc * 1024, w);
}
DI void w2_issue(u32x4 srd, u32 lds0, int w, int vo, int ct, int t) {
    w2_issue1<0>(srd, lds0, w, vo, ct, t); w2_issue1<1>(srd, lds0, w, vo, ct, t); w2_issue1<2>(srd, lds0, w, vo, ct, t);
}

template <int NF, bool LAST, int KG>
DI void g1_phase(const MainArgs& a, char* smem, int w, int lane, int pt, int u0, int nu, int ct2) {
    constexpr int P = G1_P, PW = G1_PW;
    constexpr int WP = 2 * NF, WR = (WP + 7) / 8, NLO = WP / 8, KID = LAST ? 5 : 2;
    char* ws = a.p.ws;
    const int pq = w & 3, g = lane >> 4;
    const u32x4 srdW = mk_srd(ws + OFF_W1F, W1F_BYTES), srd2 = mk_srd(ws + (LAST ? OFF_W2O : OFF_W2F), W2F_BYTES);
    const u32 lds0 = (u32)(size_t)(LAS char*)smem;
    const rsrc_t rx = mk_rsrc(ws + (LAST ? OFF_XF3 : OFF_XF1), XT_BYTES);
    const int xs = (8 * pt + 2 * pq) * 20480 + KG * 1024, vo = lane * 16;
    auto xload = [&](int st, int pu) __attribute__((always_inline)) { return asbf(__builtin_amdgcn_raw_buffer_load_b128(rx, vo, xs + pu * 20480 + st * 2048, SC1)); };
    bf16x8 xb[P][2];
#pragma unroll
    for (int st = 0; st < P; ++st) { xb[st][0] = xload(st, 0); xb[st][1] = xload(st, 1); __builtin_amdgcn_sched_barrier(0); }
    f32x4 acc[NF][2];
#pragma unroll
    for (int i = 0; i < NF; ++i) { acc[i][0] = f32x4{0, 0, 0, 0}; acc[i][1] = f32x4{0, 0, 0, 0}; }
    f32x4 gr[2] = {f32x4{0, 0, 0, 0}, f32x4{0, 0, 0, 0}};
    bf16x8 wa[2][NF];
#pragma unroll
    for (int i = 0; i < NF; ++i) wa[0][i] = *(const bf16x8*)(smem + w1slot<LAST>(0) + KG * 1024 + lane * 16 + i * 2048);
    static_for<0, 10>([&](auto ST) __attribute__((always_inline)) {
        constexpr int st = decltype(ST)::value;
        if constexpr (LAST && st == 2) { wait_lgkm(); barrier(); }
        if constexpr (st == 4 || st == 7) {
            wait_lgkm();
            wait_vm<g1_younger(st, st == 7 ? 9 : 7, NLO)>();
            barrier();
        }
        bf16x8 nx[2];
        const char* rbase = smem + w1slot<LAST>(st + 1) + KG * 1024 + lane * 16;
        constexpr int NV = WR + 2, V0 = KG == 0 ? 0 : NF - NV;
        static_for<0, NF>([&](auto I) __attribute__((always_inline)) {
            constexpr int i = decltype(I)::value;
            __builtin_amdgcn_sched_barrier(0);
            acc[i][0] = mfma16(wa[st & 1][i], xb[st % P][0], acc[i][0]);
            if constexpr (st + 1 < 10) wa[(st + 1) & 1][i] = *(const bf16x8*)(rbase + i * 2048);
            acc[i][1] = mfma16(wa[st & 1][i], xb[st % P][1], acc[i][1]);
            if constexpr (i < 2) {
                u32x4 xv = __builtin_bit_cast(u32x4, xb[st % P][i]);
                if constexpr (st == 9 && KG == 1) { if (g == 0) xv[0] &= 0x0000ffffu; }
                gr[i] = mfma16(asbf(xv), asbf(xv), gr[i]);
            }
            __builtin_amdgcn_sched_barrier(0);
            constexpr int v = i - V0;
            if constexpr (v >= 0 && v < WR) { if constexpr (st + PW < 10) w1_piece<LAST, v>(srdW, lds0, w, vo, u0, st + PW); }
            else if constexpr (v >= WR && v < NV) { if constexpr (st + P < 10) nx[v - WR] = xload(st + P, v - WR); }
            if constexpr (st >= 6) {
                constexpr int sl = i - (KG == 0 ? 0 : NF - 4), e = 4 * (st - 6) + sl;
                if constexpr (sl >= 0 && sl < 4 && e < (LAST ? 9 : 15)) w2_issue1<e % 3>(srd2, lds0, w, vo, ct2, (LAST ? 2 : 0) + e / 3);
            }
        });
        __builtin_amdgcn_sched_barrier(0);
        if constexpr (st + P < 10) { xb[st % P][0] = nx[0]; xb[st % P][1] = nx[1]; }
        if (st == 4) STAMP(KID, 2);
    });
    STAMP(KID, 3);
    u32x2 bv[NF];
#pragma unroll
    for (int i = 0; i < NF; ++i) bv[i] = *(const u32x2*)(a.b1 + min(16 * (u0 + i), F_ - 16) + 4 * g);
    __builtin_amdgcn_sched_barrier(0);
    wait_lgkm();
    barrier();
    float* sx = (float*)(smem + 8 * NF * 512);
#pragma unroll
    for (int i = 0; i < NF; ++i) {
        const f32x4 t = acc[i][1 - KG];
        u32x2 pv = {pk2(t[0], t[1]), pk2(t[2], t[3])};
        *(u32x2*)(smem + ((w * NF + i) * 64 + lane) * 8) = pv;
    }
    float st2[2];
    {
        const int n = lane & 15;
#pragma unroll
        for (int pu = 0; pu < 2; ++pu) {
            const float d01 = (n & 1) ? gr[pu][1] : gr[pu][0], d23 = (n & 1) ? gr[pu][3] : gr[pu][2];
            float d = (n & 2) ? d23 : d01;
            d = (g == (n >> 2)) ? d : 0.f;
            d += __shfl_xor(d, 16); d += __shfl_xor(d, 32);
            st2[pu] = d;
        }
    }
    if (lane < 16) sx[w * 16 + lane] = st2[1 - KG];
    wait_lgkm();
    barrier();
    STAMP(KID, 5);
    const float v = (st2[KG] + sx[(w ^ 4) * 16 + (lane & 15)]) * (1.0f / 640.0f);
    const float r1 = __builtin_amdgcn_rsqf(v + EPS);
    const float rr = r1 * __builtin_amdgcn_rsqf(v * r1 * r1 + EPS);
    const int punit = 8 * pt + 2 * pq + KG;
    u32x2 ov[NF], pv[NF];
#pragma unroll
    for (int i = 0; i < NF; ++i) pv[i] = *(const u32x2*)(smem + (((w ^ 4) * NF + i) * 64 + lane) * 8);
    static_for<0, NF>([&](auto I) __attribute__((always_inline)) {
        constexpr int i = decltype(I)::value;
        if constexpr (LAST ? i < 2 : i == 0) {
            __builtin_amdgcn_sched_barrier(0);
            w2_issue(srd2, lds0, w, vo, ct2, LAST ? i : 5);
            __builtin_amdgcn_sched_barrier(0);
        }
        const f32x2 o0 = {acc[i][KG][0] + bflo(pv[i][0]), acc[i][KG][1] + bfhi(pv[i][0])}, o1 = {acc[i][KG][2] + bflo(pv[i][1]), acc[i][KG][3] + bfhi(pv[i][1])};
        const f32x2 rr2 = {rr, rr};
        const f32x2 b0 = {bflo(bv[i][0]), bfhi(bv[i][0])}, b1 = {bflo(bv[i][1]), bfhi(bv[i][1])};
        f32x2 h0 = gelu_tanh2(o0 * rr2 + b0), h1 = gelu_tanh2(o1 * rr2 + b1);
        if (LAST) {
            const bool lin = u0 + i >= 108;
            h0[0] = lin ? o0[0] : h0[0]; h0[1] = lin ? o0[1] : h0[1]; h1[0] = lin ? o1[0] : h1[0]; h1[1] = lin ? o1[1] : h1[1];
        }
        ov[i] = u32x2{pk2(h0[0], h0[1]), pk2(h1[0], h1[1])};
    });
    __builtin_amdgcn_sched_barrier(0);
    const rsrc_t rh = mk_rsrc(ws + (LAST ? OFF_H2 : OFF_H), H_BYTES), r3 = mk_rsrc(ws + OFF_X3, X3_BYTES);
    const int hb = punit * 54 * 1024;
    auto st1 = [&](int i) __attribute__((always_inline)) {
        const int u = u0 + i;
        if (i < nu) {
            if (!LAST || u < 108) __builtin_amdgcn_raw_buffer_store_b64(ov[i], rh, lane * 16 + (u & 1) * 8, hb + (u >> 1) * 1024, SC1);
            else __builtin_amdgcn_raw_buffer_store_b64(ov[i], r3, lane * 8, (punit * 40 + (u - 108)) * 512, SC1);
        }
    };
    auto st2p = [&](int i) __attribute__((always_inline)) {
        const int u = u0 + i;
        if (i + 1 < nu && (!LAST || u + 1 < 108)) {
            u32x4 q4 = {ov[i][0], ov[i][1], ov[i + 1][0], ov[i + 1][1]};
            __builtin_amdgcn_raw_buffer_store_b128(q4, rh, lane * 16, hb + (u >> 1) * 1024, SC1);
        } else { st1(i); st1(i + 1); }
    };
    if (u0 & 1) {
        st1(0);
#pragma unroll
        for (int i = 1; i + 1 < NF; i += 2) st2p(i);
        if ((NF & 1) == 0) st1(NF - 1);
    } else {
#pragma unroll
        for (int i = 0; i + 1 < NF; i += 2) st2p(i);
        if (NF & 1) st1(NF - 1);
    }
    STAMP(KID, 4);
}

template <bool LAST>
DI void g2_phase(const MainArgs& a, char* smem, int w, int lane, int pt1, int pt, int ct) {
    constexpr int PH = G2_PH, KID = LAST ? 6 : 3;
    char* ws = a.p.ws;
    u32* cnt = (u32*)(ws + OFF_CNT + (LAST ? 8192 : 0));
    const int pg = w & 1, kq = w >> 1, n = lane & 15, q = lane >> 4;
    const int kb = kq < 2 ? 14 * kq : 28 + 13 * (kq - 2), nk = kq < 2 ? 14 : 13;
    const u32x4 srd2 = mk_srd(ws + (LAST ? OFF_W2O : OFF_W2F), W2F_BYTES);
    const u32 lds0 = (u32)(size_t)(LAS char*)smem;
    const rsrc_t rh = mk_rsrc(ws + (LAST ? OFF_H2 : OFF_H), H_BYTES), rw = mk_rsrc(ws + (LAST ? OFF_W2O : OFF_W2F), W2F_BYTES);
    const int vo = lane * 16, hs = ((4 * pt + 2 * pg) * 54 + kb) * 1024;
    bf16x8 wr[6][5];
    asm volatile("" ::: "memory");
    __builtin_amdgcn_sched_barrier(0);
#pragma unroll
    for (int t = 0; t < 3; ++t)
#pragma unroll
        for (int i = 0; i < 5; ++i) wr[t][i] = asbf(__builtin_amdgcn_raw_buffer_load_b128(rw, vo, ((5 * ct + i) * 54 + kb + 8 + t) * 1024, SC1));
    __builtin_amdgcn_sched_barrier(0);
    wait_lgkm();
    wait_vm<15>();
    barrier();
    if (threadIdx.x == 0) add_agent(cnt + 64 * pt1);
    STAMP(LAST ? 5 : 2, 15);
#pragma unroll
    for (int t = LAST ? 5 : 6; t < 8; ++t) w2_issue(srd2, lds0, w, vo, ct, t);
    __builtin_amdgcn_sched_barrier(0);
#pragma unroll
    for (int t = 3; t < 6; ++t)
#pragma unroll
        for (int i = 0; i < 5; ++i) wr[t][i] = asbf(__builtin_amdgcn_raw_buffer_load_b128(rw, vo, ((5 * ct + i) * 54 + kb + 8 + t) * 1024, SC1));
    __builtin_amdgcn_sched_barrier(0);
    if (w == 0) poll_until(cnt + 64 * (pt >> 1), 16u, ws, lane);
    wait_vm<0>();
    barrier();
    STAMP(KID, 5);
    bf16x8 hb[PH][2];
    auto hload = [&](int t, int pu) __attribute__((always_inline)) {
        const int k = t < 13 ? t : nk - 1;
        return asbf(__builtin_amdgcn_raw_buffer_load_b128(rh, vo, hs + (pu * 54 + k) * 1024, SC1));
    };
#pragma unroll
    for (int t = 0; t < PH; ++t) { hb[t][0] = hload(t, 0); hb[t][1] = hload(t, 1); __builtin_amdgcn_sched_barrier(0); }
    u32x2 xv[3], bv[3]; float bb[3];
    f32x4 acc[2][5];
#pragma unroll
    for (int i = 0; i < 5; ++i) { acc[0][i] = f32x4{0, 0, 0, 0}; acc[1][i] = f32x4{0, 0, 0, 0}; }
    bf16x8 wa[2][5];
    const char* wbase = smem + kq * 5120 + lane * 16;
#pragma unroll
    for (int i = 0; i < 5; ++i) wa[0][i] = *(const bf16x8*)(wbase + w2slot(0) + i * 1024);
    static_for<0, 14>([&](auto T) __attribute__((always_inline)) {
        constexpr int t = decltype(T)::value;
        if constexpr (t == 10) {
            __builtin_amdgcn_sched_barrier(0);
#pragma unroll
            for (int j = 0; j < 3; ++j) {
                const int tt = min(kq + 4 * j, 9), pu = 2 * pg + tt / 5, cu = tt % 5;
                if (!LAST) {
                    const int c = 80 * ct + 16 * cu + n;
                    const int off = (((4 * pt + pu) * 20 + (c >> 5)) * 64 + ((c & 31) >> 3) * 16 + 4 * q) * 16 + (c & 7) * 2;
                    const rsrc_t r1 = mk_rsrc(ws + OFF_XF1, XT_BYTES);
                    const u32 e0 = __builtin_amdgcn_raw_buffer_load_b16(r1, off, 0, SC1), e1 = __builtin_amdgcn_raw_buffer_load_b16(r1, off + 16, 0, SC1);
                    const u32 e2 = __builtin_amdgcn_raw_buffer_load_b16(r1, off + 32, 0, SC1), e3 = __builtin_amdgcn_raw_buffer_load_b16(r1, off + 48, 0, SC1);
                    xv[j] = u32x2{e0 | (e1 << 16), e2 | (e3 << 16)};
                    bb[j] = ((const float*)(ws + OFF_B2P))[c];
                } else {
                    xv[j] = __builtin_amdgcn_raw_buffer_load_b64(mk_rsrc(ws + OFF_X3, X3_BYTES), lane * 8, ((4 * pt + pu) * 40 + 5 * ct + cu) * 512, SC1);
                    bv[j] = *(const u32x2*)(a.p.b2 + 80 * ct + 16 * cu + 4 * q);
                }
            }
        }
        bf16x8 nh[2];
        auto step = [&]() __attribute__((always_inline)) {
            static_for<0, 5>([&](auto I) __attribute__((always_inline)) {
                constexpr int i = decltype(I)::value;
                __builtin_amdgcn_sched_barrier(0);
                bf16x8 wv;
                if constexpr (t < 8) wv = wa[t & 1][i]; else wv = wr[t - 8][i];
                acc[0][i] = LAST ? mfma16(wv, hb[t % PH][0], acc[0][i]) : mfma16(hb[t % PH][0], wv, acc[0][i]);
                if constexpr (t + 1 < 8) wa[(t + 1) & 1][i] = *(const bf16x8*)(wbase + w2slot(t + 1) + i * 1024);
                acc[1][i] = LAST ? mfma16(wv, hb[t % PH][1], acc[1][i]) : mfma16(hb[t % PH][1], wv, acc[1][i]);
                __builtin_amdgcn_sched_barrier(0);
                if constexpr (t + PH < 14 && (i == 1 || i == 3)) nh[i >> 1] = hload(t + PH, i >> 1);
            });
        };
        if constexpr (t < 13) step(); else { if (nk == 14) step(); }
        __builtin_amdgcn_sched_barrier(0);
        if constexpr (t + PH < 14) { hb[t % PH][0] = nh[0]; hb[t % PH][1] = nh[1]; }
        if (t == 0) STAMP(KID, 1);
        if (t == 7) STAMP(KID, 2);
    });
    STAMP(KID, 3);
    wait_lgkm();
    barrier();
#pragma unroll
    for (int t = 0; t < 10; ++t)
        if ((t & 3) != kq) *(f32x4*)(smem + ((w * 10 + t) * 64 + lane) * 16) = acc[t / 5][t % 5];
    wait_lgkm();
    barrier();
    float ssp[2][4];
#pragma unroll
    for (int r = 0; r < 4; ++r) { ssp[0][r] = 0.f; ssp[1][r] = 0.f; }
    const rsrc_t ro = mk_rsrc(ws + OFF_XT2, XT_BYTES);
    auto finish = [&](auto KQ) __attribute__((always_inline)) {
        constexpr int kqc = decltype(KQ)::value;
        static_for<0, 3>([&](auto J) __attribute__((always_inline)) {
            constexpr int j = decltype(J)::value, t = kqc + 4 * j;
            if constexpr (t < 10) {
                constexpr int pl = t / 5, cu = t % 5;
                f32x4 o = acc[pl][cu];
#pragma unroll
                for (int s = 1; s < 4; ++s) o += *(const f32x4*)(smem + (((pg + 2 * ((kqc + s) & 3)) * 10 + t) * 64 + lane) * 16);
                const int pu = 2 * pg + pl;
                if (!LAST) {
                    const int c = 80 * ct + 16 * cu + n, p = 64 * pt + 16 * pu + 4 * q;
                    const u32 o0 = pk2(bflo(xv[j][0]) + o[0] + bb[j], bfhi(xv[j][0]) + o[1] + bb[j]);
                    const u32 o1 = pk2(bflo(xv[j][1]) + o[2] + bb[j], bfhi(xv[j][1]) + o[3] + bb[j]);
                    u32x2 ov = {o0, o1};
                    __builtin_amdgcn_raw_buffer_store_b64(ov, ro, (c * S_ + p) * 2, 0, SC1);
                    const float m = c == 609 ? 0.f : 1.f;
                    ssp[pl][0] += m * bflo(o0) * bflo(o0); ssp[pl][1] += m * bfhi(o0) * bfhi(o0);
                    ssp[pl][2] += m * bflo(o1) * bflo(o1); ssp[pl][3] += m * bfhi(o1) * bfhi(o1);
                } else {
                    const int p = 16 * (4 * pt + pu) + n, d = 80 * ct + 16 * cu + 4 * q;
                    u32x2 ov = {pk2(bflo(xv[j][0]) + o[0] + bflo(bv[j][0]), bfhi(xv[j][0]) + o[1] + bfhi(bv[j][0])),
                                pk2(bflo(xv[j][1]) + o[2] + bflo(bv[j][1]), bfhi(xv[j][1]) + o[3] + bfhi(bv[j][1]))};
                    *(u32x2*)(a.out + (size_t)tok_of_p(p) * D_ + d) = ov;
                }
            }
        });
    };
    if (kq == 0) finish(std::integral_constant<int, 0>{});
    else if (kq == 1) finish(std::integral_constant<int, 1>{});
    else if (kq == 2) finish(std::integral_constant<int, 2>{});
    else finish(std::integral_constant<int, 3>{});
    if (!LAST) {
        float* sp = (float*)smem;
#pragma unroll
        for (int pl = 0; pl < 2; ++pl)
#pragma unroll
            for (int r = 0; r < 4; ++r) {
                const float v = row_sum15(ssp[pl][r]);
                if (n == 15) sp[w * 32 + 16 * pl + 4 * q + r] = v;
            }
        wait_lgkm();
        barrier();
        if (w == 0) {
            const int g2 = lane >> 5, r = lane & 31;
            const float v = (sp[g2 * 32 + r] + sp[(g2 + 2) * 32 + r]) + (sp[(g2 + 4) * 32 + r] + sp[(g2 + 6) * 32 + r]);
            __hip_atomic_fetch_add((__attribute__((address_space(1))) u64*)(ws + OFF_SS2) + 64 * pt + lane, ss_from_float(v), __ATOMIC_RELAXED, __HIP_MEMORY_SCOPE_AGENT);
        }
        wait_lgkm();
        wait_vm<0>();
        barrier();
        signal_all(ws, 1);
    }
}

template <bool LAST>
DI void mlp(const MainArgs& a, char* smem, int w, int lane, int u0) {
    constexpr int NF = LAST ? 10 : 7;
    const int x = blockIdx.x & 7, ib = blockIdx.x >> 3;
    const int pt = 4 * (x >> 1) + (ib & 3), ft = 8 * (x & 1) + (ib >> 2);
    const int pt2 = 8 * (x >> 1) + (ib & 7), ct2 = 4 * (x & 1) + (ib >> 3);
    const int nu = LAST ? (ft < 4 ? 10 : 9) : (ft < 12 ? 7 : 6);
    if (LAST) poll_all(a.p.ws, 2, 20u, w, lane);
    else if (w == 0) {
        const int sh = lane & 7, wh = (lane >> 3) & 1;
        poll_until((const u32*)(a.p.ws + OFF_CNT + 16384 + (wh ? 3 : 0) * 8192) + ((blockIdx.x >> 3) & 7) * 256 + sh * 32, wh ? (sh < 6 ? 12u : 11u) : 20u, a.p.ws, lane);
    }
    wait_vm<0>();
    barrier();
    STAMP(LAST ? 5 : 2, 1);
    if (w < 4) g1_phase<NF, LAST, 0>(a, smem, w, lane, pt, u0, nu, ct2);
    else g1_phase<NF, LAST, 1>(a, smem, w, lane, pt, u0, nu, ct2);
    g2_phase<LAST>(a, smem, w, lane, pt, pt2, ct2);
}

__global__ __launch_bounds__(512, 2) void k_main(MainArgs a) {
    extern __shared__ __attribute__((aligned(16))) char smem[];
    const int lane = threadIdx.x & 63, b = blockIdx.x;
    const int w = __builtin_amdgcn_readfirstlane(threadIdx.x >> 6);
    const int ft = 8 * (b & 1) + (b >> 5);
    const int u0a = ft < 12 ? 7 * ft : 84 + 6 * (ft - 12), u0b = ft < 4 ? 10 * ft : 40 + 9 * (ft - 4);
    if (b < 160) col_task<0>(a.p.ws, b, smem, u0a);
    else {
        w1_prefetch<false>(a.p.ws, smem, w, lane, u0a);
        if (b < 254) {
            if (b < 214) task_w2(a.p, b - 160, smem); else task_Q(a.p, b - 214);
            wait_lgkm();
            wait_vm<0>();
            barrier();
            signal_all(a.p.ws, 3);
        }
    }
    mlp<false>(a, smem, w, lane, u0a);
    END_STAMP(3);
    if (b < 160) col_task<1>(a.p.ws, b, smem, u0b); else w1_prefetch<true>(a.p.ws, smem, w, lane, u0b);
    mlp<true>(a, smem, w, lane, u0b);
    END_STAMP(6);
}

extern "C" void kernel_launch(void* const* d_in, const int* in_sizes, int n_in, void* d_out, int out_size, void* d_ws,
                              size_t ws_size, hipStream_t stream) {
    char* ws = (char*)d_ws;
    constexpr int MAIN_LDS = 163840;
    (void)hipFuncSetAttribute((const void*)k_main, hipFuncAttributeMaxDynamicSharedMemorySize, MAIN_LDS);
    PrepArgs pa{(const u16*)d_in[0], (const u16*)d_in[1], (const u16*)d_in[2], (const u16*)d_in[4], (const u16*)d_in[6], (const u16*)d_in[7], ws};
    k0_prep<<<NB_K0, 512, PREP_LDS, stream>>>(pa);
    MainArgs ma{pa, (const u16*)d_in[5], (u16*)d_out};
    k_main<<<256, 512, MAIN_LDS, stream>>>(ma);
}
```

```cpp
#include <hip/hip_runtime.h>
#include <hip/hip_bf16.h>
#include <stdint.h>
#include <type_traits>

typedef unsigned short u16;
typedef unsigned int u32;
typedef unsigned long long u64;
typedef short s16x4 __attribute__((ext_vector_type(4)));
typedef __bf16 bf16x8 __attribute__((ext_vector_type(8)));
typedef __bf16 bf16x2 __attribute__((ext_vector_type(2)));
typedef float f32x4 __attribute__((ext_vector_type(4)));
typedef float f32x2 __attribute__((ext_vector_type(2)));
typedef u32 u32x4 __attribute__((ext_vector_type(4)));
typedef u32 u32x2 __attribute__((ext_vector_type(2)));
typedef __amdgpu_buffer_rsrc_t rsrc_t;
#define LAS __attribute__((address_space(3)))
#define DI __device__ __forceinline__

constexpr int S_ = 2048, D_ = 640, F_ = 1728, DF_ = 321;
constexpr float EPS = 1e-5f;
constexpr int SC1 = 16;
constexpr float SS_SCALE = 1048576.0f;

constexpr size_t XT_BYTES = (size_t)D_ * S_ * 2;
constexpr size_t OFF_XT0 = 0;
constexpr size_t OFF_XT1 = OFF_XT0 + XT_BYTES;
constexpr size_t OFF_XT2 = OFF_XT1 + XT_BYTES;
constexpr size_t OFF_XT3 = OFF_XT2 + XT_BYTES;
constexpr size_t OFF_WT = OFF_XT3 + XT_BYTES;
constexpr size_t OFF_W1F = OFF_WT + (size_t)320 * 2048 * 4;
constexpr size_t W1F_BYTES = (size_t)148 * 20 * 1024;
constexpr size_t OFF_W2F = OFF_W1F + W1F_BYTES;
constexpr size_t W2F_BYTES = (size_t)40 * 54 * 1024;
constexpr size_t OFF_W2O = OFF_W2F + W2F_BYTES;
constexpr size_t OFF_H = OFF_W2O + W2F_BYTES;
constexpr size_t H_BYTES = (size_t)128 * 54 * 1024;
constexpr size_t OFF_H2 = OFF_H;
constexpr size_t OFF_X3 = OFF_H + 2 * H_BYTES;
constexpr size_t OFF_SS0 = OFF_X3 + (size_t)128 * 40 * 512;
constexpr size_t OFF_SS2 = OFF_SS0 + 2048 * 8;
constexpr size_t OFF_B2P = OFF_SS2 + 2048 * 8;
constexpr size_t OFF_XF1 = OFF_B2P + 4096;
constexpr size_t OFF_XF3 = OFF_XF1;
constexpr size_t X3_BYTES = (size_t)128 * 40 * 512;
constexpr size_t OFF_CNT = OFF_XF1 + 2 * XT_BYTES;
constexpr size_t WS_END = OFF_CNT + 65536;

#define STAMP(k, i)
#define CSTAMP(k, i)
#define END_STAMP(k)

DI f32x4 mfma16(bf16x8 a, bf16x8 b, f32x4 c) { return __builtin_amdgcn_mfma_f32_16x16x32_bf16(a, b, c, 0, 0, 0); }
DI u32 pk2(float a, float b) { f32x2 v = {a, b}; const bf16x2 r = __builtin_convertvector(v, bf16x2); return __builtin_bit_cast(u32, r); }
DI bf16x8 pk8(float a0, float a1, float a2, float a3, float a4, float a5, float a6, float a7) {
    u32x4 r = {pk2(a0, a1), pk2(a2, a3), pk2(a4, a5), pk2(a6, a7)};
    return __builtin_bit_cast(bf16x8, r);
}
DI bf16x8 pk8(const float (&a)[8]) { return pk8(a[0], a[1], a[2], a[3], a[4], a[5], a[6], a[7]); }
DI bf16x8 asbf(u32x4 r) { return __builtin_bit_cast(bf16x8, r); }
DI bf16x8 neg8(bf16x8 a) { u32x4 r = __builtin_bit_cast(u32x4, a); r ^= 0x80008000u; return __builtin_bit_cast(bf16x8, r); }
DI float bflo(u32 v) { return __builtin_bit_cast(float, v << 16); }
DI float bfhi(u32 v) { return __builtin_bit_cast(float, v & 0xffff0000u); }
DI float cosr(float rev) { return __builtin_amdgcn_cosf(rev); }
DI float sinr(float rev) { return __builtin_amdgcn_sinf(rev); }
DI int slot32(int q, int j) { return j < 4 ? 4 * q + j : 16 + 4 * q + (j - 4); }
DI s16x4 tr_read(const char* p) { return __builtin_amdgcn_ds_read_tr16_b64_v4i16((LAS s16x4*)p); }
DI bf16x8 cat8(s16x4 lo, s16x4 hi) { return __builtin_bit_cast(bf16x8, __builtin_shufflevector(lo, hi, 0, 1, 2, 3, 4, 5, 6, 7)); }
DI int tok_of_p(int p) { return 32 * (p & 63) + (p >> 6); }
DI rsrc_t mk_rsrc(const void* p, size_t bytes) { return __builtin_amdgcn_make_buffer_rsrc((void*)p, 0, (int)bytes, 0x00020000); }
DI u32x4 bload16(rsrc_t r, int voff, int soff) { return __builtin_amdgcn_raw_buffer_load_b128(r, voff, soff, 0); }
DI float ss_to_float(u64 v) { return (float)(u32)(v >> 32) * 4294967296.0f + (float)(u32)v; }
DI u64 ss_from_float(float s) { return (u64)(fminf(s * SS_SCALE, 1.0e18f) + 0.5f); }
DI void pin(float& x) { asm volatile("" : "+v"(x)); }
DI void pin(bf16x8& x) { asm volatile("" : "+v"(x)); }

struct F32c { bf16x8 c[2], sp[2]; };
DI void f32c_init(F32c& k, int lane) {
    const float m = (float)(lane & 15), q4 = (float)(4 * (lane >> 4));
    float c[4], s[4];
#pragma unroll
    for (int j = 0; j < 4; ++j) { const float a = __builtin_amdgcn_fractf(m * (q4 + (float)j) * (1.0f / 32.0f)); c[j] = cosr(a); s[j] = sinr(a); }
    const u32 sg = (lane & 1) ? 0x80008000u : 0u;
    const u32 c0 = pk2(c[0], c[1]), c1 = pk2(c[2], c[3]), s0 = pk2(s[0], s[1]), s1 = pk2(s[2], s[3]);
    const u32x4 cv = {c0, c1, c0 ^ sg, c1 ^ sg}, sv = {s0, s1, s0 ^ sg, s1 ^ sg};
    k.c[0] = asbf(cv); k.sp[0] = asbf(sv);
    k.c[1] = asbf(cv ^ 0x80000000u); k.sp[1] = asbf(sv ^ 0x80000000u);
}

struct RowC { bf16x8 c[2], sp[2], sm[2], c20, s20m; f32x4 twr[2], twi[2]; int off0, off1; };
constexpr int ROWC_LDS = 10 * 1024;
DI void rowc_build(char* tab, int w, int lane) {
    const int n = lane & 15, q = lane >> 4;
    const float nf = (float)n;
    u32x4* t = (u32x4*)tab + lane;
    if (w == 0) {
        F32c f; f32c_init(f, lane);
        t[0] = __builtin_bit_cast(u32x4, f.c[0]); t[64] = __builtin_bit_cast(u32x4, f.c[1]);
        t[128] = __builtin_bit_cast(u32x4, f.sp[0]); t[192] = __builtin_bit_cast(u32x4, f.sp[1]);
    } else if (w <= 2) {
        float c[8];
#pragma unroll
        for (int j = 0; j < 8; ++j) {
            const int d1 = 8 * q + j;
            const float a = __builtin_amdgcn_fractf((float)d1 * nf * 0.05f);
            const float v = w == 1 ? cosr(a) : -sinr(a);
            c[j] = (d1 < 20 && n <= 10) ? v : 0.f;
        }
        t[(3 + w) * 64] = __builtin_bit_cast(u32x4, pk8(c));
    } else if (w <= 4) {
        const float sc = 0.05590169943749474f;
        f32x4 cr, ci;
#pragma unroll
        for (int i = 0; i < 4; ++i) {
            const float a = (float)(16 * (w - 3) + 4 * q + i) * nf * (1.0f / 640.0f);
            cr[i] = cosr(a) * sc; ci[i] = -sinr(a) * sc;
        }
        t[(6 + w - 3) * 64] = __builtin_bit_cast(u32x4, cr); t[(8 + w - 3) * 64] = __builtin_bit_cast(u32x4, ci);
    }
}
DI void rowc_load(RowC& k, const char* tab, int lane) {
    const u32x4* tb = (const u32x4*)tab + lane;
    k.c[0] = asbf(tb[0]); k.c[1] = asbf(tb[64]); k.sp[0] = asbf(tb[128]); k.sp[1] = asbf(tb[192]);
    k.c20 = asbf(tb[256]); k.s20m = asbf(tb[320]);
    k.twr[0] = __builtin_bit_cast(f32x4, tb[384]); k.twr[1] = __builtin_bit_cast(f32x4, tb[448]);
    k.twi[0] = __builtin_bit_cast(f32x4, tb[512]); k.twi[1] = __builtin_bit_cast(f32x4, tb[576]);
    k.sm[0] = neg8(k.sp[0]); k.sm[1] = neg8(k.sp[1]);
    const int q = lane >> 4, qq = (lane & 15) >> 2, pp = lane & 3;
    const int r0 = min(8 * q + qq, 19), r1 = min(8 * q + 4 + qq, 19);
    k.off0 = r0 * 64 + pp * 8; k.off1 = r1 * 64 + pp * 8;
}
DI float wave_sum63(float v) {
    v += __builtin_bit_cast(float, __builtin_amdgcn_update_dpp(0, __builtin_bit_cast(int, v), 0x111, 0xf, 0xf, true));
    v += __builtin_bit_cast(float, __builtin_amdgcn_update_dpp(0, __builtin_bit_cast(int, v), 0x112, 0xf, 0xf, true));
    v += __builtin_bit_cast(float, __builtin_amdgcn_update_dpp(0, __builtin_bit_cast(int, v), 0x114, 0xf, 0xf, true));
    v += __builtin_bit_cast(float, __builtin_amdgcn_update_dpp(0, __builtin_bit_cast(int, v), 0x118, 0xf, 0xf, true));
    v += __builtin_bit_cast(float, __builtin_amdgcn_update_dpp(0, __builtin_bit_cast(int, v), 0x142, 0xa, 0xf, true));
    v += __builtin_bit_cast(float, __builtin_amdgcn_update_dpp(0, __builtin_bit_cast(int, v), 0x143, 0xc, 0xf, true));
    return v;
}
DI void rowfft(const RowC& k, const char* row, float (&A)[8], float (&B)[8], int lane) {
    f32x4 z = {0.f, 0.f, 0.f, 0.f};
    f32x4 yr[2], yi[2];
#pragma unroll
    for (int mt = 0; mt < 2; ++mt) {
        const bf16x8 a = cat8(tr_read(row + k.off0 + mt * 32), tr_read(row + k.off1 + mt * 32));
        yr[mt] = mfma16(a, k.c20, z); yi[mt] = mfma16(a, k.s20m, z);
    }
    float tr[8], ti[8];
#pragma unroll
    for (int i = 0; i < 8; ++i) {
        const float a = yr[i >> 2][i & 3], b = yi[i >> 2][i & 3], cr = k.twr[i >> 2][i & 3], ci = k.twi[i >> 2][i & 3];
        tr[i] = a * cr - b * ci; ti[i] = a * ci + b * cr;
    }
    const bf16x8 Tr = pk8(tr), Ti = pk8(ti);
    f32x4 ore[2], oim[2];
#pragma unroll
    for (int t = 0; t < 2; ++t) {
        ore[t] = mfma16(k.c[t], Tr, z); ore[t] = mfma16(k.sp[t], Ti, ore[t]);
        oim[t] = mfma16(k.sm[t], Tr, z); oim[t] = mfma16(k.c[t], Ti, oim[t]);
    }
#pragma unroll
    for (int r = 0; r < 4; ++r) { A[2 * r] = ore[0][r]; A[2 * r + 1] = oim[0][r]; B[2 * r] = ore[1][r]; B[2 * r + 1] = oim[1][r]; }
    if (lane == 0) { const float h = 0.7071067811865476f; const float dc = A[0] * h; A[0] = B[0] * h; A[1] = dc; }
}
DI int sw_off(int c) { const int ch = c >> 3; return ((ch ^ ((ch >> 3) & 7)) << 4) + (c & 7) * 2; }
DI int chunk_c0(int n, int q) { return (n == 0 ? 608 : n == 10 ? 576 : 64 * (n - 1)) + 8 * q; }

constexpr int ROWST = 1344;
constexpr int PREP_TAB = 640 * 32 + 16 * ROWST;
constexpr int PREP_LDS = PREP_TAB + ROWC_LDS;

struct PrepArgs { const u16 *x, *wr, *wi, *w1, *w2, *b2; char* ws; };

DI void task_x(const PrepArgs& a, int b, char* smem) {
    const int tid = threadIdx.x, lane = tid & 63, w = tid >> 6;
    const int p0 = 16 * ((b & 7) * 16 + (b >> 3));
    u32x4 v[3];
#pragma unroll
    for (int i = 0; i < 3; ++i) {
        const int id = min(tid + 512 * i, 1279), r = id / 80, ch = id % 80;
        v[i] = *(const u32x4*)(a.x + (size_t)tok_of_p(p0 + r) * D_ + ch * 8);
    }
    __builtin_amdgcn_sched_barrier(0);
    rowc_build(smem + PREP_TAB, w, lane);
    __builtin_amdgcn_sched_barrier(0);
    STAMP(0, 1);
    if (tid < 16) ((u64*)(a.ws + OFF_SS2))[p0 + tid] = 0ull;
    if (b < 32) ((u32*)(a.ws + OFF_CNT))[b * 512 + tid] = 0u;
#pragma unroll
    for (int i = 0; i < 3; ++i) {
        const int id = min(tid + 512 * i, 1279), r = id / 80, ch = id % 80;
        *(u32x4*)(smem + r * ROWST + ch * 16) = v[i];
    }
    STAMP(0, 2);
    __syncthreads();
    STAMP(0, 3);
    RowC k; rowc_load(k, smem + PREP_TAB, lane);
    const int n = lane & 15, q = lane >> 4, c0 = chunk_c0(n, q);
    float A[2][8], B[2][8];
#pragma unroll
    for (int i = 0; i < 2; ++i) rowfft(k, smem + (2 * w + i) * ROWST, A[i], B[i], lane);
#pragma unroll
    for (int i = 0; i < 2; ++i) {
        const int r = 2 * w + i;
        float ss = 0.f;
        if (n <= 10) {
#pragma unroll
            for (int e = 0; e < 8; ++e) ss += (lane == 0 && e == 1) ? 0.f : A[i][e] * A[i][e];
            *(bf16x8*)(smem + r * ROWST + sw_off(c0)) = pk8(A[i]);
        }
        if (n >= 1 && n <= 9) {
#pragma unroll
            for (int e = 0; e < 8; ++e) ss += B[i][e] * B[i][e];
            *(bf16x8*)(smem + r * ROWST + sw_off(c0 + 32)) = pk8(B[i]);
        }
        ss = wave_sum63(ss);
        if (lane == 63) ((u64*)(a.ws + OFF_SS0))[p0 + r] = ss_from_float(ss);
    }
    STAMP(0, 4);
    __syncthreads();
    STAMP(0, 5);
    u16* xt = (u16*)(a.ws + OFF_XT0);
    const int g = lane >> 4, i16 = lane & 15, qq = i16 >> 2, pp = i16 & 3;
#pragma unroll
    for (int i = 0; i < 3; ++i) {
        const int cbk = w + 8 * i;
        if (cbk < 20) {
            const int cb = 32 * cbk + 16 * (g & 1), ph = g >> 1;
            const char* base = smem + (8 * ph + qq) * ROWST + sw_off(cb + 4 * pp);
            const bf16x8 o = cat8(tr_read(base), tr_read(base + 4 * ROWST));
            *(bf16x8*)(xt + (size_t)(cb + i16) * S_ + p0 + 8 * ph) = o;
        }
    }
}

DI void task_W(const PrepArgs& a, int b) {
    const int lane = threadIdx.x & 63, wv = threadIdx.x >> 6, wt = b * 2 + (wv >> 2), kq = wv & 3;
    const int k1 = wt / 5, cg = wt % 5, kp = 1 + 64 * cg + lane;
    const int r = kp % 20, t = kp / 20;
    int j; bool mir = false;
    if (kp == 320) j = 304;
    else if (r == 0) j = 304 + t;
    else if (r < 10) j = 32 * (r - 1) + t;
    else if (r == 10) j = 288 + t;
    else { j = 32 * (19 - r) + 31 - t; mir = true; }
    const rsrc_t rr = mk_rsrc(a.wr, (size_t)S_ * DF_ * 2), ri = mk_rsrc(a.wi, (size_t)S_ * DF_ * 2);
    const int st = (mir ? -64 : 64) * DF_ * 2, off0 = ((mir ? 2048 - k1 : k1) * DF_ + kp) * 2 + 8 * kq * st;
    u32 re[8], im[8];
#pragma unroll
    for (int k2 = 0; k2 < 8; ++k2) {
        int off = off0 + k2 * st;
        if (k2 == 0 && mir && k1 == 0 && kq == 0) off = kp * 2;
        re[k2] = __builtin_amdgcn_raw_buffer_load_b16(rr, off, 0, 0); im[k2] = __builtin_amdgcn_raw_buffer_load_b16(ri, off, 0, 0);
    }
    const u32 sg = mir ? 0x80000000u : 0u;
    u32x4* dst = (u32x4*)(a.ws + OFF_WT + ((size_t)j * 64 + k1) * 128 + kq * 32);
#pragma unroll
    for (int i = 0; i < 2; ++i) {
        u32x4 o;
#pragma unroll
        for (int e = 0; e < 4; ++e) o[e] = (re[4 * i + e] | (im[4 * i + e] << 16)) ^ sg;
        dst[i] = o;
    }
}

DI void task_w1(const PrepArgs& a, int b, char* smem) {
    const int tid = threadIdx.x, lane = tid & 63, w = tid >> 6;
    char* raw = smem;
    char* tr = smem + 640 * 32;
    char* out = raw;
    u32x4 v[3];
#pragma unroll
    for (int i = 0; i < 3; ++i) {
        const int id = min(tid + 512 * i, 1279), d = id >> 1, h = id & 1;
        v[i] = *(const u32x4*)(a.w1 + (size_t)d * F_ + 16 * b + 8 * h);
    }
    __builtin_amdgcn_sched_barrier(0);
    rowc_build(smem + PREP_TAB, w, lane);
    __builtin_amdgcn_sched_barrier(0);
#pragma unroll
    for (int i = 0; i < 3; ++i) *(u32x4*)(raw + min(tid + 512 * i, 1279) * 16) = v[i];
    __syncthreads();
    const int g = lane >> 4, i16 = lane & 15, qq = i16 >> 2, pp = i16 & 3;
#pragma unroll
    for (int i = 0; i < 5; ++i) {
        const int d0 = 16 * (w + 8 * i) + 4 * g;
        const s16x4 t = tr_read(raw + (d0 + qq) * 32 + pp * 8);
        *(s16x4*)(tr + i16 * ROWST + d0 * 2) = t;
    }
    RowC k; rowc_load(k, smem + PREP_TAB, lane);
    __syncthreads();
    const int n = lane & 15, q = lane >> 4, c0 = chunk_c0(n, q);
    float A[2][8], B[2][8];
#pragma unroll
    for (int i = 0; i < 2; ++i) rowfft(k, tr + (2 * w + i) * ROWST, A[i], B[i], lane);
#pragma unroll
    for (int i = 0; i < 2; ++i) {
        const int f = 2 * w + i;
        if (lane == 0) A[i][1] = 0.f;
        if (n <= 10) *(bf16x8*)(out + ((c0 >> 5) * 64 + q * 16 + f) * 16) = pk8(A[i]);
        if (n >= 1 && n <= 9) *(bf16x8*)(out + (((c0 >> 5) + 1) * 64 + q * 16 + f) * 16) = pk8(B[i]);
    }
    __syncthreads();
    u32x4* dst = (u32x4*)(a.ws + OFF_W1F + (size_t)b * 20 * 1024);
#pragma unroll
    for (int i = 0; i < 3; ++i) { const int id = tid + 512 * i; if (id < 1280) dst[id] = *(const u32x4*)(out + id * 16); }
}

template <bool SW> DI void w2_frags(const char* img, char* dstbase, int b, int lane, int w) {
    const int g = lane >> 4, i16 = lane & 15, qq = i16 >> 2, pp = i16 & 3;
    const rsrc_t rd = mk_rsrc(dstbase, W2F_BYTES);
#pragma unroll
    for (int i = 0; i < 5; ++i) {
        const int cu = w + 8 * i, c = 16 * cu + 4 * pp;
        const char* base = img + (4 * g + qq) * ROWST + (SW ? sw_off(c) : c * 2);
        const bf16x8 v = cat8(tr_read(base), tr_read(base + 16 * ROWST));
        __builtin_amdgcn_raw_buffer_store_b128(__builtin_bit_cast(u32x4, v), rd, lane * 16, (cu * 54 + b) * 1024, SC1);
    }
}
constexpr int W2_TAB = 32 * ROWST;
DI void task_w2(const PrepArgs& a, int b, char* smem) {
    const int tid = threadIdx.x, lane = tid & 63, w = tid >> 6;
    u32x4 v[5];
#pragma unroll
    for (int i = 0; i < 5; ++i) {
        const int id = tid + 512 * i, r = id / 80, ch = id % 80;
        v[i] = *(const u32x4*)(a.w2 + (size_t)(32 * b + r) * D_ + ch * 8);
    }
    __builtin_amdgcn_sched_barrier(0);
    rowc_build(smem + W2_TAB, w, lane);
    __builtin_amdgcn_sched_barrier(0);
#pragma unroll
    for (int i = 0; i < 5; ++i) { const int id = tid + 512 * i, r = id / 80, ch = id % 80; *(u32x4*)(smem + r * ROWST + ch * 16) = v[i]; }
    __syncthreads();
    RowC k; rowc_load(k, smem + W2_TAB, lane);
    w2_frags<false>(smem, a.ws + OFF_W2O, b, lane, w);
    const int n = lane & 15, q = lane >> 4, c0 = chunk_c0(n, q);
    u32x4 A8[4], B8[4];
#pragma unroll
    for (int i = 0; i < 4; ++i) {
        float A[8], B[8];
        rowfft(k, smem + (4 * w + i) * ROWST, A, B, lane);
        A8[i] = __builtin_bit_cast(u32x4, pk8(A)); B8[i] = __builtin_bit_cast(u32x4, pk8(B));
    }
    __syncthreads();
#pragma unroll
    for (int i = 0; i < 4; ++i) {
        const int r = 4 * w + i;
        if (n <= 10) *(u32x4*)(smem + r * ROWST + sw_off(c0)) = A8[i];
        if (n >= 1 && n <= 9) *(u32x4*)(smem + r * ROWST + sw_off(c0 + 32)) = B8[i];
    }
    __syncthreads();
    w2_frags<true>(smem, a.ws + OFF_W2F, b, lane, w);
}

DI int kcol_of(int jc) { return jc < 288 ? (jc >> 5) + 1 + 20 * (jc & 31) : jc < 304 ? 10 + 20 * (jc - 288) : 20 * (jc - 304); }
DI void task_Q(const PrepArgs& a, int u) {
    const int tid = threadIdx.x;
#pragma unroll
    for (int ii = 0; ii < 3; ++ii) {
        const int i = tid + 512 * ii;
        if (i < 1280) {
            const int ks = i >> 6, l = i & 63, d = 16 * u + (l & 15), q = l >> 4;
            float v[8];
#pragma unroll
            for (int j = 0; j < 8; j += 2) {
                const int jc = 16 * ks + 4 * q + (j >> 1), kk = kcol_of(jc);
                const float ang = (float)((kk * d) % 640) * (1.0f / 640.0f);
                float c = cosr(ang) * 0.05590169943749474f, s = -sinr(ang) * 0.05590169943749474f;
                if (jc == 304) { c = ((d & 1) ? -1.f : 1.f) * 0.03952847075210474f; s = 0.03952847075210474f; }
                v[j] = c; v[j + 1] = s;
            }
            __builtin_amdgcn_raw_buffer_store_b128(__builtin_bit_cast(u32x4, pk8(v)), mk_rsrc(a.ws + OFF_W1F, W1F_BYTES), i * 16, (108 + u) * 20480, SC1);
        }
    }
}
DI void task_b(const PrepArgs& a, char* smem) {
    const int tid = threadIdx.x, lane = tid & 63;
    if (tid < 80) *(u32x4*)(smem + tid * 16) = *(const u32x4*)(a.b2 + tid * 8);
    rowc_build(smem + PREP_TAB, tid >> 6, lane);
    __syncthreads();
    RowC k; rowc_load(k, smem + PREP_TAB, lane);
    float A[8], B[8];
    rowfft(k, smem, A, B, lane);
    if (tid < 64) {
        const int n = lane & 15, q = lane >> 4, c0 = chunk_c0(n, q);
        float* o = (float*)(a.ws + OFF_B2P);
        if (n <= 10) for (int e = 0; e < 8; ++e) o[c0 + e] = A[e];
        if (n >= 1 && n <= 9) for (int e = 0; e < 8; ++e) o[c0 + 32 + e] = B[e];
    }
}

constexpr int NB_K0 = 128 + 108 + 160 + 1;
constexpr int MAIN_CODE_PREFETCH = 61440;
DI void prefetch_main_code(int slice, int lane) {
    u32 lo, hi;
    asm volatile("s_getpc_b64 s[40:41]\n\ts_add_u32 s40, s40, _Z6k_main8MainArgs@rel32@lo+4\n\ts_addc_u32 s41, s41, _Z6k_main8MainArgs@rel32@hi+12\n\t"
                 "s_mov_b32 %0, s40\n\ts_mov_b32 %1, s41" : "=s"(lo), "=s"(hi) :: "s40", "s41", "scc");
    const u64 base = ((u64)hi << 32) | lo;
    const char* p = (const char*)base + slice * 4096 + lane * 16;
    u32x4 t0, t1, t2, t3;
    asm volatile("global_load_dwordx4 %0, %4, off\n\tglobal_load_dwordx4 %1, %4, off offset:1024\n\tglobal_load_dwordx4 %2, %4, off offset:2048\n\t"
                 "global_load_dwordx4 %3, %4, off offset:3072\n\ts_waitcnt vmcnt(0)"
                 : "=&v"(t0), "=&v"(t1), "=&v"(t2), "=&v"(t3) : "v"(p) : "memory");
}
__global__ __launch_bounds__(512, 4) void k0_prep(PrepArgs a) {
    extern __shared__ __attribute__((aligned(16))) char smem[];
    const int b = blockIdx.x;
    STAMP(0, 0);
    if (b >= 236 && b < 236 + 8 * (MAIN_CODE_PREFETCH / 4096) && (threadIdx.x >> 6) == 7) prefetch_main_code((b - 236) >> 3, threadIdx.x & 63);
    if (b < 128) task_x(a, b, smem);
    else if (b < 236) task_w1(a, b - 128, smem);
    else if (b < 396) task_W(a, b - 236);
    else task_b(a, smem);
    END_STAMP(0);
}

template <int N> DI void wait_vm() { asm volatile("s_waitcnt vmcnt(%0)" ::"n"(N) : "memory"); }
DI void wait_lgkm() { asm volatile("s_waitcnt lgkmcnt(0)" ::: "memory"); }
DI void barrier() { __builtin_amdgcn_s_barrier(); }
template <bool SC = false> DI void dma16(u32x4 srd, int voff, int soff, u32 ldsaddr) {
    if (SC) asm volatile("s_mov_b32 m0, %0\n\ts_nop 0\n\tbuffer_load_dwordx4 %1, %2, %3 offen sc1 lds" ::"s"(ldsaddr), "v"(voff), "s"(srd), "s"(soff) : "memory", "m0");
    else asm volatile("s_mov_b32 m0, %0\n\ts_nop 0\n\tbuffer_load_dwordx4 %1, %2, %3 offen lds" ::"s"(ldsaddr), "v"(voff), "s"(srd), "s"(soff) : "memory", "m0");
}
template <int LIM, bool SC = false> DI void dma16_lim(u32x4 srd, int voff, int soff, u32 ldsaddr, int w) {
    if (SC) asm volatile("s_cmp_lt_i32 %4, %5\n\ts_cbranch_scc0 .Lskip%=\n\ts_mov_b32 m0, %0\n\ts_nop 0\n\tbuffer_load_dwordx4 %1, %2, %3 offen sc1 lds\n.Lskip%=:"
                 ::"s"(ldsaddr), "v"(voff), "s"(srd), "s"(soff), "s"(w), "n"(LIM) : "memory", "m0", "scc");
    else asm volatile("s_cmp_lt_i32 %4, %5\n\ts_cbranch_scc0 .Lskip%=\n\ts_mov_b32 m0, %0\n\ts_nop 0\n\tbuffer_load_dwordx4 %1, %2, %3 offen lds\n.Lskip%=:"
                 ::"s"(ldsaddr), "v"(voff), "s"(srd), "s"(soff), "s"(w), "n"(LIM) : "memory", "m0", "scc");
}
DI u32x4 mk_srd(const void* p, size_t bytes) {
    const u64 a = (u64)p;
    u32x4 r = {(u32)a, (u32)(a >> 32) & 0xffffu, (u32)bytes, 0x00020000u};
    return r;
}
DI float row_sum15(float v) {
    v += __builtin_bit_cast(float, __builtin_amdgcn_update_dpp(0, __builtin_bit_cast(int, v), 0x111, 0xf, 0xf, true));
    v += __builtin_bit_cast(float, __builtin_amdgcn_update_dpp(0, __builtin_bit_cast(int, v), 0x112, 0xf, 0xf, true));
    v += __builtin_bit_cast(float, __builtin_amdgcn_update_dpp(0, __builtin_bit_cast(int, v), 0x114, 0xf, 0xf, true));
    v += __builtin_bit_cast(float, __builtin_amdgcn_update_dpp(0, __builtin_bit_cast(int, v), 0x118, 0xf, 0xf, true));
    return v;
}
template <int I, int N, class F> DI void static_for(F&& f) {
    if constexpr (I < N) { f(std::integral_constant<int, I>{}); static_for<I + 1, N>(f); }
}

#define G1_P_ 3
constexpr int G1_P = G1_P_, G1_PW = 5;
typedef __attribute__((address_space(1))) u32 gu32;
DI u32 ld_agent(const u32* p) { return __hip_atomic_load((gu32*)p, __ATOMIC_RELAXED, __HIP_MEMORY_SCOPE_AGENT); }
DI void add_agent(u32* p) { __hip_atomic_fetch_add((gu32*)p, 1u, __ATOMIC_RELAXED, __HIP_MEMORY_SCOPE_AGENT); }
DI void signal_all(char* ws, int which) {
    if (threadIdx.x == 0) {
        u32* c = (u32*)(ws + OFF_CNT + 16384 + which * 8192) + (blockIdx.x & 7) * 32;
#pragma unroll
        for (int r = 0; r < 8; ++r) add_agent(c + r * 256);
    }
}
#define POLL_SLEEP 16
DI void poll_until(const u32* p, u32 want, char* ws, int lane) {
    for (u32 spins = 0;; ++spins) {
        if (__all(ld_agent(p) >= want)) break;
        __builtin_amdgcn_s_sleep(POLL_SLEEP);
        if (spins > (1u << 18)) { if (lane == 0) __hip_atomic_store((gu32*)(ws + OFF_CNT + 49152), 1u, __ATOMIC_RELAXED, __HIP_MEMORY_SCOPE_AGENT); break; }
    }
}
DI void poll_all(char* ws, int which, u32 per_shard, int w, int lane) {
    if (w == 0) poll_until((const u32*)(ws + OFF_CNT + 16384 + which * 8192) + ((blockIdx.x >> 3) & 7) * 256 + (lane & 7) * 32, per_shard, ws, lane);
}

struct MainArgs { PrepArgs p; const u16* b1; u16* out; };
DI int img128(int row, int ch) { return row * 128 + ((ch ^ ((row >> 1) & 7)) << 4); }
template <bool LAST> DI void w1_prefetch(char* ws, char* smem, int w, int lane, int u0);

template <int L>
DI void col_task(char* ws, int b, char* smem, int u0) {
    const int tid = threadIdx.x & 255, lane = tid & 63, w = tid >> 6, half = threadIdx.x >> 8;
    const int j = 2 * b + half;
    char* Ure = smem + half * 24576; char* Uim = Ure + 4096; char* Ere = Ure + 8192; char* Eim = Ure + 12288;
    char* Rre = Ure + 16384; char* Rim = Ure + 20480;
    const int n = lane & 15, q = lane >> 4;
    const int wv = __builtin_amdgcn_readfirstlane(threadIdx.x >> 6);
    const rsrc_t rx = mk_rsrc(ws + (L == 0 ? OFF_XT0 : OFF_XT2), XT_BYTES), rs = mk_rsrc(ws + (L == 0 ? OFF_SS0 : OFF_SS2), 2048 * 8);
    u32x4 vr, vi, sv[4];
    auto load_in = [&]() __attribute__((always_inline)) {
        vr = __builtin_amdgcn_raw_buffer_load_b128(rx, (2 * j * S_ + tid * 8) * 2, 0, L == 0 ? 0 : SC1);
        vi = __builtin_amdgcn_raw_buffer_load_b128(rx, ((2 * j + 1) * S_ + tid * 8) * 2, 0, L == 0 ? 0 : SC1);
#pragma unroll
        for (int i = 0; i < 4; ++i) sv[i] = __builtin_amdgcn_raw_buffer_load_b128(rs, tid * 64 + 16 * i, 0, L == 0 ? 0 : SC1);
    };
    if (L == 0) load_in();
    u32x4 wf[2];
#pragma unroll
    for (int t = 0; t < 2; ++t) wf[t] = *(const u32x4*)((const u32*)(ws + OFF_WT) + ((size_t)j * 64 + 16 * w + n) * 32 + 16 * t + 4 * q);
    __builtin_amdgcn_sched_barrier(0);
    F32c f; f32c_init(f, lane);
    bf16x8 c32[2] = {f.c[0], f.c[1]}, s32p[2] = {f.sp[0], f.sp[1]}, s32m[2];
    pin(c32[0]); pin(c32[1]); pin(s32p[0]); pin(s32p[1]);
    bf16x8 c64[2], s64p[2], s64m[2];
    const float k1f = (float)(16 * w + n);
    {
        float c[8], s[8];
#pragma unroll
        for (int e = 0; e < 8; ++e) {
            const float ang = __builtin_amdgcn_fractf((float)(8 * q + e) * k1f * (1.0f / 64.0f));
            c[e] = cosr(ang); s[e] = sinr(ang);
        }
        c64[0] = pk8(c); s64p[0] = pk8(s);
        const u32 sg = (lane & 1) ? 0x80008000u : 0u;
        c64[1] = asbf(__builtin_bit_cast(u32x4, c64[0]) ^ sg); s64p[1] = asbf(__builtin_bit_cast(u32x4, s64p[0]) ^ sg);
        pin(c64[0]); pin(c64[1]); pin(s64p[0]); pin(s64p[1]);
    }
    float twr[8], twi[8], ivr[8], ivi[8];
#pragma unroll
    for (int i = 0; i < 8; ++i) {
        const float af = (float)(16 * (i >> 2) + 4 * q + (i & 3)) * k1f * (1.0f / 2048.0f);
        twr[i] = cosr(af); twi[i] = -sinr(af);
        const float ai = (float)(16 * w + 4 * q + (i & 3)) * (float)(16 * (i >> 2) + n) * (1.0f / 2048.0f);
        ivr[i] = cosr(ai) * (1.0f / 2048.0f); ivi[i] = sinr(ai) * (1.0f / 2048.0f);
        pin(twr[i]); pin(twi[i]); pin(ivr[i]); pin(ivi[i]);
    }
    __builtin_amdgcn_sched_barrier(0);
    STAMP(L ? 4 : 1, 1);
    if (L == 1) {
        poll_all(ws, 1, 32u, wv, lane);
        barrier();
        STAMP(4, 5);
        load_in();
        __builtin_amdgcn_sched_barrier(0);
    }
    s64m[0] = neg8(s64p[0]); s64m[1] = neg8(s64p[1]);
    {
        float r[8];
#pragma unroll
        for (int i = 0; i < 8; ++i) {
            const float s = (float)sv[i >> 1][2 * (i & 1) + 1] * 4294967296.0f + (float)sv[i >> 1][2 * (i & 1)];
            r[i] = __builtin_amdgcn_rsqf(s * (1.0f / (SS_SCALE * 640.0f)) + EPS);
        }
        const bf16x8 ur = pk8(bflo(vr[0]) * r[0], bfhi(vr[0]) * r[1], bflo(vr[1]) * r[2], bfhi(vr[1]) * r[3],
                              bflo(vr[2]) * r[4], bfhi(vr[2]) * r[5], bflo(vr[3]) * r[6], bfhi(vr[3]) * r[7]);
        bf16x8 ui = pk8(bflo(vi[0]) * r[0], bfhi(vi[0]) * r[1], bflo(vi[1]) * r[2], bfhi(vi[1]) * r[3],
                        bflo(vi[2]) * r[4], bfhi(vi[2]) * r[5], bflo(vi[3]) * r[6], bfhi(vi[3]) * r[7]);
        if (j == 304) ui = asbf(u32x4{0u, 0u, 0u, 0u});
        const int o = img128(tid >> 3, tid & 7);
        *(bf16x8*)(Ure + o) = ur; *(bf16x8*)(Uim + o) = ui;
        *(u32x4*)(Rre + o) = vr; *(u32x4*)(Rim + o) = vi;
    }
    __syncthreads();
    STAMP(L ? 4 : 1, 2);
    const f32x4 z = {0.f, 0.f, 0.f, 0.f};
    f32x4 dr[2], di[2];
#pragma unroll
    for (int mt = 0; mt < 2; ++mt) {
        dr[mt] = z; di[mt] = z;
#pragma unroll
        for (int ks = 0; ks < 2; ++ks) {
            const int o = img128(16 * mt + n, 4 * ks + q);
            const bf16x8 ar = *(const bf16x8*)(Ure + o), ai = *(const bf16x8*)(Uim + o);
            dr[mt] = mfma16(ar, c64[ks], dr[mt]); dr[mt] = mfma16(ai, s64p[ks], dr[mt]);
            di[mt] = mfma16(ar, s64m[ks], di[mt]); di[mt] = mfma16(ai, c64[ks], di[mt]);
        }
    }
    float tr[8], ti[8];
#pragma unroll
    for (int i = 0; i < 8; ++i) {
        const float x = dr[i >> 2][i & 3], y = di[i >> 2][i & 3];
        tr[i] = x * twr[i] - y * twi[i]; ti[i] = x * twi[i] + y * twr[i];
    }
    bf16x8 Tr = pk8(tr), Ti = pk8(ti);
    s32m[0] = neg8(s32p[0]); s32m[1] = neg8(s32p[1]);
    f32x4 orr[2], oi[2];
#pragma unroll
    for (int t = 0; t < 2; ++t) {
        orr[t] = mfma16(c32[t], Tr, z); orr[t] = mfma16(s32p[t], Ti, orr[t]);
        oi[t] = mfma16(s32m[t], Tr, z); oi[t] = mfma16(c32[t], Ti, oi[t]);
    }
#pragma unroll
    for (int i = 0; i < 8; ++i) {
        const float x = orr[i >> 2][i & 3], y = oi[i >> 2][i & 3];
        const u32 wv = wf[i >> 2][i & 3];
        const float wre = bflo(wv), wim = bfhi(wv);
        tr[i] = x * wre - y * wim; ti[i] = x * wim + y * wre;
    }
    Tr = pk8(tr); Ti = pk8(ti);
#pragma unroll
    for (int nt = 0; nt < 2; ++nt) {
        f32x4 zr = mfma16(Tr, c32[nt], z); zr = mfma16(Ti, s32m[nt], zr);
        f32x4 zi = mfma16(Tr, s32p[nt], z); zi = mfma16(Ti, c32[nt], zi);
        float er[4], ei[4];
#pragma unroll
        for (int r = 0; r < 4; ++r) {
            const float cr = ivr[4 * nt + r], ci = ivi[4 * nt + r];
            er[r] = zr[r] * cr - zi[r] * ci; ei[r] = zr[r] * ci + zi[r] * cr;
        }
        const int o = img128(16 * nt + n, 2 * w + (q >> 1)) + (q & 1) * 8;
        u32x2 pr = {pk2(er[0], er[1]), pk2(er[2], er[3])}, pi = {pk2(ei[0], ei[1]), pk2(ei[2], ei[3])};
        *(u32x2*)(Ere + o) = pr; *(u32x2*)(Eim + o) = pi;
    }
    __syncthreads();
    STAMP(L ? 4 : 1, 3);
    const rsrc_t rf = mk_rsrc(ws + (L == 0 ? OFF_XF1 : OFF_XF3), XT_BYTES);
    char* O = Ure;
#pragma unroll
    for (int nt = 0; nt < 2; ++nt) {
        f32x4 yr = z, yi = z;
#pragma unroll
        for (int ks = 0; ks < 2; ++ks) {
            const int o = img128(16 * nt + n, 4 * ks + q);
            const bf16x8 br = *(const bf16x8*)(Ere + o), bi = *(const bf16x8*)(Eim + o);
            yr = mfma16(c64[ks], br, yr); yr = mfma16(s64m[ks], bi, yr);
            yi = mfma16(s64p[ks], br, yi); yi = mfma16(c64[ks], bi, yi);
        }
        if (j == 304) yi = z;
        const int p = (16 * nt + n) * 64 + 16 * w + 4 * q;
        u32x2 rr[2], ri[2];
        { const int o = img128(16 * nt + n, 2 * w + (q >> 1)) + (q & 1) * 8; rr[nt] = *(const u32x2*)(Rre + o); ri[nt] = *(const u32x2*)(Rim + o); }
        const float fr[4] = {bflo(rr[nt][0]) + yr[0], bfhi(rr[nt][0]) + yr[1], bflo(rr[nt][1]) + yr[2], bfhi(rr[nt][1]) + yr[3]};
        const float fi[4] = {bflo(ri[nt][0]) + yi[0], bfhi(ri[nt][0]) + yi[1], bflo(ri[nt][1]) + yi[2], bfhi(ri[nt][1]) + yi[3]};
        u32x4 ov = {pk2(fr[0], fi[0]), pk2(fr[1], fi[1]), pk2(fr[2], fi[2]), pk2(fr[3], fi[3])};
        *(u32x4*)(O + p * 4) = ov;
    }
    __syncthreads();
    {
        const int c0 = 4 * b, dst = ((c0 >> 5) * 64 + ((c0 & 31) >> 3) * 16) * 16 + (c0 & 4) * 2;
        const u32* O0 = (const u32*)smem; const u32* O1 = (const u32*)(smem + 24576);
#pragma unroll
        for (int i = 0; i < 4; ++i) {
            const int p = threadIdx.x + 512 * i;
            u32x2 v = {O0[p], O1[p]};
            __builtin_amdgcn_raw_buffer_store_b64(v, rf, (p >> 4) * 20480 + (p & 15) * 16, dst, SC1);
        }
    }
    STAMP(L ? 4 : 1, 4);
    wait_lgkm();
    wait_vm<0>();
    barrier();
    signal_all(ws, L == 0 ? 0 : 2);
    w1_prefetch<L == 1>(ws, smem, wv, lane, u0);
    STAMP(L ? 4 : 1, 15);
}

DI f32x2 gelu_tanh2(f32x2 x) {
    const float k1 = -2.302208198f, k2 = -0.1029432397f;
    const f32x2 c1 = {k1, k1}, c2 = {k2, k2}, one = {1.0f, 1.0f};
    const f32x2 t = x * (x * x * c2 + c1);
    f32x2 e = {__builtin_amdgcn_exp2f(t[0]), __builtin_amdgcn_exp2f(t[1])};
    e += one;
    const f32x2 r = {__builtin_amdgcn_rcpf(e[0]), __builtin_amdgcn_rcpf(e[1])};
    return x * r;
}
#define G2_PH_ 3
constexpr int G2_PH = G2_PH_;
constexpr int W2STG = 20480;
DI u32 w2slot(int t) { return 163840 - ((t & 7) + 1) * W2STG; }
template <bool LAST> constexpr int w1slot(int st) { return LAST ? 163840 - (st % 8 + 1) * 20480 : 163840 - (st + 1) * 14336; }

template <bool LAST, int I> DI void w1_piece(u32x4 srd, u32 lds0, int w, int vo, int u0, int st) {
    constexpr int WP = LAST ? 20 : 14;
    const int pc = w + 8 * I;
    const u32 dst = lds0 + w1slot<LAST>(st) + pc * 1024;
    const int so = (u0 + (pc >> 1)) * 20480 + st * 2048 + (pc & 1) * 1024;
    if (8 * I + 7 < WP) dma16<LAST>(srd, vo, so, dst); else dma16_lim<(WP & 7), LAST>(srd, vo, so, dst, w);
}
template <bool LAST> DI void w1_prefetch(char* ws, char* smem, int w, int lane, int u0) {
    const u32x4 srd = mk_srd(ws + OFF_W1F, W1F_BYTES);
    const u32 lds0 = (u32)(size_t)(LAS char*)smem;
#pragma unroll
    for (int st = 0; st < G1_PW; ++st) {
        w1_piece<LAST, 0>(srd, lds0, w, lane * 16, u0, st);
        w1_piece<LAST, 1>(srd, lds0, w, lane * 16, u0, st);
        if (LAST) w1_piece<LAST, 2>(srd, lds0, w, lane * 16, u0, st);
    }
}

constexpr int g1_younger(int ssync, int t, int nlo) {
    int cnt = 0; bool seen = false;
    for (int s = 0; s < ssync; ++s) {
        if (s + G1_PW < 10) { if (seen) cnt += nlo; if (s + G1_PW == t) seen = true; }
        if (s + G1_P < 10 && seen) cnt += 2;
    }
    return cnt;
}

template <int I> DI void w2_issue1(u32x4 srd, u32 lds0, int w, int vo, int ct, int t) {
    const int pc = w + 8 * I, q = pc / 5, cu = pc - 5 * q;
    const int so = ((5 * ct + cu) * 54 + (q < 2 ? 14 * q : 28 + 13 * (q - 2)) + t) * 1024;
    if (I < 2) dma16<true>(srd, vo, so, lds0 + w2slot(t) + pc * 1024); else dma16_lim<4, true>(srd, vo, so, lds0 + w2slot(t) + pc * 1024, w);
}
DI void w2_issue(u32x4 srd, u32 lds0, int w, int vo, int ct, int t) {
    w2_issue1<0>(srd, lds0, w, vo, ct, t); w2_issue1<1>(srd, lds0, w, vo, ct, t); w2_issue1<2>(srd, lds0, w, vo, ct, t);
}

template <int NF, bool LAST, int KG>
DI void g1_phase(const MainArgs& a, char* smem, int w, int lane, int pt, int u0, int nu, int ct2) {
    constexpr int P = G1_P, PW = G1_PW;
    constexpr int WP = 2 * NF, WR = (WP + 7) / 8, NLO = WP / 8, KID = LAST ? 5 : 2;
    char* ws = a.p.ws;
    const int pq = w & 3, g = lane >> 4;
    const u32x4 srdW = mk_srd(ws + OFF_W1F, W1F_BYTES), srd2 = mk_srd(ws + (LAST ? OFF_W2O : OFF_W2F), W2F_BYTES);
    const u32 lds0 = (u32)(size_t)(LAS char*)smem;
    const rsrc_t rx = mk_rsrc(ws + (LAST ? OFF_XF3 : OFF_XF1), XT_BYTES);
    const int xs = (8 * pt + 2 * pq) * 20480 + KG * 1024, vo = lane * 16;
    auto xload = [&](int st, int pu) __attribute__((always_inline)) { return asbf(__builtin_amdgcn_raw_buffer_load_b128(rx, vo, xs + pu * 20480 + st * 2048, SC1)); };
    bf16x8 xb[P][2];
#pragma unroll
    for (int st = 0; st < P; ++st) { xb[st][0] = xload(st, 0); xb[st][1] = xload(st, 1); __builtin_amdgcn_sched_barrier(0); }
    f32x4 acc[NF][2];
#pragma unroll
    for (int i = 0; i < NF; ++i) { acc[i][0] = f32x4{0, 0, 0, 0}; acc[i][1] = f32x4{0, 0, 0, 0}; }
    f32x4 gr[2] = {f32x4{0, 0, 0, 0}, f32x4{0, 0, 0, 0}};
    bf16x8 wa[2][NF];
#pragma unroll
    for (int i = 0; i < NF; ++i) wa[0][i] = *(const bf16x8*)(smem + w1slot<LAST>(0) + KG * 1024 + lane * 16 + i * 2048);
    static_for<0, 10>([&](auto ST) __attribute__((always_inline)) {
        constexpr int st = decltype(ST)::value;
        if constexpr (LAST && st == 2) { wait_lgkm(); barrier(); }
        if constexpr (st == 4 || st == 7) {
            wait_lgkm();
            wait_vm<g1_younger(st, st == 7 ? 9 : 7, NLO)>();
            barrier();
        }
        bf16x8 nx[2];
        const char* rbase = smem + w1slot<LAST>(st + 1) + KG * 1024 + lane * 16;
        constexpr int NV = WR + 2, V0 = KG == 0 ? 0 : NF - NV;
        static_for<0, NF>([&](auto I) __attribute__((always_inline)) {
            constexpr int i = decltype(I)::value;
            __builtin_amdgcn_sched_barrier(0);
            acc[i][0] = mfma16(wa[st & 1][i], xb[st % P][0], acc[i][0]);
            if constexpr (st + 1 < 10) wa[(st + 1) & 1][i] = *(const bf16x8*)(rbase + i * 2048);
            acc[i][1] = mfma16(wa[st & 1][i], xb[st % P][1], acc[i][1]);
            if constexpr (i < 2) {
                u32x4 xv = __builtin_bit_cast(u32x4, xb[st % P][i]);
                if constexpr (st == 9 && KG == 1) { if (g == 0) xv[0] &= 0x0000ffffu; }
                gr[i] = mfma16(asbf(xv), asbf(xv), gr[i]);
            }
            __builtin_amdgcn_sched_barrier(0);
            constexpr int v = i - V0;
            if constexpr (v >= 0 && v < WR) { if constexpr (st + PW < 10) w1_piece<LAST, v>(srdW, lds0, w, vo, u0, st + PW); }
            else if constexpr (v >= WR && v < NV) { if constexpr (st + P < 10) nx[v - WR] = xload(st + P, v - WR); }
            if constexpr (st >= 6) {
                constexpr int sl = i - (KG == 0 ? 0 : NF - 4), e = 4 * (st - 6) + sl;
                if constexpr (sl >= 0 && sl < 4 && e < (LAST ? 9 : 15)) w2_issue1<e % 3>(srd2, lds0, w, vo, ct2, (LAST ? 2 : 0) + e / 3);
            }
        });
        __builtin_amdgcn_sched_barrier(0);
        if constexpr (st + P < 10) { xb[st % P][0] = nx[0]; xb[st % P][1] = nx[1]; }
        if (st == 4) STAMP(KID, 2);
    });
    STAMP(KID, 3);
    u32x2 bv[NF];
#pragma unroll
    for (int i = 0; i < NF; ++i) bv[i] = *(const u32x2*)(a.b1 + min(16 * (u0 + i), F_ - 16) + 4 * g);
    __builtin_amdgcn_sched_barrier(0);
    wait_lgkm();
    barrier();
    float* sx = (float*)(smem + 8 * NF * 512);
#pragma unroll
    for (int i = 0; i < NF; ++i) {
        const f32x4 t = acc[i][1 - KG];
        u32x2 pv = {pk2(t[0], t[1]), pk2(t[2], t[3])};
        *(u32x2*)(smem + ((w * NF + i) * 64 + lane) * 8) = pv;
    }
    float st2[2];
    {
        const int n = lane & 15;
#pragma unroll
        for (int pu = 0; pu < 2; ++pu) {
            const float d01 = (n & 1) ? gr[pu][1] : gr[pu][0], d23 = (n & 1) ? gr[pu][3] : gr[pu][2];
            float d = (n & 2) ? d23 : d01;
            d = (g == (n >> 2)) ? d : 0.f;
            d += __shfl_xor(d, 16); d += __shfl_xor(d, 32);
            st2[pu] = d;
        }
    }
    if (lane < 16) sx[w * 16 + lane] = st2[1 - KG];
    wait_lgkm();
    barrier();
    STAMP(KID, 5);
    const float v = (st2[KG] + sx[(w ^ 4) * 16 + (lane & 15)]) * (1.0f / 640.0f);
    const float r1 = __builtin_amdgcn_rsqf(v + EPS);
    const float rr = r1 * __builtin_amdgcn_rsqf(v * r1 * r1 + EPS);
    const int punit = 8 * pt + 2 * pq + KG;
    u32x2 ov[NF], pv[NF];
#pragma unroll
    for (int i = 0; i < NF; ++i) pv[i] = *(const u32x2*)(smem + (((w ^ 4) * NF + i) * 64 + lane) * 8);
    static_for<0, NF>([&](auto I) __attribute__((always_inline)) {
        constexpr int i = decltype(I)::value;
        if constexpr (LAST ? i < 2 : i == 0) {
            __builtin_amdgcn_sched_barrier(0);
            w2_issue(srd2, lds0, w, vo, ct2, LAST ? i : 5);
            __builtin_amdgcn_sched_barrier(0);
        }
        const f32x2 o0 = {acc[i][KG][0] + bflo(pv[i][0]), acc[i][KG][1] + bfhi(pv[i][0])}, o1 = {acc[i][KG][2] + bflo(pv[i][1]), acc[i][KG][3] + bfhi(pv[i][1])};
        const f32x2 rr2 = {rr, rr};
        const f32x2 b0 = {bflo(bv[i][0]), bfhi(bv[i][0])}, b1 = {bflo(bv[i][1]), bfhi(bv[i][1])};
        f32x2 h0 = gelu_tanh2(o0 * rr2 + b0), h1 = gelu_tanh2(o1 * rr2 + b1);
        if (LAST) {
            const bool lin = u0 + i >= 108;
            h0[0] = lin ? o0[0] : h0[0]; h0[1] = lin ? o0[1] : h0[1]; h1[0] = lin ? o1[0] : h1[0]; h1[1] = lin ? o1[1] : h1[1];
        }
        ov[i] = u32x2{pk2(h0[0], h0[1]), pk2(h1[0], h1[1])};
    });
    __builtin_amdgcn_sched_barrier(0);
    const rsrc_t rh = mk_rsrc(ws + (LAST ? OFF_H2 : OFF_H), H_BYTES), r3 = mk_rsrc(ws + OFF_X3, X3_BYTES);
    const int hb = punit * 54 * 1024;
    auto st1 = [&](int i) __attribute__((always_inline)) {
        const int u = u0 + i;
        if (i < nu) {
            if (!LAST || u < 108) __builtin_amdgcn_raw_buffer_store_b64(ov[i], rh, lane * 16 + (u & 1) * 8, hb + (u >> 1) * 1024, SC1);
            else __builtin_amdgcn_raw_buffer_store_b64(ov[i], r3, lane * 8, (punit * 40 + (u - 108)) * 512, SC1);
        }
    };
    auto st2p = [&](int i) __attribute__((always_inline)) {
        const int u = u0 + i;
        if (i + 1 < nu && (!LAST || u + 1 < 108)) {
            u32x4 q4 = {ov[i][0], ov[i][1], ov[i + 1][0], ov[i + 1][1]};
            __builtin_amdgcn_raw_buffer_store_b128(q4, rh, lane * 16, hb + (u >> 1) * 1024, SC1);
        } else { st1(i); st1(i + 1); }
    };
    if (u0 & 1) {
        st1(0);
#pragma unroll
        for (int i = 1; i + 1 < NF; i += 2) st2p(i);
        if ((NF & 1) == 0) st1(NF - 1);
    } else {
#pragma unroll
        for (int i = 0; i + 1 < NF; i += 2) st2p(i);
        if (NF & 1) st1(NF - 1);
    }
    STAMP(KID, 4);
}

template <bool LAST>
DI void g2_phase(const MainArgs& a, char* smem, int w, int lane, int pt1, int pt, int ct) {
    constexpr int PH = G2_PH, KID = LAST ? 6 : 3;
    char* ws = a.p.ws;
    u32* cnt = (u32*)(ws + OFF_CNT + (LAST ? 8192 : 0));
    const int pg = w & 1, kq = w >> 1, n = lane & 15, q = lane >> 4;
    const int kb = kq < 2 ? 14 * kq : 28 + 13 * (kq - 2), nk = kq < 2 ? 14 : 13;
    const u32x4 srd2 = mk_srd(ws + (LAST ? OFF_W2O : OFF_W2F), W2F_BYTES);
    const u32 lds0 = (u32)(size_t)(LAS char*)smem;
    const rsrc_t rh = mk_rsrc(ws + (LAST ? OFF_H2 : OFF_H), H_BYTES), rw = mk_rsrc(ws + (LAST ? OFF_W2O : OFF_W2F), W2F_BYTES);
    const int vo = lane * 16, hs = ((4 * pt + 2 * pg) * 54 + kb) * 1024;
    bf16x8 wr[6][5];
    asm volatile("" ::: "memory");
    __builtin_amdgcn_sched_barrier(0);
#pragma unroll
    for (int t = 0; t < 3; ++t)
#pragma unroll
        for (int i = 0; i < 5; ++i) wr[t][i] = asbf(__builtin_amdgcn_raw_buffer_load_b128(rw, vo, ((5 * ct + i) * 54 + kb + 8 + t) * 1024, SC1));
    __builtin_amdgcn_sched_barrier(0);
    wait_lgkm();
    wait_vm<15>();
    barrier();
    if (threadIdx.x == 0) add_agent(cnt + 64 * pt1);
    STAMP(LAST ? 5 : 2, 15);
#pragma unroll
    for (int t = LAST ? 5 : 6; t < 8; ++t) w2_issue(srd2, lds0, w, vo, ct, t);
    __builtin_amdgcn_sched_barrier(0);
#pragma unroll
    for (int t = 3; t < 6; ++t)
#pragma unroll
        for (int i = 0; i < 5; ++i) wr[t][i] = asbf(__builtin_amdgcn_raw_buffer_load_b128(rw, vo, ((5 * ct + i) * 54 + kb + 8 + t) * 1024, SC1));
    __builtin_amdgcn_sched_barrier(0);
    if (w == 0) poll_until(cnt + 64 * (pt >> 1), 16u, ws, lane);
    wait_vm<0>();
    barrier();
    STAMP(KID, 5);
    bf16x8 hb[PH][2];
    auto hload = [&](int t, int pu) __attribute__((always_inline)) {
        const int k = t < 13 ? t : nk - 1;
        return asbf(__builtin_amdgcn_raw_buffer_load_b128(rh, vo, hs + (pu * 54 + k) * 1024, SC1));
    };
#pragma unroll
    for (int t = 0; t < PH; ++t) { hb[t][0] = hload(t, 0); hb[t][1] = hload(t, 1); __builtin_amdgcn_sched_barrier(0); }
    u32x2 xv[3], bv[3]; float bb[3];
    f32x4 acc[2][5];
#pragma unroll
    for (int i = 0; i < 5; ++i) { acc[0][i] = f32x4{0, 0, 0, 0}; acc[1][i] = f32x4{0, 0, 0, 0}; }
    bf16x8 wa[2][5];
    const char* wbase = smem + kq * 5120 + lane * 16;
#pragma unroll
    for (int i = 0; i < 5; ++i) wa[0][i] = *(const bf16x8*)(wbase + w2slot(0) + i * 1024);
    static_for<0, 14>([&](auto T) __attribute__((always_inline)) {
        constexpr int t = decltype(T)::value;
        if constexpr (t == 10) {
            __builtin_amdgcn_sched_barrier(0);
#pragma unroll
            for (int j = 0; j < 3; ++j) {
                const int tt = min(kq + 4 * j, 9), pu = 2 * pg + tt / 5, cu = tt % 5;
                if (!LAST) {
                    const int c = 80 * ct + 16 * cu + n;
                    const int off = (((4 * pt + pu) * 20 + (c >> 5)) * 64 + ((c & 31) >> 3) * 16 + 4 * q) * 16 + (c & 7) * 2;
                    const rsrc_t r1 = mk_rsrc(ws + OFF_XF1, XT_BYTES);
                    const u32 e0 = __builtin_amdgcn_raw_buffer_load_b16(r1, off, 0, SC1), e1 = __builtin_amdgcn_raw_buffer_load_b16(r1, off + 16, 0, SC1);
                    const u32 e2 = __builtin_amdgcn_raw_buffer_load_b16(r1, off + 32, 0, SC1), e3 = __builtin_amdgcn_raw_buffer_load_b16(r1, off + 48, 0, SC1);
                    xv[j] = u32x2{e0 | (e1 << 16), e2 | (e3 << 16)};
                    bb[j] = ((const float*)(ws + OFF_B2P))[c];
                } else {
                    xv[j] = __builtin_amdgcn_raw_buffer_load_b64(mk_rsrc(ws + OFF_X3, X3_BYTES), lane * 8, ((4 * pt + pu) * 40 + 5 * ct + cu) * 512, SC1);
                    bv[j] = *(const u32x2*)(a.p.b2 + 80 * ct + 16 * cu + 4 * q);
                }
            }
        }
        bf16x8 nh[2];
        auto step = [&]() __attribute__((always_inline)) {
            static_for<0, 5>([&](auto I) __attribute__((always_inline)) {
                constexpr int i = decltype(I)::value;
                __builtin_amdgcn_sched_barrier(0);
                bf16x8 wv;
                if constexpr (t < 8) wv = wa[t & 1][i]; else wv = wr[t - 8][i];
                acc[0][i] = LAST ? mfma16(wv, hb[t % PH][0], acc[0][i]) : mfma16(hb[t % PH][0], wv, acc[0][i]);
                if constexpr (t + 1 < 8) wa[(t + 1) & 1][i] = *(const bf16x8*)(wbase + w2slot(t + 1) + i * 1024);
                acc[1][i] = LAST ? mfma16(wv, hb[t % PH][1], acc[1][i]) : mfma16(hb[t % PH][1], wv, acc[1][i]);
                __builtin_amdgcn_sched_barrier(0);
                if constexpr (t + PH < 14 && (i == 1 || i == 3)) nh[i >> 1] = hload(t + PH, i >> 1);
            });
        };
        if constexpr (t < 13) step(); else { if (nk == 14) step(); }
        __builtin_amdgcn_sched_barrier(0);
        if constexpr (t + PH < 14) { hb[t % PH][0] = nh[0]; hb[t % PH][1] = nh[1]; }
        if (t == 0) STAMP(KID, 1);
        if (t == 7) STAMP(KID, 2);
    });
    STAMP(KID, 3);
    wait_lgkm();
    barrier();
#pragma unroll
    for (int t = 0; t < 10; ++t)
        if ((t & 3) != kq) *(f32x4*)(smem + ((w * 10 + t) * 64 + lane) * 16) = acc[t / 5][t % 5];
    wait_lgkm();
    barrier();
    float ssp[2][4];
#pragma unroll
    for (int r = 0; r < 4; ++r) { ssp[0][r] = 0.f; ssp[1][r] = 0.f; }
    const rsrc_t ro = mk_rsrc(ws + OFF_XT2, XT_BYTES);
    auto finish = [&](auto KQ) __attribute__((always_inline)) {
        constexpr int kqc = decltype(KQ)::value;
        static_for<0, 3>([&](auto J) __attribute__((always_inline)) {
            constexpr int j = decltype(J)::value, t = kqc + 4 * j;
            if constexpr (t < 10) {
                constexpr int pl = t / 5, cu = t % 5;
                f32x4 o = acc[pl][cu];
#pragma unroll
                for (int s = 1; s < 4; ++s) o += *(const f32x4*)(smem + (((pg + 2 * ((kqc + s) & 3)) * 10 + t) * 64 + lane) * 16);
                const int pu = 2 * pg + pl;
                if (!LAST) {
                    const int c = 80 * ct + 16 * cu + n, p = 64 * pt + 16 * pu + 4 * q;
                    const u32 o0 = pk2(bflo(xv[j][0]) + o[0] + bb[j], bfhi(xv[j][0]) + o[1] + bb[j]);
                    const u32 o1 = pk2(bflo(xv[j][1]) + o[2] + bb[j], bfhi(xv[j][1]) + o[3] + bb[j]);
                    u32x2 ov = {o0, o1};
                    __builtin_amdgcn_raw_buffer_store_b64(ov, ro, (c * S_ + p) * 2, 0, SC1);
                    const float m = c == 609 ? 0.f : 1.f;
                    ssp[pl][0] += m * bflo(o0) * bflo(o0); ssp[pl][1] += m * bfhi(o0) * bfhi(o0);
                    ssp[pl][2] += m * bflo(o1) * bflo(o1); ssp[pl][3] += m * bfhi(o1) * bfhi(o1);
                } else {
                    const int p = 16 * (4 * pt + pu) + n, d = 80 * ct + 16 * cu + 4 * q;
                    u32x2 ov = {pk2(bflo(xv[j][0]) + o[0] + bflo(bv[j][0]), bfhi(xv[j][0]) + o[1] + bfhi(bv[j][0])),
                                pk2(bflo(xv[j][1]) + o[2] + bflo(bv[j][1]), bfhi(xv[j][1]) + o[3] + bfhi(bv[j][1]))};
                    *(u32x2*)(a.out + (size_t)tok_of_p(p) * D_ + d) = ov;
                }
            }
        });
    };
    if (kq == 0) finish(std::integral_constant<int, 0>{});
    else if (kq == 1) finish(std::integral_constant<int, 1>{});
    else if (kq == 2) finish(std::integral_constant<int, 2>{});
    else finish(std::integral_constant<int, 3>{});
    if (!LAST) {
        float* sp = (float*)smem;
#pragma unroll
        for (int pl = 0; pl < 2; ++pl)
#pragma unroll
            for (int r = 0; r < 4; ++r) {
                const float v = row_sum15(ssp[pl][r]);
                if (n == 15) sp[w * 32 + 16 * pl + 4 * q + r] = v;
            }
        wait_lgkm();
        barrier();
        if (w == 0) {
            const int g2 = lane >> 5, r = lane & 31;
            const float v = (sp[g2 * 32 + r] + sp[(g2 + 2) * 32 + r]) + (sp[(g2 + 4) * 32 + r] + sp[(g2 + 6) * 32 + r]);
            __hip_atomic_fetch_add((__attribute__((address_space(1))) u64*)(ws + OFF_SS2) + 64 * pt + lane, ss_from_float(v), __ATOMIC_RELAXED, __HIP_MEMORY_SCOPE_AGENT);
        }
        wait_lgkm();
        wait_vm<0>();
        barrier();
        signal_all(ws, 1);
    }
}

template <bool LAST>
DI void mlp(const MainArgs& a, char* smem, int w, int lane, int u0) {
    constexpr int NF = LAST ? 10 : 7;
    const int x = blockIdx.x & 7, ib = blockIdx.x >> 3;
    const int pt = 4 * (x >> 1) + (ib & 3), ft = 8 * (x & 1) + (ib >> 2);
    const int pt2 = 8 * (x >> 1) + (ib & 7), ct2 = 4 * (x & 1) + (ib >> 3);
    const int nu = LAST ? (ft < 4 ? 10 : 9) : (ft < 12 ? 7 : 6);
    if (LAST) poll_all(a.p.ws, 2, 20u, w, lane);
    else if (w == 0) {
        const int sh = lane & 7, wh = (lane >> 3) & 1;
        poll_until((const u32*)(a.p.ws + OFF_CNT + 16384 + (wh ? 3 : 0) * 8192) + ((blockIdx.x >> 3) & 7) * 256 + sh * 32, wh ? (sh < 6 ? 12u : 11u) : 20u, a.p.ws, lane);
    }
    wait_vm<0>();
    barrier();
    STAMP(LAST ? 5 : 2, 1);
    if (w >= 4) __builtin_amdgcn_s_setprio(1);
    if (w < 4) g1_phase<NF, LAST, 0>(a, smem, w, lane, pt, u0, nu, ct2);
    else g1_phase<NF, LAST, 1>(a, smem, w, lane, pt, u0, nu, ct2);
    __builtin_amdgcn_s_setprio(0);
    g2_phase<LAST>(a, smem, w, lane, pt, pt2, ct2);
}

__global__ __launch_bounds__(512, 2) void k_main(MainArgs a) {
    extern __shared__ __attribute__((aligned(16))) char smem[];
    const int lane = threadIdx.x & 63, b = blockIdx.x;
    const int w = __builtin_amdgcn_readfirstlane(threadIdx.x >> 6);
    const int ft = 8 * (b & 1) + (b >> 5);
    const int u0a = ft < 12 ? 7 * ft : 84 + 6 * (ft - 12), u0b = ft < 4 ? 10 * ft : 40 + 9 * (ft - 4);
    if (b < 160) col_task<0>(a.p.ws, b, smem, u0a);
    else {
        w1_prefetch<false>(a.p.ws, smem, w, lane, u0a);
        if (b < 254) {
            if (b < 214) task_w2(a.p, b - 160, smem); else task_Q(a.p, b - 214);
            wait_lgkm();
            wait_vm<0>();
            barrier();
            signal_all(a.p.ws, 3);
        }
    }
    mlp<false>(a, smem, w, lane, u0a);
    END_STAMP(3);
    if (b < 160) col_task<1>(a.p.ws, b, smem, u0b); else w1_prefetch<true>(a.p.ws, smem, w, lane, u0b);
    mlp<true>(a, smem, w, lane, u0b);
    END_STAMP(6);
}

extern "C" void kernel_launch(void* const* d_in, const int* in_sizes, int n_in, void* d_out, int out_size, void* d_ws,
                              size_t ws_size, hipStream_t stream) {
    char* ws = (char*)d_ws;
    constexpr int MAIN_LDS = 163840;
    (void)hipFuncSetAttribute((const void*)k_main, hipFuncAttributeMaxDynamicSharedMemorySize, MAIN_LDS);
    PrepArgs pa{(const u16*)d_in[0], (const u16*)d_in[1], (const u16*)d_in[2], (const u16*)d_in[4], (const u16*)d_in[6], (const u16*)d_in[7], ws};
    k0_prep<<<NB_K0, 512, PREP_LDS, stream>>>(pa);
    MainArgs ma{pa, (const u16*)d_in[5], (u16*)d_out};
    k_main<<<256, 512, MAIN_LDS, stream>>>(ma);
}
```

```cpp
#include <hip/hip_runtime.h>
#include <hip/hip_bf16.h>
#include <stdint.h>
#include <type_traits>

typedef unsigned short u16;
typedef unsigned int u32;
typedef unsigned long long u64;
typedef short s16x4 __attribute__((ext_vector_type(4)));
typedef __bf16 bf16x8 __attribute__((ext_vector_type(8)));
typedef __bf16 bf16x2 __attribute__((ext_vector_type(2)));
typedef float f32x4 __attribute__((ext_vector_type(4)));
typedef float f32x2 __attribute__((ext_vector_type(2)));
typedef u32 u32x4 __attribute__((ext_vector_type(4)));
typedef u32 u32x2 __attribute__((ext_vector_type(2)));
typedef __amdgpu_buffer_rsrc_t rsrc_t;
#define LAS __attribute__((address_space(3)))
#define DI __device__ __forceinline__

constexpr int S_ = 2048, D_ = 640, F_ = 1728, DF_ = 321;
constexpr float EPS = 1e-5f;
constexpr int SC1 = 16;
constexpr float SS_SCALE = 1048576.0f;

constexpr size_t XT_BYTES = (size_t)D_ * S_ * 2;
constexpr size_t OFF_XT0 = 0;
constexpr size_t OFF_XT2 = OFF_XT0 + XT_BYTES;
constexpr size_t OFF_WT = OFF_XT2 + XT_BYTES;
constexpr size_t OFF_W1F = OFF_WT + (size_t)320 * 2048 * 4;
constexpr size_t W1F_BYTES = (size_t)148 * 20 * 1024;
constexpr size_t OFF_W2F = OFF_W1F + W1F_BYTES;
constexpr size_t W2F_BYTES = (size_t)40 * 54 * 1024;
constexpr size_t OFF_W2O = OFF_W2F + W2F_BYTES;
constexpr size_t OFF_H = OFF_W2O + W2F_BYTES;
constexpr size_t H_BYTES = (size_t)128 * 54 * 1024;
constexpr size_t OFF_H2 = OFF_H;
constexpr size_t OFF_X3 = OFF_H + H_BYTES;
constexpr size_t OFF_SS0 = OFF_X3 + (size_t)128 * 40 * 512;
constexpr size_t OFF_SS2 = OFF_SS0 + 2048 * 8;
constexpr size_t OFF_B2P = OFF_SS2 + 2048 * 8;
constexpr size_t OFF_XF1 = OFF_B2P + 4096;
constexpr size_t OFF_XF3 = OFF_XF1;
constexpr size_t X3_BYTES = (size_t)128 * 40 * 512;
constexpr size_t OFF_CNT = OFF_XF1 + XT_BYTES;
constexpr size_t WS_END = OFF_CNT + 65536;

DI f32x4 mfma16(bf16x8 a, bf16x8 b, f32x4 c) { return __builtin_amdgcn_mfma_f32_16x16x32_bf16(a, b, c, 0, 0, 0); }
DI u32 pk2(float a, float b) { f32x2 v = {a, b}; const bf16x2 r = __builtin_convertvector(v, bf16x2); return __builtin_bit_cast(u32, r); }
DI bf16x8 pk8(float a0, float a1, float a2, float a3, float a4, float a5, float a6, float a7) {
    u32x4 r = {pk2(a0, a1), pk2(a2, a3), pk2(a4, a5), pk2(a6, a7)};
    return __builtin_bit_cast(bf16x8, r);
}
DI bf16x8 pk8(const float (&a)[8]) { return pk8(a[0], a[1], a[2], a[3], a[4], a[5], a[6], a[7]); }
DI bf16x8 asbf(u32x4 r) { return __builtin_bit_cast(bf16x8, r); }
DI bf16x8 neg8(bf16x8 a) { u32x4 r = __builtin_bit_cast(u32x4, a); r ^= 0x80008000u; return __builtin_bit_cast(bf16x8, r); }
DI float bflo(u32 v) { return __builtin_bit_cast(float, v << 16); }
DI float bfhi(u32 v) { return __builtin_bit_cast(float, v & 0xffff0000u); }
DI float cosr(float rev) { return __builtin_amdgcn_cosf(rev); }
DI float sinr(float rev) { return __builtin_amdgcn_sinf(rev); }
DI s16x4 tr_read(const char* p) { return __builtin_amdgcn_ds_read_tr16_b64_v4i16((LAS s16x4*)p); }
DI bf16x8 cat8(s16x4 lo, s16x4 hi) { return __builtin_bit_cast(bf16x8, __builtin_shufflevector(lo, hi, 0, 1, 2, 3, 4, 5, 6, 7)); }
DI int tok_of_p(int p) { return 32 * (p & 63) + (p >> 6); }
DI rsrc_t mk_rsrc(const void* p, size_t bytes) { return __builtin_amdgcn_make_buffer_rsrc((void*)p, 0, (int)bytes, 0x00020000); }
DI u32x4 bload16(rsrc_t r, int voff, int soff) { return __builtin_amdgcn_raw_buffer_load_b128(r, voff, soff, 0); }
DI u64 ss_from_float(float s) { return (u64)(fminf(s * SS_SCALE, 1.0e18f) + 0.5f); }
DI void pin(float& x) { asm volatile("" : "+v"(x)); }
DI void pin(bf16x8& x) { asm volatile("" : "+v"(x)); }

struct F32c { bf16x8 c[2], sp[2]; };
DI void f32c_init(F32c& k, int lane) {
    const float m = (float)(lane & 15), q4 = (float)(4 * (lane >> 4));
    float c[4], s[4];
#pragma unroll
    for (int j = 0; j < 4; ++j) { const float a = __builtin_amdgcn_fractf(m * (q4 + (float)j) * (1.0f / 32.0f)); c[j] = cosr(a); s[j] = sinr(a); }
    const u32 sg = (lane & 1) ? 0x80008000u : 0u;
    const u32 c0 = pk2(c[0], c[1]), c1 = pk2(c[2], c[3]), s0 = pk2(s[0], s[1]), s1 = pk2(s[2], s[3]);
    const u32x4 cv = {c0, c1, c0 ^ sg, c1 ^ sg}, sv = {s0, s1, s0 ^ sg, s1 ^ sg};
    k.c[0] = asbf(cv); k.sp[0] = asbf(sv);
    k.c[1] = asbf(cv ^ 0x80000000u); k.sp[1] = asbf(sv ^ 0x80000000u);
}

struct RowC { bf16x8 c[2], sp[2], sm[2], c20, s20m; f32x4 twr[2], twi[2]; int off0, off1; };
constexpr int ROWC_LDS = 10 * 1024;
DI void rowc_build(char* tab, int w, int lane) {
    const int n = lane & 15, q = lane >> 4;
    const float nf = (float)n;
    u32x4* t = (u32x4*)tab + lane;
    if (w == 0) {
        F32c f; f32c_init(f, lane);
        t[0] = __builtin_bit_cast(u32x4, f.c[0]); t[64] = __builtin_bit_cast(u32x4, f.c[1]);
        t[128] = __builtin_bit_cast(u32x4, f.sp[0]); t[192] = __builtin_bit_cast(u32x4, f.sp[1]);
    } else if (w <= 2) {
        float c[8];
#pragma unroll
        for (int j = 0; j < 8; ++j) {
            const int d1 = 8 * q + j;
            const float a = __builtin_amdgcn_fractf((float)d1 * nf * 0.05f);
            const float v = w == 1 ? cosr(a) : -sinr(a);
            c[j] = (d1 < 20 && n <= 10) ? v : 0.f;
        }
        t[(3 + w) * 64] = __builtin_bit_cast(u32x4, pk8(c));
    } else if (w <= 4) {
        const float sc = 0.05590169943749474f;
        f32x4 cr, ci;
#pragma unroll
        for (int i = 0; i < 4; ++i) {
            const float a = (float)(16 * (w - 3) + 4 * q + i) * nf * (1.0f / 640.0f);
            cr[i] = cosr(a) * sc; ci[i] = -sinr(a) * sc;
        }
        t[(6 + w - 3) * 64] = __builtin_bit_cast(u32x4, cr); t[(8 + w - 3) * 64] = __builtin_bit_cast(u32x4, ci);
    }
}
DI void rowc_load(RowC& k, const char* tab, int lane) {
    const u32x4* tb = (const u32x4*)tab + lane;
    k.c[0] = asbf(tb[0]); k.c[1] = asbf(tb[64]); k.sp[0] = asbf(tb[128]); k.sp[1] = asbf(tb[192]);
    k.c20 = asbf(tb[256]); k.s20m = asbf(tb[320]);
    k.twr[0] = __builtin_bit_cast(f32x4, tb[384]); k.twr[1] = __builtin_bit_cast(f32x4, tb[448]);
    k.twi[0] = __builtin_bit_cast(f32x4, tb[512]); k.twi[1] = __builtin_bit_cast(f32x4, tb[576]);
    k.sm[0] = neg8(k.sp[0]); k.sm[1] = neg8(k.sp[1]);
    const int q = lane >> 4, qq = (lane & 15) >> 2, pp = lane & 3;
    const int r0 = min(8 * q + qq, 19), r1 = min(8 * q + 4 + qq, 19);
    k.off0 = r0 * 64 + pp * 8; k.off1 = r1 * 64 + pp * 8;
}
DI float wave_sum63(float v) {
    v += __builtin_bit_cast(float, __builtin_amdgcn_update_dpp(0, __builtin_bit_cast(int, v), 0x111, 0xf, 0xf, true));
    v += __builtin_bit_cast(float, __builtin_amdgcn_update_dpp(0, __builtin_bit_cast(int, v), 0x112, 0xf, 0xf, true));
    v += __builtin_bit_cast(float, __builtin_amdgcn_update_dpp(0, __builtin_bit_cast(int, v), 0x114, 0xf, 0xf, true));
    v += __builtin_bit_cast(float, __builtin_amdgcn_update_dpp(0, __builtin_bit_cast(int, v), 0x118, 0xf, 0xf, true));
    v += __builtin_bit_cast(float, __builtin_amdgcn_update_dpp(0, __builtin_bit_cast(int, v), 0x142, 0xa, 0xf, true));
    v += __builtin_bit_cast(float, __builtin_amdgcn_update_dpp(0, __builtin_bit_cast(int, v), 0x143, 0xc, 0xf, true));
    return v;
}
DI void rowfft(const RowC& k, const char* row, float (&A)[8], float (&B)[8], int lane) {
    f32x4 z = {0.f, 0.f, 0.f, 0.f};
    f32x4 yr[2], yi[2];
#pragma unroll
    for (int mt = 0; mt < 2; ++mt) {
        const bf16x8 a = cat8(tr_read(row + k.off0 + mt * 32), tr_read(row + k.off1 + mt * 32));
        yr[mt] = mfma16(a, k.c20, z); yi[mt] = mfma16(a, k.s20m, z);
    }
    float tr[8], ti[8];
#pragma unroll
    for (int i = 0; i < 8; ++i) {
        const float a = yr[i >> 2][i & 3], b = yi[i >> 2][i & 3], cr = k.twr[i >> 2][i & 3], ci = k.twi[i >> 2][i & 3];
        tr[i] = a * cr - b * ci; ti[i] = a * ci + b * cr;
    }
    const bf16x8 Tr = pk8(tr), Ti = pk8(ti);
    f32x4 ore[2], oim[2];
#pragma unroll
    for (int t = 0; t < 2; ++t) {
        ore[t] = mfma16(k.c[t], Tr, z); ore[t] = mfma16(k.sp[t], Ti, ore[t]);
        oim[t] = mfma16(k.sm[t], Tr, z); oim[t] = mfma16(k.c[t], Ti, oim[t]);
    }
#pragma unroll
    for (int r = 0; r < 4; ++r) { A[2 * r] = ore[0][r]; A[2 * r + 1] = oim[0][r]; B[2 * r] = ore[1][r]; B[2 * r + 1] = oim[1][r]; }
    if (lane == 0) { const float h = 0.7071067811865476f; const float dc = A[0] * h; A[0] = B[0] * h; A[1] = dc; }
}
DI int sw_off(int c) { const int ch = c >> 3; return ((ch ^ ((ch >> 3) & 7)) << 4) + (c & 7) * 2; }
DI int chunk_c0(int n, int q) { return (n == 0 ? 608 : n == 10 ? 576 : 64 * (n - 1)) + 8 * q; }

constexpr int ROWST = 1344;
constexpr int PREP_TAB = 640 * 32 + 16 * ROWST;
constexpr int PREP_LDS = PREP_TAB + ROWC_LDS;

struct PrepArgs { const u16 *x, *wr, *wi, *w1, *w2, *b2; char* ws; };

DI void task_x(const PrepArgs& a, int b, char* smem) {
    const int tid = threadIdx.x, lane = tid & 63, w = tid >> 6;
    const int p0 = 16 * ((b & 7) * 16 + (b >> 3));
    u32x4 v[3];
#pragma unroll
    for (int i = 0; i < 3; ++i) {
        const int id = min(tid + 512 * i, 1279), r = id / 80, ch = id % 80;
        v[i] = *(const u32x4*)(a.x + (size_t)tok_of_p(p0 + r) * D_ + ch * 8);
    }
    __builtin_amdgcn_sched_barrier(0);
    rowc_build(smem + PREP_TAB, w, lane);
    __builtin_amdgcn_sched_barrier(0);
    if (tid < 16) ((u64*)(a.ws + OFF_SS2))[p0 + tid] = 0ull;
    if (b < 32) ((u32*)(a.ws + OFF_CNT))[b * 512 + tid] = 0u;
#pragma unroll
    for (int i = 0; i < 3; ++i) {
        const int id = min(tid + 512 * i, 1279), r = id / 80, ch = id % 80;
        *(u32x4*)(smem + r * ROWST + ch * 16) = v[i];
    }
    __syncthreads();
    RowC k; rowc_load(k, smem + PREP_TAB, lane);
    const int n = lane & 15, q = lane >> 4, c0 = chunk_c0(n, q);
    float A[2][8], B[2][8];
#pragma unroll
    for (int i = 0; i < 2; ++i) rowfft(k, smem + (2 * w + i) * ROWST, A[i], B[i], lane);
#pragma unroll
    for (int i = 0; i < 2; ++i) {
        const int r = 2 * w + i;
        float ss = 0.f;
        if (n <= 10) {
#pragma unroll
            for (int e = 0; e < 8; ++e) ss += (lane == 0 && e == 1) ? 0.f : A[i][e] * A[i][e];
            *(bf16x8*)(smem + r * ROWST + sw_off(c0)) = pk8(A[i]);
        }
        if (n >= 1 && n <= 9) {
#pragma unroll
            for (int e = 0; e < 8; ++e) ss += B[i][e] * B[i][e];
            *(bf16x8*)(smem + r * ROWST + sw_off(c0 + 32)) = pk8(B[i]);
        }
        ss = wave_sum63(ss);
        if (lane == 63) ((u64*)(a.ws + OFF_SS0))[p0 + r] = ss_from_float(ss);
    }
    __syncthreads();
    u16* xt = (u16*)(a.ws + OFF_XT0);
    const int g = lane >> 4, i16 = lane & 15, qq = i16 >> 2, pp = i16 & 3;
#pragma unroll
    for (int i = 0; i < 3; ++i) {
        const int cbk = w + 8 * i;
        if (cbk < 20) {
            const int cb = 32 * cbk + 16 * (g & 1), ph = g >> 1;
            const char* base = smem + (8 * ph + qq) * ROWST + sw_off(cb + 4 * pp);
            const bf16x8 o = cat8(tr_read(base), tr_read(base + 4 * ROWST));
            *(bf16x8*)(xt + (size_t)(cb + i16) * S_ + p0 + 8 * ph) = o;
        }
    }
}

DI void task_W(const PrepArgs& a, int b) {
    const int lane = threadIdx.x & 63, wv = threadIdx.x >> 6, wt = b * 2 + (wv >> 2), kq = wv & 3;
    const int k1 = wt / 5, cg = wt % 5, kp = 1 + 64 * cg + lane;
    const int r = kp % 20, t = kp / 20;
    int j; bool mir = false;
    if (kp == 320) j = 304;
    else if (r == 0) j = 304 + t;
    else if (r < 10) j = 32 * (r - 1) + t;
    else if (r == 10) j = 288 + t;
    else { j = 32 * (19 - r) + 31 - t; mir = true; }
    const rsrc_t rr = mk_rsrc(a.wr, (size_t)S_ * DF_ * 2), ri = mk_rsrc(a.wi, (size_t)S_ * DF_ * 2);
    const int st = (mir ? -64 : 64) * DF_ * 2, off0 = ((mir ? 2048 - k1 : k1) * DF_ + kp) * 2 + 8 * kq * st;
    u32 re[8], im[8];
#pragma unroll
    for (int k2 = 0; k2 < 8; ++k2) {
        int off = off0 + k2 * st;
        if (k2 == 0 && mir && k1 == 0 && kq == 0) off = kp * 2;
        re[k2] = __builtin_amdgcn_raw_buffer_load_b16(rr, off, 0, 0); im[k2] = __builtin_amdgcn_raw_buffer_load_b16(ri, off, 0, 0);
    }
    const u32 sg = mir ? 0x80000000u : 0u;
    u32x4* dst = (u32x4*)(a.ws + OFF_WT + ((size_t)j * 64 + k1) * 128 + kq * 32);
#pragma unroll
    for (int i = 0; i < 2; ++i) {
        u32x4 o;
#pragma unroll
        for (int e = 0; e < 4; ++e) o[e] = (re[4 * i + e] | (im[4 * i + e] << 16)) ^ sg;
        dst[i] = o;
    }
}

DI void task_w1(const PrepArgs& a, int b, char* smem) {
    const int tid = threadIdx.x, lane = tid & 63, w = tid >> 6;
    char* raw = smem;
    char* tr = smem + 640 * 32;
    char* out = raw;
    u32x4 v[3];
#pragma unroll
    for (int i = 0; i < 3; ++i) {
        const int id = min(tid + 512 * i, 1279), d = id >> 1, h = id & 1;
        v[i] = *(const u32x4*)(a.w1 + (size_t)d * F_ + 16 * b + 8 * h);
    }
    __builtin_amdgcn_sched_barrier(0);
    rowc_build(smem + PREP_TAB, w, lane);
    __builtin_amdgcn_sched_barrier(0);
#pragma unroll
    for (int i = 0; i < 3; ++i) *(u32x4*)(raw + min(tid + 512 * i, 1279) * 16) = v[i];
    __syncthreads();
    const int g = lane >> 4, i16 = lane & 15, qq = i16 >> 2, pp = i16 & 3;
#pragma unroll
    for (int i = 0; i < 5; ++i) {
        const int d0 = 16 * (w + 8 * i) + 4 * g;
        const s16x4 t = tr_read(raw + (d0 + qq) * 32 + pp * 8);
        *(s16x4*)(tr + i16 * ROWST + d0 * 2) = t;
    }
    RowC k; rowc_load(k, smem + PREP_TAB, lane);
    __syncthreads();
    const int n = lane & 15, q = lane >> 4, c0 = chunk_c0(n, q);
    float A[2][8], B[2][8];
#pragma unroll
    for (int i = 0; i < 2; ++i) rowfft(k, tr + (2 * w + i) * ROWST, A[i], B[i], lane);
#pragma unroll
    for (int i = 0; i < 2; ++i) {
        const int f = 2 * w + i;
        if (lane == 0) A[i][1] = 0.f;
        if (n <= 10) *(bf16x8*)(out + ((c0 >> 5) * 64 + q * 16 + f) * 16) = pk8(A[i]);
        if (n >= 1 && n <= 9) *(bf16x8*)(out + (((c0 >> 5) + 1) * 64 + q * 16 + f) * 16) = pk8(B[i]);
    }
    __syncthreads();
    u32x4* dst = (u32x4*)(a.ws + OFF_W1F + (size_t)b * 20 * 1024);
#pragma unroll
    for (int i = 0; i < 3; ++i) { const int id = tid + 512 * i; if (id < 1280) dst[id] = *(const u32x4*)(out + id * 16); }
}

template <bool SW> DI void w2_frags(const char* img, char* dstbase, int b, int lane, int w) {
    const int g = lane >> 4, i16 = lane & 15, qq = i16 >> 2, pp = i16 & 3;
    const rsrc_t rd = mk_rsrc(dstbase, W2F_BYTES);
#pragma unroll
    for (int i = 0; i < 5; ++i) {
        const int cu = w + 8 * i, c = 16 * cu + 4 * pp;
        const char* base = img + (4 * g + qq) * ROWST + (SW ? sw_off(c) : c * 2);
        const bf16x8 v = cat8(tr_read(base), tr_read(base + 16 * ROWST));
        __builtin_amdgcn_raw_buffer_store_b128(__builtin_bit_cast(u32x4, v), rd, lane * 16, (cu * 54 + b) * 1024, SC1);
    }
}
constexpr int W2_TAB = 32 * ROWST;
DI void task_w2(const PrepArgs& a, int b, char* smem) {
    const int tid = threadIdx.x, lane = tid & 63, w = tid >> 6;
    u32x4 v[5];
#pragma unroll
    for (int i = 0; i < 5; ++i) {
        const int id = tid + 512 * i, r = id / 80, ch = id % 80;
        v[i] = *(const u32x4*)(a.w2 + (size_t)(32 * b + r) * D_ + ch * 8);
    }
    __builtin_amdgcn_sched_barrier(0);
    rowc_build(smem + W2_TAB, w, lane);
    __builtin_amdgcn_sched_barrier(0);
#pragma unroll
    for (int i = 0; i < 5; ++i) { const int id = tid + 512 * i, r = id / 80, ch = id % 80; *(u32x4*)(smem + r * ROWST + ch * 16) = v[i]; }
    __syncthreads();
    RowC k; rowc_load(k, smem + W2_TAB, lane);
    w2_frags<false>(smem, a.ws + OFF_W2O, b, lane, w);
    const int n = lane & 15, q = lane >> 4, c0 = chunk_c0(n, q);
    u32x4 A8[4], B8[4];
#pragma unroll
    for (int i = 0; i < 4; ++i) {
        float A[8], B[8];
        rowfft(k, smem + (4 * w + i) * ROWST, A, B, lane);
        A8[i] = __builtin_bit_cast(u32x4, pk8(A)); B8[i] = __builtin_bit_cast(u32x4, pk8(B));
    }
    __syncthreads();
#pragma unroll
    for (int i = 0; i < 4; ++i) {
        const int r = 4 * w + i;
        if (n <= 10) *(u32x4*)(smem + r * ROWST + sw_off(c0)) = A8[i];
        if (n >= 1 && n <= 9) *(u32x4*)(smem + r * ROWST + sw_off(c0 + 32)) = B8[i];
    }
    __syncthreads();
    w2_frags<true>(smem, a.ws + OFF_W2F, b, lane, w);
}

DI int kcol_of(int jc) { return jc < 288 ? (jc >> 5) + 1 + 20 * (jc & 31) : jc < 304 ? 10 + 20 * (jc - 288) : 20 * (jc - 304); }
DI void task_Q(const PrepArgs& a, int u) {
    const int tid = threadIdx.x;
#pragma unroll
    for (int ii = 0; ii < 3; ++ii) {
        const int i = tid + 512 * ii;
        if (i < 1280) {
            const int ks = i >> 6, l = i & 63, d = 16 * u + (l & 15), q = l >> 4;
            float v[8];
#pragma unroll
            for (int j = 0; j < 8; j += 2) {
                const int jc = 16 * ks + 4 * q + (j >> 1), kk = kcol_of(jc);
                const float ang = (float)((kk * d) % 640) * (1.0f / 640.0f);
                float c = cosr(ang) * 0.05590169943749474f, s = -sinr(ang) * 0.05590169943749474f;
                if (jc == 304) { c = ((d & 1) ? -1.f : 1.f) * 0.03952847075210474f; s = 0.03952847075210474f; }
                v[j] = c; v[j + 1] = s;
            }
            __builtin_amdgcn_raw_buffer_store_b128(__builtin_bit_cast(u32x4, pk8(v)), mk_rsrc(a.ws + OFF_W1F, W1F_BYTES), i * 16, (108 + u) * 20480, SC1);
        }
    }
}
DI void task_b(const PrepArgs& a, char* smem) {
    const int tid = threadIdx.x, lane = tid & 63;
    if (tid < 80) *(u32x4*)(smem + tid * 16) = *(const u32x4*)(a.b2 + tid * 8);
    rowc_build(smem + PREP_TAB, tid >> 6, lane);
    __syncthreads();
    RowC k; rowc_load(k, smem + PREP_TAB, lane);
    float A[8], B[8];
    rowfft(k, smem, A, B, lane);
    if (tid < 64) {
        const int n = lane & 15, q = lane >> 4, c0 = chunk_c0(n, q);
        float* o = (float*)(a.ws + OFF_B2P);
        if (n <= 10) for (int e = 0; e < 8; ++e) o[c0 + e] = A[e];
        if (n >= 1 && n <= 9) for (int e = 0; e < 8; ++e) o[c0 + 32 + e] = B[e];
    }
}

constexpr int NB_K0 = 128 + 108 + 160 + 1;
constexpr int MAIN_CODE_PREFETCH = 61440;
DI void prefetch_main_code(int slice, int lane) {
    u32 lo, hi;
    asm volatile("s_getpc_b64 s[40:41]\n\ts_add_u32 s40, s40, _Z6k_main8MainArgs@rel32@lo+4\n\ts_addc_u32 s41, s41, _Z6k_main8MainArgs@rel32@hi+12\n\t"
                 "s_mov_b32 %0, s40\n\ts_mov_b32 %1, s41" : "=s"(lo), "=s"(hi) :: "s40", "s41", "scc");
    const u64 base = ((u64)hi << 32) | lo;
    const char* p = (const char*)base + slice * 4096 + lane * 16;
    u32x4 t0, t1, t2, t3;
    asm volatile("global_load_dwordx4 %0, %4, off\n\tglobal_load_dwordx4 %1, %4, off offset:1024\n\tglobal_load_dwordx4 %2, %4, off offset:2048\n\t"
                 "global_load_dwordx4 %3, %4, off offset:3072\n\ts_waitcnt vmcnt(0)"
                 : "=&v"(t0), "=&v"(t1), "=&v"(t2), "=&v"(t3) : "v"(p) : "memory");
}
__global__ __launch_bounds__(512, 4) void k0_prep(PrepArgs a) {
    extern __shared__ __attribute__((aligned(16))) char smem[];
    const int b = blockIdx.x;
    if (b >= 236 && b < 236 + 8 * (MAIN_CODE_PREFETCH / 4096) && (threadIdx.x >> 6) == 7) prefetch_main_code((b - 236) >> 3, threadIdx.x & 63);
    if (b < 128) task_x(a, b, smem);
    else if (b < 236) task_w1(a, b - 128, smem);
    else if (b < 396) task_W(a, b - 236);
    else task_b(a, smem);
}

template <int N> DI void wait_vm() { asm volatile("s_waitcnt vmcnt(%0)" ::"n"(N) : "memory"); }
DI void wait_lgkm() { asm volatile("s_waitcnt lgkmcnt(0)" ::: "memory"); }
DI void barrier() { __builtin_amdgcn_s_barrier(); }
template <bool SC = false> DI void dma16(u32x4 srd, int voff, int soff, u32 ldsaddr) {
    if (SC) asm volatile("s_mov_b32 m0, %0\n\ts_nop 0\n\tbuffer_load_dwordx4 %1, %2, %3 offen sc1 lds" ::"s"(ldsaddr), "v"(voff), "s"(srd), "s"(soff) : "memory", "m0");
    else asm volatile("s_mov_b32 m0, %0\n\ts_nop 0\n\tbuffer_load_dwordx4 %1, %2, %3 offen lds" ::"s"(ldsaddr), "v"(voff), "s"(srd), "s"(soff) : "memory", "m0");
}
template <int LIM, bool SC = false> DI void dma16_lim(u32x4 srd, int voff, int soff, u32 ldsaddr, int w) {
    if (SC) asm volatile("s_cmp_lt_i32 %4, %5\n\ts_cbranch_scc0 .Lskip%=\n\ts_mov_b32 m0, %0\n\ts_nop 0\n\tbuffer_load_dwordx4 %1, %2, %3 offen sc1 lds\n.Lskip%=:"
                 ::"s"(ldsaddr), "v"(voff), "s"(srd), "s"(soff), "s"(w), "n"(LIM) : "memory", "m0", "scc");
    else asm volatile("s_cmp_lt_i32 %4, %5\n\ts_cbranch_scc0 .Lskip%=\n\ts_mov_b32 m0, %0\n\ts_nop 0\n\tbuffer_load_dwordx4 %1, %2, %3 offen lds\n.Lskip%=:"
                 ::"s"(ldsaddr), "v"(voff), "s"(srd), "s"(soff), "s"(w), "n"(LIM) : "memory", "m0", "scc");
}
DI u32x4 mk_srd(const void* p, size_t bytes) {
    const u64 a = (u64)p;
    u32x4 r = {(u32)a, (u32)(a >> 32) & 0xffffu, (u32)bytes, 0x00020000u};
    return r;
}
DI float row_sum15(float v) {
    v += __builtin_bit_cast(float, __builtin_amdgcn_update_dpp(0, __builtin_bit_cast(int, v), 0x111, 0xf, 0xf, true));
    v += __builtin_bit_cast(float, __builtin_amdgcn_update_dpp(0, __builtin_bit_cast(int, v), 0x112, 0xf, 0xf, true));
    v += __builtin_bit_cast(float, __builtin_amdgcn_update_dpp(0, __builtin_bit_cast(int, v), 0x114, 0xf, 0xf, true));
    v += __builtin_bit_cast(float, __builtin_amdgcn_update_dpp(0, __builtin_bit_cast(int, v), 0x118, 0xf, 0xf, true));
    return v;
}
template <int I, int N, class F> DI void static_for(F&& f) {
    if constexpr (I < N) { f(std::integral_constant<int, I>{}); static_for<I + 1, N>(f); }
}

constexpr int G1_P = 3, G1_PW = 5;
typedef __attribute__((address_space(1))) u32 gu32;
DI u32 ld_agent(const u32* p) { return __hip_atomic_load((gu32*)p, __ATOMIC_RELAXED, __HIP_MEMORY_SCOPE_AGENT); }
DI void add_agent(u32* p) { __hip_atomic_fetch_add((gu32*)p, 1u, __ATOMIC_RELAXED, __HIP_MEMORY_SCOPE_AGENT); }
DI void signal_all(char* ws, int which) {
    if (threadIdx.x == 0) {
        u32* c = (u32*)(ws + OFF_CNT + 16384 + which * 8192) + (blockIdx.x & 7) * 32;
#pragma unroll
        for (int r = 0; r < 8; ++r) add_agent(c + r * 256);
    }
}
DI void poll_until(const u32* p, u32 want, char* ws, int lane) {
    for (u32 spins = 0;; ++spins) {
        if (__all(ld_agent(p) >= want)) break;
        __builtin_amdgcn_s_sleep(16);
        if (spins > (1u << 18)) { if (lane == 0) __hip_atomic_store((gu32*)(ws + OFF_CNT + 49152), 1u, __ATOMIC_RELAXED, __HIP_MEMORY_SCOPE_AGENT); break; }
    }
}
DI void poll_all(char* ws, int which, u32 per_shard, int w, int lane) {
    if (w == 0) poll_until((const u32*)(ws + OFF_CNT + 16384 + which * 8192) + ((blockIdx.x >> 3) & 7) * 256 + (lane & 7) * 32, per_shard, ws, lane);
}

struct MainArgs { PrepArgs p; const u16* b1; u16* out; };
DI int img128(int row, int ch) { return row * 128 + ((ch ^ ((row >> 1) & 7)) << 4); }
template <bool LAST> DI void w1_prefetch(char* ws, char* smem, int w, int lane, int u0);

template <int L>
DI void col_task(char* ws, int b, char* smem, int u0) {
    const int tid = threadIdx.x & 255, lane = tid & 63, w = tid >> 6, half = threadIdx.x >> 8;
    const int j = 2 * b + half;
    char* Ure = smem + half * 24576; char* Uim = Ure + 4096; char* Ere = Ure + 8192; char* Eim = Ure + 12288;
    char* Rre = Ure + 16384; char* Rim = Ure + 20480;
    const int n = lane & 15, q = lane >> 4;
    const int wv = __builtin_amdgcn_readfirstlane(threadIdx.x >> 6);
    const rsrc_t rx = mk_rsrc(ws + (L == 0 ? OFF_XT0 : OFF_XT2), XT_BYTES), rs = mk_rsrc(ws + (L == 0 ? OFF_SS0 : OFF_SS2), 2048 * 8);
    u32x4 vr, vi, sv[4];
    auto load_in = [&]() __attribute__((always_inline)) {
        vr = __builtin_amdgcn_raw_buffer_load_b128(rx, (2 * j * S_ + tid * 8) * 2, 0, L == 0 ? 0 : SC1);
        vi = __builtin_amdgcn_raw_buffer_load_b128(rx, ((2 * j + 1) * S_ + tid * 8) * 2, 0, L == 0 ? 0 : SC1);
#pragma unroll
        for (int i = 0; i < 4; ++i) sv[i] = __builtin_amdgcn_raw_buffer_load_b128(rs, tid * 64 + 16 * i, 0, L == 0 ? 0 : SC1);
    };
    if (L == 0) load_in();
    u32x4 wf[2];
#pragma unroll
    for (int t = 0; t < 2; ++t) wf[t] = *(const u32x4*)((const u32*)(ws + OFF_WT) + ((size_t)j * 64 + 16 * w + n) * 32 + 16 * t + 4 * q);
    __builtin_amdgcn_sched_barrier(0);
    F32c f; f32c_init(f, lane);
    bf16x8 c32[2] = {f.c[0], f.c[1]}, s32p[2] = {f.sp[0], f.sp[1]}, s32m[2];
    pin(c32[0]); pin(c32[1]); pin(s32p[0]); pin(s32p[1]);
    bf16x8 c64[2], s64p[2], s64m[2];
    const float k1f = (float)(16 * w + n);
    {
        float c[8], s[8];
#pragma unroll
        for (int e = 0; e < 8; ++e) {
            const float ang = __builtin_amdgcn_fractf((float)(8 * q + e) * k1f * (1.0f / 64.0f));
            c[e] = cosr(ang); s[e] = sinr(ang);
        }
        c64[0] = pk8(c); s64p[0] = pk8(s);
        const u32 sg = (lane & 1) ? 0x80008000u : 0u;
        c64[1] = asbf(__builtin_bit_cast(u32x4, c64[0]) ^ sg); s64p[1] = asbf(__builtin_bit_cast(u32x4, s64p[0]) ^ sg);
        pin(c64[0]); pin(c64[1]); pin(s64p[0]); pin(s64p[1]);
    }
    float twr[8], twi[8], ivr[8], ivi[8];
#pragma unroll
    for (int i = 0; i < 8; ++i) {
        const float af = (float)(16 * (i >> 2) + 4 * q + (i & 3)) * k1f * (1.0f / 2048.0f);
        twr[i] = cosr(af); twi[i] = -sinr(af);
        const float ai = (float)(16 * w + 4 * q + (i & 3)) * (float)(16 * (i >> 2) + n) * (1.0f / 2048.0f);
        ivr[i] = cosr(ai) * (1.0f / 2048.0f); ivi[i] = sinr(ai) * (1.0f / 2048.0f);
        pin(twr[i]); pin(twi[i]); pin(ivr[i]); pin(ivi[i]);
    }
    __builtin_amdgcn_sched_barrier(0);
    if (L == 1) {
        poll_all(ws, 1, 32u, wv, lane);
        barrier();
        load_in();
        __builtin_amdgcn_sched_barrier(0);
    }
    s64m[0] = neg8(s64p[0]); s64m[1] = neg8(s64p[1]);
    {
        float r[8];
#pragma unroll
        for (int i = 0; i < 8; ++i) {
            const float s = (float)sv[i >> 1][2 * (i & 1) + 1] * 4294967296.0f + (float)sv[i >> 1][2 * (i & 1)];
            r[i] = __builtin_amdgcn_rsqf(s * (1.0f / (SS_SCALE * 640.0f)) + EPS);
        }
        const bf16x8 ur = pk8(bflo(vr[0]) * r[0], bfhi(vr[0]) * r[1], bflo(vr[1]) * r[2], bfhi(vr[1]) * r[3],
                              bflo(vr[2]) * r[4], bfhi(vr[2]) * r[5], bflo(vr[3]) * r[6], bfhi(vr[3]) * r[7]);
        bf16x8 ui = pk8(bflo(vi[0]) * r[0], bfhi(vi[0]) * r[1], bflo(vi[1]) * r[2], bfhi(vi[1]) * r[3],
                        bflo(vi[2]) * r[4], bfhi(vi[2]) * r[5], bflo(vi[3]) * r[6], bfhi(vi[3]) * r[7]);
        if (j == 304) ui = asbf(u32x4{0u, 0u, 0u, 0u});
        const int o = img128(tid >> 3, tid & 7);
        *(bf16x8*)(Ure + o) = ur; *(bf16x8*)(Uim + o) = ui;
        *(u32x4*)(Rre + o) = vr; *(u32x4*)(Rim + o) = vi;
    }
    __syncthreads();
    const f32x4 z = {0.f, 0.f, 0.f, 0.f};
    f32x4 dr[2], di[2];
#pragma unroll
    for (int mt = 0; mt < 2; ++mt) {
        dr[mt] = z; di[mt] = z;
#pragma unroll
        for (int ks = 0; ks < 2; ++ks) {
            const int o = img128(16 * mt + n, 4 * ks + q);
            const bf16x8 ar = *(const bf16x8*)(Ure + o), ai = *(const bf16x8*)(Uim + o);
            dr[mt] = mfma16(ar, c64[ks], dr[mt]); dr[mt] = mfma16(ai, s64p[ks], dr[mt]);
            di[mt] = mfma16(ar, s64m[ks], di[mt]); di[mt] = mfma16(ai, c64[ks], di[mt]);
        }
    }
    float tr[8], ti[8];
#pragma unroll
    for (int i = 0; i < 8; ++i) {
        const float x = dr[i >> 2][i & 3], y = di[i >> 2][i & 3];
        tr[i] = x * twr[i] - y * twi[i]; ti[i] = x * twi[i] + y * twr[i];
    }
    bf16x8 Tr = pk8(tr), Ti = pk8(ti);
    s32m[0] = neg8(s32p[0]); s32m[1] = neg8(s32p[1]);
    f32x4 orr[2], oi[2];
#pragma unroll
    for (int t = 0; t < 2; ++t) {
        orr[t] = mfma16(c32[t], Tr, z); orr[t] = mfma16(s32p[t], Ti, orr[t]);
        oi[t] = mfma16(s32m[t], Tr, z); oi[t] = mfma16(c32[t], Ti, oi[t]);
    }
#pragma unroll
    for (int i = 0; i < 8; ++i) {
        const float x = orr[i >> 2][i & 3], y = oi[i >> 2][i & 3];
        const u32 wv = wf[i >> 2][i & 3];
        const float wre = bflo(wv), wim = bfhi(wv);
        tr[i] = x * wre - y * wim; ti[i] = x * wim + y * wre;
    }
    Tr = pk8(tr); Ti = pk8(ti);
#pragma unroll
    for (int nt = 0; nt < 2; ++nt) {
        f32x4 zr = mfma16(Tr, c32[nt], z); zr = mfma16(Ti, s32m[nt], zr);
        f32x4 zi = mfma16(Tr, s32p[nt], z); zi = mfma16(Ti, c32[nt], zi);
        float er[4], ei[4];
#pragma unroll
        for (int r = 0; r < 4; ++r) {
            const float cr = ivr[4 * nt + r], ci = ivi[4 * nt + r];
            er[r] = zr[r] * cr - zi[r] * ci; ei[r] = zr[r] * ci + zi[r] * cr;
        }
        const int o = img128(16 * nt + n, 2 * w + (q >> 1)) + (q & 1) * 8;
        u32x2 pr = {pk2(er[0], er[1]), pk2(er[2], er[3])}, pi = {pk2(ei[0], ei[1]), pk2(ei[2], ei[3])};
        *(u32x2*)(Ere + o) = pr; *(u32x2*)(Eim + o) = pi;
    }
    __syncthreads();
    const rsrc_t rf = mk_rsrc(ws + (L == 0 ? OFF_XF1 : OFF_XF3), XT_BYTES);
    char* O = Ure;
#pragma unroll
    for (int nt = 0; nt < 2; ++nt) {
        f32x4 yr = z, yi = z;
#pragma unroll
        for (int ks = 0; ks < 2; ++ks) {
            const int o = img128(16 * nt + n, 4 * ks + q);
            const bf16x8 br = *(const bf16x8*)(Ere + o), bi = *(const bf16x8*)(Eim + o);
            yr = mfma16(c64[ks], br, yr); yr = mfma16(s64m[ks], bi, yr);
            yi = mfma16(s64p[ks], br, yi); yi = mfma16(c64[ks], bi, yi);
        }
        if (j == 304) yi = z;
        const int p = (16 * nt + n) * 64 + 16 * w + 4 * q;
        u32x2 rr[2], ri[2];
        { const int o = img128(16 * nt + n, 2 * w + (q >> 1)) + (q & 1) * 8; rr[nt] = *(const u32x2*)(Rre + o); ri[nt] = *(const u32x2*)(Rim + o); }
        const float fr[4] = {bflo(rr[nt][0]) + yr[0], bfhi(rr[nt][0]) + yr[1], bflo(rr[nt][1]) + yr[2], bfhi(rr[nt][1]) + yr[3]};
        const float fi[4] = {bflo(ri[nt][0]) + yi[0], bfhi(ri[nt][0]) + yi[1], bflo(ri[nt][1]) + yi[2], bfhi(ri[nt][1]) + yi[3]};
        u32x4 ov = {pk2(fr[0], fi[0]), pk2(fr[1], fi[1]), pk2(fr[2], fi[2]), pk2(fr[3], fi[3])};
        *(u32x4*)(O + p * 4) = ov;
    }
    __syncthreads();
    {
        const int c0 = 4 * b, dst = ((c0 >> 5) * 64 + ((c0 & 31) >> 3) * 16) * 16 + (c0 & 4) * 2;
        const u32* O0 = (const u32*)smem; const u32* O1 = (const u32*)(smem + 24576);
#pragma unroll
        for (int i = 0; i < 4; ++i) {
            const int p = threadIdx.x + 512 * i;
            u32x2 v = {O0[p], O1[p]};
            __builtin_amdgcn_raw_buffer_store_b64(v, rf, (p >> 4) * 20480 + (p & 15) * 16, dst, SC1);
        }
    }
    wait_lgkm();
    wait_vm<0>();
    barrier();
    signal_all(ws, L == 0 ? 0 : 2);
    w1_prefetch<L == 1>(ws, smem, wv, lane, u0);
}

DI f32x2 gelu_tanh2(f32x2 x) {
    const float k1 = -2.302208198f, k2 = -0.1029432397f;
    const f32x2 c1 = {k1, k1}, c2 = {k2, k2}, one = {1.0f, 1.0f};
    const f32x2 t = x * (x * x * c2 + c1);
    f32x2 e = {__builtin_amdgcn_exp2f(t[0]), __builtin_amdgcn_exp2f(t[1])};
    e += one;
    const f32x2 r = {__builtin_amdgcn_rcpf(e[0]), __builtin_amdgcn_rcpf(e[1])};
    return x * r;
}
constexpr int G2_PH = 3;
constexpr int W2STG = 20480;
DI u32 w2slot(int t) { return 163840 - ((t & 7) + 1) * W2STG; }
template <bool LAST> constexpr int w1slot(int st) { return LAST ? 163840 - (st % 8 + 1) * 20480 : 163840 - (st + 1) * 14336; }

template <bool LAST, int I> DI void w1_piece(u32x4 srd, u32 lds0, int w, int vo, int u0, int st) {
    constexpr int WP = LAST ? 20 : 14;
    const int pc = w + 8 * I;
    const u32 dst = lds0 + w1slot<LAST>(st) + pc * 1024;
    const int so = (u0 + (pc >> 1)) * 20480 + st * 2048 + (pc & 1) * 1024;
    if (8 * I + 7 < WP) dma16<LAST>(srd, vo, so, dst); else dma16_lim<(WP & 7), LAST>(srd, vo, so, dst, w);
}
template <bool LAST> DI void w1_prefetch(char* ws, char* smem, int w, int lane, int u0) {
    const u32x4 srd = mk_srd(ws + OFF_W1F, W1F_BYTES);
    const u32 lds0 = (u32)(size_t)(LAS char*)smem;
#pragma unroll
    for (int st = 0; st < G1_PW; ++st) {
        w1_piece<LAST, 0>(srd, lds0, w, lane * 16, u0, st);
        w1_piece<LAST, 1>(srd, lds0, w, lane * 16, u0, st);
        if (LAST) w1_piece<LAST, 2>(srd, lds0, w, lane * 16, u0, st);
    }
}

constexpr int g1_younger(int ssync, int t, int nlo) {
    int cnt = 0; bool seen = false;
    for (int s = 0; s < ssync; ++s) {
        if (s + G1_PW < 10) { if (seen) cnt += nlo; if (s + G1_PW == t) seen = true; }
        if (s + G1_P < 10 && seen) cnt += 2;
    }
    return cnt;
}

template <int I> DI void w2_issue1(u32x4 srd, u32 lds0, int w, int vo, int ct, int t) {
    const int pc = w + 8 * I, q = pc / 5, cu = pc - 5 * q;
    const int so = ((5 * ct + cu) * 54 + (q < 2 ? 14 * q : 28 + 13 * (q - 2)) + t) * 1024;
    if (I < 2) dma16<true>(srd, vo, so, lds0 + w2slot(t) + pc * 1024); else dma16_lim<4, true>(srd, vo, so, lds0 + w2slot(t) + pc * 1024, w);
}
DI void w2_issue(u32x4 srd, u32 lds0, int w, int vo, int ct, int t) {
    w2_issue1<0>(srd, lds0, w, vo, ct, t); w2_issue1<1>(srd, lds0, w, vo, ct, t); w2_issue1<2>(srd, lds0, w, vo, ct, t);
}

template <int NF, bool LAST, int KG>
DI void g1_phase(const MainArgs& a, char* smem, int w, int lane, int pt, int u0, int nu, int ct2) {
    constexpr int P = G1_P, PW = G1_PW;
    constexpr int WP = 2 * NF, WR = (WP + 7) / 8, NLO = WP / 8, KID = LAST ? 5 : 2;
    char* ws = a.p.ws;
    const int pq = w & 3, g = lane >> 4;
    const u32x4 srdW = mk_srd(ws + OFF_W1F, W1F_BYTES), srd2 = mk_srd(ws + (LAST ? OFF_W2O : OFF_W2F), W2F_BYTES);
    const u32 lds0 = (u32)(size_t)(LAS char*)smem;
    const rsrc_t rx = mk_rsrc(ws + (LAST ? OFF_XF3 : OFF_XF1), XT_BYTES);
    const int xs = (8 * pt + 2 * pq) * 20480 + KG * 1024, vo = lane * 16;
    auto xload = [&](int st, int pu) __attribute__((always_inline)) { return asbf(__builtin_amdgcn_raw_buffer_load_b128(rx, vo, xs + pu * 20480 + st * 2048, SC1)); };
    bf16x8 xb[P][2];
#pragma unroll
    for (int st = 0; st < P; ++st) { xb[st][0] = xload(st, 0); xb[st][1] = xload(st, 1); __builtin_amdgcn_sched_barrier(0); }
    f32x4 acc[NF][2];
#pragma unroll
    for (int i = 0; i < NF; ++i) { acc[i][0] = f32x4{0, 0, 0, 0}; acc[i][1] = f32x4{0, 0, 0, 0}; }
    f32x4 gr[2] = {f32x4{0, 0, 0, 0}, f32x4{0, 0, 0, 0}};
    bf16x8 wa[2][NF];
#pragma unroll
    for (int i = 0; i < NF; ++i) wa[0][i] = *(const bf16x8*)(smem + w1slot<LAST>(0) + KG * 1024 + lane * 16 + i * 2048);
    static_for<0, 10>([&](auto ST) __attribute__((always_inline)) {
        constexpr int st = decltype(ST)::value;
        if constexpr (LAST && st == 2) { wait_lgkm(); barrier(); }
        if constexpr (st == 4 || st == 7) {
            wait_lgkm();
            wait_vm<g1_younger(st, st == 7 ? 9 : 7, NLO)>();
            barrier();
        }
        bf16x8 nx[2];
        const char* rbase = smem + w1slot<LAST>(st + 1) + KG * 1024 + lane * 16;
        constexpr int NV = WR + 2, V0 = KG == 0 ? 0 : NF - NV;
        static_for<0, NF>([&](auto I) __attribute__((always_inline)) {
            constexpr int i = decltype(I)::value;
            __builtin_amdgcn_sched_barrier(0);
            acc[i][0] = mfma16(wa[st & 1][i], xb[st % P][0], acc[i][0]);
            if constexpr (st + 1 < 10) wa[(st + 1) & 1][i] = *(const bf16x8*)(rbase + i * 2048);
            acc[i][1] = mfma16(wa[st & 1][i], xb[st % P][1], acc[i][1]);
            if constexpr (i < 2) {
                u32x4 xv = __builtin_bit_cast(u32x4, xb[st % P][i]);
                if constexpr (st == 9 && KG == 1) { if (g == 0) xv[0] &= 0x0000ffffu; }
                gr[i] = mfma16(asbf(xv), asbf(xv), gr[i]);
            }
            __builtin_amdgcn_sched_barrier(0);
            constexpr int v = i - V0;
            if constexpr (v >= 0 && v < WR) { if constexpr (st + PW < 10) w1_piece<LAST, v>(srdW, lds0, w, vo, u0, st + PW); }
            else if constexpr (v >= WR && v < NV) { if constexpr (st + P < 10) nx[v - WR] = xload(st + P, v - WR); }
            if constexpr (st >= 6) {
                constexpr int sl = i - (KG == 0 ? 0 : NF - 4), e = 4 * (st - 6) + sl;
                if constexpr (sl >= 0 && sl < 4 && e < (LAST ? 9 : 15)) w2_issue1<e % 3>(srd2, lds0, w, vo, ct2, (LAST ? 2 : 0) + e / 3);
            }
        });
        __builtin_amdgcn_sched_barrier(0);
        if constexpr (st + P < 10) { xb[st % P][0] = nx[0]; xb[st % P][1] = nx[1]; }
    });
    u32x2 bv[NF];
#pragma unroll
    for (int i = 0; i < NF; ++i) bv[i] = *(const u32x2*)(a.b1 + min(16 * (u0 + i), F_ - 16) + 4 * g);
    __builtin_amdgcn_sched_barrier(0);
    wait_lgkm();
    barrier();
    float* sx = (float*)(smem + 8 * NF * 512);
#pragma unroll
    for (int i = 0; i < NF; ++i) {
        const f32x4 t = acc[i][1 - KG];
        u32x2 pv = {pk2(t[0], t[1]), pk2(t[2], t[3])};
        *(u32x2*)(smem + ((w * NF + i) * 64 + lane) * 8) = pv;
    }
    float st2[2];
    {
        const int n = lane & 15;
#pragma unroll
        for (int pu = 0; pu < 2; ++pu) {
            const float d01 = (n & 1) ? gr[pu][1] : gr[pu][0], d23 = (n & 1) ? gr[pu][3] : gr[pu][2];
            float d = (n & 2) ? d23 : d01;
            d = (g == (n >> 2)) ? d : 0.f;
            d += __shfl_xor(d, 16); d += __shfl_xor(d, 32);
            st2[pu] = d;
        }
    }
    if (lane < 16) sx[w * 16 + lane] = st2[1 - KG];
    wait_lgkm();
    barrier();
    const float v = (st2[KG] + sx[(w ^ 4) * 16 + (lane & 15)]) * (1.0f / 640.0f);
    const float r1 = __builtin_amdgcn_rsqf(v + EPS);
    const float rr = r1 * __builtin_amdgcn_rsqf(v * r1 * r1 + EPS);
    const int punit = 8 * pt + 2 * pq + KG;
    u32x2 ov[NF], pv[NF];
#pragma unroll
    for (int i = 0; i < NF; ++i) pv[i] = *(const u32x2*)(smem + (((w ^ 4) * NF + i) * 64 + lane) * 8);
    static_for<0, NF>([&](auto I) __attribute__((always_inline)) {
        constexpr int i = decltype(I)::value;
        if constexpr (LAST ? i < 2 : i == 0) {
            __builtin_amdgcn_sched_barrier(0);
            w2_issue(srd2, lds0, w, vo, ct2, LAST ? i : 5);
            __builtin_amdgcn_sched_barrier(0);
        }
        const f32x2 o0 = {acc[i][KG][0] + bflo(pv[i][0]), acc[i][KG][1] + bfhi(pv[i][0])}, o1 = {acc[i][KG][2] + bflo(pv[i][1]), acc[i][KG][3] + bfhi(pv[i][1])};
        const f32x2 rr2 = {rr, rr};
        const f32x2 b0 = {bflo(bv[i][0]), bfhi(bv[i][0])}, b1 = {bflo(bv[i][1]), bfhi(bv[i][1])};
        f32x2 h0 = gelu_tanh2(o0 * rr2 + b0), h1 = gelu_tanh2(o1 * rr2 + b1);
        if (LAST) {
            const bool lin = u0 + i >= 108;
            h0[0] = lin ? o0[0] : h0[0]; h0[1] = lin ? o0[1] : h0[1]; h1[0] = lin ? o1[0] : h1[0]; h1[1] = lin ? o1[1] : h1[1];
        }
        ov[i] = u32x2{pk2(h0[0], h0[1]), pk2(h1[0], h1[1])};
    });
    __builtin_amdgcn_sched_barrier(0);
    const rsrc_t rh = mk_rsrc(ws + (LAST ? OFF_H2 : OFF_H), H_BYTES), r3 = mk_rsrc(ws + OFF_X3, X3_BYTES);
    const int hb = punit * 54 * 1024;
    auto st1 = [&](int i) __attribute__((always_inline)) {
        const int u = u0 + i;
        if (i < nu) {
            if (!LAST || u < 108) __builtin_amdgcn_raw_buffer_store_b64(ov[i], rh, lane * 16 + (u & 1) * 8, hb + (u >> 1) * 1024, SC1);
            else __builtin_amdgcn_raw_buffer_store_b64(ov[i], r3, lane * 8, (punit * 40 + (u - 108)) * 512, SC1);
        }
    };
    auto st2p = [&](int i) __attribute__((always_inline)) {
        const int u = u0 + i;
        if (i + 1 < nu && (!LAST || u + 1 < 108)) {
            u32x4 q4 = {ov[i][0], ov[i][1], ov[i + 1][0], ov[i + 1][1]};
            __builtin_amdgcn_raw_buffer_store_b128(q4, rh, lane * 16, hb + (u >> 1) * 1024, SC1);
        } else { st1(i); st1(i + 1); }
    };
    if (u0 & 1) {
        st1(0);
#pragma unroll
        for (int i = 1; i + 1 < NF; i += 2) st2p(i);
        if ((NF & 1) == 0) st1(NF - 1);
    } else {
#pragma unroll
        for (int i = 0; i + 1 < NF; i += 2) st2p(i);
        if (NF & 1) st1(NF - 1);
    }
}

template <bool LAST>
DI void g2_phase(const MainArgs& a, char* smem, int w, int lane, int pt1, int pt, int ct) {
    constexpr int PH = G2_PH, KID = LAST ? 6 : 3;
    char* ws = a.p.ws;
    u32* cnt = (u32*)(ws + OFF_CNT + (LAST ? 8192 : 0));
    const int pg = w & 1, kq = w >> 1, n = lane & 15, q = lane >> 4;
    const int kb = kq < 2 ? 14 * kq : 28 + 13 * (kq - 2), nk = kq < 2 ? 14 : 13;
    const u32x4 srd2 = mk_srd(ws + (LAST ? OFF_W2O : OFF_W2F), W2F_BYTES);
    const u32 lds0 = (u32)(size_t)(LAS char*)smem;
    const rsrc_t rh = mk_rsrc(ws + (LAST ? OFF_H2 : OFF_H), H_BYTES), rw = mk_rsrc(ws + (LAST ? OFF_W2O : OFF_W2F), W2F_BYTES);
    const int vo = lane * 16, hs = ((4 * pt + 2 * pg) * 54 + kb) * 1024;
    bf16x8 wr[6][5];
    asm volatile("" ::: "memory");
    __builtin_amdgcn_sched_barrier(0);
#pragma unroll
    for (int t = 0; t < 3; ++t)
#pragma unroll
        for (int i = 0; i < 5; ++i) wr[t][i] = asbf(__builtin_amdgcn_raw_buffer_load_b128(rw, vo, ((5 * ct + i) * 54 + kb + 8 + t) * 1024, SC1));
    __builtin_amdgcn_sched_barrier(0);
    wait_lgkm();
    wait_vm<15>();
    barrier();
    if (threadIdx.x == 0) add_agent(cnt + 64 * pt1);
#pragma unroll
    for (int t = LAST ? 5 : 6; t < 8; ++t) w2_issue(srd2, lds0, w, vo, ct, t);
    __builtin_amdgcn_sched_barrier(0);
#pragma unroll
    for (int t = 3; t < 6; ++t)
#pragma unroll
        for (int i = 0; i < 5; ++i) wr[t][i] = asbf(__builtin_amdgcn_raw_buffer_load_b128(rw, vo, ((5 * ct + i) * 54 + kb + 8 + t) * 1024, SC1));
    __builtin_amdgcn_sched_barrier(0);
    if (w == 0) poll_until(cnt + 64 * (pt >> 1), 16u, ws, lane);
    wait_vm<0>();
    barrier();
    bf16x8 hb[PH][2];
    auto hload = [&](int t, int pu) __attribute__((always_inline)) {
        const int k = t < 13 ? t : nk - 1;
        return asbf(__builtin_amdgcn_raw_buffer_load_b128(rh, vo, hs + (pu * 54 + k) * 1024, SC1));
    };
#pragma unroll
    for (int t = 0; t < PH; ++t) { hb[t][0] = hload(t, 0); hb[t][1] = hload(t, 1); __builtin_amdgcn_sched_barrier(0); }
    u32x2 xv[3], bv[3]; float bb[3];
    f32x4 acc[2][5];
#pragma unroll
    for (int i = 0; i < 5; ++i) { acc[0][i] = f32x4{0, 0, 0, 0}; acc[1][i] = f32x4{0, 0, 0, 0}; }
    bf16x8 wa[2][5];
    const char* wbase = smem + kq * 5120 + lane * 16;
#pragma unroll
    for (int i = 0; i < 5; ++i) wa[0][i] = *(const bf16x8*)(wbase + w2slot(0) + i * 1024);
    static_for<0, 14>([&](auto T) __attribute__((always_inline)) {
        constexpr int t = decltype(T)::value;
        if constexpr (t == 10) {
            __builtin_amdgcn_sched_barrier(0);
#pragma unroll
            for (int j = 0; j < 3; ++j) {
                const int tt = min(kq + 4 * j, 9), pu = 2 * pg + tt / 5, cu = tt % 5;
                if (!LAST) {
                    const int c = 80 * ct + 16 * cu + n;
                    const int off = (((4 * pt + pu) * 20 + (c >> 5)) * 64 + ((c & 31) >> 3) * 16 + 4 * q) * 16 + (c & 7) * 2;
                    const rsrc_t r1 = mk_rsrc(ws + OFF_XF1, XT_BYTES);
                    const u32 e0 = __builtin_amdgcn_raw_buffer_load_b16(r1, off, 0, SC1), e1 = __builtin_amdgcn_raw_buffer_load_b16(r1, off + 16, 0, SC1);
                    const u32 e2 = __builtin_amdgcn_raw_buffer_load_b16(r1, off + 32, 0, SC1), e3 = __builtin_amdgcn_raw_buffer_load_b16(r1, off + 48, 0, SC1);
                    xv[j] = u32x2{e0 | (e1 << 16), e2 | (e3 << 16)};
                    bb[j] = ((const float*)(ws + OFF_B2P))[c];
                } else {
                    xv[j] = __builtin_amdgcn_raw_buffer_load_b64(mk_rsrc(ws + OFF_X3, X3_BYTES), lane * 8, ((4 * pt + pu) * 40 + 5 * ct + cu) * 512, SC1);
                    bv[j] = *(const u32x2*)(a.p.b2 + 80 * ct + 16 * cu + 4 * q);
                }
            }
        }
        bf16x8 nh[2];
        auto step = [&]() __attribute__((always_inline)) {
            static_for<0, 5>([&](auto I) __attribute__((always_inline)) {
                constexpr int i = decltype(I)::value;
                __builtin_amdgcn_sched_barrier(0);
                bf16x8 wv;
                if constexpr (t < 8) wv = wa[t & 1][i]; else wv = wr[t - 8][i];
                acc[0][i] = LAST ? mfma16(wv, hb[t % PH][0], acc[0][i]) : mfma16(hb[t % PH][0], wv, acc[0][i]);
                if constexpr (t + 1 < 8) wa[(t + 1) & 1][i] = *(const bf16x8*)(wbase + w2slot(t + 1) + i * 1024);
                acc[1][i] = LAST ? mfma16(wv, hb[t % PH][1], acc[1][i]) : mfma16(hb[t % PH][1], wv, acc[1][i]);
                __builtin_amdgcn_sched_barrier(0);
                if constexpr (t + PH < 14 && (i == 1 || i == 3)) nh[i >> 1] = hload(t + PH, i >> 1);
            });
        };
        if constexpr (t < 13) step(); else { if (nk == 14) step(); }
        __builtin_amdgcn_sched_barrier(0);
        if constexpr (t + PH < 14) { hb[t % PH][0] = nh[0]; hb[t % PH][1] = nh[1]; }
    });
    wait_lgkm();
    barrier();
#pragma unroll
    for (int t = 0; t < 10; ++t)
        if ((t & 3) != kq) *(f32x4*)(smem + ((w * 10 + t) * 64 + lane) * 16) = acc[t / 5][t % 5];
    wait_lgkm();
    barrier();
    float ssp[2][4];
#pragma unroll
    for (int r = 0; r < 4; ++r) { ssp[0][r] = 0.f; ssp[1][r] = 0.f; }
    const rsrc_t ro = mk_rsrc(ws + OFF_XT2, XT_BYTES);
    auto finish = [&](auto KQ) __attribute__((always_inline)) {
        constexpr int kqc = decltype(KQ)::value;
        static_for<0, 3>([&](auto J) __attribute__((always_inline)) {
            constexpr int j = decltype(J)::value, t = kqc + 4 * j;
            if constexpr (t < 10) {
                constexpr int pl = t / 5, cu = t % 5;
                f32x4 o = acc[pl][cu];
#pragma unroll
                for (int s = 1; s < 4; ++s) o += *(const f32x4*)(smem + (((pg + 2 * ((kqc + s) & 3)) * 10 + t) * 64 + lane) * 16);
                const int pu = 2 * pg + pl;
                if (!LAST) {
                    const int c = 80 * ct + 16 * cu + n, p = 64 * pt + 16 * pu + 4 * q;
                    const u32 o0 = pk2(bflo(xv[j][0]) + o[0] + bb[j], bfhi(xv[j][0]) + o[1] + bb[j]);
                    const u32 o1 = pk2(bflo(xv[j][1]) + o[2] + bb[j], bfhi(xv[j][1]) + o[3] + bb[j]);
                    u32x2 ov = {o0, o1};
                    __builtin_amdgcn_raw_buffer_store_b64(ov, ro, (c * S_ + p) * 2, 0, SC1);
                    const float m = c == 609 ? 0.f : 1.f;
                    ssp[pl][0] += m * bflo(o0) * bflo(o0); ssp[pl][1] += m * bfhi(o0) * bfhi(o0);
                    ssp[pl][2] += m * bflo(o1) * bflo(o1); ssp[pl][3] += m * bfhi(o1) * bfhi(o1);
                } else {
                    const int p = 16 * (4 * pt + pu) + n, d = 80 * ct + 16 * cu + 4 * q;
                    u32x2 ov = {pk2(bflo(xv[j][0]) + o[0] + bflo(bv[j][0]), bfhi(xv[j][0]) + o[1] + bfhi(bv[j][0])),
                                pk2(bflo(xv[j][1]) + o[2] + bflo(bv[j][1]), bfhi(xv[j][1]) + o[3] + bfhi(bv[j][1]))};
                    *(u32x2*)(a.out + (size_t)tok_of_p(p) * D_ + d) = ov;
                }
            }
        });
    };
    if (kq == 0) finish(std::integral_constant<int, 0>{});
    else if (kq == 1) finish(std::integral_constant<int, 1>{});
    else if (kq == 2) finish(std::integral_constant<int, 2>{});
    else finish(std::integral_constant<int, 3>{});
    if (!LAST) {
        float* sp = (float*)smem;
#pragma unroll
        for (int pl = 0; pl < 2; ++pl)
#pragma unroll
            for (int r = 0; r < 4; ++r) {
                const float v = row_sum15(ssp[pl][r]);
                if (n == 15) sp[w * 32 + 16 * pl + 4 * q + r] = v;
            }
        wait_lgkm();
        barrier();
        if (w == 0) {
            const int g2 = lane >> 5, r = lane & 31;
            const float v = (sp[g2 * 32 + r] + sp[(g2 + 2) * 32 + r]) + (sp[(g2 + 4) * 32 + r] + sp[(g2 + 6) * 32 + r]);
            __hip_atomic_fetch_add((__attribute__((address_space(1))) u64*)(ws + OFF_SS2) + 64 * pt + lane, ss_from_float(v), __ATOMIC_RELAXED, __HIP_MEMORY_SCOPE_AGENT);
        }
        wait_lgkm();
        wait_vm<0>();
        barrier();
        signal_all(ws, 1);
    }
}

template <bool LAST>
DI void mlp(const MainArgs& a, char* smem, int w, int lane, int u0) {
    constexpr int NF = LAST ? 10 : 7;
    const int x = blockIdx.x & 7, ib = blockIdx.x >> 3;
    const int pt = 4 * (x >> 1) + (ib & 3), ft = 8 * (x & 1) + (ib >> 2);
    const int pt2 = 8 * (x >> 1) + (ib & 7), ct2 = 4 * (x & 1) + (ib >> 3);
    const int nu = LAST ? (ft < 4 ? 10 : 9) : (ft < 12 ? 7 : 6);
    if (LAST) poll_all(a.p.ws, 2, 20u, w, lane);
    else if (w == 0) {
        const int sh = lane & 7, wh = (lane >> 3) & 1;
        poll_until((const u32*)(a.p.ws + OFF_CNT + 16384 + (wh ? 3 : 0) * 8192) + ((blockIdx.x >> 3) & 7) * 256 + sh * 32, wh ? (sh < 6 ? 12u : 11u) : 20u, a.p.ws, lane);
    }
    wait_vm<0>();
    barrier();
    if (w >= 4) __builtin_amdgcn_s_setprio(1);
    if (w < 4) g1_phase<NF, LAST, 0>(a, smem, w, lane, pt, u0, nu, ct2);
    else g1_phase<NF, LAST, 1>(a, smem, w, lane, pt, u0, nu, ct2);
    __builtin_amdgcn_s_setprio(0);
    g2_phase<LAST>(a, smem, w, lane, pt, pt2, ct2);
}

__global__ __launch_bounds__(512, 2) void k_main(MainArgs a) {
    extern __shared__ __attribute__((aligned(16))) char smem[];
    const int lane = threadIdx.x & 63, b = blockIdx.x;
    const int w = __builtin_amdgcn_readfirstlane(threadIdx.x >> 6);
    const int ft = 8 * (b & 1) + (b >> 5);
    const int u0a = ft < 12 ? 7 * ft : 84 + 6 * (ft - 12), u0b = ft < 4 ? 10 * ft : 40 + 9 * (ft - 4);
    if (b < 160) col_task<0>(a.p.ws, b, smem, u0a);
    else {
        w1_prefetch<false>(a.p.ws, smem, w, lane, u0a);
        if (b < 254) {
            if (b < 214) task_w2(a.p, b - 160, smem); else task_Q(a.p, b - 214);
            wait_lgkm();
            wait_vm<0>();
            barrier();
            signal_all(a.p.ws, 3);
        }
    }
    mlp<false>(a, smem, w, lane, u0a);
    if (b < 160) col_task<1>(a.p.ws, b, smem, u0b); else w1_prefetch<true>(a.p.ws, smem, w, lane, u0b);
    mlp<true>(a, smem, w, lane, u0b);
}

extern "C" void kernel_launch(void* const* d_in, const int* in_sizes, int n_in, void* d_out, int out_size, void* d_ws,
                              size_t ws_size, hipStream_t stream) {
    char* ws = (char*)d_ws;
    constexpr int MAIN_LDS = 163840;
    (void)hipFuncSetAttribute((const void*)k_main, hipFuncAttributeMaxDynamicSharedMemorySize, MAIN_LDS);
    PrepArgs pa{(const u16*)d_in[0], (const u16*)d_in[1], (const u16*)d_in[2], (const u16*)d_in[4], (const u16*)d_in[6], (const u16*)d_in[7], ws};
    k0_prep<<<NB_K0, 512, PREP_LDS, stream>>>(pa);
    MainArgs ma{pa, (const u16*)d_in[5], (u16*)d_out};
    k_main<<<256, 512, MAIN_LDS, stream>>>(ma);
}
```

```cpp
#include <hip/hip_runtime.h>
#include <hip/hip_bf16.h>
#include <stdint.h>
#include <type_traits>

typedef unsigned short u16;
typedef unsigned int u32;
typedef unsigned long long u64;
typedef short s16x4 __attribute__((ext_vector_type(4)));
typedef __bf16 bf16x8 __attribute__((ext_vector_type(8)));
typedef __bf16 bf16x2 __attribute__((ext_vector_type(2)));
typedef float f32x4 __attribute__((ext_vector_type(4)));
typedef float f32x2 __attribute__((ext_vector_type(2)));
typedef u32 u32x4 __attribute__((ext_vector_type(4)));
typedef u32 u32x2 __attribute__((ext_vector_type(2)));
typedef __amdgpu_buffer_rsrc_t rsrc_t;
#define LAS __attribute__((address_space(3)))
#define DI __device__ __forceinline__

constexpr int S_ = 2048, D_ = 640, F_ = 1728, DF_ = 321;
constexpr float EPS = 1e-5f;
constexpr int SC1 = 16;
constexpr float SS_SCALE = 1048576.0f;

constexpr size_t XT_BYTES = (size_t)D_ * S_ * 2;
constexpr size_t OFF_XT0 = 0;
constexpr size_t OFF_XT1 = OFF_XT0 + XT_BYTES;
constexpr size_t OFF_XT2 = OFF_XT1 + XT_BYTES;
constexpr size_t OFF_XT3 = OFF_XT2 + XT_BYTES;
constexpr size_t OFF_WT = OFF_XT3 + XT_BYTES;
constexpr size_t OFF_W1F = OFF_WT + (size_t)320 * 2048 * 4;
constexpr size_t W1F_BYTES = (size_t)148 * 20 * 1024;
constexpr size_t OFF_W2F = OFF_W1F + W1F_BYTES;
constexpr size_t W2F_BYTES = (size_t)40 * 54 * 1024;
constexpr size_t OFF_W2O = OFF_W2F + W2F_BYTES;
constexpr size_t OFF_H = OFF_W2O + W2F_BYTES;
constexpr size_t H_BYTES = (size_t)128 * 54 * 1024;
constexpr size_t OFF_H2 = OFF_H;
constexpr size_t OFF_X3 = OFF_H + 2 * H_BYTES;
constexpr size_t OFF_SS0 = OFF_X3 + (size_t)128 * 40 * 512;
constexpr size_t OFF_SS2 = OFF_SS0 + 2048 * 8;
constexpr size_t OFF_B2P = OFF_SS2 + 2048 * 8;
constexpr size_t OFF_XF1 = OFF_B2P + 4096;
constexpr size_t OFF_XF3 = OFF_XF1;
constexpr size_t X3_BYTES = (size_t)128 * 40 * 512;
constexpr size_t OFF_CNT = OFF_XF1 + 2 * XT_BYTES;
constexpr size_t WS_END = OFF_CNT + 65536;

#define STAMP(k, i)
#define CSTAMP(k, i)
#define END_STAMP(k)

DI f32x4 mfma16(bf16x8 a, bf16x8 b, f32x4 c) { return __builtin_amdgcn_mfma_f32_16x16x32_bf16(a, b, c, 0, 0, 0); }
DI u32 pk2(float a, float b) { f32x2 v = {a, b}; const bf16x2 r = __builtin_convertvector(v, bf16x2); return __builtin_bit_cast(u32, r); }
DI bf16x8 pk8(float a0, float a1, float a2, float a3, float a4, float a5, float a6, float a7) {
    u32x4 r = {pk2(a0, a1), pk2(a2, a3), pk2(a4, a5), pk2(a6, a7)};
    return __builtin_bit_cast(bf16x8, r);
}
DI bf16x8 pk8(const float (&a)[8]) { return pk8(a[0], a[1], a[2], a[3], a[4], a[5], a[6], a[7]); }
DI bf16x8 asbf(u32x4 r) { return __builtin_bit_cast(bf16x8, r); }
DI bf16x8 neg8(bf16x8 a) { u32x4 r = __builtin_bit_cast(u32x4, a); r ^= 0x80008000u; return __builtin_bit_cast(bf16x8, r); }
DI float bflo(u32 v) { return __builtin_bit_cast(float, v << 16); }
DI float bfhi(u32 v) { return __builtin_bit_cast(float, v & 0xffff0000u); }
DI float cosr(float rev) { return __builtin_amdgcn_cosf(rev); }
DI float sinr(float rev) { return __builtin_amdgcn_sinf(rev); }
DI int slot32(int q, int j) { return j < 4 ? 4 * q + j : 16 + 4 * q + (j - 4); }
DI s16x4 tr_read(const char* p) { return __builtin_amdgcn_ds_read_tr16_b64_v4i16((LAS s16x4*)p); }
DI bf16x8 cat8(s16x4 lo, s16x4 hi) { return __builtin_bit_cast(bf16x8, __builtin_shufflevector(lo, hi, 0, 1, 2, 3, 4, 5, 6, 7)); }
DI int tok_of_p(int p) { return 32 * (p & 63) + (p >> 6); }
DI rsrc_t mk_rsrc(const void* p, size_t bytes) { return __builtin_amdgcn_make_buffer_rsrc((void*)p, 0, (int)bytes, 0x00020000); }
DI u32x4 bload16(rsrc_t r, int voff, int soff) { return __builtin_amdgcn_raw_buffer_load_b128(r, voff, soff, 0); }
DI float ss_to_float(u64 v) { return (float)(u32)(v >> 32) * 4294967296.0f + (float)(u32)v; }
DI u64 ss_from_float(float s) { return (u64)(fminf(s * SS_SCALE, 1.0e18f) + 0.5f); }
DI void pin(float& x) { asm volatile("" : "+v"(x)); }
DI void pin(bf16x8& x) { asm volatile("" : "+v"(x)); }

struct F32c { bf16x8 c[2], sp[2]; };
DI void f32c_init(F32c& k, int lane) {
    const float m = (float)(lane & 15), q4 = (float)(4 * (lane >> 4));
    float c[4], s[4];
#pragma unroll
    for (int j = 0; j < 4; ++j) { const float a = __builtin_amdgcn_fractf(m * (q4 + (float)j) * (1.0f / 32.0f)); c[j] = cosr(a); s[j] = sinr(a); }
    const u32 sg = (lane & 1) ? 0x80008000u : 0u;
    const u32 c0 = pk2(c[0], c[1]), c1 = pk2(c[2], c[3]), s0 = pk2(s[0], s[1]), s1 = pk2(s[2], s[3]);
    const u32x4 cv = {c0, c1, c0 ^ sg, c1 ^ sg}, sv = {s0, s1, s0 ^ sg, s1 ^ sg};
    k.c[0] = asbf(cv); k.sp[0] = asbf(sv);
    k.c[1] = asbf(cv ^ 0x80000000u); k.sp[1] = asbf(sv ^ 0x80000000u);
}

struct RowC { bf16x8 c[2], sp[2], sm[2], c20, s20m; f32x4 twr[2], twi[2]; int off0, off1; };
constexpr int ROWC_LDS = 10 * 1024;
DI void rowc_build(char* tab, int w, int lane) {
    const int n = lane & 15, q = lane >> 4;
    const float nf = (float)n;
    u32x4* t = (u32x4*)tab + lane;
    if (w == 0) {
        F32c f; f32c_init(f, lane);
        t[0] = __builtin_bit_cast(u32x4, f.c[0]); t[64] = __builtin_bit_cast(u32x4, f.c[1]);
        t[128] = __builtin_bit_cast(u32x4, f.sp[0]); t[192] = __builtin_bit_cast(u32x4, f.sp[1]);
    } else if (w <= 2) {
        float c[8];
#pragma unroll
        for (int j = 0; j < 8; ++j) {
            const int d1 = 8 * q + j;
            const float a = __builtin_amdgcn_fractf((float)d1 * nf * 0.05f);
            const float v = w == 1 ? cosr(a) : -sinr(a);
            c[j] = (d1 < 20 && n <= 10) ? v : 0.f;
        }
        t[(3 + w) * 64] = __builtin_bit_cast(u32x4, pk8(c));
    } else if (w <= 4) {
        const float sc = 0.05590169943749474f;
        f32x4 cr, ci;
#pragma unroll
        for (int i = 0; i < 4; ++i) {
            const float a = (float)(16 * (w - 3) + 4 * q + i) * nf * (1.0f / 640.0f);
            cr[i] = cosr(a) * sc; ci[i] = -sinr(a) * sc;
        }
        t[(6 + w - 3) * 64] = __builtin_bit_cast(u32x4, cr); t[(8 + w - 3) * 64] = __builtin_bit_cast(u32x4, ci);
    }
}
DI void rowc_load(RowC& k, const char* tab, int lane) {
    const u32x4* tb = (const u32x4*)tab + lane;
    k.c[0] = asbf(tb[0]); k.c[1] = asbf(tb[64]); k.sp[0] = asbf(tb[128]); k.sp[1] = asbf(tb[192]);
    k.c20 = asbf(tb[256]); k.s20m = asbf(tb[320]);
    k.twr[0] = __builtin_bit_cast(f32x4, tb[384]); k.twr[1] = __builtin_bit_cast(f32x4, tb[448]);
    k.twi[0] = __builtin_bit_cast(f32x4, tb[512]); k.twi[1] = __builtin_bit_cast(f32x4, tb[576]);
    k.sm[0] = neg8(k.sp[0]); k.sm[1] = neg8(k.sp[1]);
    const int q = lane >> 4, qq = (lane & 15) >> 2, pp = lane & 3;
    const int r0 = min(8 * q + qq, 19), r1 = min(8 * q + 4 + qq, 19);
    k.off0 = r0 * 64 + pp * 8; k.off1 = r1 * 64 + pp * 8;
}
DI float wave_sum63(float v) {
    v += __builtin_bit_cast(float, __builtin_amdgcn_update_dpp(0, __builtin_bit_cast(int, v), 0x111, 0xf, 0xf, true));
    v += __builtin_bit_cast(float, __builtin_amdgcn_update_dpp(0, __builtin_bit_cast(int, v), 0x112, 0xf, 0xf, true));
    v += __builtin_bit_cast(float, __builtin_amdgcn_update_dpp(0, __builtin_bit_cast(int, v), 0x114, 0xf, 0xf, true));
    v += __builtin_bit_cast(float, __builtin_amdgcn_update_dpp(0, __builtin_bit_cast(int, v), 0x118, 0xf, 0xf, true));
    v += __builtin_bit_cast(float, __builtin_amdgcn_update_dpp(0, __builtin_bit_cast(int, v), 0x142, 0xa, 0xf, true));
    v += __builtin_bit_cast(float, __builtin_amdgcn_update_dpp(0, __builtin_bit_cast(int, v), 0x143, 0xc, 0xf, true));
    return v;
}
DI void rowfft(const RowC& k, const char* row, float (&A)[8], float (&B)[8], int lane) {
    f32x4 z = {0.f, 0.f, 0.f, 0.f};
    f32x4 yr[2], yi[2];
#pragma unroll
    for (int mt = 0; mt < 2; ++mt) {
        const bf16x8 a = cat8(tr_read(row + k.off0 + mt * 32), tr_read(row + k.off1 + mt * 32));
        yr[mt] = mfma16(a, k.c20, z); yi[mt] = mfma16(a, k.s20m, z);
    }
    float tr[8], ti[8];
#pragma unroll
    for (int i = 0; i < 8; ++i) {
        const float a = yr[i >> 2][i & 3], b = yi[i >> 2][i & 3], cr = k.twr[i >> 2][i & 3], ci = k.twi[i >> 2][i & 3];
        tr[i] = a * cr - b * ci; ti[i] = a * ci + b * cr;
    }
    const bf16x8 Tr = pk8(tr), Ti = pk8(ti);
    f32x4 ore[2], oim[2];
#pragma unroll
    for (int t = 0; t < 2; ++t) {
        ore[t] = mfma16(k.c[t], Tr, z); ore[t] = mfma16(k.sp[t], Ti, ore[t]);
        oim[t] = mfma16(k.sm[t], Tr, z); oim[t] = mfma16(k.c[t], Ti, oim[t]);
    }
#pragma unroll
    for (int r = 0; r < 4; ++r) { A[2 * r] = ore[0][r]; A[2 * r + 1] = oim[0][r]; B[2 * r] = ore[1][r]; B[2 * r + 1] = oim[1][r]; }
    if (lane == 0) { const float h = 0.7071067811865476f; const float dc = A[0] * h; A[0] = B[0] * h; A[1] = dc; }
}
DI int sw_off(int c) { const int ch = c >> 3; return ((ch ^ ((ch >> 3) & 7)) << 4) + (c & 7) * 2; }
DI int chunk_c0(int n, int q) { return (n == 0 ? 608 : n == 10 ? 576 : 64 * (n - 1)) + 8 * q; }

constexpr int ROWST = 1344;
constexpr int PREP_TAB = 640 * 32 + 16 * ROWST;
constexpr int PREP_LDS = PREP_TAB + ROWC_LDS;

struct PrepArgs { const u16 *x, *wr, *wi, *w1, *w2, *b2; char* ws; };

DI void task_x(const PrepArgs& a, int b, char* smem) {
    const int tid = threadIdx.x, lane = tid & 63, w = tid >> 6;
    const int p0 = 16 * ((b & 7) * 16 + (b >> 3));
    u32x4 v[3];
#pragma unroll
    for (int i = 0; i < 3; ++i) {
        const int id = min(tid + 512 * i, 1279), r = id / 80, ch = id % 80;
        v[i] = *(const u32x4*)(a.x + (size_t)tok_of_p(p0 + r) * D_ + ch * 8);
    }
    __builtin_amdgcn_sched_barrier(0);
    rowc_build(smem + PREP_TAB, w, lane);
    __builtin_amdgcn_sched_barrier(0);
    STAMP(0, 1);
    if (tid < 16) ((u64*)(a.ws + OFF_SS2))[p0 + tid] = 0ull;
    if (b < 32) ((u32*)(a.ws + OFF_CNT))[b * 512 + tid] = 0u;
#pragma unroll
    for (int i = 0; i < 3; ++i) {
        const int id = min(tid + 512 * i, 1279), r = id / 80, ch = id % 80;
        *(u32x4*)(smem + r * ROWST + ch * 16) = v[i];
    }
    STAMP(0, 2);
    __syncthreads();
    STAMP(0, 3);
    RowC k; rowc_load(k, smem + PREP_TAB, lane);
    const int n = lane & 15, q = lane >> 4, c0 = chunk_c0(n, q);
    float A[2][8], B[2][8];
#pragma unroll
    for (int i = 0; i < 2; ++i) rowfft(k, smem + (2 * w + i) * ROWST, A[i], B[i], lane);
#pragma unroll
    for (int i = 0; i < 2; ++i) {
        const int r = 2 * w + i;
        float ss = 0.f;
        if (n <= 10) {
#pragma unroll
            for (int e = 0; e < 8; ++e) ss += (lane == 0 && e == 1) ? 0.f : A[i][e] * A[i][e];
            *(bf16x8*)(smem + r * ROWST + sw_off(c0)) = pk8(A[i]);
        }
        if (n >= 1 && n <= 9) {
#pragma unroll
            for (int e = 0; e < 8; ++e) ss += B[i][e] * B[i][e];
            *(bf16x8*)(smem + r * ROWST + sw_off(c0 + 32)) = pk8(B[i]);
        }
        ss = wave_sum63(ss);
        if (lane == 63) ((u64*)(a.ws + OFF_SS0))[p0 + r] = ss_from_float(ss);
    }
    STAMP(0, 4);
    __syncthreads();
    STAMP(0, 5);
    u16* xt = (u16*)(a.ws + OFF_XT0);
    const int g = lane >> 4, i16 = lane & 15, qq = i16 >> 2, pp = i16 & 3;
#pragma unroll
    for (int i = 0; i < 3; ++i) {
        const int cbk = w + 8 * i;
        if (cbk < 20) {
            const int cb = 32 * cbk + 16 * (g & 1), ph = g >> 1;
            const char* base = smem + (8 * ph + qq) * ROWST + sw_off(cb + 4 * pp);
            const bf16x8 o = cat8(tr_read(base), tr_read(base + 4 * ROWST));
            *(bf16x8*)(xt + (size_t)(cb + i16) * S_ + p0 + 8 * ph) = o;
        }
    }
}

DI void task_W(const PrepArgs& a, int b) {
    const int lane = threadIdx.x & 63, wv = threadIdx.x >> 6, wt = b * 2 + (wv >> 2), kq = wv & 3;
    const int k1 = wt / 5, cg = wt % 5, kp = 1 + 64 * cg + lane;
    const int r = kp % 20, t = kp / 20;
    int j; bool mir = false;
    if (kp == 320) j = 304;
    else if (r == 0) j = 304 + t;
    else if (r < 10) j = 32 * (r - 1) + t;
    else if (r == 10) j = 288 + t;
    else { j = 32 * (19 - r) + 31 - t; mir = true; }
    const rsrc_t rr = mk_rsrc(a.wr, (size_t)S_ * DF_ * 2), ri = mk_rsrc(a.wi, (size_t)S_ * DF_ * 2);
    const int st = (mir ? -64 : 64) * DF_ * 2, off0 = ((mir ? 2048 - k1 : k1) * DF_ + kp) * 2 + 8 * kq * st;
    u32 re[8], im[8];
#pragma unroll
    for (int k2 = 0; k2 < 8; ++k2) {
        int off = off0 + k2 * st;
        if (k2 == 0 && mir && k1 == 0 && kq == 0) off = kp * 2;
        re[k2] = __builtin_amdgcn_raw_buffer_load_b16(rr, off, 0, 0); im[k2] = __builtin_amdgcn_raw_buffer_load_b16(ri, off, 0, 0);
    }
    const u32 sg = mir ? 0x80000000u : 0u;
    u32x4* dst = (u32x4*)(a.ws + OFF_WT + ((size_t)j * 64 + k1) * 128 + kq * 32);
#pragma unroll
    for (int i = 0; i < 2; ++i) {
        u32x4 o;
#pragma unroll
        for (int e = 0; e < 4; ++e) o[e] = (re[4 * i + e] | (im[4 * i + e] << 16)) ^ sg;
        dst[i] = o;
    }
}

DI void task_w1(const PrepArgs& a, int b, char* smem) {
    const int tid = threadIdx.x, lane = tid & 63, w = tid >> 6;
    char* raw = smem;
    char* tr = smem + 640 * 32;
    char* out = raw;
    u32x4 v[3];
#pragma unroll
    for (int i = 0; i < 3; ++i) {
        const int id = min(tid + 512 * i, 1279), d = id >> 1, h = id & 1;
        v[i] = *(const u32x4*)(a.w1 + (size_t)d * F_ + 16 * b + 8 * h);
    }
    __builtin_amdgcn_sched_barrier(0);
    rowc_build(smem + PREP_TAB, w, lane);
    __builtin_amdgcn_sched_barrier(0);
#pragma unroll
    for (int i = 0; i < 3; ++i) *(u32x4*)(raw + min(tid + 512 * i, 1279) * 16) = v[i];
    __syncthreads();
    const int g = lane >> 4, i16 = lane & 15, qq = i16 >> 2, pp = i16 & 3;
#pragma unroll
    for (int i = 0; i < 5; ++i) {
        const int d0 = 16 * (w + 8 * i) + 4 * g;
        const s16x4 t = tr_read(raw + (d0 + qq) * 32 + pp * 8);
        *(s16x4*)(tr + i16 * ROWST + d0 * 2) = t;
    }
    RowC k; rowc_load(k, smem + PREP_TAB, lane);
    __syncthreads();
    const int n = lane & 15, q = lane >> 4, c0 = chunk_c0(n, q);
    float A[2][8], B[2][8];
#pragma unroll
    for (int i = 0; i < 2; ++i) rowfft(k, tr + (2 * w + i) * ROWST, A[i], B[i], lane);
#pragma unroll
    for (int i = 0; i < 2; ++i) {
        const int f = 2 * w + i;
        if (lane == 0) A[i][1] = 0.f;
        if (n <= 10) *(bf16x8*)(out + ((c0 >> 5) * 64 + q * 16 + f) * 16) = pk8(A[i]);
        if (n >= 1 && n <= 9) *(bf16x8*)(out + (((c0 >> 5) + 1) * 64 + q * 16 + f) * 16) = pk8(B[i]);
    }
    __syncthreads();
    u32x4* dst = (u32x4*)(a.ws + OFF_W1F + (size_t)b * 20 * 1024);
#pragma unroll
    for (int i = 0; i < 3; ++i) { const int id = tid + 512 * i; if (id < 1280) dst[id] = *(const u32x4*)(out + id * 16); }
}

template <bool SW> DI void w2_frags(const char* img, char* dstbase, int b, int lane, int w) {
    const int g = lane >> 4, i16 = lane & 15, qq = i16 >> 2, pp = i16 & 3;
    const rsrc_t rd = mk_rsrc(dstbase, W2F_BYTES);
#pragma unroll
    for (int i = 0; i < 5; ++i) {
        const int cu = w + 8 * i, c = 16 * cu + 4 * pp;
        const char* base = img + (4 * g + qq) * ROWST + (SW ? sw_off(c) : c * 2);
        const bf16x8 v = cat8(tr_read(base), tr_read(base + 16 * ROWST));
        __builtin_amdgcn_raw_buffer_store_b128(__builtin_bit_cast(u32x4, v), rd, lane * 16, (cu * 54 + b) * 1024, SC1);
    }
}
constexpr int W2_TAB = 32 * ROWST;
DI void task_w2(const PrepArgs& a, int b, char* smem) {
    const int tid = threadIdx.x, lane = tid & 63, w = tid >> 6;
    u32x4 v[5];
#pragma unroll
    for (int i = 0; i < 5; ++i) {
        const int id = tid + 512 * i, r = id / 80, ch = id % 80;
        v[i] = *(const u32x4*)(a.w2 + (size_t)(32 * b + r) * D_ + ch * 8);
    }
    __builtin_amdgcn_sched_barrier(0);
    rowc_build(smem + W2_TAB, w, lane);
    __builtin_amdgcn_sched_barrier(0);
#pragma unroll
    for (int i = 0; i < 5; ++i) { const int id = tid + 512 * i, r = id / 80, ch = id % 80; *(u32x4*)(smem + r * ROWST + ch * 16) = v[i]; }
    __syncthreads();
    RowC k; rowc_load(k, smem + W2_TAB, lane);
    w2_frags<false>(smem, a.ws + OFF_W2O, b, lane, w);
    const int n = lane & 15, q = lane >> 4, c0 = chunk_c0(n, q);
    u32x4 A8[4], B8[4];
#pragma unroll
    for (int i = 0; i < 4; ++i) {
        float A[8], B[8];
        rowfft(k, smem + (4 * w + i) * ROWST, A, B, lane);
        A8[i] = __builtin_bit_cast(u32x4, pk8(A)); B8[i] = __builtin_bit_cast(u32x4, pk8(B));
    }
    __syncthreads();
#pragma unroll
    for (int i = 0; i < 4; ++i) {
        const int r = 4 * w + i;
        if (n <= 10) *(u32x4*)(smem + r * ROWST + sw_off(c0)) = A8[i];
        if (n >= 1 && n <= 9) *(u32x4*)(smem + r * ROWST + sw_off(c0 + 32)) = B8[i];
    }
    __syncthreads();
    w2_frags<true>(smem, a.ws + OFF_W2F, b, lane, w);
}

DI int kcol_of(int jc) { return jc < 288 ? (jc >> 5) + 1 + 20 * (jc & 31) : jc < 304 ? 10 + 20 * (jc - 288) : 20 * (jc - 304); }
DI void task_Q(const PrepArgs& a, int u) {
    const int tid = threadIdx.x;
#pragma unroll
    for (int ii = 0; ii < 3; ++ii) {
        const int i = tid + 512 * ii;
        if (i < 1280) {
            const int ks = i >> 6, l = i & 63, d = 16 * u + (l & 15), q = l >> 4;
            float v[8];
#pragma unroll
            for (int j = 0; j < 8; j += 2) {
                const int jc = 16 * ks + 4 * q + (j >> 1), kk = kcol_of(jc);
                const float ang = (float)((kk * d) % 640) * (1.0f / 640.0f);
                float c = cosr(ang) * 0.05590169943749474f, s = -sinr(ang) * 0.05590169943749474f;
                if (jc == 304) { c = ((d & 1) ? -1.f : 1.f) * 0.03952847075210474f; s = 0.03952847075210474f; }
                v[j] = c; v[j + 1] = s;
            }
            __builtin_amdgcn_raw_buffer_store_b128(__builtin_bit_cast(u32x4, pk8(v)), mk_rsrc(a.ws + OFF_W1F, W1F_BYTES), i * 16, (108 + u) * 20480, SC1);
        }
    }
}
DI void task_b(const PrepArgs& a, char* smem) {
    const int tid = threadIdx.x, lane = tid & 63;
    if (tid < 80) *(u32x4*)(smem + tid * 16) = *(const u32x4*)(a.b2 + tid * 8);
    rowc_build(smem + PREP_TAB, tid >> 6, lane);
    __syncthreads();
    RowC k; rowc_load(k, smem + PREP_TAB, lane);
    float A[8], B[8];
    rowfft(k, smem, A, B, lane);
    if (tid < 64) {
        const int n = lane & 15, q = lane >> 4, c0 = chunk_c0(n, q);
        float* o = (float*)(a.ws + OFF_B2P);
        if (n <= 10) for (int e = 0; e < 8; ++e) o[c0 + e] = A[e];
        if (n >= 1 && n <= 9) for (int e = 0; e < 8; ++e) o[c0 + 32 + e] = B[e];
    }
}

constexpr int NB_K0 = 128 + 108 + 160 + 1;
constexpr int MAIN_CODE_PREFETCH = 61440;
DI void prefetch_main_code(int slice, int lane) {
    u32 lo, hi;
    asm volatile("s_getpc_b64 s[40:41]\n\ts_add_u32 s40, s40, _Z6k_main8MainArgs@rel32@lo+4\n\ts_addc_u32 s41, s41, _Z6k_main8MainArgs@rel32@hi+12\n\t"
                 "s_mov_b32 %0, s40\n\ts_mov_b32 %1, s41" : "=s"(lo), "=s"(hi) :: "s40", "s41", "scc");
    const u64 base = ((u64)hi << 32) | lo;
    const char* p = (const char*)base + slice * 4096 + lane * 16;
    u32x4 t0, t1, t2, t3;
    asm volatile("global_load_dwordx4 %0, %4, off\n\tglobal_load_dwordx4 %1, %4, off offset:1024\n\tglobal_load_dwordx4 %2, %4, off offset:2048\n\t"
                 "global_load_dwordx4 %3, %4, off offset:3072\n\ts_waitcnt vmcnt(0)"
                 : "=&v"(t0), "=&v"(t1), "=&v"(t2), "=&v"(t3) : "v"(p) : "memory");
}
__global__ __launch_bounds__(512, 4) void k0_prep(PrepArgs a) {
    extern __shared__ __attribute__((aligned(16))) char smem[];
    const int b = blockIdx.x;
    STAMP(0, 0);
    if (b >= 236 && b < 236 + 8 * (MAIN_CODE_PREFETCH / 4096) && (threadIdx.x >> 6) == 7) prefetch_main_code((b - 236) >> 3, threadIdx.x & 63);
    if (b < 128) task_x(a, b, smem);
    else if (b < 236) task_w1(a, b - 128, smem);
    else if (b < 396) task_W(a, b - 236);
    else task_b(a, smem);
    END_STAMP(0);
}

template <int N> DI void wait_vm() { asm volatile("s_waitcnt vmcnt(%0)" ::"n"(N) : "memory"); }
DI void wait_lgkm() { asm volatile("s_waitcnt lgkmcnt(0)" ::: "memory"); }
DI void barrier() { __builtin_amdgcn_s_barrier(); }
template <bool SC = false> DI void dma16(u32x4 srd, int voff, int soff, u32 ldsaddr) {
    if (SC) asm volatile("s_mov_b32 m0, %0\n\ts_nop 0\n\tbuffer_load_dwordx4 %1, %2, %3 offen sc1 lds" ::"s"(ldsaddr), "v"(voff), "s"(srd), "s"(soff) : "memory", "m0");
    else asm volatile("s_mov_b32 m0, %0\n\ts_nop 0\n\tbuffer_load_dwordx4 %1, %2, %3 offen lds" ::"s"(ldsaddr), "v"(voff), "s"(srd), "s"(soff) : "memory", "m0");
}
template <int LIM, bool SC = false> DI void dma16_lim(u32x4 srd, int voff, int soff, u32 ldsaddr, int w) {
    if (SC) asm volatile("s_cmp_lt_i32 %4, %5\n\ts_cbranch_scc0 .Lskip%=\n\ts_mov_b32 m0, %0\n\ts_nop 0\n\tbuffer_load_dwordx4 %1, %2, %3 offen sc1 lds\n.Lskip%=:"
                 ::"s"(ldsaddr), "v"(voff), "s"(srd), "s"(soff), "s"(w), "n"(LIM) : "memory", "m0", "scc");
    else asm volatile("s_cmp_lt_i32 %4, %5\n\ts_cbranch_scc0 .Lskip%=\n\ts_mov_b32 m0, %0\n\ts_nop 0\n\tbuffer_load_dwordx4 %1, %2, %3 offen lds\n.Lskip%=:"
                 ::"s"(ldsaddr), "v"(voff), "s"(srd), "s"(soff), "s"(w), "n"(LIM) : "memory", "m0", "scc");
}
DI u32x4 mk_srd(const void* p, size_t bytes) {
    const u64 a = (u64)p;
    u32x4 r = {(u32)a, (u32)(a >> 32) & 0xffffu, (u32)bytes, 0x00020000u};
    return r;
}
DI float row_sum15(float v) {
    v += __builtin_bit_cast(float, __builtin_amdgcn_update_dpp(0, __builtin_bit_cast(int, v), 0x111, 0xf, 0xf, true));
    v += __builtin_bit_cast(float, __builtin_amdgcn_update_dpp(0, __builtin_bit_cast(int, v), 0x112, 0xf, 0xf, true));
    v += __builtin_bit_cast(float, __builtin_amdgcn_update_dpp(0, __builtin_bit_cast(int, v), 0x114, 0xf, 0xf, true));
    v += __builtin_bit_cast(float, __builtin_amdgcn_update_dpp(0, __builtin_bit_cast(int, v), 0x118, 0xf, 0xf, true));
    return v;
}
template <int I, int N, class F> DI void static_for(F&& f) {
    if constexpr (I < N) { f(std::integral_constant<int, I>{}); static_for<I + 1, N>(f); }
}

#define G1_P_ 3
constexpr int G1_P = G1_P_, G1_PW = 5;
typedef __attribute__((address_space(1))) u32 gu32;
DI u32 ld_agent(const u32* p) { return __hip_atomic_load((gu32*)p, __ATOMIC_RELAXED, __HIP_MEMORY_SCOPE_AGENT); }
DI void add_agent(u32* p) { __hip_atomic_fetch_add((gu32*)p, 1u, __ATOMIC_RELAXED, __HIP_MEMORY_SCOPE_AGENT); }
DI void signal_all(char* ws, int which) {
    if (threadIdx.x == 0) {
        u32* c = (u32*)(ws + OFF_CNT + 16384 + which * 8192) + (blockIdx.x & 7) * 32;
#pragma unroll
        for (int r = 0; r < 8; ++r) add_agent(c + r * 256);
    }
}
#define POLL_SLEEP 16
DI void poll_until(const u32* p, u32 want, char* ws, int lane) {
    for (u32 spins = 0;; ++spins) {
        if (__all(ld_agent(p) >= want)) break;
        __builtin_amdgcn_s_sleep(POLL_SLEEP);
        if (spins > (1u << 18)) { if (lane == 0) __hip_atomic_store((gu32*)(ws + OFF_CNT + 49152), 1u, __ATOMIC_RELAXED, __HIP_MEMORY_SCOPE_AGENT); break; }
    }
}
DI void poll_all(char* ws, int which, u32 per_shard, int w, int lane) {
    if (w == 0) poll_until((const u32*)(ws + OFF_CNT + 16384 + which * 8192) + ((blockIdx.x >> 3) & 7) * 256 + (lane & 7) * 32, per_shard, ws, lane);
}

struct MainArgs { PrepArgs p; const u16* b1; u16* out; };
DI int img128(int row, int ch) { return row * 128 + ((ch ^ ((row >> 1) & 7)) << 4); }
template <bool LAST> DI void w1_prefetch(char* ws, char* smem, int w, int lane, int u0);

template <int L>
DI void col_task(char* ws, int b, char* smem, int u0) {
    const int tid = threadIdx.x & 255, lane = tid & 63, w = tid >> 6, half = threadIdx.x >> 8;
    const int j = 2 * b + half;
    char* Ure = smem + half * 24576; char* Uim = Ure + 4096; char* Ere = Ure + 8192; char* Eim = Ure + 12288;
    char* Rre = Ure + 16384; char* Rim = Ure + 20480;
    const int n = lane & 15, q = lane >> 4;
    const int wv = __builtin_amdgcn_readfirstlane(threadIdx.x >> 6);
    const rsrc_t rx = mk_rsrc(ws + (L == 0 ? OFF_XT0 : OFF_XT2), XT_BYTES), rs = mk_rsrc(ws + (L == 0 ? OFF_SS0 : OFF_SS2), 2048 * 8);
    u32x4 vr, vi, sv[4];
    auto load_in = [&]() __attribute__((always_inline)) {
        vr = __builtin_amdgcn_raw_buffer_load_b128(rx, (2 * j * S_ + tid * 8) * 2, 0, L == 0 ? 0 : SC1);
        vi = __builtin_amdgcn_raw_buffer_load_b128(rx, ((2 * j + 1) * S_ + tid * 8) * 2, 0, L == 0 ? 0 : SC1);
#pragma unroll
        for (int i = 0; i < 4; ++i) sv[i] = __builtin_amdgcn_raw_buffer_load_b128(rs, tid * 64 + 16 * i, 0, L == 0 ? 0 : SC1);
    };
    if (L == 0) load_in();
    u32x4 wf[2];
#pragma unroll
    for (int t = 0; t < 2; ++t) wf[t] = *(const u32x4*)((const u32*)(ws + OFF_WT) + ((size_t)j * 64 + 16 * w + n) * 32 + 16 * t + 4 * q);
    __builtin_amdgcn_sched_barrier(0);
    F32c f; f32c_init(f, lane);
    bf16x8 c32[2] = {f.c[0], f.c[1]}, s32p[2] = {f.sp[0], f.sp[1]}, s32m[2];
    pin(c32[0]); pin(c32[1]); pin(s32p[0]); pin(s32p[1]);
    bf16x8 c64[2], s64p[2], s64m[2];
    const float k1f = (float)(16 * w + n);
    {
        float c[8], s[8];
#pragma unroll
        for (int e = 0; e < 8; ++e) {
            const float ang = __builtin_amdgcn_fractf((float)(8 * q + e) * k1f * (1.0f / 64.0f));
            c[e] = cosr(ang); s[e] = sinr(ang);
        }
        c64[0] = pk8(c); s64p[0] = pk8(s);
        const u32 sg = (lane & 1) ? 0x80008000u : 0u;
        c64[1] = asbf(__builtin_bit_cast(u32x4, c64[0]) ^ sg); s64p[1] = asbf(__builtin_bit_cast(u32x4, s64p[0]) ^ sg);
        pin(c64[0]); pin(c64[1]); pin(s64p[0]); pin(s64p[1]);
    }
    float twr[8], twi[8], ivr[8], ivi[8];
#pragma unroll
    for (int i = 0; i < 8; ++i) {
        const float af = (float)(16 * (i >> 2) + 4 * q + (i & 3)) * k1f * (1.0f / 2048.0f);
        twr[i] = cosr(af); twi[i] = -sinr(af);
        const float ai = (float)(16 * w + 4 * q + (i & 3)) * (float)(16 * (i >> 2) + n) * (1.0f / 2048.0f);
        ivr[i] = cosr(ai) * (1.0f / 2048.0f); ivi[i] = sinr(ai) * (1.0f / 2048.0f);
        pin(twr[i]); pin(twi[i]); pin(ivr[i]); pin(ivi[i]);
    }
    __builtin_amdgcn_sched_barrier(0);
    STAMP(L ? 4 : 1, 1);
    if (L == 1) {
        poll_all(ws, 1, 32u, wv, lane);
        barrier();
        STAMP(4, 5);
        load_in();
        __builtin_amdgcn_sched_barrier(0);
    }
    s64m[0] = neg8(s64p[0]); s64m[1] = neg8(s64p[1]);
    {
        float r[8];
#pragma unroll
        for (int i = 0; i < 8; ++i) {
            const float s = (float)sv[i >> 1][2 * (i & 1) + 1] * 4294967296.0f + (float)sv[i >> 1][2 * (i & 1)];
            r[i] = __builtin_amdgcn_rsqf(s * (1.0f / (SS_SCALE * 640.0f)) + EPS);
        }
        const bf16x8 ur = pk8(bflo(vr[0]) * r[0], bfhi(vr[0]) * r[1], bflo(vr[1]) * r[2], bfhi(vr[1]) * r[3],
                              bflo(vr[2]) * r[4], bfhi(vr[2]) * r[5], bflo(vr[3]) * r[6], bfhi(vr[3]) * r[7]);
        bf16x8 ui = pk8(bflo(vi[0]) * r[0], bfhi(vi[0]) * r[1], bflo(vi[1]) * r[2], bfhi(vi[1]) * r[3],
                        bflo(vi[2]) * r[4], bfhi(vi[2]) * r[5], bflo(vi[3]) * r[6], bfhi(vi[3]) * r[7]);
        if (j == 304) ui = asbf(u32x4{0u, 0u, 0u, 0u});
        const int o = img128(tid >> 3, tid & 7);
        *(bf16x8*)(Ure + o) = ur; *(bf16x8*)(Uim + o) = ui;
        *(u32x4*)(Rre + o) = vr; *(u32x4*)(Rim + o) = vi;
    }
    __syncthreads();
    STAMP(L ? 4 : 1, 2);
    const f32x4 z = {0.f, 0.f, 0.f, 0.f};
    f32x4 dr[2], di[2];
#pragma unroll
    for (int mt = 0; mt < 2; ++mt) {
        dr[mt] = z; di[mt] = z;
#pragma unroll
        for (int ks = 0; ks < 2; ++ks) {
            const int o = img128(16 * mt + n, 4 * ks + q);
            const bf16x8 ar = *(const bf16x8*)(Ure + o), ai = *(const bf16x8*)(Uim + o);
            dr[mt] = mfma16(ar, c64[ks], dr[mt]); dr[mt] = mfma16(ai, s64p[ks], dr[mt]);
            di[mt] = mfma16(ar, s64m[ks], di[mt]); di[mt] = mfma16(ai, c64[ks], di[mt]);
        }
    }
    float tr[8], ti[8];
#pragma unroll
    for (int i = 0; i < 8; ++i) {
        const float x = dr[i >> 2][i & 3], y = di[i >> 2][i & 3];
        tr[i] = x * twr[i] - y * twi[i]; ti[i] = x * twi[i] + y * twr[i];
    }
    bf16x8 Tr = pk8(tr), Ti = pk8(ti);
    s32m[0] = neg8(s32p[0]); s32m[1] = neg8(s32p[1]);
    f32x4 orr[2], oi[2];
#pragma unroll
    for (int t = 0; t < 2; ++t) {
        orr[t] = mfma16(c32[t], Tr, z); orr[t] = mfma16(s32p[t], Ti, orr[t]);
        oi[t] = mfma16(s32m[t], Tr, z); oi[t] = mfma16(c32[t], Ti, oi[t]);
    }
#pragma unroll
    for (int i = 0; i < 8; ++i) {
        const float x = orr[i >> 2][i & 3], y = oi[i >> 2][i & 3];
        const u32 wv = wf[i >> 2][i & 3];
        const float wre = bflo(wv), wim = bfhi(wv);
        tr[i] = x * wre - y * wim; ti[i] = x * wim + y * wre;
    }
    Tr = pk8(tr); Ti = pk8(ti);
#pragma unroll
    for (int nt = 0; nt < 2; ++nt) {
        f32x4 zr = mfma16(Tr, c32[nt], z); zr = mfma16(Ti, s32m[nt], zr);
        f32x4 zi = mfma16(Tr, s32p[nt], z); zi = mfma16(Ti, c32[nt], zi);
        float er[4], ei[4];
#pragma unroll
        for (int r = 0; r < 4; ++r) {
            const float cr = ivr[4 * nt + r], ci = ivi[4 * nt + r];
            er[r] = zr[r] * cr - zi[r] * ci; ei[r] = zr[r] * ci + zi[r] * cr;
        }
        const int o = img128(16 * nt + n, 2 * w + (q >> 1)) + (q & 1) * 8;
        u32x2 pr = {pk2(er[0], er[1]), pk2(er[2], er[3])}, pi = {pk2(ei[0], ei[1]), pk2(ei[2], ei[3])};
        *(u32x2*)(Ere + o) = pr; *(u32x2*)(Eim + o) = pi;
    }
    __syncthreads();
    STAMP(L ? 4 : 1, 3);
    const rsrc_t rf = mk_rsrc(ws + (L == 0 ? OFF_XF1 : OFF_XF3), XT_BYTES);
    char* O = Ure;
#pragma unroll
    for (int nt = 0; nt < 2; ++nt) {
        f32x4 yr = z, yi = z;
#pragma unroll
        for (int ks = 0; ks < 2; ++ks) {
            const int o = img128(16 * nt + n, 4 * ks + q);
            const bf16x8 br = *(const bf16x8*)(Ere + o), bi = *(const bf16x8*)(Eim + o);
            yr = mfma16(c64[ks], br, yr); yr = mfma16(s64m[ks], bi, yr);
            yi = mfma16(s64p[ks], br, yi); yi = mfma16(c64[ks], bi, yi);
        }
        if (j == 304) yi = z;
        const int p = (16 * nt + n) * 64 + 16 * w + 4 * q;
        u32x2 rr[2], ri[2];
        { const int o = img128(16 * nt + n, 2 * w + (q >> 1)) + (q & 1) * 8; rr[nt] = *(const u32x2*)(Rre + o); ri[nt] = *(const u32x2*)(Rim + o); }
        const float fr[4] = {bflo(rr[nt][0]) + yr[0], bfhi(rr[nt][0]) + yr[1], bflo(rr[nt][1]) + yr[2], bfhi(rr[nt][1]) + yr[3]};
        const float fi[4] = {bflo(ri[nt][0]) + yi[0], bfhi(ri[nt][0]) + yi[1], bflo(ri[nt][1]) + yi[2], bfhi(ri[nt][1]) + yi[3]};
        u32x4 ov = {pk2(fr[0], fi[0]), pk2(fr[1], fi[1]), pk2(fr[2], fi[2]), pk2(fr[3], fi[3])};
        *(u32x4*)(O + p * 4) = ov;
    }
    __syncthreads();
    {
        const int c0 = 4 * b, dst = ((c0 >> 5) * 64 + ((c0 & 31) >> 3) * 16) * 16 + (c0 & 4) * 2;
        const u32* O0 = (const u32*)smem; const u32* O1 = (const u32*)(smem + 24576);
#pragma unroll
        for (int i = 0; i < 4; ++i) {
            const int p = threadIdx.x + 512 * i;
            u32x2 v = {O0[p], O1[p]};
            __builtin_amdgcn_raw_buffer_store_b64(v, rf, (p >> 4) * 20480 + (p & 15) * 16, dst, SC1);
        }
    }
    STAMP(L ? 4 : 1, 4);
    wait_lgkm();
    wait_vm<0>();
    barrier();
    signal_all(ws, L == 0 ? 0 : 2);
    w1_prefetch<L == 1>(ws, smem, wv, lane, u0);
    STAMP(L ? 4 : 1, 15);
}

DI f32x2 gelu_tanh2(f32x2 x) {
    const float k1 = -2.302208198f, k2 = -0.1029432397f;
    const f32x2 c1 = {k1, k1}, c2 = {k2, k2}, one = {1.0f, 1.0f};
    const f32x2 t = x * (x * x * c2 + c1);
    f32x2 e = {__builtin_amdgcn_exp2f(t[0]), __builtin_amdgcn_exp2f(t[1])};
    e += one;
    const f32x2 r = {__builtin_amdgcn_rcpf(e[0]), __builtin_amdgcn_rcpf(e[1])};
    return x * r;
}
#define G2_PH_ 3
constexpr int G2_PH = G2_PH_;
constexpr int W2STG = 20480;
DI u32 w2slot(int t) { return 163840 - ((t & 7) + 1) * W2STG; }
template <bool LAST> constexpr int w1slot(int st) { return LAST ? 163840 - (st % 8 + 1) * 20480 : 163840 - (st + 1) * 14336; }

template <bool LAST, int I> DI void w1_piece(u32x4 srd, u32 lds0, int w, int vo, int u0, int st) {
    constexpr int WP = LAST ? 20 : 14;
    const int pc = w + 8 * I;
    const u32 dst = lds0 + w1slot<LAST>(st) + pc * 1024;
    const int so = (u0 + (pc >> 1)) * 20480 + st * 2048 + (pc & 1) * 1024;
    if (8 * I + 7 < WP) dma16<LAST>(srd, vo, so, dst); else dma16_lim<(WP & 7), LAST>(srd, vo, so, dst, w);
}
template <bool LAST> DI void w1_prefetch(char* ws, char* smem, int w, int lane, int u0) {
    const u32x4 srd = mk_srd(ws + OFF_W1F, W1F_BYTES);
    const u32 lds0 = (u32)(size_t)(LAS char*)smem;
#pragma unroll
    for (int st = 0; st < (LAST ? 8 : 10); ++st) {
        w1_piece<LAST, 0>(srd, lds0, w, lane * 16, u0, st);
        w1_piece<LAST, 1>(srd, lds0, w, lane * 16, u0, st);
        if (LAST) w1_piece<LAST, 2>(srd, lds0, w, lane * 16, u0, st);
    }
}

constexpr int g1_younger(int ssync, int t, int nlo) {
    int cnt = 0; bool seen = false;
    for (int s = 0; s < ssync; ++s) {
        if (s + G1_PW < 10 && s + G1_PW >= 8) { if (seen) cnt += nlo; if (s + G1_PW == t) seen = true; }
        if (s + G1_P < 10 && seen) cnt += 2;
    }
    return cnt;
}

template <int I> DI void w2_issue1(u32x4 srd, u32 lds0, int w, int vo, int ct, int t) {
    const int pc = w + 8 * I, q = pc / 5, cu = pc - 5 * q;
    const int so = ((5 * ct + cu) * 54 + (q < 2 ? 14 * q : 28 + 13 * (q - 2)) + t) * 1024;
    if (I < 2) dma16<true>(srd, vo, so, lds0 + w2slot(t) + pc * 1024); else dma16_lim<4, true>(srd, vo, so, lds0 + w2slot(t) + pc * 1024, w);
}
DI void w2_issue(u32x4 srd, u32 lds0, int w, int vo, int ct, int t) {
    w2_issue1<0>(srd, lds0, w, vo, ct, t); w2_issue1<1>(srd, lds0, w, vo, ct, t); w2_issue1<2>(srd, lds0, w, vo, ct, t);
}

template <int NF, bool LAST, int KG>
DI void g1_phase(const MainArgs& a, char* smem, int w, int lane, int pt, int u0, int nu, int ct2) {
    constexpr int P = G1_P, PW = G1_PW;
    constexpr int WP = 2 * NF, WR = (WP + 7) / 8, NLO = WP / 8, KID = LAST ? 5 : 2;
    char* ws = a.p.ws;
    const int pq = w & 3, g = lane >> 4;
    const u32x4 srdW = mk_srd(ws + OFF_W1F, W1F_BYTES), srd2 = mk_srd(ws + (LAST ? OFF_W2O : OFF_W2F), W2F_BYTES);
    const u32 lds0 = (u32)(size_t)(LAS char*)smem;
    const rsrc_t rx = mk_rsrc(ws + (LAST ? OFF_XF3 : OFF_XF1), XT_BYTES);
    const int xs = (8 * pt + 2 * pq) * 20480 + KG * 1024, vo = lane * 16;
    auto xload = [&](int st, int pu) __attribute__((always_inline)) { return asbf(__builtin_amdgcn_raw_buffer_load_b128(rx, vo, xs + pu * 20480 + st * 2048, SC1)); };
    bf16x8 xb[P][2];
#pragma unroll
    for (int st = 0; st < P; ++st) { xb[st][0] = xload(st, 0); xb[st][1] = xload(st, 1); __builtin_amdgcn_sched_barrier(0); }
    f32x4 acc[NF][2];
#pragma unroll
    for (int i = 0; i < NF; ++i) { acc[i][0] = f32x4{0, 0, 0, 0}; acc[i][1] = f32x4{0, 0, 0, 0}; }
    f32x4 gr[2] = {f32x4{0, 0, 0, 0}, f32x4{0, 0, 0, 0}};
    bf16x8 wa[2][NF];
#pragma unroll
    for (int i = 0; i < NF; ++i) wa[0][i] = *(const bf16x8*)(smem + w1slot<LAST>(0) + KG * 1024 + lane * 16 + i * 2048);
    static_for<0, 10>([&](auto ST) __attribute__((always_inline)) {
        constexpr int st = decltype(ST)::value;
        if constexpr (LAST && st == 2) { wait_lgkm(); barrier(); }
        if constexpr (st == 4 || st == 7) {
            wait_lgkm();
            constexpr int nx4 = 2 * P + 2 * 4, nx7 = 2 * P + 2 * (10 - P < 7 ? 10 - P : 7);
            if constexpr (st == 4) wait_vm<(LAST ? NLO : 2 * NLO) + nx4>();
            else if constexpr (LAST) wait_vm<g1_younger(7, 9, NLO)>();
            else wait_vm<nx7>();
            barrier();
        }
        bf16x8 nx[2];
        const char* rbase = smem + w1slot<LAST>(st + 1) + KG * 1024 + lane * 16;
        constexpr int NV = WR + 2, V0 = KG == 0 ? 0 : NF - NV;
        static_for<0, NF>([&](auto I) __attribute__((always_inline)) {
            constexpr int i = decltype(I)::value;
            __builtin_amdgcn_sched_barrier(0);
            acc[i][0] = mfma16(wa[st & 1][i], xb[st % P][0], acc[i][0]);
            if constexpr (st + 1 < 10) wa[(st + 1) & 1][i] = *(const bf16x8*)(rbase + i * 2048);
            acc[i][1] = mfma16(wa[st & 1][i], xb[st % P][1], acc[i][1]);
            if constexpr (i < 2) {
                u32x4 xv = __builtin_bit_cast(u32x4, xb[st % P][i]);
                if constexpr (st == 9 && KG == 1) { if (g == 0) xv[0] &= 0x0000ffffu; }
                gr[i] = mfma16(asbf(xv), asbf(xv), gr[i]);
            }
            __builtin_amdgcn_sched_barrier(0);
            constexpr int v = i - V0;
            if constexpr (v >= 0 && v < WR) { if constexpr (LAST && st + PW >= 8 && st + PW < 10) w1_piece<LAST, v>(srdW, lds0, w, vo, u0, st + PW); }
            else if constexpr (v >= WR && v < NV) { if constexpr (st + P < 10) nx[v - WR] = xload(st + P, v - WR); }
            if constexpr (st >= 6) {
                constexpr int sl = i - (KG == 0 ? 0 : NF - 4), e = 4 * (st - 6) + sl;
                if constexpr (sl >= 0 && sl < 4 && e < (LAST ? 9 : 15)) w2_issue1<e % 3>(srd2, lds0, w, vo, ct2, (LAST ? 2 : 0) + e / 3);
            }
        });
        __builtin_amdgcn_sched_barrier(0);
        if constexpr (st + P < 10) { xb[st % P][0] = nx[0]; xb[st % P][1] = nx[1]; }
        if (st == 4) STAMP(KID, 2);
    });
    STAMP(KID, 3);
    u32x2 bv[NF];
#pragma unroll
    for (int i = 0; i < NF; ++i) bv[i] = *(const u32x2*)(a.b1 + min(16 * (u0 + i), F_ - 16) + 4 * g);
    __builtin_amdgcn_sched_barrier(0);
    wait_lgkm();
    barrier();
    float* sx = (float*)(smem + 8 * NF * 512);
#pragma unroll
    for (int i = 0; i < NF; ++i) {
        const f32x4 t = acc[i][1 - KG];
        u32x2 pv = {pk2(t[0], t[1]), pk2(t[2], t[3])};
        *(u32x2*)(smem + ((w * NF + i) * 64 + lane) * 8) = pv;
    }
    float st2[2];
    {
        const int n = lane & 15;
#pragma unroll
        for (int pu = 0; pu < 2; ++pu) {
            const float d01 = (n & 1) ? gr[pu][1] : gr[pu][0], d23 = (n & 1) ? gr[pu][3] : gr[pu][2];
            float d = (n & 2) ? d23 : d01;
            d = (g == (n >> 2)) ? d : 0.f;
            d += __shfl_xor(d, 16); d += __shfl_xor(d, 32);
            st2[pu] = d;
        }
    }
    if (lane < 16) sx[w * 16 + lane] = st2[1 - KG];
    wait_lgkm();
    barrier();
    STAMP(KID, 5);
    const float v = (st2[KG] + sx[(w ^ 4) * 16 + (lane & 15)]) * (1.0f / 640.0f);
    const float r1 = __builtin_amdgcn_rsqf(v + EPS);
    const float rr = r1 * __builtin_amdgcn_rsqf(v * r1 * r1 + EPS);
    const int punit = 8 * pt + 2 * pq + KG;
    u32x2 ov[NF], pv[NF];
#pragma unroll
    for (int i = 0; i < NF; ++i) pv[i] = *(const u32x2*)(smem + (((w ^ 4) * NF + i) * 64 + lane) * 8);
    static_for<0, NF>([&](auto I) __attribute__((always_inline)) {
        constexpr int i = decltype(I)::value;
        if constexpr (LAST ? i < 2 : i == 0) {
            __builtin_amdgcn_sched_barrier(0);
            w2_issue(srd2, lds0, w, vo, ct2, LAST ? i : 5);
            __builtin_amdgcn_sched_barrier(0);
        }
        const f32x2 o0 = {acc[i][KG][0] + bflo(pv[i][0]), acc[i][KG][1] + bfhi(pv[i][0])}, o1 = {acc[i][KG][2] + bflo(pv[i][1]), acc[i][KG][3] + bfhi(pv[i][1])};
        const f32x2 rr2 = {rr, rr};
        const f32x2 b0 = {bflo(bv[i][0]), bfhi(bv[i][0])}, b1 = {bflo(bv[i][1]), bfhi(bv[i][1])};
        f32x2 h0 = gelu_tanh2(o0 * rr2 + b0), h1 = gelu_tanh2(o1 * rr2 + b1);
        if (LAST) {
            const bool lin = u0 + i >= 108;
            h0[0] = lin ? o0[0] : h0[0]; h0[1] = lin ? o0[1] : h0[1]; h1[0] = lin ? o1[0] : h1[0]; h1[1] = lin ? o1[1] : h1[1];
        }
        ov[i] = u32x2{pk2(h0[0], h0[1]), pk2(h1[0], h1[1])};
    });
    __builtin_amdgcn_sched_barrier(0);
    const rsrc_t rh = mk_rsrc(ws + (LAST ? OFF_H2 : OFF_H), H_BYTES), r3 = mk_rsrc(ws + OFF_X3, X3_BYTES);
    const int hb = punit * 54 * 1024;
    auto st1 = [&](int i) __attribute__((always_inline)) {
        const int u = u0 + i;
        if (i < nu) {
            if (!LAST || u < 108) __builtin_amdgcn_raw_buffer_store_b64(ov[i], rh, lane * 16 + (u & 1) * 8, hb + (u >> 1) * 1024, SC1);
            else __builtin_amdgcn_raw_buffer_store_b64(ov[i], r3, lane * 8, (punit * 40 + (u - 108)) * 512, SC1);
        }
    };
    auto st2p = [&](int i) __attribute__((always_inline)) {
        const int u = u0 + i;
        if (i + 1 < nu && (!LAST || u + 1 < 108)) {
            u32x4 q4 = {ov[i][0], ov[i][1], ov[i + 1][0], ov[i + 1][1]};
            __builtin_amdgcn_raw_buffer_store_b128(q4, rh, lane * 16, hb + (u >> 1) * 1024, SC1);
        } else { st1(i); st1(i + 1); }
    };
    if (u0 & 1) {
        st1(0);
#pragma unroll
        for (int i = 1; i + 1 < NF; i += 2) st2p(i);
        if ((NF & 1) == 0) st1(NF - 1);
    } else {
#pragma unroll
        for (int i = 0; i + 1 < NF; i += 2) st2p(i);
        if (NF & 1) st1(NF - 1);
    }
    STAMP(KID, 4);
}

template <bool LAST>
DI void g2_phase(const MainArgs& a, char* smem, int w, int lane, int pt1, int pt, int ct) {
    constexpr int PH = G2_PH, KID = LAST ? 6 : 3;
    char* ws = a.p.ws;
    u32* cnt = (u32*)(ws + OFF_CNT + (LAST ? 8192 : 0));
    const int pg = w & 1, kq = w >> 1, n = lane & 15, q = lane >> 4;
    const int kb = kq < 2 ? 14 * kq : 28 + 13 * (kq - 2), nk = kq < 2 ? 14 : 13;
    const u32x4 srd2 = mk_srd(ws + (LAST ? OFF_W2O : OFF_W2F), W2F_BYTES);
    const u32 lds0 = (u32)(size_t)(LAS char*)smem;
    const rsrc_t rh = mk_rsrc(ws + (LAST ? OFF_H2 : OFF_H), H_BYTES), rw = mk_rsrc(ws + (LAST ? OFF_W2O : OFF_W2F), W2F_BYTES);
    const int vo = lane * 16, hs = ((4 * pt + 2 * pg) * 54 + kb) * 1024;
    bf16x8 wr[6][5];
    asm volatile("" ::: "memory");
    __builtin_amdgcn_sched_barrier(0);
#pragma unroll
    for (int t = 0; t < 3; ++t)
#pragma unroll
        for (int i = 0; i < 5; ++i) wr[t][i] = asbf(__builtin_amdgcn_raw_buffer_load_b128(rw, vo, ((5 * ct + i) * 54 + kb + 8 + t) * 1024, SC1));
    __builtin_amdgcn_sched_barrier(0);
    wait_lgkm();
    wait_vm<15>();
    barrier();
    if (threadIdx.x == 0) add_agent(cnt + 64 * pt1);
    STAMP(LAST ? 5 : 2, 15);
#pragma unroll
    for (int t = LAST ? 5 : 6; t < 8; ++t) w2_issue(srd2, lds0, w, vo, ct, t);
    __builtin_amdgcn_sched_barrier(0);
#pragma unroll
    for (int t = 3; t < 6; ++t)
#pragma unroll
        for (int i = 0; i < 5; ++i) wr[t][i] = asbf(__builtin_amdgcn_raw_buffer_load_b128(rw, vo, ((5 * ct + i) * 54 + kb + 8 + t) * 1024, SC1));
    __builtin_amdgcn_sched_barrier(0);
    if (w == 0) poll_until(cnt + 64 * (pt >> 1), 16u, ws, lane);
    wait_vm<0>();
    barrier();
    STAMP(KID, 5);
    bf16x8 hb[PH][2];
    auto hload = [&](int t, int pu) __attribute__((always_inline)) {
        const int k = t < 13 ? t : nk - 1;
        return asbf(__builtin_amdgcn_raw_buffer_load_b128(rh, vo, hs + (pu * 54 + k) * 1024, SC1));
    };
#pragma unroll
    for (int t = 0; t < PH; ++t) { hb[t][0] = hload(t, 0); hb[t][1] = hload(t, 1); __builtin_amdgcn_sched_barrier(0); }
    u32x2 xv[3], bv[3]; float bb[3];
    f32x4 acc[2][5];
#pragma unroll
    for (int i = 0; i < 5; ++i) { acc[0][i] = f32x4{0, 0, 0, 0}; acc[1][i] = f32x4{0, 0, 0, 0}; }
    bf16x8 wa[2][5];
    const char* wbase = smem + kq * 5120 + lane * 16;
#pragma unroll
    for (int i = 0; i < 5; ++i) wa[0][i] = *(const bf16x8*)(wbase + w2slot(0) + i * 1024);
    static_for<0, 14>([&](auto T) __attribute__((always_inline)) {
        constexpr int t = decltype(T)::value;
        if constexpr (t == 10) {
            __builtin_amdgcn_sched_barrier(0);
#pragma unroll
            for (int j = 0; j < 3; ++j) {
                const int tt = min(kq + 4 * j, 9), pu = 2 * pg + tt / 5, cu = tt % 5;
                if (!LAST) {
                    const int c = 80 * ct + 16 * cu + n;
                    const int off = (((4 * pt + pu) * 20 + (c >> 5)) * 64 + ((c & 31) >> 3) * 16 + 4 * q) * 16 + (c & 7) * 2;
                    const rsrc_t r1 = mk_rsrc(ws + OFF_XF1, XT_BYTES);
                    const u32 e0 = __builtin_amdgcn_raw_buffer_load_b16(r1, off, 0, SC1), e1 = __builtin_amdgcn_raw_buffer_load_b16(r1, off + 16, 0, SC1);
                    const u32 e2 = __builtin_amdgcn_raw_buffer_load_b16(r1, off + 32, 0, SC1), e3 = __builtin_amdgcn_raw_buffer_load_b16(r1, off + 48, 0, SC1);
                    xv[j] = u32x2{e0 | (e1 << 16), e2 | (e3 << 16)};
                    bb[j] = ((const float*)(ws + OFF_B2P))[c];
                } else {
                    xv[j] = __builtin_amdgcn_raw_buffer_load_b64(mk_rsrc(ws + OFF_X3, X3_BYTES), lane * 8, ((4 * pt + pu) * 40 + 5 * ct + cu) * 512, SC1);
                    bv[j] = *(const u32x2*)(a.p.b2 + 80 * ct + 16 * cu + 4 * q);
                }
            }
        }
        bf16x8 nh[2];
        auto step = [&]() __attribute__((always_inline)) {
            static_for<0, 5>([&](auto I) __attribute__((always_inline)) {
                constexpr int i = decltype(I)::value;
                __builtin_amdgcn_sched_barrier(0);
                bf16x8 wv;
                if constexpr (t < 8) wv = wa[t & 1][i]; else wv = wr[t - 8][i];
                acc[0][i] = LAST ? mfma16(wv, hb[t % PH][0], acc[0][i]) : mfma16(hb[t % PH][0], wv, acc[0][i]);
                if constexpr (t + 1 < 8) wa[(t + 1) & 1][i] = *(const bf16x8*)(wbase + w2slot(t + 1) + i * 1024);
                acc[1][i] = LAST ? mfma16(wv, hb[t % PH][1], acc[1][i]) : mfma16(hb[t % PH][1], wv, acc[1][i]);
                __builtin_amdgcn_sched_barrier(0);
                if constexpr (t + PH < 14 && (i == 1 || i == 3)) nh[i >> 1] = hload(t + PH, i >> 1);
            });
        };
        if constexpr (t < 13) step(); else { if (nk == 14) step(); }
        __builtin_amdgcn_sched_barrier(0);
        if constexpr (t + PH < 14) { hb[t % PH][0] = nh[0]; hb[t % PH][1] = nh[1]; }
        if (t == 0) STAMP(KID, 1);
        if (t == 7) STAMP(KID, 2);
    });
    STAMP(KID, 3);
    wait_lgkm();
    barrier();
#pragma unroll
    for (int t = 0; t < 10; ++t)
        if ((t & 3) != kq) *(f32x4*)(smem + ((w * 10 + t) * 64 + lane) * 16) = acc[t / 5][t % 5];
    wait_lgkm();
    barrier();
    float ssp[2][4];
#pragma unroll
    for (int r = 0; r < 4; ++r) { ssp[0][r] = 0.f; ssp[1][r] = 0.f; }
    const rsrc_t ro = mk_rsrc(ws + OFF_XT2, XT_BYTES);
    auto finish = [&](auto KQ) __attribute__((always_inline)) {
        constexpr int kqc = decltype(KQ)::value;
        static_for<0, 3>([&](auto J) __attribute__((always_inline)) {
            constexpr int j = decltype(J)::value, t = kqc + 4 * j;
            if constexpr (t < 10) {
                constexpr int pl = t / 5, cu = t % 5;
                f32x4 o = acc[pl][cu];
#pragma unroll
                for (int s = 1; s < 4; ++s) o += *(const f32x4*)(smem + (((pg + 2 * ((kqc + s) & 3)) * 10 + t) * 64 + lane) * 16);
                const int pu = 2 * pg + pl;
                if (!LAST) {
                    const int c = 80 * ct + 16 * cu + n, p = 64 * pt + 16 * pu + 4 * q;
                    const u32 o0 = pk2(bflo(xv[j][0]) + o[0] + bb[j], bfhi(xv[j][0]) + o[1] + bb[j]);
                    const u32 o1 = pk2(bflo(xv[j][1]) + o[2] + bb[j], bfhi(xv[j][1]) + o[3] + bb[j]);
                    u32x2 ov = {o0, o1};
                    __builtin_amdgcn_raw_buffer_store_b64(ov, ro, (c * S_ + p) * 2, 0, SC1);
                    const float m = c == 609 ? 0.f : 1.f;
                    ssp[pl][0] += m * bflo(o0) * bflo(o0); ssp[pl][1] += m * bfhi(o0) * bfhi(o0);
                    ssp[pl][2] += m * bflo(o1) * bflo(o1); ssp[pl][3] += m * bfhi(o1) * bfhi(o1);
                } else {
                    const int p = 16 * (4 * pt + pu) + n, d = 80 * ct + 16 * cu + 4 * q;
                    u32x2 ov = {pk2(bflo(xv[j][0]) + o[0] + bflo(bv[j][0]), bfhi(xv[j][0]) + o[1] + bfhi(bv[j][0])),
                                pk2(bflo(xv[j][1]) + o[2] + bflo(bv[j][1]), bfhi(xv[j][1]) + o[3] + bfhi(bv[j][1]))};
                    *(u32x2*)(a.out + (size_t)tok_of_p(p) * D_ + d) = ov;
                }
            }
        });
    };
    if (kq == 0) finish(std::integral_constant<int, 0>{});
    else if (kq == 1) finish(std::integral_constant<int, 1>{});
    else if (kq == 2) finish(std::integral_constant<int, 2>{});
    else finish(std::integral_constant<int, 3>{});
    if (!LAST) {
        float* sp = (float*)smem;
#pragma unroll
        for (int pl = 0; pl < 2; ++pl)
#pragma unroll
            for (int r = 0; r < 4; ++r) {
                const float v = row_sum15(ssp[pl][r]);
                if (n == 15) sp[w * 32 + 16 * pl + 4 * q + r] = v;
            }
        wait_lgkm();
        barrier();
        if (w == 0) {
            const int g2 = lane >> 5, r = lane & 31;
            const float v = (sp[g2 * 32 + r] + sp[(g2 + 2) * 32 + r]) + (sp[(g2 + 4) * 32 + r] + sp[(g2 + 6) * 32 + r]);
            __hip_atomic_fetch_add((__attribute__((address_space(1))) u64*)(ws + OFF_SS2) + 64 * pt + lane, ss_from_float(v), __ATOMIC_RELAXED, __HIP_MEMORY_SCOPE_AGENT);
        }
        wait_lgkm();
        wait_vm<0>();
        barrier();
        signal_all(ws, 1);
    }
}

template <bool LAST>
DI void mlp(const MainArgs& a, char* smem, int w, int lane, int u0) {
    constexpr int NF = LAST ? 10 : 7;
    const int x = blockIdx.x & 7, ib = blockIdx.x >> 3;
    const int pt = 4 * (x >> 1) + (ib & 3), ft = 8 * (x & 1) + (ib >> 2);
    const int pt2 = 8 * (x >> 1) + (ib & 7), ct2 = 4 * (x & 1) + (ib >> 3);
    const int nu = LAST ? (ft < 4 ? 10 : 9) : (ft < 12 ? 7 : 6);
    if (LAST) poll_all(a.p.ws, 2, 20u, w, lane);
    else if (w == 0) {
        const int sh = lane & 7, wh = (lane >> 3) & 1;
        poll_until((const u32*)(a.p.ws + OFF_CNT + 16384 + (wh ? 3 : 0) * 8192) + ((blockIdx.x >> 3) & 7) * 256 + sh * 32, wh ? (sh < 6 ? 12u : 11u) : 20u, a.p.ws, lane);
    }
    wait_vm<(LAST ? 6 : 5)>();
    barrier();
    STAMP(LAST ? 5 : 2, 1);
    if (w >= 4) __builtin_amdgcn_s_setprio(1);
    if (w < 4) g1_phase<NF, LAST, 0>(a, smem, w, lane, pt, u0, nu, ct2);
    else g1_phase<NF, LAST, 1>(a, smem, w, lane, pt, u0, nu, ct2);
    __builtin_amdgcn_s_setprio(0);
    g2_phase<LAST>(a, smem, w, lane, pt, pt2, ct2);
}

__global__ __launch_bounds__(512, 2) void k_main(MainArgs a) {
    extern __shared__ __attribute__((aligned(16))) char smem[];
    const int lane = threadIdx.x & 63, b = blockIdx.x;
    const int w = __builtin_amdgcn_readfirstlane(threadIdx.x >> 6);
    const int ft = 8 * (b & 1) + (b >> 5);
    const int u0a = ft < 12 ? 7 * ft : 84 + 6 * (ft - 12), u0b = ft < 4 ? 10 * ft : 40 + 9 * (ft - 4);
    if (b < 160) col_task<0>(a.p.ws, b, smem, u0a);
    else {
        if (b < 254) {
            if (b < 214) task_w2(a.p, b - 160, smem); else task_Q(a.p, b - 214);
            wait_lgkm();
            wait_vm<0>();
            barrier();
            signal_all(a.p.ws, 3);
        }
        w1_prefetch<false>(a.p.ws, smem, w, lane, u0a);
    }
    mlp<false>(a, smem, w, lane, u0a);
    END_STAMP(3);
    if (b < 160) col_task<1>(a.p.ws, b, smem, u0b); else w1_prefetch<true>(a.p.ws, smem, w, lane, u0b);
    mlp<true>(a, smem, w, lane, u0b);
    END_STAMP(6);
}

extern "C" void kernel_launch(void* const* d_in, const int* in_sizes, int n_in, void* d_out, int out_size, void* d_ws,
                              size_t ws_size, hipStream_t stream) {
    char* ws = (char*)d_ws;
    constexpr int MAIN_LDS = 163840;
    (void)hipFuncSetAttribute((const void*)k_main, hipFuncAttributeMaxDynamicSharedMemorySize, MAIN_LDS);
    PrepArgs pa{(const u16*)d_in[0], (const u16*)d_in[1], (const u16*)d_in[2], (const u16*)d_in[4], (const u16*)d_in[6], (const u16*)d_in[7], ws};
    k0_prep<<<NB_K0, 512, PREP_LDS, stream>>>(pa);
    MainArgs ma{pa, (const u16*)d_in[5], (u16*)d_out};
    k_main<<<256, 512, MAIN_LDS, stream>>>(ma);
}
```

```cpp
#include <hip/hip_runtime.h>
#include <hip/hip_bf16.h>
#include <stdint.h>
#include <type_traits>

typedef unsigned short u16;
typedef unsigned int u32;
typedef unsigned long long u64;
typedef short s16x4 __attribute__((ext_vector_type(4)));
typedef __bf16 bf16x8 __attribute__((ext_vector_type(8)));
typedef __bf16 bf16x2 __attribute__((ext_vector_type(2)));
typedef float f32x4 __attribute__((ext_vector_type(4)));
typedef float f32x2 __attribute__((ext_vector_type(2)));
typedef u32 u32x4 __attribute__((ext_vector_type(4)));
typedef u32 u32x2 __attribute__((ext_vector_type(2)));
typedef __amdgpu_buffer_rsrc_t rsrc_t;
#define LAS __attribute__((address_space(3)))
#define DI __device__ __forceinline__

constexpr int S_ = 2048, D_ = 640, F_ = 1728, DF_ = 321;
constexpr float EPS = 1e-5f;
constexpr int SC1 = 16;
constexpr float SS_SCALE = 1048576.0f;

constexpr size_t XT_BYTES = (size_t)D_ * S_ * 2;
constexpr size_t OFF_XT0 = 0;
constexpr size_t OFF_XT2 = OFF_XT0 + XT_BYTES;
constexpr size_t OFF_WT = OFF_XT2 + XT_BYTES;
constexpr size_t OFF_W1F = OFF_WT + (size_t)320 * 2048 * 4;
constexpr size_t W1F_BYTES = (size_t)148 * 20 * 1024;
constexpr size_t OFF_W2F = OFF_W1F + W1F_BYTES;
constexpr size_t W2F_BYTES = (size_t)40 * 54 * 1024;
constexpr size_t OFF_W2O = OFF_W2F + W2F_BYTES;
constexpr size_t OFF_H = OFF_W2O + W2F_BYTES;
constexpr size_t H_BYTES = (size_t)128 * 54 * 1024;
constexpr size_t OFF_H2 = OFF_H;
constexpr size_t OFF_X3 = OFF_H + H_BYTES;
constexpr size_t OFF_SS0 = OFF_X3 + (size_t)128 * 40 * 512;
constexpr size_t OFF_SS2 = OFF_SS0 + 2048 * 8;
constexpr size_t OFF_B2P = OFF_SS2 + 2048 * 8;
constexpr size_t OFF_XF1 = OFF_B2P + 4096;
constexpr size_t OFF_XF3 = OFF_XF1;
constexpr size_t X3_BYTES = (size_t)128 * 40 * 512;
constexpr size_t OFF_CNT = OFF_XF1 + XT_BYTES;
constexpr size_t WS_END = OFF_CNT + 65536;

DI f32x4 mfma16(bf16x8 a, bf16x8 b, f32x4 c) { return __builtin_amdgcn_mfma_f32_16x16x32_bf16(a, b, c, 0, 0, 0); }
DI u32 pk2(float a, float b) { f32x2 v = {a, b}; const bf16x2 r = __builtin_convertvector(v, bf16x2); return __builtin_bit_cast(u32, r); }
DI bf16x8 pk8(float a0, float a1, float a2, float a3, float a4, float a5, float a6, float a7) {
    u32x4 r = {pk2(a0, a1), pk2(a2, a3), pk2(a4, a5), pk2(a6, a7)};
    return __builtin_bit_cast(bf16x8, r);
}
DI bf16x8 pk8(const float (&a)[8]) { return pk8(a[0], a[1], a[2], a[3], a[4], a[5], a[6], a[7]); }
DI bf16x8 asbf(u32x4 r) { return __builtin_bit_cast(bf16x8, r); }
DI bf16x8 neg8(bf16x8 a) { u32x4 r = __builtin_bit_cast(u32x4, a); r ^= 0x80008000u; return __builtin_bit_cast(bf16x8, r); }
DI float bflo(u32 v) { return __builtin_bit_cast(float, v << 16); }
DI float bfhi(u32 v) { return __builtin_bit_cast(float, v & 0xffff0000u); }
DI float cosr(float rev) { return __builtin_amdgcn_cosf(rev); }
DI float sinr(float rev) { return __builtin_amdgcn_sinf(rev); }
DI s16x4 tr_read(const char* p) { return __builtin_amdgcn_ds_read_tr16_b64_v4i16((LAS s16x4*)p); }
DI bf16x8 cat8(s16x4 lo, s16x4 hi) { return __builtin_bit_cast(bf16x8, __builtin_shufflevector(lo, hi, 0, 1, 2, 3, 4, 5, 6, 7)); }
DI int tok_of_p(int p) { return 32 * (p & 63) + (p >> 6); }
DI rsrc_t mk_rsrc(const void* p, size_t bytes) { return __builtin_amdgcn_make_buffer_rsrc((void*)p, 0, (int)bytes, 0x00020000); }
DI u32x4 bload16(rsrc_t r, int voff, int soff) { return __builtin_amdgcn_raw_buffer_load_b128(r, voff, soff, 0); }
DI u64 ss_from_float(float s) { return (u64)(fminf(s * SS_SCALE, 1.0e18f) + 0.5f); }
DI void pin(float& x) { asm volatile("" : "+v"(x)); }
DI void pin(bf16x8& x) { asm volatile("" : "+v"(x)); }

struct F32c { bf16x8 c[2], sp[2]; };
DI void f32c_init(F32c& k, int lane) {
    const float m = (float)(lane & 15), q4 = (float)(4 * (lane >> 4));
    float c[4], s[4];
#pragma unroll
    for (int j = 0; j < 4; ++j) { const float a = __builtin_amdgcn_fractf(m * (q4 + (float)j) * (1.0f / 32.0f)); c[j] = cosr(a); s[j] = sinr(a); }
    const u32 sg = (lane & 1) ? 0x80008000u : 0u;
    const u32 c0 = pk2(c[0], c[1]), c1 = pk2(c[2], c[3]), s0 = pk2(s[0], s[1]), s1 = pk2(s[2], s[3]);
    const u32x4 cv = {c0, c1, c0 ^ sg, c1 ^ sg}, sv = {s0, s1, s0 ^ sg, s1 ^ sg};
    k.c[0] = asbf(cv); k.sp[0] = asbf(sv);
    k.c[1] = asbf(cv ^ 0x80000000u); k.sp[1] = asbf(sv ^ 0x80000000u);
}

struct RowC { bf16x8 c[2], sp[2], sm[2], c20, s20m; f32x4 twr[2], twi[2]; int off0, off1; };
constexpr int ROWC_LDS = 10 * 1024;
DI void rowc_build(char* tab, int w, int lane) {
    const int n = lane & 15, q = lane >> 4;
    const float nf = (float)n;
    u32x4* t = (u32x4*)tab + lane;
    if (w == 0) {
        F32c f; f32c_init(f, lane);
        t[0] = __builtin_bit_cast(u32x4, f.c[0]); t[64] = __builtin_bit_cast(u32x4, f.c[1]);
        t[128] = __builtin_bit_cast(u32x4, f.sp[0]); t[192] = __builtin_bit_cast(u32x4, f.sp[1]);
    } else if (w <= 2) {
        float c[8];
#pragma unroll
        for (int j = 0; j < 8; ++j) {
            const int d1 = 8 * q + j;
            const float a = __builtin_amdgcn_fractf((float)d1 * nf * 0.05f);
            const float v = w == 1 ? cosr(a) : -sinr(a);
            c[j] = (d1 < 20 && n <= 10) ? v : 0.f;
        }
        t[(3 + w) * 64] = __builtin_bit_cast(u32x4, pk8(c));
    } else if (w <= 4) {
        const float sc = 0.05590169943749474f;
        f32x4 cr, ci;
#pragma unroll
        for (int i = 0; i < 4; ++i) {
            const float a = (float)(16 * (w - 3) + 4 * q + i) * nf * (1.0f / 640.0f);
            cr[i] = cosr(a) * sc; ci[i] = -sinr(a) * sc;
        }
        t[(6 + w - 3) * 64] = __builtin_bit_cast(u32x4, cr); t[(8 + w - 3) * 64] = __builtin_bit_cast(u32x4, ci);
    }
}
DI void rowc_load(RowC& k, const char* tab, int lane) {
    const u32x4* tb = (const u32x4*)tab + lane;
    k.c[0] = asbf(tb[0]); k.c[1] = asbf(tb[64]); k.sp[0] = asbf(tb[128]); k.sp[1] = asbf(tb[192]);
    k.c20 = asbf(tb[256]); k.s20m = asbf(tb[320]);
    k.twr[0] = __builtin_bit_cast(f32x4, tb[384]); k.twr[1] = __builtin_bit_cast(f32x4, tb[448]);
    k.twi[0] = __builtin_bit_cast(f32x4, tb[512]); k.twi[1] = __builtin_bit_cast(f32x4, tb[576]);
    k.sm[0] = neg8(k.sp[0]); k.sm[1] = neg8(k.sp[1]);
    const int q = lane >> 4, qq = (lane & 15) >> 2, pp = lane & 3;
    const int r0 = min(8 * q + qq, 19), r1 = min(8 * q + 4 + qq, 19);
    k.off0 = r0 * 64 + pp * 8; k.off1 = r1 * 64 + pp * 8;
}
DI float wave_sum63(float v) {
    v += __builtin_bit_cast(float, __builtin_amdgcn_update_dpp(0, __builtin_bit_cast(int, v), 0x111, 0xf, 0xf, true));
    v += __builtin_bit_cast(float, __builtin_amdgcn_update_dpp(0, __builtin_bit_cast(int, v), 0x112, 0xf, 0xf, true));
    v += __builtin_bit_cast(float, __builtin_amdgcn_update_dpp(0, __builtin_bit_cast(int, v), 0x114, 0xf, 0xf, true));
    v += __builtin_bit_cast(float, __builtin_amdgcn_update_dpp(0, __builtin_bit_cast(int, v), 0x118, 0xf, 0xf, true));
    v += __builtin_bit_cast(float, __builtin_amdgcn_update_dpp(0, __builtin_bit_cast(int, v), 0x142, 0xa, 0xf, true));
    v += __builtin_bit_cast(float, __builtin_amdgcn_update_dpp(0, __builtin_bit_cast(int, v), 0x143, 0xc, 0xf, true));
    return v;
}
DI void rowfft(const RowC& k, const char* row, float (&A)[8], float (&B)[8], int lane) {
    f32x4 z = {0.f, 0.f, 0.f, 0.f};
    f32x4 yr[2], yi[2];
#pragma unroll
    for (int mt = 0; mt < 2; ++mt) {
        const bf16x8 a = cat8(tr_read(row + k.off0 + mt * 32), tr_read(row + k.off1 + mt * 32));
        yr[mt] = mfma16(a, k.c20, z); yi[mt] = mfma16(a, k.s20m, z);
    }
    float tr[8], ti[8];
#pragma unroll
    for (int i = 0; i < 8; ++i) {
        const float a = yr[i >> 2][i & 3], b = yi[i >> 2][i & 3], cr = k.twr[i >> 2][i & 3], ci = k.twi[i >> 2][i & 3];
        tr[i] = a * cr - b * ci; ti[i] = a * ci + b * cr;
    }
    const bf16x8 Tr = pk8(tr), Ti = pk8(ti);
    f32x4 ore[2], oim[2];
#pragma unroll
    for (int t = 0; t < 2; ++t) {
        ore[t] = mfma16(k.c[t], Tr, z); ore[t] = mfma16(k.sp[t], Ti, ore[t]);
        oim[t] = mfma16(k.sm[t], Tr, z); oim[t] = mfma16(k.c[t], Ti, oim[t]);
    }
#pragma unroll
    for (int r = 0; r < 4; ++r) { A[2 * r] = ore[0][r]; A[2 * r + 1] = oim[0][r]; B[2 * r] = ore[1][r]; B[2 * r + 1] = oim[1][r]; }
    if (lane == 0) { const float h = 0.7071067811865476f; const float dc = A[0] * h; A[0] = B[0] * h; A[1] = dc; }
}
DI int sw_off(int c) { const int ch = c >> 3; return ((ch ^ ((ch >> 3) & 7)) << 4) + (c & 7) * 2; }
DI int chunk_c0(int n, int q) { return (n == 0 ? 608 : n == 10 ? 576 : 64 * (n - 1)) + 8 * q; }

constexpr int ROWST = 1344;
constexpr int PREP_TAB = 640 * 32 + 16 * ROWST;
constexpr int PREP_LDS = PREP_TAB + ROWC_LDS;

struct PrepArgs { const u16 *x, *wr, *wi, *w1, *w2, *b2; char* ws; };

DI void task_x(const PrepArgs& a, int b, char* smem) {
    const int tid = threadIdx.x, lane = tid & 63, w = tid >> 6;
    const int p0 = 16 * ((b & 7) * 16 + (b >> 3));
    u32x4 v[3];
#pragma unroll
    for (int i = 0; i < 3; ++i) {
        const int id = min(tid + 512 * i, 1279), r = id / 80, ch = id % 80;
        v[i] = *(const u32x4*)(a.x + (size_t)tok_of_p(p0 + r) * D_ + ch * 8);
    }
    __builtin_amdgcn_sched_barrier(0);
    rowc_build(smem + PREP_TAB, w, lane);
    __builtin_amdgcn_sched_barrier(0);
    if (tid < 16) ((u64*)(a.ws + OFF_SS2))[p0 + tid] = 0ull;
    if (b < 32) ((u32*)(a.ws + OFF_CNT))[b * 512 + tid] = 0u;
#pragma unroll
    for (int i = 0; i < 3; ++i) {
        const int id = min(tid + 512 * i, 1279), r = id / 80, ch = id % 80;
        *(u32x4*)(smem + r * ROWST + ch * 16) = v[i];
    }
    __syncthreads();
    RowC k; rowc_load(k, smem + PREP_TAB, lane);
    const int n = lane & 15, q = lane >> 4, c0 = chunk_c0(n, q);
    float A[2][8], B[2][8];
#pragma unroll
    for (int i = 0; i < 2; ++i) rowfft(k, smem + (2 * w + i) * ROWST, A[i], B[i], lane);
#pragma unroll
    for (int i = 0; i < 2; ++i) {
        const int r = 2 * w + i;
        float ss = 0.f;
        if (n <= 10) {
#pragma unroll
            for (int e = 0; e < 8; ++e) ss += (lane == 0 && e == 1) ? 0.f : A[i][e] * A[i][e];
            *(bf16x8*)(smem + r * ROWST + sw_off(c0)) = pk8(A[i]);
        }
        if (n >= 1 && n <= 9) {
#pragma unroll
            for (int e = 0; e < 8; ++e) ss += B[i][e] * B[i][e];
            *(bf16x8*)(smem + r * ROWST + sw_off(c0 + 32)) = pk8(B[i]);
        }
        ss = wave_sum63(ss);
        if (lane == 63) ((u64*)(a.ws + OFF_SS0))[p0 + r] = ss_from_float(ss);
    }
    __syncthreads();
    u16* xt = (u16*)(a.ws + OFF_XT0);
    const int g = lane >> 4, i16 = lane & 15, qq = i16 >> 2, pp = i16 & 3;
#pragma unroll
    for (int i = 0; i < 3; ++i) {
        const int cbk = w + 8 * i;
        if (cbk < 20) {
            const int cb = 32 * cbk + 16 * (g & 1), ph = g >> 1;
            const char* base = smem + (8 * ph + qq) * ROWST + sw_off(cb + 4 * pp);
            const bf16x8 o = cat8(tr_read(base), tr_read(base + 4 * ROWST));
            *(bf16x8*)(xt + (size_t)(cb + i16) * S_ + p0 + 8 * ph) = o;
        }
    }
}

DI void task_W(const PrepArgs& a, int b) {
    const int lane = threadIdx.x & 63, wv = threadIdx.x >> 6, wt = b * 2 + (wv >> 2), kq = wv & 3;
    const int k1 = wt / 5, cg = wt % 5, kp = 1 + 64 * cg + lane;
    const int r = kp % 20, t = kp / 20;
    int j; bool mir = false;
    if (kp == 320) j = 304;
    else if (r == 0) j = 304 + t;
    else if (r < 10) j = 32 * (r - 1) + t;
    else if (r == 10) j = 288 + t;
    else { j = 32 * (19 - r) + 31 - t; mir = true; }
    const rsrc_t rr = mk_rsrc(a.wr, (size_t)S_ * DF_ * 2), ri = mk_rsrc(a.wi, (size_t)S_ * DF_ * 2);
    const int st = (mir ? -64 : 64) * DF_ * 2, off0 = ((mir ? 2048 - k1 : k1) * DF_ + kp) * 2 + 8 * kq * st;
    u32 re[8], im[8];
#pragma unroll
    for (int k2 = 0; k2 < 8; ++k2) {
        int off = off0 + k2 * st;
        if (k2 == 0 && mir && k1 == 0 && kq == 0) off = kp * 2;
        re[k2] = __builtin_amdgcn_raw_buffer_load_b16(rr, off, 0, 0); im[k2] = __builtin_amdgcn_raw_buffer_load_b16(ri, off, 0, 0);
    }
    const u32 sg = mir ? 0x80000000u : 0u;
    u32x4* dst = (u32x4*)(a.ws + OFF_WT + ((size_t)j * 64 + k1) * 128 + kq * 32);
#pragma unroll
    for (int i = 0; i < 2; ++i) {
        u32x4 o;
#pragma unroll
        for (int e = 0; e < 4; ++e) o[e] = (re[4 * i + e] | (im[4 * i + e] << 16)) ^ sg;
        dst[i] = o;
    }
}

DI void task_w1(const PrepArgs& a, int b, char* smem) {
    const int tid = threadIdx.x, lane = tid & 63, w = tid >> 6;
    char* raw = smem;
    char* tr = smem + 640 * 32;
    char* out = raw;
    u32x4 v[3];
#pragma unroll
    for (int i = 0; i < 3; ++i) {
        const int id = min(tid + 512 * i, 1279), d = id >> 1, h = id & 1;
        v[i] = *(const u32x4*)(a.w1 + (size_t)d * F_ + 16 * b + 8 * h);
    }
    __builtin_amdgcn_sched_barrier(0);
    rowc_build(smem + PREP_TAB, w, lane);
    __builtin_amdgcn_sched_barrier(0);
#pragma unroll
    for (int i = 0; i < 3; ++i) *(u32x4*)(raw + min(tid + 512 * i, 1279) * 16) = v[i];
    __syncthreads();
    const int g = lane >> 4, i16 = lane & 15, qq = i16 >> 2, pp = i16 & 3;
#pragma unroll
    for (int i = 0; i < 5; ++i) {
        const int d0 = 16 * (w + 8 * i) + 4 * g;
        const s16x4 t = tr_read(raw + (d0 + qq) * 32 + pp * 8);
        *(s16x4*)(tr + i16 * ROWST + d0 * 2) = t;
    }
    RowC k; rowc_load(k, smem + PREP_TAB, lane);
    __syncthreads();
    const int n = lane & 15, q = lane >> 4, c0 = chunk_c0(n, q);
    float A[2][8], B[2][8];
#pragma unroll
    for (int i = 0; i < 2; ++i) rowfft(k, tr + (2 * w + i) * ROWST, A[i], B[i], lane);
#pragma unroll
    for (int i = 0; i < 2; ++i) {
        const int f = 2 * w + i;
        if (lane == 0) A[i][1] = 0.f;
        if (n <= 10) *(bf16x8*)(out + ((c0 >> 5) * 64 + q * 16 + f) * 16) = pk8(A[i]);
        if (n >= 1 && n <= 9) *(bf16x8*)(out + (((c0 >> 5) + 1) * 64 + q * 16 + f) * 16) = pk8(B[i]);
    }
    __syncthreads();
    u32x4* dst = (u32x4*)(a.ws + OFF_W1F + (size_t)b * 20 * 1024);
#pragma unroll
    for (int i = 0; i < 3; ++i) { const int id = tid + 512 * i; if (id < 1280) dst[id] = *(const u32x4*)(out + id * 16); }
}

template <bool SW> DI void w2_frags(const char* img, char* dstbase, int b, int lane, int w) {
    const int g = lane >> 4, i16 = lane & 15, qq = i16 >> 2, pp = i16 & 3;
    const rsrc_t rd = mk_rsrc(dstbase, W2F_BYTES);
#pragma unroll
    for (int i = 0; i < 5; ++i) {
        const int cu = w + 8 * i, c = 16 * cu + 4 * pp;
        const char* base = img + (4 * g + qq) * ROWST + (SW ? sw_off(c) : c * 2);
        const bf16x8 v = cat8(tr_read(base), tr_read(base + 16 * ROWST));
        __builtin_amdgcn_raw_buffer_store_b128(__builtin_bit_cast(u32x4, v), rd, lane * 16, (cu * 54 + b) * 1024, SC1);
    }
}
constexpr int W2_TAB = 32 * ROWST;
DI void task_w2(const PrepArgs& a, int b, char* smem) {
    const int tid = threadIdx.x, lane = tid & 63, w = tid >> 6;
    u32x4 v[5];
#pragma unroll
    for (int i = 0; i < 5; ++i) {
        const int id = tid + 512 * i, r = id / 80, ch = id % 80;
        v[i] = *(const u32x4*)(a.w2 + (size_t)(32 * b + r) * D_ + ch * 8);
    }
    __builtin_amdgcn_sched_barrier(0);
    rowc_build(smem + W2_TAB, w, lane);
    __builtin_amdgcn_sched_barrier(0);
#pragma unroll
    for (int i = 0; i < 5; ++i) { const int id = tid + 512 * i, r = id / 80, ch = id % 80; *(u32x4*)(smem + r * ROWST + ch * 16) = v[i]; }
    __syncthreads();
    RowC k; rowc_load(k, smem + W2_TAB, lane);
    w2_frags<false>(smem, a.ws + OFF_W2O, b, lane, w);
    const int n = lane & 15, q = lane >> 4, c0 = chunk_c0(n, q);
    u32x4 A8[4], B8[4];
#pragma unroll
    for (int i = 0; i < 4; ++i) {
        float A[8], B[8];
        rowfft(k, smem + (4 * w + i) * ROWST, A, B, lane);
        A8[i] = __builtin_bit_cast(u32x4, pk8(A)); B8[i] = __builtin_bit_cast(u32x4, pk8(B));
    }
    __syncthreads();
#pragma unroll
    for (int i = 0; i < 4; ++i) {
        const int r = 4 * w + i;
        if (n <= 10) *(u32x4*)(smem + r * ROWST + sw_off(c0)) = A8[i];
        if (n >= 1 && n <= 9) *(u32x4*)(smem + r * ROWST + sw_off(c0 + 32)) = B8[i];
    }
    __syncthreads();
    w2_frags<true>(smem, a.ws + OFF_W2F, b, lane, w);
}

DI int kcol_of(int jc) { return jc < 288 ? (jc >> 5) + 1 + 20 * (jc & 31) : jc < 304 ? 10 + 20 * (jc - 288) : 20 * (jc - 304); }
DI void task_Q(const PrepArgs& a, int u) {
    const int tid = threadIdx.x;
#pragma unroll
    for (int ii = 0; ii < 3; ++ii) {
        const int i = tid + 512 * ii;
        if (i < 1280) {
            const int ks = i >> 6, l = i & 63, d = 16 * u + (l & 15), q = l >> 4;
            float v[8];
#pragma unroll
            for (int j = 0; j < 8; j += 2) {
                const int jc = 16 * ks + 4 * q + (j >> 1), kk = kcol_of(jc);
                const float ang = (float)((kk * d) % 640) * (1.0f / 640.0f);
                float c = cosr(ang) * 0.05590169943749474f, s = -sinr(ang) * 0.05590169943749474f;
                if (jc == 304) { c = ((d & 1) ? -1.f : 1.f) * 0.03952847075210474f; s = 0.03952847075210474f; }
                v[j] = c; v[j + 1] = s;
            }
            __builtin_amdgcn_raw_buffer_store_b128(__builtin_bit_cast(u32x4, pk8(v)), mk_rsrc(a.ws + OFF_W1F, W1F_BYTES), i * 16, (108 + u) * 20480, SC1);
        }
    }
}
DI void task_b(const PrepArgs& a, char* smem) {
    const int tid = threadIdx.x, lane = tid & 63;
    if (tid < 80) *(u32x4*)(smem + tid * 16) = *(const u32x4*)(a.b2 + tid * 8);
    rowc_build(smem + PREP_TAB, tid >> 6, lane);
    __syncthreads();
    RowC k; rowc_load(k, smem + PREP_TAB, lane);
    float A[8], B[8];
    rowfft(k, smem, A, B, lane);
    if (tid < 64) {
        const int n = lane & 15, q = lane >> 4, c0 = chunk_c0(n, q);
        float* o = (float*)(a.ws + OFF_B2P);
        if (n <= 10) for (int e = 0; e < 8; ++e) o[c0 + e] = A[e];
        if (n >= 1 && n <= 9) for (int e = 0; e < 8; ++e) o[c0 + 32 + e] = B[e];
    }
}

constexpr int NB_K0 = 128 + 108 + 160 + 1;
constexpr int MAIN_CODE_PREFETCH = 61440;
DI void prefetch_main_code(int slice, int lane) {
    u32 lo, hi;
    asm volatile("s_getpc_b64 s[40:41]\n\ts_add_u32 s40, s40, _Z6k_main8MainArgs@rel32@lo+4\n\ts_addc_u32 s41, s41, _Z6k_main8MainArgs@rel32@hi+12\n\t"
                 "s_mov_b32 %0, s40\n\ts_mov_b32 %1, s41" : "=s"(lo), "=s"(hi) :: "s40", "s41", "scc");
    const u64 base = ((u64)hi << 32) | lo;
    const char* p = (const char*)base + slice * 4096 + lane * 16;
    u32x4 t0, t1, t2, t3;
    asm volatile("global_load_dwordx4 %0, %4, off\n\tglobal_load_dwordx4 %1, %4, off offset:1024\n\tglobal_load_dwordx4 %2, %4, off offset:2048\n\t"
                 "global_load_dwordx4 %3, %4, off offset:3072\n\ts_waitcnt vmcnt(0)"
                 : "=&v"(t0), "=&v"(t1), "=&v"(t2), "=&v"(t3) : "v"(p) : "memory");
}
__global__ __launch_bounds__(512, 4) void k0_prep(PrepArgs a) {
    extern __shared__ __attribute__((aligned(16))) char smem[];
    const int b = blockIdx.x;
    if (b >= 236 && b < 236 + 8 * (MAIN_CODE_PREFETCH / 4096) && (threadIdx.x >> 6) == 7) prefetch_main_code((b - 236) >> 3, threadIdx.x & 63);
    if (b < 128) task_x(a, b, smem);
    else if (b < 236) task_w1(a, b - 128, smem);
    else if (b < 396) task_W(a, b - 236);
    else task_b(a, smem);
}

template <int N> DI void wait_vm() { asm volatile("s_waitcnt vmcnt(%0)" ::"n"(N) : "memory"); }
DI void wait_lgkm() { asm volatile("s_waitcnt lgkmcnt(0)" ::: "memory"); }
DI void barrier() { __builtin_amdgcn_s_barrier(); }
template <bool SC = false> DI void dma16(u32x4 srd, int voff, int soff, u32 ldsaddr) {
    if (SC) asm volatile("s_mov_b32 m0, %0\n\ts_nop 0\n\tbuffer_load_dwordx4 %1, %2, %3 offen sc1 lds" ::"s"(ldsaddr), "v"(voff), "s"(srd), "s"(soff) : "memory", "m0");
    else asm volatile("s_mov_b32 m0, %0\n\ts_nop 0\n\tbuffer_load_dwordx4 %1, %2, %3 offen lds" ::"s"(ldsaddr), "v"(voff), "s"(srd), "s"(soff) : "memory", "m0");
}
template <int LIM, bool SC = false> DI void dma16_lim(u32x4 srd, int voff, int soff, u32 ldsaddr, int w) {
    if (SC) asm volatile("s_cmp_lt_i32 %4, %5\n\ts_cbranch_scc0 .Lskip%=\n\ts_mov_b32 m0, %0\n\ts_nop 0\n\tbuffer_load_dwordx4 %1, %2, %3 offen sc1 lds\n.Lskip%=:"
                 ::"s"(ldsaddr), "v"(voff), "s"(srd), "s"(soff), "s"(w), "n"(LIM) : "memory", "m0", "scc");
    else asm volatile("s_cmp_lt_i32 %4, %5\n\ts_cbranch_scc0 .Lskip%=\n\ts_mov_b32 m0, %0\n\ts_nop 0\n\tbuffer_load_dwordx4 %1, %2, %3 offen lds\n.Lskip%=:"
                 ::"s"(ldsaddr), "v"(voff), "s"(srd), "s"(soff), "s"(w), "n"(LIM) : "memory", "m0", "scc");
}
DI u32x4 mk_srd(const void* p, size_t bytes) {
    const u64 a = (u64)p;
    u32x4 r = {(u32)a, (u32)(a >> 32) & 0xffffu, (u32)bytes, 0x00020000u};
    return r;
}
DI float row_sum15(float v) {
    v += __builtin_bit_cast(float, __builtin_amdgcn_update_dpp(0, __builtin_bit_cast(int, v), 0x111, 0xf, 0xf, true));
    v += __builtin_bit_cast(float, __builtin_amdgcn_update_dpp(0, __builtin_bit_cast(int, v), 0x112, 0xf, 0xf, true));
    v += __builtin_bit_cast(float, __builtin_amdgcn_update_dpp(0, __builtin_bit_cast(int, v), 0x114, 0xf, 0xf, true));
    v += __builtin_bit_cast(float, __builtin_amdgcn_update_dpp(0, __builtin_bit_cast(int, v), 0x118, 0xf, 0xf, true));
    return v;
}
template <int I, int N, class F> DI void static_for(F&& f) {
    if constexpr (I < N) { f(std::integral_constant<int, I>{}); static_for<I + 1, N>(f); }
}

constexpr int G1_P = 3, G1_PW = 5;
typedef __attribute__((address_space(1))) u32 gu32;
DI u32 ld_agent(const u32* p) { return __hip_atomic_load((gu32*)p, __ATOMIC_RELAXED, __HIP_MEMORY_SCOPE_AGENT); }
DI void add_agent(u32* p) { __hip_atomic_fetch_add((gu32*)p, 1u, __ATOMIC_RELAXED, __HIP_MEMORY_SCOPE_AGENT); }
DI void signal_all(char* ws, int which) {
    if (threadIdx.x == 0) {
        u32* c = (u32*)(ws + OFF_CNT + 16384 + which * 8192) + (blockIdx.x & 7) * 32;
#pragma unroll
        for (int r = 0; r < 8; ++r) add_agent(c + r * 256);
    }
}
DI void poll_until(const u32* p, u32 want, char* ws, int lane) {
    for (u32 spins = 0;; ++spins) {
        if (__all(ld_agent(p) >= want)) break;
        __builtin_amdgcn_s_sleep(16);
        if (spins > (1u << 18)) { if (lane == 0) __hip_atomic_store((gu32*)(ws + OFF_CNT + 49152), 1u, __ATOMIC_RELAXED, __HIP_MEMORY_SCOPE_AGENT); break; }
    }
}
DI void poll_all(char* ws, int which, u32 per_shard, int w, int lane) {
    if (w == 0) poll_until((const u32*)(ws + OFF_CNT + 16384 + which * 8192) + ((blockIdx.x >> 3) & 7) * 256 + (lane & 7) * 32, per_shard, ws, lane);
}

struct MainArgs { PrepArgs p; const u16* b1; u16* out; };
DI int img128(int row, int ch) { return row * 128 + ((ch ^ ((row >> 1) & 7)) << 4); }
template <bool LAST> DI void w1_prefetch(char* ws, char* smem, int w, int lane, int u0);

template <int L>
DI void col_task(char* ws, int b, char* smem, int u0) {
    const int tid = threadIdx.x & 255, lane = tid & 63, w = tid >> 6, half = threadIdx.x >> 8;
    const int j = 2 * b + half;
    char* Ure = smem + half * 24576; char* Uim = Ure + 4096; char* Ere = Ure + 8192; char* Eim = Ure + 12288;
    char* Rre = Ure + 16384; char* Rim = Ure + 20480;
    const int n = lane & 15, q = lane >> 4;
    const int wv = __builtin_amdgcn_readfirstlane(threadIdx.x >> 6);
    const rsrc_t rx = mk_rsrc(ws + (L == 0 ? OFF_XT0 : OFF_XT2), XT_BYTES), rs = mk_rsrc(ws + (L == 0 ? OFF_SS0 : OFF_SS2), 2048 * 8);
    u32x4 vr, vi, sv[4];
    auto load_in = [&]() __attribute__((always_inline)) {
        vr = __builtin_amdgcn_raw_buffer_load_b128(rx, (2 * j * S_ + tid * 8) * 2, 0, L == 0 ? 0 : SC1);
        vi = __builtin_amdgcn_raw_buffer_load_b128(rx, ((2 * j + 1) * S_ + tid * 8) * 2, 0, L == 0 ? 0 : SC1);
#pragma unroll
        for (int i = 0; i < 4; ++i) sv[i] = __builtin_amdgcn_raw_buffer_load_b128(rs, tid * 64 + 16 * i, 0, L == 0 ? 0 : SC1);
    };
    if (L == 0) load_in();
    u32x4 wf[2];
#pragma unroll
    for (int t = 0; t < 2; ++t) wf[t] = *(const u32x4*)((const u32*)(ws + OFF_WT) + ((size_t)j * 64 + 16 * w + n) * 32 + 16 * t + 4 * q);
    __builtin_amdgcn_sched_barrier(0);
    F32c f; f32c_init(f, lane);
    bf16x8 c32[2] = {f.c[0], f.c[1]}, s32p[2] = {f.sp[0], f.sp[1]}, s32m[2];
    pin(c32[0]); pin(c32[1]); pin(s32p[0]); pin(s32p[1]);
    bf16x8 c64[2], s64p[2], s64m[2];
    const float k1f = (float)(16 * w + n);
    {
        float c[8], s[8];
#pragma unroll
        for (int e = 0; e < 8; ++e) {
            const float ang = __builtin_amdgcn_fractf((float)(8 * q + e) * k1f * (1.0f / 64.0f));
            c[e] = cosr(ang); s[e] = sinr(ang);
        }
        c64[0] = pk8(c); s64p[0] = pk8(s);
        const u32 sg = (lane & 1) ? 0x80008000u : 0u;
        c64[1] = asbf(__builtin_bit_cast(u32x4, c64[0]) ^ sg); s64p[1] = asbf(__builtin_bit_cast(u32x4, s64p[0]) ^ sg);
        pin(c64[0]); pin(c64[1]); pin(s64p[0]); pin(s64p[1]);
    }
    float twr[8], twi[8], ivr[8], ivi[8];
#pragma unroll
    for (int i = 0; i < 8; ++i) {
        const float af = (float)(16 * (i >> 2) + 4 * q + (i & 3)) * k1f * (1.0f / 2048.0f);
        twr[i] = cosr(af); twi[i] = -sinr(af);
        const float ai = (float)(16 * w + 4 * q + (i & 3)) * (float)(16 * (i >> 2) + n) * (1.0f / 2048.0f);
        ivr[i] = cosr(ai) * (1.0f / 2048.0f); ivi[i] = sinr(ai) * (1.0f / 2048.0f);
        pin(twr[i]); pin(twi[i]); pin(ivr[i]); pin(ivi[i]);
    }
    __builtin_amdgcn_sched_barrier(0);
    if (L == 1) {
        poll_all(ws, 1, 32u, wv, lane);
        barrier();
        load_in();
        __builtin_amdgcn_sched_barrier(0);
    }
    s64m[0] = neg8(s64p[0]); s64m[1] = neg8(s64p[1]);
    {
        float r[8];
#pragma unroll
        for (int i = 0; i < 8; ++i) {
            const float s = (float)sv[i >> 1][2 * (i & 1) + 1] * 4294967296.0f + (float)sv[i >> 1][2 * (i & 1)];
            r[i] = __builtin_amdgcn_rsqf(s * (1.0f / (SS_SCALE * 640.0f)) + EPS);
        }
        const bf16x8 ur = pk8(bflo(vr[0]) * r[0], bfhi(vr[0]) * r[1], bflo(vr[1]) * r[2], bfhi(vr[1]) * r[3],
                              bflo(vr[2]) * r[4], bfhi(vr[2]) * r[5], bflo(vr[3]) * r[6], bfhi(vr[3]) * r[7]);
        bf16x8 ui = pk8(bflo(vi[0]) * r[0], bfhi(vi[0]) * r[1], bflo(vi[1]) * r[2], bfhi(vi[1]) * r[3],
                        bflo(vi[2]) * r[4], bfhi(vi[2]) * r[5], bflo(vi[3]) * r[6], bfhi(vi[3]) * r[7]);
        if (j == 304) ui = asbf(u32x4{0u, 0u, 0u, 0u});
        const int o = img128(tid >> 3, tid & 7);
        *(bf16x8*)(Ure + o) = ur; *(bf16x8*)(Uim + o) = ui;
        *(u32x4*)(Rre + o) = vr; *(u32x4*)(Rim + o) = vi;
    }
    __syncthreads();
    const f32x4 z = {0.f, 0.f, 0.f, 0.f};
    f32x4 dr[2], di[2];
#pragma unroll
    for (int mt = 0; mt < 2; ++mt) {
        dr[mt] = z; di[mt] = z;
#pragma unroll
        for (int ks = 0; ks < 2; ++ks) {
            const int o = img128(16 * mt + n, 4 * ks + q);
            const bf16x8 ar = *(const bf16x8*)(Ure + o), ai = *(const bf16x8*)(Uim + o);
            dr[mt] = mfma16(ar, c64[ks], dr[mt]); dr[mt] = mfma16(ai, s64p[ks], dr[mt]);
            di[mt] = mfma16(ar, s64m[ks], di[mt]); di[mt] = mfma16(ai, c64[ks], di[mt]);
        }
    }
    float tr[8], ti[8];
#pragma unroll
    for (int i = 0; i < 8; ++i) {
        const float x = dr[i >> 2][i & 3], y = di[i >> 2][i & 3];
        tr[i] = x * twr[i] - y * twi[i]; ti[i] = x * twi[i] + y * twr[i];
    }
    bf16x8 Tr = pk8(tr), Ti = pk8(ti);
    s32m[0] = neg8(s32p[0]); s32m[1] = neg8(s32p[1]);
    f32x4 orr[2], oi[2];
#pragma unroll
    for (int t = 0; t < 2; ++t) {
        orr[t] = mfma16(c32[t], Tr, z); orr[t] = mfma16(s32p[t], Ti, orr[t]);
        oi[t] = mfma16(s32m[t], Tr, z); oi[t] = mfma16(c32[t], Ti, oi[t]);
    }
#pragma unroll
    for (int i = 0; i < 8; ++i) {
        const float x = orr[i >> 2][i & 3], y = oi[i >> 2][i & 3];
        const u32 wv = wf[i >> 2][i & 3];
        const float wre = bflo(wv), wim = bfhi(wv);
        tr[i] = x * wre - y * wim; ti[i] = x * wim + y * wre;
    }
    Tr = pk8(tr); Ti = pk8(ti);
#pragma unroll
    for (int nt = 0; nt < 2; ++nt) {
        f32x4 zr = mfma16(Tr, c32[nt], z); zr = mfma16(Ti, s32m[nt], zr);
        f32x4 zi = mfma16(Tr, s32p[nt], z); zi = mfma16(Ti, c32[nt], zi);
        float er[4], ei[4];
#pragma unroll
        for (int r = 0; r < 4; ++r) {
            const float cr = ivr[4 * nt + r], ci = ivi[4 * nt + r];
            er[r] = zr[r] * cr - zi[r] * ci; ei[r] = zr[r] * ci + zi[r] * cr;
        }
        const int o = img128(16 * nt + n, 2 * w + (q >> 1)) + (q & 1) * 8;
        u32x2 pr = {pk2(er[0], er[1]), pk2(er[2], er[3])}, pi = {pk2(ei[0], ei[1]), pk2(ei[2], ei[3])};
        *(u32x2*)(Ere + o) = pr; *(u32x2*)(Eim + o) = pi;
    }
    __syncthreads();
    const rsrc_t rf = mk_rsrc(ws + (L == 0 ? OFF_XF1 : OFF_XF3), XT_BYTES);
    char* O = Ure;
#pragma unroll
    for (int nt = 0; nt < 2; ++nt) {
        f32x4 yr = z, yi = z;
#pragma unroll
        for (int ks = 0; ks < 2; ++ks) {
            const int o = img128(16 * nt + n, 4 * ks + q);
            const bf16x8 br = *(const bf16x8*)(Ere + o), bi = *(const bf16x8*)(Eim + o);
            yr = mfma16(c64[ks], br, yr); yr = mfma16(s64m[ks], bi, yr);
            yi = mfma16(s64p[ks], br, yi); yi = mfma16(c64[ks], bi, yi);
        }
        if (j == 304) yi = z;
        const int p = (16 * nt + n) * 64 + 16 * w + 4 * q;
        u32x2 rr[2], ri[2];
        { const int o = img128(16 * nt + n, 2 * w + (q >> 1)) + (q & 1) * 8; rr[nt] = *(const u32x2*)(Rre + o); ri[nt] = *(const u32x2*)(Rim + o); }
        const float fr[4] = {bflo(rr[nt][0]) + yr[0], bfhi(rr[nt][0]) + yr[1], bflo(rr[nt][1]) + yr[2], bfhi(rr[nt][1]) + yr[3]};
        const float fi[4] = {bflo(ri[nt][0]) + yi[0], bfhi(ri[nt][0]) + yi[1], bflo(ri[nt][1]) + yi[2], bfhi(ri[nt][1]) + yi[3]};
        u32x4 ov = {pk2(fr[0], fi[0]), pk2(fr[1], fi[1]), pk2(fr[2], fi[2]), pk2(fr[3], fi[3])};
        *(u32x4*)(O + p * 4) = ov;
    }
    __syncthreads();
    {
        const int c0 = 4 * b, dst = ((c0 >> 5) * 64 + ((c0 & 31) >> 3) * 16) * 16 + (c0 & 4) * 2;
        const u32* O0 = (const u32*)smem; const u32* O1 = (const u32*)(smem + 24576);
#pragma unroll
        for (int i = 0; i < 4; ++i) {
            const int p = threadIdx.x + 512 * i;
            u32x2 v = {O0[p], O1[p]};
            __builtin_amdgcn_raw_buffer_store_b64(v, rf, (p >> 4) * 20480 + (p & 15) * 16, dst, SC1);
        }
    }
    wait_lgkm();
    wait_vm<0>();
    barrier();
    signal_all(ws, L == 0 ? 0 : 2);
    w1_prefetch<L == 1>(ws, smem, wv, lane, u0);
}

DI f32x2 gelu_tanh2(f32x2 x) {
    const float k1 = -2.302208198f, k2 = -0.1029432397f;
    const f32x2 c1 = {k1, k1}, c2 = {k2, k2}, one = {1.0f, 1.0f};
    const f32x2 t = x * (x * x * c2 + c1);
    f32x2 e = {__builtin_amdgcn_exp2f(t[0]), __builtin_amdgcn_exp2f(t[1])};
    e += one;
    const f32x2 r = {__builtin_amdgcn_rcpf(e[0]), __builtin_amdgcn_rcpf(e[1])};
    return x * r;
}
constexpr int G2_PH = 3;
constexpr int W2STG = 20480;
DI u32 w2slot(int t) { return 163840 - ((t & 7) + 1) * W2STG; }
template <bool LAST> constexpr int w1slot(int st) { return LAST ? 163840 - (st % 8 + 1) * 20480 : 163840 - (st + 1) * 14336; }

template <bool LAST, int I> DI void w1_piece(u32x4 srd, u32 lds0, int w, int vo, int u0, int st) {
    constexpr int WP = LAST ? 20 : 14;
    const int pc = w + 8 * I;
    const u32 dst = lds0 + w1slot<LAST>(st) + pc * 1024;
    const int so = (u0 + (pc >> 1)) * 20480 + st * 2048 + (pc & 1) * 1024;
    if (8 * I + 7 < WP) dma16<LAST>(srd, vo, so, dst); else dma16_lim<(WP & 7), LAST>(srd, vo, so, dst, w);
}
template <bool LAST> DI void w1_prefetch(char* ws, char* smem, int w, int lane, int u0) {
    const u32x4 srd = mk_srd(ws + OFF_W1F, W1F_BYTES);
    const u32 lds0 = (u32)(size_t)(LAS char*)smem;
#pragma unroll
    for (int st = 0; st < (LAST ? 8 : 10); ++st) {
        w1_piece<LAST, 0>(srd, lds0, w, lane * 16, u0, st);
        w1_piece<LAST, 1>(srd, lds0, w, lane * 16, u0, st);
        if (LAST) w1_piece<LAST, 2>(srd, lds0, w, lane * 16, u0, st);
    }
}

constexpr int g1_younger(int ssync, int t, int nlo) {
    int cnt = 0; bool seen = false;
    for (int s = 0; s < ssync; ++s) {
        if (s + G1_PW < 10 && s + G1_PW >= 8) { if (seen) cnt += nlo; if (s + G1_PW == t) seen = true; }
        if (s + G1_P < 10 && seen) cnt += 2;
    }
    return cnt;
}

template <int I> DI void w2_issue1(u32x4 srd, u32 lds0, int w, int vo, int ct, int t) {
    const int pc = w + 8 * I, q = pc / 5, cu = pc - 5 * q;
    const int so = ((5 * ct + cu) * 54 + (q < 2 ? 14 * q : 28 + 13 * (q - 2)) + t) * 1024;
    if (I < 2) dma16<true>(srd, vo, so, lds0 + w2slot(t) + pc * 1024); else dma16_lim<4, true>(srd, vo, so, lds0 + w2slot(t) + pc * 1024, w);
}
DI void w2_issue(u32x4 srd, u32 lds0, int w, int vo, int ct, int t) {
    w2_issue1<0>(srd, lds0, w, vo, ct, t); w2_issue1<1>(srd, lds0, w, vo, ct, t); w2_issue1<2>(srd, lds0, w, vo, ct, t);
}

template <int NF, bool LAST, int KG>
DI void g1_phase(const MainArgs& a, char* smem, int w, int lane, int pt, int u0, int nu, int ct2) {
    constexpr int P = G1_P, PW = G1_PW;
    constexpr int WP = 2 * NF, WR = (WP + 7) / 8, NLO = WP / 8, KID = LAST ? 5 : 2;
    char* ws = a.p.ws;
    const int pq = w & 3, g = lane >> 4;
    const u32x4 srdW = mk_srd(ws + OFF_W1F, W1F_BYTES), srd2 = mk_srd(ws + (LAST ? OFF_W2O : OFF_W2F), W2F_BYTES);
    const u32 lds0 = (u32)(size_t)(LAS char*)smem;
    const rsrc_t rx = mk_rsrc(ws + (LAST ? OFF_XF3 : OFF_XF1), XT_BYTES);
    const int xs = (8 * pt + 2 * pq) * 20480 + KG * 1024, vo = lane * 16;
    auto xload = [&](int st, int pu) __attribute__((always_inline)) { return asbf(__builtin_amdgcn_raw_buffer_load_b128(rx, vo, xs + pu * 20480 + st * 2048, SC1)); };
    bf16x8 xb[P][2];
#pragma unroll
    for (int st = 0; st < P; ++st) { xb[st][0] = xload(st, 0); xb[st][1] = xload(st, 1); __builtin_amdgcn_sched_barrier(0); }
    f32x4 acc[NF][2];
#pragma unroll
    for (int i = 0; i < NF; ++i) { acc[i][0] = f32x4{0, 0, 0, 0}; acc[i][1] = f32x4{0, 0, 0, 0}; }
    f32x4 gr[2] = {f32x4{0, 0, 0, 0}, f32x4{0, 0, 0, 0}};
    bf16x8 wa[2][NF];
#pragma unroll
    for (int i = 0; i < NF; ++i) wa[0][i] = *(const bf16x8*)(smem + w1slot<LAST>(0) + KG * 1024 + lane * 16 + i * 2048);
    static_for<0, 10>([&](auto ST) __attribute__((always_inline)) {
        constexpr int st = decltype(ST)::value;
        if constexpr (LAST && st == 2) { wait_lgkm(); barrier(); }
        if constexpr (st == 4 || st == 7) {
            wait_lgkm();
            constexpr int nx4 = 2 * P + 2 * 4, nx7 = 2 * P + 2 * (10 - P < 7 ? 10 - P : 7);
            if constexpr (st == 4) wait_vm<(LAST ? NLO : 2 * NLO) + nx4>();
            else if constexpr (LAST) wait_vm<g1_younger(7, 9, NLO)>();
            else wait_vm<nx7>();
            barrier();
        }
        bf16x8 nx[2];
        const char* rbase = smem + w1slot<LAST>(st + 1) + KG * 1024 + lane * 16;
        constexpr int NV = WR + 2, V0 = KG == 0 ? 0 : NF - NV;
        static_for<0, NF>([&](auto I) __attribute__((always_inline)) {
            constexpr int i = decltype(I)::value;
            __builtin_amdgcn_sched_barrier(0);
            acc[i][0] = mfma16(wa[st & 1][i], xb[st % P][0], acc[i][0]);
            if constexpr (st + 1 < 10) wa[(st + 1) & 1][i] = *(const bf16x8*)(rbase + i * 2048);
            acc[i][1] = mfma16(wa[st & 1][i], xb[st % P][1], acc[i][1]);
            if constexpr (i < 2) {
                u32x4 xv = __builtin_bit_cast(u32x4, xb[st % P][i]);
                if constexpr (st == 9 && KG == 1) { if (g == 0) xv[0] &= 0x0000ffffu; }
                gr[i] = mfma16(asbf(xv), asbf(xv), gr[i]);
            }
            __builtin_amdgcn_sched_barrier(0);
            constexpr int v = i - V0;
            if constexpr (v >= 0 && v < WR) { if constexpr (LAST && st + PW >= 8 && st + PW < 10) w1_piece<LAST, v>(srdW, lds0, w, vo, u0, st + PW); }
            else if constexpr (v >= WR && v < NV) { if constexpr (st + P < 10) nx[v - WR] = xload(st + P, v - WR); }
            if constexpr (st >= 6) {
                constexpr int sl = i - (KG == 0 ? 0 : NF - 4), e = 4 * (st - 6) + sl;
                if constexpr (sl >= 0 && sl < 4 && e < (LAST ? 9 : 15)) w2_issue1<e % 3>(srd2, lds0, w, vo, ct2, (LAST ? 2 : 0) + e / 3);
            }
        });
        __builtin_amdgcn_sched_barrier(0);
        if constexpr (st + P < 10) { xb[st % P][0] = nx[0]; xb[st % P][1] = nx[1]; }
    });
    u32x2 bv[NF];
#pragma unroll
    for (int i = 0; i < NF; ++i) bv[i] = *(const u32x2*)(a.b1 + min(16 * (u0 + i), F_ - 16) + 4 * g);
    __builtin_amdgcn_sched_barrier(0);
    wait_lgkm();
    barrier();
    float* sx = (float*)(smem + 8 * NF * 512);
#pragma unroll
    for (int i = 0; i < NF; ++i) {
        const f32x4 t = acc[i][1 - KG];
        u32x2 pv = {pk2(t[0], t[1]), pk2(t[2], t[3])};
        *(u32x2*)(smem + ((w * NF + i) * 64 + lane) * 8) = pv;
    }
    float st2[2];
    {
        const int n = lane & 15;
#pragma unroll
        for (int pu = 0; pu < 2; ++pu) {
            const float d01 = (n & 1) ? gr[pu][1] : gr[pu][0], d23 = (n & 1) ? gr[pu][3] : gr[pu][2];
            float d = (n & 2) ? d23 : d01;
            d = (g == (n >> 2)) ? d : 0.f;
            d += __shfl_xor(d, 16); d += __shfl_xor(d, 32);
            st2[pu] = d;
        }
    }
    if (lane < 16) sx[w * 16 + lane] = st2[1 - KG];
    wait_lgkm();
    barrier();
    const float v = (st2[KG] + sx[(w ^ 4) * 16 + (lane & 15)]) * (1.0f / 640.0f);
    const float r1 = __builtin_amdgcn_rsqf(v + EPS);
    const float rr = r1 * __builtin_amdgcn_rsqf(v * r1 * r1 + EPS);
    const int punit = 8 * pt + 2 * pq + KG;
    u32x2 ov[NF], pv[NF];
#pragma unroll
    for (int i = 0; i < NF; ++i) pv[i] = *(const u32x2*)(smem + (((w ^ 4) * NF + i) * 64 + lane) * 8);
    static_for<0, NF>([&](auto I) __attribute__((always_inline)) {
        constexpr int i = decltype(I)::value;
        if constexpr (LAST ? i < 2 : i == 0) {
            __builtin_amdgcn_sched_barrier(0);
            w2_issue(srd2, lds0, w, vo, ct2, LAST ? i : 5);
            __builtin_amdgcn_sched_barrier(0);
        }
        const f32x2 o0 = {acc[i][KG][0] + bflo(pv[i][0]), acc[i][KG][1] + bfhi(pv[i][0])}, o1 = {acc[i][KG][2] + bflo(pv[i][1]), acc[i][KG][3] + bfhi(pv[i][1])};
        const f32x2 rr2 = {rr, rr};
        const f32x2 b0 = {bflo(bv[i][0]), bfhi(bv[i][0])}, b1 = {bflo(bv[i][1]), bfhi(bv[i][1])};
        f32x2 h0 = gelu_tanh2(o0 * rr2 + b0), h1 = gelu_tanh2(o1 * rr2 + b1);
        if (LAST) {
            const bool lin = u0 + i >= 108;
            h0[0] = lin ? o0[0] : h0[0]; h0[1] = lin ? o0[1] : h0[1]; h1[0] = lin ? o1[0] : h1[0]; h1[1] = lin ? o1[1] : h1[1];
        }
        ov[i] = u32x2{pk2(h0[0], h0[1]), pk2(h1[0], h1[1])};
    });
    __builtin_amdgcn_sched_barrier(0);
    const rsrc_t rh = mk_rsrc(ws + (LAST ? OFF_H2 : OFF_H), H_BYTES), r3 = mk_rsrc(ws + OFF_X3, X3_BYTES);
    const int hb = punit * 54 * 1024;
    auto st1 = [&](int i) __attribute__((always_inline)) {
        const int u = u0 + i;
        if (i < nu) {
            if (!LAST || u < 108) __builtin_amdgcn_raw_buffer_store_b64(ov[i], rh, lane * 16 + (u & 1) * 8, hb + (u >> 1) * 1024, SC1);
            else __builtin_amdgcn_raw_buffer_store_b64(ov[i], r3, lane * 8, (punit * 40 + (u - 108)) * 512, SC1);
        }
    };
    auto st2p = [&](int i) __attribute__((always_inline)) {
        const int u = u0 + i;
        if (i + 1 < nu && (!LAST || u + 1 < 108)) {
            u32x4 q4 = {ov[i][0], ov[i][1], ov[i + 1][0], ov[i + 1][1]};
            __builtin_amdgcn_raw_buffer_store_b128(q4, rh, lane * 16, hb + (u >> 1) * 1024, SC1);
        } else { st1(i); st1(i + 1); }
    };
    if (u0 & 1) {
        st1(0);
#pragma unroll
        for (int i = 1; i + 1 < NF; i += 2) st2p(i);
        if ((NF & 1) == 0) st1(NF - 1);
    } else {
#pragma unroll
        for (int i = 0; i + 1 < NF; i += 2) st2p(i);
        if (NF & 1) st1(NF - 1);
    }
}

template <bool LAST>
DI void g2_phase(const MainArgs& a, char* smem, int w, int lane, int pt1, int pt, int ct) {
    constexpr int PH = G2_PH, KID = LAST ? 6 : 3;
    char* ws = a.p.ws;
    u32* cnt = (u32*)(ws + OFF_CNT + (LAST ? 8192 : 0));
    const int pg = w & 1, kq = w >> 1, n = lane & 15, q = lane >> 4;
    const int kb = kq < 2 ? 14 * kq : 28 + 13 * (kq - 2), nk = kq < 2 ? 14 : 13;
    const u32x4 srd2 = mk_srd(ws + (LAST ? OFF_W2O : OFF_W2F), W2F_BYTES);
    const u32 lds0 = (u32)(size_t)(LAS char*)smem;
    const rsrc_t rh = mk_rsrc(ws + (LAST ? OFF_H2 : OFF_H), H_BYTES), rw = mk_rsrc(ws + (LAST ? OFF_W2O : OFF_W2F), W2F_BYTES);
    const int vo = lane * 16, hs = ((4 * pt + 2 * pg) * 54 + kb) * 1024;
    bf16x8 wr[6][5];
    asm volatile("" ::: "memory");
    __builtin_amdgcn_sched_barrier(0);
#pragma unroll
    for (int t = 0; t < 3; ++t)
#pragma unroll
        for (int i = 0; i < 5; ++i) wr[t][i] = asbf(__builtin_amdgcn_raw_buffer_load_b128(rw, vo, ((5 * ct + i) * 54 + kb + 8 + t) * 1024, SC1));
    __builtin_amdgcn_sched_barrier(0);
    wait_lgkm();
    wait_vm<15>();
    barrier();
    if (threadIdx.x == 0) add_agent(cnt + 64 * pt1);
#pragma unroll
    for (int t = LAST ? 5 : 6; t < 8; ++t) w2_issue(srd2, lds0, w, vo, ct, t);
    __builtin_amdgcn_sched_barrier(0);
#pragma unroll
    for (int t = 3; t < 6; ++t)
#pragma unroll
        for (int i = 0; i < 5; ++i) wr[t][i] = asbf(__builtin_amdgcn_raw_buffer_load_b128(rw, vo, ((5 * ct + i) * 54 + kb + 8 + t) * 1024, SC1));
    __builtin_amdgcn_sched_barrier(0);
    if (w == 0) poll_until(cnt + 64 * (pt >> 1), 16u, ws, lane);
    wait_vm<0>();
    barrier();
    bf16x8 hb[PH][2];
    auto hload = [&](int t, int pu) __attribute__((always_inline)) {
        const int k = t < 13 ? t : nk - 1;
        return asbf(__builtin_amdgcn_raw_buffer_load_b128(rh, vo, hs + (pu * 54 + k) * 1024, SC1));
    };
#pragma unroll
    for (int t = 0; t < PH; ++t) { hb[t][0] = hload(t, 0); hb[t][1] = hload(t, 1); __builtin_amdgcn_sched_barrier(0); }
    u32x2 xv[3], bv[3]; float bb[3];
    f32x4 acc[2][5];
#pragma unroll
    for (int i = 0; i < 5; ++i) { acc[0][i] = f32x4{0, 0, 0, 0}; acc[1][i] = f32x4{0, 0, 0, 0}; }
    bf16x8 wa[2][5];
    const char* wbase = smem + kq * 5120 + lane * 16;
#pragma unroll
    for (int i = 0; i < 5; ++i) wa[0][i] = *(const bf16x8*)(wbase + w2slot(0) + i * 1024);
    static_for<0, 14>([&](auto T) __attribute__((always_inline)) {
        constexpr int t = decltype(T)::value;
        if constexpr (t == 10) {
            __builtin_amdgcn_sched_barrier(0);
#pragma unroll
            for (int j = 0; j < 3; ++j) {
                const int tt = min(kq + 4 * j, 9), pu = 2 * pg + tt / 5, cu = tt % 5;
                if (!LAST) {
                    const int c = 80 * ct + 16 * cu + n;
                    const int off = (((4 * pt + pu) * 20 + (c >> 5)) * 64 + ((c & 31) >> 3) * 16 + 4 * q) * 16 + (c & 7) * 2;
                    const rsrc_t r1 = mk_rsrc(ws + OFF_XF1, XT_BYTES);
                    const u32 e0 = __builtin_amdgcn_raw_buffer_load_b16(r1, off, 0, SC1), e1 = __builtin_amdgcn_raw_buffer_load_b16(r1, off + 16, 0, SC1);
                    const u32 e2 = __builtin_amdgcn_raw_buffer_load_b16(r1, off + 32, 0, SC1), e3 = __builtin_amdgcn_raw_buffer_load_b16(r1, off + 48, 0, SC1);
                    xv[j] = u32x2{e0 | (e1 << 16), e2 | (e3 << 16)};
                    bb[j] = ((const float*)(ws + OFF_B2P))[c];
                } else {
                    xv[j] = __builtin_amdgcn_raw_buffer_load_b64(mk_rsrc(ws + OFF_X3, X3_BYTES), lane * 8, ((4 * pt + pu) * 40 + 5 * ct + cu) * 512, SC1);
                    bv[j] = *(const u32x2*)(a.p.b2 + 80 * ct + 16 * cu + 4 * q);
                }
            }
        }
        bf16x8 nh[2];
        auto step = [&]() __attribute__((always_inline)) {
            static_for<0, 5>([&](auto I) __attribute__((always_inline)) {
                constexpr int i = decltype(I)::value;
                __builtin_amdgcn_sched_barrier(0);
                bf16x8 wv;
                if constexpr (t < 8) wv = wa[t & 1][i]; else wv = wr[t - 8][i];
                acc[0][i] = LAST ? mfma16(wv, hb[t % PH][0], acc[0][i]) : mfma16(hb[t % PH][0], wv, acc[0][i]);
                if constexpr (t + 1 < 8) wa[(t + 1) & 1][i] = *(const bf16x8*)(wbase + w2slot(t + 1) + i * 1024);
                acc[1][i] = LAST ? mfma16(wv, hb[t % PH][1], acc[1][i]) : mfma16(hb[t % PH][1], wv, acc[1][i]);
                __builtin_amdgcn_sched_barrier(0);
                if constexpr (t + PH < 14 && (i == 1 || i == 3)) nh[i >> 1] = hload(t + PH, i >> 1);
            });
        };
        if constexpr (t < 13) step(); else { if (nk == 14) step(); }
        __builtin_amdgcn_sched_barrier(0);
        if constexpr (t + PH < 14) { hb[t % PH][0] = nh[0]; hb[t % PH][1] = nh[1]; }
    });
    wait_lgkm();
    barrier();
#pragma unroll
    for (int t = 0; t < 10; ++t)
        if ((t & 3) != kq) *(f32x4*)(smem + ((w * 10 + t) * 64 + lane) * 16) = acc[t / 5][t % 5];
    wait_lgkm();
    barrier();
    float ssp[2][4];
#pragma unroll
    for (int r = 0; r < 4; ++r) { ssp[0][r] = 0.f; ssp[1][r] = 0.f; }
    const rsrc_t ro = mk_rsrc(ws + OFF_XT2, XT_BYTES);
    auto finish = [&](auto KQ) __attribute__((always_inline)) {
        constexpr int kqc = decltype(KQ)::value;
        static_for<0, 3>([&](auto J) __attribute__((always_inline)) {
            constexpr int j = decltype(J)::value, t = kqc + 4 * j;
            if constexpr (t < 10) {
                constexpr int pl = t / 5, cu = t % 5;
                f32x4 o = acc[pl][cu];
#pragma unroll
                for (int s = 1; s < 4; ++s) o += *(const f32x4*)(smem + (((pg + 2 * ((kqc + s) & 3)) * 10 + t) * 64 + lane) * 16);
                const int pu = 2 * pg + pl;
                if (!LAST) {
                    const int c = 80 * ct + 16 * cu + n, p = 64 * pt + 16 * pu + 4 * q;
                    const u32 o0 = pk2(bflo(xv[j][0]) + o[0] + bb[j], bfhi(xv[j][0]) + o[1] + bb[j]);
                    const u32 o1 = pk2(bflo(xv[j][1]) + o[2] + bb[j], bfhi(xv[j][1]) + o[3] + bb[j]);
                    u32x2 ov = {o0, o1};
                    __builtin_amdgcn_raw_buffer_store_b64(ov, ro, (c * S_ + p) * 2, 0, SC1);
                    const float m = c == 609 ? 0.f : 1.f;
                    ssp[pl][0] += m * bflo(o0) * bflo(o0); ssp[pl][1] += m * bfhi(o0) * bfhi(o0);
                    ssp[pl][2] += m * bflo(o1) * bflo(o1); ssp[pl][3] += m * bfhi(o1) * bfhi(o1);
                } else {
                    const int p = 16 * (4 * pt + pu) + n, d = 80 * ct + 16 * cu + 4 * q;
                    u32x2 ov = {pk2(bflo(xv[j][0]) + o[0] + bflo(bv[j][0]), bfhi(xv[j][0]) + o[1] + bfhi(bv[j][0])),
                                pk2(bflo(xv[j][1]) + o[2] + bflo(bv[j][1]), bfhi(xv[j][1]) + o[3] + bfhi(bv[j][1]))};
                    *(u32x2*)(a.out + (size_t)tok_of_p(p) * D_ + d) = ov;
                }
            }
        });
    };
    if (kq == 0) finish(std::integral_constant<int, 0>{});
    else if (kq == 1) finish(std::integral_constant<int, 1>{});
    else if (kq == 2) finish(std::integral_constant<int, 2>{});
    else finish(std::integral_constant<int, 3>{});
    if (!LAST) {
        float* sp = (float*)smem;
#pragma unroll
        for (int pl = 0; pl < 2; ++pl)
#pragma unroll
            for (int r = 0; r < 4; ++r) {
                const float v = row_sum15(ssp[pl][r]);
                if (n == 15) sp[w * 32 + 16 * pl + 4 * q + r] = v;
            }
        wait_lgkm();
        barrier();
        if (w == 0) {
            const int g2 = lane >> 5, r = lane & 31;
            const float v = (sp[g2 * 32 + r] + sp[(g2 + 2) * 32 + r]) + (sp[(g2 + 4) * 32 + r] + sp[(g2 + 6) * 32 + r]);
            __hip_atomic_fetch_add((__attribute__((address_space(1))) u64*)(ws + OFF_SS2) + 64 * pt + lane, ss_from_float(v), __ATOMIC_RELAXED, __HIP_MEMORY_SCOPE_AGENT);
        }
        wait_lgkm();
        wait_vm<0>();
        barrier();
        signal_all(ws, 1);
    }
}

template <bool LAST>
DI void mlp(const MainArgs& a, char* smem, int w, int lane, int u0) {
    constexpr int NF = LAST ? 10 : 7;
    const int x = blockIdx.x & 7, ib = blockIdx.x >> 3;
    const int pt = 4 * (x >> 1) + (ib & 3), ft = 8 * (x & 1) + (ib >> 2);
    const int pt2 = 8 * (x >> 1) + (ib & 7), ct2 = 4 * (x & 1) + (ib >> 3);
    const int nu = LAST ? (ft < 4 ? 10 : 9) : (ft < 12 ? 7 : 6);
    if (LAST) poll_all(a.p.ws, 2, 20u, w, lane);
    else if (w == 0) {
        const int sh = lane & 7, wh = (lane >> 3) & 1;
        poll_until((const u32*)(a.p.ws + OFF_CNT + 16384 + (wh ? 3 : 0) * 8192) + ((blockIdx.x >> 3) & 7) * 256 + sh * 32, wh ? (sh < 6 ? 12u : 11u) : 20u, a.p.ws, lane);
    }
    wait_vm<(LAST ? 6 : 5)>();
    barrier();
    if (w >= 4) __builtin_amdgcn_s_setprio(1);
    if (w < 4) g1_phase<NF, LAST, 0>(a, smem, w, lane, pt, u0, nu, ct2);
    else g1_phase<NF, LAST, 1>(a, smem, w, lane, pt, u0, nu, ct2);
    __builtin_amdgcn_s_setprio(0);
    g2_phase<LAST>(a, smem, w, lane, pt, pt2, ct2);
}

__global__ __launch_bounds__(512, 2) void k_main(MainArgs a) {
    extern __shared__ __attribute__((aligned(16))) char smem[];
    const int lane = threadIdx.x & 63, b = blockIdx.x;
    const int w = __builtin_amdgcn_readfirstlane(threadIdx.x >> 6);
    const int ft = 8 * (b & 1) + (b >> 5);
    const int u0a = ft < 12 ? 7 * ft : 84 + 6 * (ft - 12), u0b = ft < 4 ? 10 * ft : 40 + 9 * (ft - 4);
    if (b < 160) col_task<0>(a.p.ws, b, smem, u0a);
    else {
        if (b < 254) {
            if (b < 214) task_w2(a.p, b - 160, smem); else task_Q(a.p, b - 214);
            wait_lgkm();
            wait_vm<0>();
            barrier();
            signal_all(a.p.ws, 3);
        }
        w1_prefetch<false>(a.p.ws, smem, w, lane, u0a);
    }
    mlp<false>(a, smem, w, lane, u0a);
    if (b < 160) col_task<1>(a.p.ws, b, smem, u0b); else w1_prefetch<true>(a.p.ws, smem, w, lane, u0b);
    mlp<true>(a, smem, w, lane, u0b);
}

extern "C" void kernel_launch(void* const* d_in, const int* in_sizes, int n_in, void* d_out, int out_size, void* d_ws,
                              size_t ws_size, hipStream_t stream) {
    char* ws = (char*)d_ws;
    constexpr int MAIN_LDS = 163840;
    (void)hipFuncSetAttribute((const void*)k_main, hipFuncAttributeMaxDynamicSharedMemorySize, MAIN_LDS);
    PrepArgs pa{(const u16*)d_in[0], (const u16*)d_in[1], (const u16*)d_in[2], (const u16*)d_in[4], (const u16*)d_in[6], (const u16*)d_in[7], ws};
    k0_prep<<<NB_K0, 512, PREP_LDS, stream>>>(pa);
    MainArgs ma{pa, (const u16*)d_in[5], (u16*)d_out};
    k_main<<<256, 512, MAIN_LDS, stream>>>(ma);
}
```

```cpp
#include <hip/hip_runtime.h>
#include <hip/hip_bf16.h>
#include <stdint.h>
#include <type_traits>

typedef unsigned short u16;
typedef unsigned int u32;
typedef unsigned long long u64;
typedef short s16x4 __attribute__((ext_vector_type(4)));
typedef __bf16 bf16x8 __attribute__((ext_vector_type(8)));
typedef __bf16 bf16x2 __attribute__((ext_vector_type(2)));
typedef float f32x4 __attribute__((ext_vector_type(4)));
typedef float f32x2 __attribute__((ext_vector_type(2)));
typedef u32 u32x4 __attribute__((ext_vector_type(4)));
typedef u32 u32x2 __attribute__((ext_vector_type(2)));
typedef __amdgpu_buffer_rsrc_t rsrc_t;
#define LAS __attribute__((address_space(3)))
#define DI __device__ __forceinline__

constexpr int S_ = 2048, D_ = 640, F_ = 1728, DF_ = 321;
constexpr float EPS = 1e-5f;
constexpr int SC1 = 16;
constexpr float SS_SCALE = 1048576.0f;

constexpr size_t XT_BYTES = (size_t)D_ * S_ * 2;
constexpr size_t OFF_XT0 = 0;
constexpr size_t OFF_XT1 = OFF_XT0 + XT_BYTES;
constexpr size_t OFF_XT2 = OFF_XT1 + XT_BYTES;
constexpr size_t OFF_XT3 = OFF_XT2 + XT_BYTES;
constexpr size_t OFF_WT = OFF_XT3 + XT_BYTES;
constexpr size_t OFF_W1F = OFF_WT + (size_t)320 * 2048 * 4;
constexpr size_t W1F_BYTES = (size_t)148 * 20 * 1024;
constexpr size_t OFF_W2F = OFF_W1F + W1F_BYTES;
constexpr size_t W2F_BYTES = (size_t)40 * 54 * 1024;
constexpr size_t OFF_W2O = OFF_W2F + W2F_BYTES;
constexpr size_t OFF_H = OFF_W2O + W2F_BYTES;
constexpr size_t H_BYTES = (size_t)128 * 54 * 1024;
constexpr size_t OFF_H2 = OFF_H;
constexpr size_t OFF_X3 = OFF_H + 2 * H_BYTES;
constexpr size_t OFF_SS0 = OFF_X3 + (size_t)128 * 40 * 512;
constexpr size_t OFF_SS2 = OFF_SS0 + 2048 * 8;
constexpr size_t OFF_B2P = OFF_SS2 + 2048 * 8;
constexpr size_t OFF_XF1 = OFF_B2P + 4096;
constexpr size_t OFF_XF3 = OFF_XF1;
constexpr size_t X3_BYTES = (size_t)128 * 40 * 512;
constexpr size_t OFF_CNT = OFF_XF1 + 2 * XT_BYTES;
constexpr size_t WS_END = OFF_CNT + 65536;

#define STAMP(k, i)
#define CSTAMP(k, i)
#define END_STAMP(k)

DI f32x4 mfma16(bf16x8 a, bf16x8 b, f32x4 c) { return __builtin_amdgcn_mfma_f32_16x16x32_bf16(a, b, c, 0, 0, 0); }
DI u32 pk2(float a, float b) { f32x2 v = {a, b}; const bf16x2 r = __builtin_convertvector(v, bf16x2); return __builtin_bit_cast(u32, r); }
DI bf16x8 pk8(float a0, float a1, float a2, float a3, float a4, float a5, float a6, float a7) {
    u32x4 r = {pk2(a0, a1), pk2(a2, a3), pk2(a4, a5), pk2(a6, a7)};
    return __builtin_bit_cast(bf16x8, r);
}
DI bf16x8 pk8(const float (&a)[8]) { return pk8(a[0], a[1], a[2], a[3], a[4], a[5], a[6], a[7]); }
DI bf16x8 asbf(u32x4 r) { return __builtin_bit_cast(bf16x8, r); }
DI bf16x8 neg8(bf16x8 a) { u32x4 r = __builtin_bit_cast(u32x4, a); r ^= 0x80008000u; return __builtin_bit_cast(bf16x8, r); }
DI float bflo(u32 v) { return __builtin_bit_cast(float, v << 16); }
DI float bfhi(u32 v) { return __builtin_bit_cast(float, v & 0xffff0000u); }
DI float cosr(float rev) { return __builtin_amdgcn_cosf(rev); }
DI float sinr(float rev) { return __builtin_amdgcn_sinf(rev); }
DI int slot32(int q, int j) { return j < 4 ? 4 * q + j : 16 + 4 * q + (j - 4); }
DI s16x4 tr_read(const char* p) { return __builtin_amdgcn_ds_read_tr16_b64_v4i16((LAS s16x4*)p); }
DI bf16x8 cat8(s16x4 lo, s16x4 hi) { return __builtin_bit_cast(bf16x8, __builtin_shufflevector(lo, hi, 0, 1, 2, 3, 4, 5, 6, 7)); }
DI int tok_of_p(int p) { return 32 * (p & 63) + (p >> 6); }
DI rsrc_t mk_rsrc(const void* p, size_t bytes) { return __builtin_amdgcn_make_buffer_rsrc((void*)p, 0, (int)bytes, 0x00020000); }
DI u32x4 bload16(rsrc_t r, int voff, int soff) { return __builtin_amdgcn_raw_buffer_load_b128(r, voff, soff, 0); }
DI float ss_to_float(u64 v) { return (float)(u32)(v >> 32) * 4294967296.0f + (float)(u32)v; }
DI u64 ss_from_float(float s) { return (u64)(fminf(s * SS_SCALE, 1.0e18f) + 0.5f); }
DI void pin(float& x) { asm volatile("" : "+v"(x)); }
DI void pin(bf16x8& x) { asm volatile("" : "+v"(x)); }

struct F32c { bf16x8 c[2], sp[2]; };
DI void f32c_init(F32c& k, int lane) {
    const float m = (float)(lane & 15), q4 = (float)(4 * (lane >> 4));
    float c[4], s[4];
#pragma unroll
    for (int j = 0; j < 4; ++j) { const float a = __builtin_amdgcn_fractf(m * (q4 + (float)j) * (1.0f / 32.0f)); c[j] = cosr(a); s[j] = sinr(a); }
    const u32 sg = (lane & 1) ? 0x80008000u : 0u;
    const u32 c0 = pk2(c[0], c[1]), c1 = pk2(c[2], c[3]), s0 = pk2(s[0], s[1]), s1 = pk2(s[2], s[3]);
    const u32x4 cv = {c0, c1, c0 ^ sg, c1 ^ sg}, sv = {s0, s1, s0 ^ sg, s1 ^ sg};
    k.c[0] = asbf(cv); k.sp[0] = asbf(sv);
    k.c[1] = asbf(cv ^ 0x80000000u); k.sp[1] = asbf(sv ^ 0x80000000u);
}

struct RowC { bf16x8 c[2], sp[2], sm[2], c20, s20m; f32x4 twr[2], twi[2]; int off0, off1; };
constexpr int ROWC_LDS = 10 * 1024;
DI void rowc_build(char* tab, int w, int lane) {
    const int n = lane & 15, q = lane >> 4;
    const float nf = (float)n;
    u32x4* t = (u32x4*)tab + lane;
    if (w == 0) {
        F32c f; f32c_init(f, lane);
        t[0] = __builtin_bit_cast(u32x4, f.c[0]); t[64] = __builtin_bit_cast(u32x4, f.c[1]);
        t[128] = __builtin_bit_cast(u32x4, f.sp[0]); t[192] = __builtin_bit_cast(u32x4, f.sp[1]);
    } else if (w <= 2) {
        float c[8];
#pragma unroll
        for (int j = 0; j < 8; ++j) {
            const int d1 = 8 * q + j;
            const float a = __builtin_amdgcn_fractf((float)d1 * nf * 0.05f);
            const float v = w == 1 ? cosr(a) : -sinr(a);
            c[j] = (d1 < 20 && n <= 10) ? v : 0.f;
        }
        t[(3 + w) * 64] = __builtin_bit_cast(u32x4, pk8(c));
    } else if (w <= 4) {
        const float sc = 0.05590169943749474f;
        f32x4 cr, ci;
#pragma unroll
        for (int i = 0; i < 4; ++i) {
            const float a = (float)(16 * (w - 3) + 4 * q + i) * nf * (1.0f / 640.0f);
            cr[i] = cosr(a) * sc; ci[i] = -sinr(a) * sc;
        }
        t[(6 + w - 3) * 64] = __builtin_bit_cast(u32x4, cr); t[(8 + w - 3) * 64] = __builtin_bit_cast(u32x4, ci);
    }
}
DI void rowc_load(RowC& k, const char* tab, int lane) {
    const u32x4* tb = (const u32x4*)tab + lane;
    k.c[0] = asbf(tb[0]); k.c[1] = asbf(tb[64]); k.sp[0] = asbf(tb[128]); k.sp[1] = asbf(tb[192]);
    k.c20 = asbf(tb[256]); k.s20m = asbf(tb[320]);
    k.twr[0] = __builtin_bit_cast(f32x4, tb[384]); k.twr[1] = __builtin_bit_cast(f32x4, tb[448]);
    k.twi[0] = __builtin_bit_cast(f32x4, tb[512]); k.twi[1] = __builtin_bit_cast(f32x4, tb[576]);
    k.sm[0] = neg8(k.sp[0]); k.sm[1] = neg8(k.sp[1]);
    const int q = lane >> 4, qq = (lane & 15) >> 2, pp = lane & 3;
    const int r0 = min(8 * q + qq, 19), r1 = min(8 * q + 4 + qq, 19);
    k.off0 = r0 * 64 + pp * 8; k.off1 = r1 * 64 + pp * 8;
}
DI float wave_sum63(float v) {
    v += __builtin_bit_cast(float, __builtin_amdgcn_update_dpp(0, __builtin_bit_cast(int, v), 0x111, 0xf, 0xf, true));
    v += __builtin_bit_cast(float, __builtin_amdgcn_update_dpp(0, __builtin_bit_cast(int, v), 0x112, 0xf, 0xf, true));
    v += __builtin_bit_cast(float, __builtin_amdgcn_update_dpp(0, __builtin_bit_cast(int, v), 0x114, 0xf, 0xf, true));
    v += __builtin_bit_cast(float, __builtin_amdgcn_update_dpp(0, __builtin_bit_cast(int, v), 0x118, 0xf, 0xf, true));
    v += __builtin_bit_cast(float, __builtin_amdgcn_update_dpp(0, __builtin_bit_cast(int, v), 0x142, 0xa, 0xf, true));
    v += __builtin_bit_cast(float, __builtin_amdgcn_update_dpp(0, __builtin_bit_cast(int, v), 0x143, 0xc, 0xf, true));
    return v;
}
DI void rowfft(const RowC& k, const char* row, float (&A)[8], float (&B)[8], int lane) {
    f32x4 z = {0.f, 0.f, 0.f, 0.f};
    f32x4 yr[2], yi[2];
#pragma unroll
    for (int mt = 0; mt < 2; ++mt) {
        const bf16x8 a = cat8(tr_read(row + k.off0 + mt * 32), tr_read(row + k.off1 + mt * 32));
        yr[mt] = mfma16(a, k.c20, z); yi[mt] = mfma16(a, k.s20m, z);
    }
    float tr[8], ti[8];
#pragma unroll
    for (int i = 0; i < 8; ++i) {
        const float a = yr[i >> 2][i & 3], b = yi[i >> 2][i & 3], cr = k.twr[i >> 2][i & 3], ci = k.twi[i >> 2][i & 3];
        tr[i] = a * cr - b * ci; ti[i] = a * ci + b * cr;
    }
    const bf16x8 Tr = pk8(tr), Ti = pk8(ti);
    f32x4 ore[2], oim[2];
#pragma unroll
    for (int t = 0; t < 2; ++t) {
        ore[t] = mfma16(k.c[t], Tr, z); ore[t] = mfma16(k.sp[t], Ti, ore[t]);
        oim[t] = mfma16(k.sm[t], Tr, z); oim[t] = mfma16(k.c[t], Ti, oim[t]);
    }
#pragma unroll
    for (int r = 0; r < 4; ++r) { A[2 * r] = ore[0][r]; A[2 * r + 1] = oim[0][r]; B[2 * r] = ore[1][r]; B[2 * r + 1] = oim[1][r]; }
    if (lane == 0) { const float h = 0.7071067811865476f; const float dc = A[0] * h; A[0] = B[0] * h; A[1] = dc; }
}
DI int sw_off(int c) { const int ch = c >> 3; return ((ch ^ ((ch >> 3) & 7)) << 4) + (c & 7) * 2; }
DI int chunk_c0(int n, int q) { return (n == 0 ? 608 : n == 10 ? 576 : 64 * (n - 1)) + 8 * q; }

constexpr int ROWST = 1344;
constexpr int PREP_TAB = 640 * 32 + 16 * ROWST;
constexpr int PREP_LDS = PREP_TAB + ROWC_LDS;

struct PrepArgs { const u16 *x, *wr, *wi, *w1, *w2, *b2; char* ws; };

DI void task_x(const PrepArgs& a, int b, char* smem) {
    const int tid = threadIdx.x, lane = tid & 63, w = tid >> 6;
    const int p0 = 16 * ((b & 7) * 16 + (b >> 3));
    u32x4 v[3];
#pragma unroll
    for (int i = 0; i < 3; ++i) {
        const int id = min(tid + 512 * i, 1279), r = id / 80, ch = id % 80;
        v[i] = *(const u32x4*)(a.x + (size_t)tok_of_p(p0 + r) * D_ + ch * 8);
    }
    __builtin_amdgcn_sched_barrier(0);
    rowc_build(smem + PREP_TAB, w, lane);
    __builtin_amdgcn_sched_barrier(0);
    STAMP(0, 1);
    if (tid < 16) ((u64*)(a.ws + OFF_SS2))[p0 + tid] = 0ull;
    if (b < 32) ((u32*)(a.ws + OFF_CNT))[b * 512 + tid] = 0u;
#pragma unroll
    for (int i = 0; i < 3; ++i) {
        const int id = min(tid + 512 * i, 1279), r = id / 80, ch = id % 80;
        *(u32x4*)(smem + r * ROWST + ch * 16) = v[i];
    }
    STAMP(0, 2);
    __syncthreads();
    STAMP(0, 3);
    RowC k; rowc_load(k, smem + PREP_TAB, lane);
    const int n = lane & 15, q = lane >> 4, c0 = chunk_c0(n, q);
    float A[2][8], B[2][8];
#pragma unroll
    for (int i = 0; i < 2; ++i) rowfft(k, smem + (2 * w + i) * ROWST, A[i], B[i], lane);
#pragma unroll
    for (int i = 0; i < 2; ++i) {
        const int r = 2 * w + i;
        float ss = 0.f;
        if (n <= 10) {
#pragma unroll
            for (int e = 0; e < 8; ++e) ss += (lane == 0 && e == 1) ? 0.f : A[i][e] * A[i][e];
            *(bf16x8*)(smem + r * ROWST + sw_off(c0)) = pk8(A[i]);
        }
        if (n >= 1 && n <= 9) {
#pragma unroll
            for (int e = 0; e < 8; ++e) ss += B[i][e] * B[i][e];
            *(bf16x8*)(smem + r * ROWST + sw_off(c0 + 32)) = pk8(B[i]);
        }
        ss = wave_sum63(ss);
        if (lane == 63) ((u64*)(a.ws + OFF_SS0))[p0 + r] = ss_from_float(ss);
    }
    STAMP(0, 4);
    __syncthreads();
    STAMP(0, 5);
    u16* xt = (u16*)(a.ws + OFF_XT0);
    const int g = lane >> 4, i16 = lane & 15, qq = i16 >> 2, pp = i16 & 3;
#pragma unroll
    for (int i = 0; i < 3; ++i) {
        const int cbk = w + 8 * i;
        if (cbk < 20) {
            const int cb = 32 * cbk + 16 * (g & 1), ph = g >> 1;
            const char* base = smem + (8 * ph + qq) * ROWST + sw_off(cb + 4 * pp);
            const bf16x8 o = cat8(tr_read(base), tr_read(base + 4 * ROWST));
            *(bf16x8*)(xt + (size_t)(cb + i16) * S_ + p0 + 8 * ph) = o;
        }
    }
}

DI void task_W(const PrepArgs& a, int b) {
    const int lane = threadIdx.x & 63, wv = threadIdx.x >> 6, wt = b * 2 + (wv >> 2), kq = wv & 3;
    const int k1 = wt / 5, cg = wt % 5, kp = 1 + 64 * cg + lane;
    const int r = kp % 20, t = kp / 20;
    int j; bool mir = false;
    if (kp == 320) j = 304;
    else if (r == 0) j = 304 + t;
    else if (r < 10) j = 32 * (r - 1) + t;
    else if (r == 10) j = 288 + t;
    else { j = 32 * (19 - r) + 31 - t; mir = true; }
    const rsrc_t rr = mk_rsrc(a.wr, (size_t)S_ * DF_ * 2), ri = mk_rsrc(a.wi, (size_t)S_ * DF_ * 2);
    const int st = (mir ? -64 : 64) * DF_ * 2, off0 = ((mir ? 2048 - k1 : k1) * DF_ + kp) * 2 + 8 * kq * st;
    u32 re[8], im[8];
#pragma unroll
    for (int k2 = 0; k2 < 8; ++k2) {
        int off = off0 + k2 * st;
        if (k2 == 0 && mir && k1 == 0 && kq == 0) off = kp * 2;
        re[k2] = __builtin_amdgcn_raw_buffer_load_b16(rr, off, 0, 0); im[k2] = __builtin_amdgcn_raw_buffer_load_b16(ri, off, 0, 0);
    }
    const u32 sg = mir ? 0x80000000u : 0u;
    u32x4* dst = (u32x4*)(a.ws + OFF_WT + ((size_t)j * 64 + k1) * 128 + kq * 32);
#pragma unroll
    for (int i = 0; i < 2; ++i) {
        u32x4 o;
#pragma unroll
        for (int e = 0; e < 4; ++e) o[e] = (re[4 * i + e] | (im[4 * i + e] << 16)) ^ sg;
        dst[i] = o;
    }
}

DI void task_w1(const PrepArgs& a, int b, char* smem) {
    const int tid = threadIdx.x, lane = tid & 63, w = tid >> 6;
    char* raw = smem;
    char* tr = smem + 640 * 32;
    char* out = raw;
    u32x4 v[3];
#pragma unroll
    for (int i = 0; i < 3; ++i) {
        const int id = min(tid + 512 * i, 1279), d = id >> 1, h = id & 1;
        v[i] = *(const u32x4*)(a.w1 + (size_t)d * F_ + 16 * b + 8 * h);
    }
    __builtin_amdgcn_sched_barrier(0);
    rowc_build(smem + PREP_TAB, w, lane);
    __builtin_amdgcn_sched_barrier(0);
#pragma unroll
    for (int i = 0; i < 3; ++i) *(u32x4*)(raw + min(tid + 512 * i, 1279) * 16) = v[i];
    __syncthreads();
    const int g = lane >> 4, i16 = lane & 15, qq = i16 >> 2, pp = i16 & 3;
#pragma unroll
    for (int i = 0; i < 5; ++i) {
        const int d0 = 16 * (w + 8 * i) + 4 * g;
        const s16x4 t = tr_read(raw + (d0 + qq) * 32 + pp * 8);
        *(s16x4*)(tr + i16 * ROWST + d0 * 2) = t;
    }
    RowC k; rowc_load(k, smem + PREP_TAB, lane);
    __syncthreads();
    const int n = lane & 15, q = lane >> 4, c0 = chunk_c0(n, q);
    float A[2][8], B[2][8];
#pragma unroll
    for (int i = 0; i < 2; ++i) rowfft(k, tr + (2 * w + i) * ROWST, A[i], B[i], lane);
#pragma unroll
    for (int i = 0; i < 2; ++i) {
        const int f = 2 * w + i;
        if (lane == 0) A[i][1] = 0.f;
        if (n <= 10) *(bf16x8*)(out + ((c0 >> 5) * 64 + q * 16 + f) * 16) = pk8(A[i]);
        if (n >= 1 && n <= 9) *(bf16x8*)(out + (((c0 >> 5) + 1) * 64 + q * 16 + f) * 16) = pk8(B[i]);
    }
    __syncthreads();
    u32x4* dst = (u32x4*)(a.ws + OFF_W1F + (size_t)b * 20 * 1024);
#pragma unroll
    for (int i = 0; i < 3; ++i) { const int id = tid + 512 * i; if (id < 1280) dst[id] = *(const u32x4*)(out + id * 16); }
}

template <bool SW> DI void w2_frags(const char* img, char* dstbase, int b, int lane, int w) {
    const int g = lane >> 4, i16 = lane & 15, qq = i16 >> 2, pp = i16 & 3;
    const rsrc_t rd = mk_rsrc(dstbase, W2F_BYTES);
#pragma unroll
    for (int i = 0; i < 5; ++i) {
        const int cu = w + 8 * i, c = 16 * cu + 4 * pp;
        const char* base = img + (4 * g + qq) * ROWST + (SW ? sw_off(c) : c * 2);
        const bf16x8 v = cat8(tr_read(base), tr_read(base + 16 * ROWST));
        __builtin_amdgcn_raw_buffer_store_b128(__builtin_bit_cast(u32x4, v), rd, lane * 16, (cu * 54 + b) * 1024, SC1);
    }
}
constexpr int W2_TAB = 32 * ROWST;
DI void task_w2(const PrepArgs& a, int b, char* smem) {
    const int tid = threadIdx.x, lane = tid & 63, w = tid >> 6;
    u32x4 v[5];
#pragma unroll
    for (int i = 0; i < 5; ++i) {
        const int id = tid + 512 * i, r = id / 80, ch = id % 80;
        v[i] = *(const u32x4*)(a.w2 + (size_t)(32 * b + r) * D_ + ch * 8);
    }
    __builtin_amdgcn_sched_barrier(0);
    rowc_build(smem + W2_TAB, w, lane);
    __builtin_amdgcn_sched_barrier(0);
#pragma unroll
    for (int i = 0; i < 5; ++i) { const int id = tid + 512 * i, r = id / 80, ch = id % 80; *(u32x4*)(smem + r * ROWST + ch * 16) = v[i]; }
    __syncthreads();
    RowC k; rowc_load(k, smem + W2_TAB, lane);
    w2_frags<false>(smem, a.ws + OFF_W2O, b, lane, w);
    const int n = lane & 15, q = lane >> 4, c0 = chunk_c0(n, q);
    u32x4 A8[4], B8[4];
#pragma unroll
    for (int i = 0; i < 4; ++i) {
        float A[8], B[8];
        rowfft(k, smem + (4 * w + i) * ROWST, A, B, lane);
        A8[i] = __builtin_bit_cast(u32x4, pk8(A)); B8[i] = __builtin_bit_cast(u32x4, pk8(B));
    }
    __syncthreads();
#pragma unroll
    for (int i = 0; i < 4; ++i) {
        const int r = 4 * w + i;
        if (n <= 10) *(u32x4*)(smem + r * ROWST + sw_off(c0)) = A8[i];
        if (n >= 1 && n <= 9) *(u32x4*)(smem + r * ROWST + sw_off(c0 + 32)) = B8[i];
    }
    __syncthreads();
    w2_frags<true>(smem, a.ws + OFF_W2F, b, lane, w);
}

DI int kcol_of(int jc) { return jc < 288 ? (jc >> 5) + 1 + 20 * (jc & 31) : jc < 304 ? 10 + 20 * (jc - 288) : 20 * (jc - 304); }
DI void task_Q(const PrepArgs& a, int u) {
    const int tid = threadIdx.x;
#pragma unroll
    for (int ii = 0; ii < 3; ++ii) {
        const int i = tid + 512 * ii;
        if (i < 1280) {
            const int ks = i >> 6, l = i & 63, d = 16 * u + (l & 15), q = l >> 4;
            float v[8];
#pragma unroll
            for (int j = 0; j < 8; j += 2) {
                const int jc = 16 * ks + 4 * q + (j >> 1), kk = kcol_of(jc);
                const float ang = (float)((kk * d) % 640) * (1.0f / 640.0f);
                float c = cosr(ang) * 0.05590169943749474f, s = -sinr(ang) * 0.05590169943749474f;
                if (jc == 304) { c = ((d & 1) ? -1.f : 1.f) * 0.03952847075210474f; s = 0.03952847075210474f; }
                v[j] = c; v[j + 1] = s;
            }
            __builtin_amdgcn_raw_buffer_store_b128(__builtin_bit_cast(u32x4, pk8(v)), mk_rsrc(a.ws + OFF_W1F, W1F_BYTES), i * 16, (108 + u) * 20480, SC1);
        }
    }
}
DI void task_b(const PrepArgs& a, char* smem) {
    const int tid = threadIdx.x, lane = tid & 63;
    if (tid < 80) *(u32x4*)(smem + tid * 16) = *(const u32x4*)(a.b2 + tid * 8);
    rowc_build(smem + PREP_TAB, tid >> 6, lane);
    __syncthreads();
    RowC k; rowc_load(k, smem + PREP_TAB, lane);
    float A[8], B[8];
    rowfft(k, smem, A, B, lane);
    if (tid < 64) {
        const int n = lane & 15, q = lane >> 4, c0 = chunk_c0(n, q);
        float* o = (float*)(a.ws + OFF_B2P);
        if (n <= 10) for (int e = 0; e < 8; ++e) o[c0 + e] = A[e];
        if (n >= 1 && n <= 9) for (int e = 0; e < 8; ++e) o[c0 + 32 + e] = B[e];
    }
}

constexpr int NB_K0 = 128 + 108 + 160 + 1;
constexpr int MAIN_CODE_PREFETCH = 61440;
DI void prefetch_main_code(int slice, int lane) {
    u32 lo, hi;
    asm volatile("s_getpc_b64 s[40:41]\n\ts_add_u32 s40, s40, _Z6k_main8MainArgs@rel32@lo+4\n\ts_addc_u32 s41, s41, _Z6k_main8MainArgs@rel32@hi+12\n\t"
                 "s_mov_b32 %0, s40\n\ts_mov_b32 %1, s41" : "=s"(lo), "=s"(hi) :: "s40", "s41", "scc");
    const u64 base = ((u64)hi << 32) | lo;
    const char* p = (const char*)base + slice * 4096 + lane * 16;
    u32x4 t0, t1, t2, t3;
    asm volatile("global_load_dwordx4 %0, %4, off\n\tglobal_load_dwordx4 %1, %4, off offset:1024\n\tglobal_load_dwordx4 %2, %4, off offset:2048\n\t"
                 "global_load_dwordx4 %3, %4, off offset:3072\n\ts_waitcnt vmcnt(0)"
                 : "=&v"(t0), "=&v"(t1), "=&v"(t2), "=&v"(t3) : "v"(p) : "memory");
}
__global__ __launch_bounds__(512, 4) void k0_prep(PrepArgs a) {
    extern __shared__ __attribute__((aligned(16))) char smem[];
    const int b = blockIdx.x;
    STAMP(0, 0);
    if (b >= 236 && b < 236 + 8 * (MAIN_CODE_PREFETCH / 4096) && (threadIdx.x >> 6) == 7) prefetch_main_code((b - 236) >> 3, threadIdx.x & 63);
    if (b < 128) task_x(a, b, smem);
    else if (b < 236) task_w1(a, b - 128, smem);
    else if (b < 396) task_W(a, b - 236);
    else task_b(a, smem);
    END_STAMP(0);
}

template <int N> DI void wait_vm() { asm volatile("s_waitcnt vmcnt(%0)" ::"n"(N) : "memory"); }
DI void wait_lgkm() { asm volatile("s_waitcnt lgkmcnt(0)" ::: "memory"); }
DI void barrier() { __builtin_amdgcn_s_barrier(); }
template <bool SC = false> DI void dma16(u32x4 srd, int voff, int soff, u32 ldsaddr) {
    if (SC) asm volatile("s_mov_b32 m0, %0\n\ts_nop 0\n\tbuffer_load_dwordx4 %1, %2, %3 offen sc1 lds" ::"s"(ldsaddr), "v"(voff), "s"(srd), "s"(soff) : "memory", "m0");
    else asm volatile("s_mov_b32 m0, %0\n\ts_nop 0\n\tbuffer_load_dwordx4 %1, %2, %3 offen lds" ::"s"(ldsaddr), "v"(voff), "s"(srd), "s"(soff) : "memory", "m0");
}
template <int LIM, bool SC = false> DI void dma16_lim(u32x4 srd, int voff, int soff, u32 ldsaddr, int w) {
    if (SC) asm volatile("s_cmp_lt_i32 %4, %5\n\ts_cbranch_scc0 .Lskip%=\n\ts_mov_b32 m0, %0\n\ts_nop 0\n\tbuffer_load_dwordx4 %1, %2, %3 offen sc1 lds\n.Lskip%=:"
                 ::"s"(ldsaddr), "v"(voff), "s"(srd), "s"(soff), "s"(w), "n"(LIM) : "memory", "m0", "scc");
    else asm volatile("s_cmp_lt_i32 %4, %5\n\ts_cbranch_scc0 .Lskip%=\n\ts_mov_b32 m0, %0\n\ts_nop 0\n\tbuffer_load_dwordx4 %1, %2, %3 offen lds\n.Lskip%=:"
                 ::"s"(ldsaddr), "v"(voff), "s"(srd), "s"(soff), "s"(w), "n"(LIM) : "memory", "m0", "scc");
}
DI u32x4 mk_srd(const void* p, size_t bytes) {
    const u64 a = (u64)p;
    u32x4 r = {(u32)a, (u32)(a >> 32) & 0xffffu, (u32)bytes, 0x00020000u};
    return r;
}
DI float row_sum15(float v) {
    v += __builtin_bit_cast(float, __builtin_amdgcn_update_dpp(0, __builtin_bit_cast(int, v), 0x111, 0xf, 0xf, true));
    v += __builtin_bit_cast(float, __builtin_amdgcn_update_dpp(0, __builtin_bit_cast(int, v), 0x112, 0xf, 0xf, true));
    v += __builtin_bit_cast(float, __builtin_amdgcn_update_dpp(0, __builtin_bit_cast(int, v), 0x114, 0xf, 0xf, true));
    v += __builtin_bit_cast(float, __builtin_amdgcn_update_dpp(0, __builtin_bit_cast(int, v), 0x118, 0xf, 0xf, true));
    return v;
}
template <int I, int N, class F> DI void static_for(F&& f) {
    if constexpr (I < N) { f(std::integral_constant<int, I>{}); static_for<I + 1, N>(f); }
}

#define G1_P_ 3
constexpr int G1_P = G1_P_, G1_PW = 5;
typedef __attribute__((address_space(1))) u32 gu32;
DI u32 ld_agent(const u32* p) { return __hip_atomic_load((gu32*)p, __ATOMIC_RELAXED, __HIP_MEMORY_SCOPE_AGENT); }
DI void add_agent(u32* p) { __hip_atomic_fetch_add((gu32*)p, 1u, __ATOMIC_RELAXED, __HIP_MEMORY_SCOPE_AGENT); }
DI void signal_all(char* ws, int which) {
    if (threadIdx.x == 0) {
        u32* c = (u32*)(ws + OFF_CNT + 16384 + which * 8192) + (blockIdx.x & 7) * 32;
#pragma unroll
        for (int r = 0; r < 8; ++r) add_agent(c + r * 256);
    }
}
#define POLL_SLEEP 16
DI void poll_until(const u32* p, u32 want, char* ws, int lane) {
    for (u32 spins = 0;; ++spins) {
        if (__all(ld_agent(p) >= want)) break;
        __builtin_amdgcn_s_sleep(POLL_SLEEP);
        if (spins > (1u << 18)) { if (lane == 0) __hip_atomic_store((gu32*)(ws + OFF_CNT + 49152), 1u, __ATOMIC_RELAXED, __HIP_MEMORY_SCOPE_AGENT); break; }
    }
}
DI void poll_all(char* ws, int which, u32 per_shard, int w, int lane) {
    if (w == 0) poll_until((const u32*)(ws + OFF_CNT + 16384 + which * 8192) + ((blockIdx.x >> 3) & 7) * 256 + (lane & 7) * 32, per_shard, ws, lane);
}

struct MainArgs { PrepArgs p; const u16* b1; u16* out; };
DI int img128(int row, int ch) { return row * 128 + ((ch ^ ((row >> 1) & 7)) << 4); }
template <bool LAST> DI void w1_prefetch(char* ws, char* smem, int w, int lane, int u0);

template <int L>
DI void col_task(char* ws, int b, char* smem, int u0) {
    const int tid = threadIdx.x & 255, lane = tid & 63, w = tid >> 6, half = threadIdx.x >> 8;
    const int j = 2 * b + half;
    char* Ure = smem + half * 24576; char* Uim = Ure + 4096; char* Ere = Ure + 8192; char* Eim = Ure + 12288;
    char* Rre = Ure + 16384; char* Rim = Ure + 20480;
    const int n = lane & 15, q = lane >> 4;
    const int wv = __builtin_amdgcn_readfirstlane(threadIdx.x >> 6);
    const rsrc_t rx = mk_rsrc(ws + (L == 0 ? OFF_XT0 : OFF_XT2), XT_BYTES), rs = mk_rsrc(ws + (L == 0 ? OFF_SS0 : OFF_SS2), 2048 * 8);
    u32x4 vr, vi, sv[4];
    auto load_in = [&]() __attribute__((always_inline)) {
        vr = __builtin_amdgcn_raw_buffer_load_b128(rx, (2 * j * S_ + tid * 8) * 2, 0, L == 0 ? 0 : SC1);
        vi = __builtin_amdgcn_raw_buffer_load_b128(rx, ((2 * j + 1) * S_ + tid * 8) * 2, 0, L == 0 ? 0 : SC1);
#pragma unroll
        for (int i = 0; i < 4; ++i) sv[i] = __builtin_amdgcn_raw_buffer_load_b128(rs, tid * 64 + 16 * i, 0, L == 0 ? 0 : SC1);
    };
    if (L == 0) load_in();
    u32x4 wf[2];
#pragma unroll
    for (int t = 0; t < 2; ++t) wf[t] = *(const u32x4*)((const u32*)(ws + OFF_WT) + ((size_t)j * 64 + 16 * w + n) * 32 + 16 * t + 4 * q);
    __builtin_amdgcn_sched_barrier(0);
    F32c f; f32c_init(f, lane);
    bf16x8 c32[2] = {f.c[0], f.c[1]}, s32p[2] = {f.sp[0], f.sp[1]}, s32m[2];
    pin(c32[0]); pin(c32[1]); pin(s32p[0]); pin(s32p[1]);
    bf16x8 c64[2], s64p[2], s64m[2];
    const float k1f = (float)(16 * w + n);
    {
        float c[8], s[8];
#pragma unroll
        for (int e = 0; e < 8; ++e) {
            const float ang = __builtin_amdgcn_fractf((float)(8 * q + e) * k1f * (1.0f / 64.0f));
            c[e] = cosr(ang); s[e] = sinr(ang);
        }
        c64[0] = pk8(c); s64p[0] = pk8(s);
        const u32 sg = (lane & 1) ? 0x80008000u : 0u;
        c64[1] = asbf(__builtin_bit_cast(u32x4, c64[0]) ^ sg); s64p[1] = asbf(__builtin_bit_cast(u32x4, s64p[0]) ^ sg);
        pin(c64[0]); pin(c64[1]); pin(s64p[0]); pin(s64p[1]);
    }
    float twr[8], twi[8], ivr[8], ivi[8];
#pragma unroll
    for (int i = 0; i < 8; ++i) {
        const float af = (float)(16 * (i >> 2) + 4 * q + (i & 3)) * k1f * (1.0f / 2048.0f);
        twr[i] = cosr(af); twi[i] = -sinr(af);
        const float ai = (float)(16 * w + 4 * q + (i & 3)) * (float)(16 * (i >> 2) + n) * (1.0f / 2048.0f);
        ivr[i] = cosr(ai) * (1.0f / 2048.0f); ivi[i] = sinr(ai) * (1.0f / 2048.0f);
        pin(twr[i]); pin(twi[i]); pin(ivr[i]); pin(ivi[i]);
    }
    __builtin_amdgcn_sched_barrier(0);
    STAMP(L ? 4 : 1, 1);
    if (L == 1) {
        poll_all(ws, 1, 32u, wv, lane);
        barrier();
        STAMP(4, 5);
        load_in();
        __builtin_amdgcn_sched_barrier(0);
    }
    s64m[0] = neg8(s64p[0]); s64m[1] = neg8(s64p[1]);
    {
        float r[8];
#pragma unroll
        for (int i = 0; i < 8; ++i) {
            const float s = (float)sv[i >> 1][2 * (i & 1) + 1] * 4294967296.0f + (float)sv[i >> 1][2 * (i & 1)];
            r[i] = __builtin_amdgcn_rsqf(s * (1.0f / (SS_SCALE * 640.0f)) + EPS);
        }
        const bf16x8 ur = pk8(bflo(vr[0]) * r[0], bfhi(vr[0]) * r[1], bflo(vr[1]) * r[2], bfhi(vr[1]) * r[3],
                              bflo(vr[2]) * r[4], bfhi(vr[2]) * r[5], bflo(vr[3]) * r[6], bfhi(vr[3]) * r[7]);
        bf16x8 ui = pk8(bflo(vi[0]) * r[0], bfhi(vi[0]) * r[1], bflo(vi[1]) * r[2], bfhi(vi[1]) * r[3],
                        bflo(vi[2]) * r[4], bfhi(vi[2]) * r[5], bflo(vi[3]) * r[6], bfhi(vi[3]) * r[7]);
        if (j == 304) ui = asbf(u32x4{0u, 0u, 0u, 0u});
        const int o = img128(tid >> 3, tid & 7);
        *(bf16x8*)(Ure + o) = ur; *(bf16x8*)(Uim + o) = ui;
        *(u32x4*)(Rre + o) = vr; *(u32x4*)(Rim + o) = vi;
    }
    __syncthreads();
    STAMP(L ? 4 : 1, 2);
    const f32x4 z = {0.f, 0.f, 0.f, 0.f};
    f32x4 dr[2], di[2];
#pragma unroll
    for (int mt = 0; mt < 2; ++mt) {
        dr[mt] = z; di[mt] = z;
#pragma unroll
        for (int ks = 0; ks < 2; ++ks) {
            const int o = img128(16 * mt + n, 4 * ks + q);
            const bf16x8 ar = *(const bf16x8*)(Ure + o), ai = *(const bf16x8*)(Uim + o);
            dr[mt] = mfma16(ar, c64[ks], dr[mt]); dr[mt] = mfma16(ai, s64p[ks], dr[mt]);
            di[mt] = mfma16(ar, s64m[ks], di[mt]); di[mt] = mfma16(ai, c64[ks], di[mt]);
        }
    }
    float tr[8], ti[8];
#pragma unroll
    for (int i = 0; i < 8; ++i) {
        const float x = dr[i >> 2][i & 3], y = di[i >> 2][i & 3];
        tr[i] = x * twr[i] - y * twi[i]; ti[i] = x * twi[i] + y * twr[i];
    }
    bf16x8 Tr = pk8(tr), Ti = pk8(ti);
    s32m[0] = neg8(s32p[0]); s32m[1] = neg8(s32p[1]);
    f32x4 orr[2], oi[2];
#pragma unroll
    for (int t = 0; t < 2; ++t) {
        orr[t] = mfma16(c32[t], Tr, z); orr[t] = mfma16(s32p[t], Ti, orr[t]);
        oi[t] = mfma16(s32m[t], Tr, z); oi[t] = mfma16(c32[t], Ti, oi[t]);
    }
#pragma unroll
    for (int i = 0; i < 8; ++i) {
        const float x = orr[i >> 2][i & 3], y = oi[i >> 2][i & 3];
        const u32 wv = wf[i >> 2][i & 3];
        const float wre = bflo(wv), wim = bfhi(wv);
        tr[i] = x * wre - y * wim; ti[i] = x * wim + y * wre;
    }
    Tr = pk8(tr); Ti = pk8(ti);
#pragma unroll
    for (int nt = 0; nt < 2; ++nt) {
        f32x4 zr = mfma16(Tr, c32[nt], z); zr = mfma16(Ti, s32m[nt], zr);
        f32x4 zi = mfma16(Tr, s32p[nt], z); zi = mfma16(Ti, c32[nt], zi);
        float er[4], ei[4];
#pragma unroll
        for (int r = 0; r < 4; ++r) {
            const float cr = ivr[4 * nt + r], ci = ivi[4 * nt + r];
            er[r] = zr[r] * cr - zi[r] * ci; ei[r] = zr[r] * ci + zi[r] * cr;
        }
        const int o = img128(16 * nt + n, 2 * w + (q >> 1)) + (q & 1) * 8;
        u32x2 pr = {pk2(er[0], er[1]), pk2(er[2], er[3])}, pi = {pk2(ei[0], ei[1]), pk2(ei[2], ei[3])};
        *(u32x2*)(Ere + o) = pr; *(u32x2*)(Eim + o) = pi;
    }
    __syncthreads();
    STAMP(L ? 4 : 1, 3);
    const rsrc_t rf = mk_rsrc(ws + (L == 0 ? OFF_XF1 : OFF_XF3), XT_BYTES);
    char* O = Ure;
#pragma unroll
    for (int nt = 0; nt < 2; ++nt) {
        f32x4 yr = z, yi = z;
#pragma unroll
        for (int ks = 0; ks < 2; ++ks) {
            const int o = img128(16 * nt + n, 4 * ks + q);
            const bf16x8 br = *(const bf16x8*)(Ere + o), bi = *(const bf16x8*)(Eim + o);
            yr = mfma16(c64[ks], br, yr); yr = mfma16(s64m[ks], bi, yr);
            yi = mfma16(s64p[ks], br, yi); yi = mfma16(c64[ks], bi, yi);
        }
        if (j == 304) yi = z;
        const int p = (16 * nt + n) * 64 + 16 * w + 4 * q;
        u32x2 rr[2], ri[2];
        { const int o = img128(16 * nt + n, 2 * w + (q >> 1)) + (q & 1) * 8; rr[nt] = *(const u32x2*)(Rre + o); ri[nt] = *(const u32x2*)(Rim + o); }
        const float fr[4] = {bflo(rr[nt][0]) + yr[0], bfhi(rr[nt][0]) + yr[1], bflo(rr[nt][1]) + yr[2], bfhi(rr[nt][1]) + yr[3]};
        const float fi[4] = {bflo(ri[nt][0]) + yi[0], bfhi(ri[nt][0]) + yi[1], bflo(ri[nt][1]) + yi[2], bfhi(ri[nt][1]) + yi[3]};
        u32x4 ov = {pk2(fr[0], fi[0]), pk2(fr[1], fi[1]), pk2(fr[2], fi[2]), pk2(fr[3], fi[3])};
        *(u32x4*)(O + p * 4) = ov;
    }
    __syncthreads();
    {
        const int c0 = 4 * b, dst = (((c0 >> 5) * 2 + ((c0 >> 2) & 1)) * 64 + ((c0 & 31) >> 3) * 16) * 8;
        const u32* O0 = (const u32*)smem; const u32* O1 = (const u32*)(smem + 24576);
#pragma unroll
        for (int i = 0; i < 4; ++i) {
            const int p = threadIdx.x + 512 * i;
            u32x2 v = {O0[p], O1[p]};
            __builtin_amdgcn_raw_buffer_store_b64(v, rf, (p >> 4) * 20480 + (p & 15) * 8, dst, SC1);
        }
    }
    STAMP(L ? 4 : 1, 4);
    wait_lgkm();
    wait_vm<0>();
    barrier();
    signal_all(ws, L == 0 ? 0 : 2);
    w1_prefetch<L == 1>(ws, smem, wv, lane, u0);
    STAMP(L ? 4 : 1, 15);
}

DI f32x2 gelu_tanh2(f32x2 x) {
    const float k1 = -2.302208198f, k2 = -0.1029432397f;
    const f32x2 c1 = {k1, k1}, c2 = {k2, k2}, one = {1.0f, 1.0f};
    const f32x2 t = x * (x * x * c2 + c1);
    f32x2 e = {__builtin_amdgcn_exp2f(t[0]), __builtin_amdgcn_exp2f(t[1])};
    e += one;
    const f32x2 r = {__builtin_amdgcn_rcpf(e[0]), __builtin_amdgcn_rcpf(e[1])};
    return x * r;
}
#define G2_PH_ 3
constexpr int G2_PH = G2_PH_;
constexpr int W2STG = 20480;
DI u32 w2slot(int t) { return 163840 - ((t & 7) + 1) * W2STG; }
template <bool LAST> constexpr int w1slot(int st) { return LAST ? 163840 - (st % 8 + 1) * 20480 : 163840 - (st + 1) * 14336; }

template <bool LAST, int I> DI void w1_piece(u32x4 srd, u32 lds0, int w, int vo, int u0, int st) {
    constexpr int WP = LAST ? 20 : 14;
    const int pc = w + 8 * I;
    const u32 dst = lds0 + w1slot<LAST>(st) + pc * 1024;
    const int so = (u0 + (pc >> 1)) * 20480 + st * 2048 + (pc & 1) * 1024;
    if (8 * I + 7 < WP) dma16<LAST>(srd, vo, so, dst); else dma16_lim<(WP & 7), LAST>(srd, vo, so, dst, w);
}
template <bool LAST> DI void w1_prefetch(char* ws, char* smem, int w, int lane, int u0) {
    const u32x4 srd = mk_srd(ws + OFF_W1F, W1F_BYTES);
    const u32 lds0 = (u32)(size_t)(LAS char*)smem;
#pragma unroll
    for (int st = 0; st < (LAST ? 8 : 10); ++st) {
        w1_piece<LAST, 0>(srd, lds0, w, lane * 16, u0, st);
        w1_piece<LAST, 1>(srd, lds0, w, lane * 16, u0, st);
        if (LAST) w1_piece<LAST, 2>(srd, lds0, w, lane * 16, u0, st);
    }
}

constexpr int g1_younger(int ssync, int t, int nlo) {
    int cnt = 0; bool seen = false;
    for (int s = 0; s < ssync; ++s) {
        if (s + G1_PW < 10 && s + G1_PW >= 8) { if (seen) cnt += nlo; if (s + G1_PW == t) seen = true; }
        if (s + G1_P < 10 && seen) cnt += 4;
    }
    return cnt;
}

template <int I> DI void w2_issue1(u32x4 srd, u32 lds0, int w, int vo, int ct, int t) {
    const int pc = w + 8 * I, q = pc / 5, cu = pc - 5 * q;
    const int so = ((5 * ct + cu) * 54 + (q < 2 ? 14 * q : 28 + 13 * (q - 2)) + t) * 1024;
    if (I < 2) dma16<true>(srd, vo, so, lds0 + w2slot(t) + pc * 1024); else dma16_lim<4, true>(srd, vo, so, lds0 + w2slot(t) + pc * 1024, w);
}
DI void w2_issue(u32x4 srd, u32 lds0, int w, int vo, int ct, int t) {
    w2_issue1<0>(srd, lds0, w, vo, ct, t); w2_issue1<1>(srd, lds0, w, vo, ct, t); w2_issue1<2>(srd, lds0, w, vo, ct, t);
}

template <int NF, bool LAST, int KG>
DI void g1_phase(const MainArgs& a, char* smem, int w, int lane, int pt, int u0, int nu, int ct2) {
    constexpr int P = G1_P, PW = G1_PW;
    constexpr int WP = 2 * NF, WR = (WP + 7) / 8, NLO = WP / 8, KID = LAST ? 5 : 2;
    char* ws = a.p.ws;
    const int pq = w & 3, g = lane >> 4;
    const u32x4 srdW = mk_srd(ws + OFF_W1F, W1F_BYTES), srd2 = mk_srd(ws + (LAST ? OFF_W2O : OFF_W2F), W2F_BYTES);
    const u32 lds0 = (u32)(size_t)(LAS char*)smem;
    const rsrc_t rx = mk_rsrc(ws + (LAST ? OFF_XF3 : OFF_XF1), XT_BYTES);
    const int xs = (8 * pt + 2 * pq) * 20480 + KG * 1024, vo = lane * 16;
    auto xload = [&](int st, int v) __attribute__((always_inline)) {
        return __builtin_amdgcn_raw_buffer_load_b64(rx, lane * 8, xs + (v >> 1) * 20480 + st * 2048 + (v & 1) * 512, SC1);
    };
    u32x2 xh[P][4];
#pragma unroll
    for (int st = 0; st < P; ++st) {
#pragma unroll
        for (int v = 0; v < 4; ++v) xh[st][v] = xload(st, v);
        __builtin_amdgcn_sched_barrier(0);
    }
    auto xfrag = [&](int st, int pu) __attribute__((always_inline)) { return asbf(u32x4{xh[st % P][2 * pu][0], xh[st % P][2 * pu][1], xh[st % P][2 * pu + 1][0], xh[st % P][2 * pu + 1][1]}); };
    f32x4 acc[NF][2];
#pragma unroll
    for (int i = 0; i < NF; ++i) { acc[i][0] = f32x4{0, 0, 0, 0}; acc[i][1] = f32x4{0, 0, 0, 0}; }
    f32x4 gr[2] = {f32x4{0, 0, 0, 0}, f32x4{0, 0, 0, 0}};
    bf16x8 wa[2][NF];
#pragma unroll
    for (int i = 0; i < NF; ++i) wa[0][i] = *(const bf16x8*)(smem + w1slot<LAST>(0) + KG * 1024 + lane * 16 + i * 2048);
    static_for<0, 10>([&](auto ST) __attribute__((always_inline)) {
        constexpr int st = decltype(ST)::value;
        if constexpr (LAST && st == 2) { wait_lgkm(); barrier(); }
        if constexpr (st == 4 || st == 7) {
            wait_lgkm();
            constexpr int nx4 = 4 * P + 4 * 4, nx7 = 4 * P + 4 * (10 - P < 7 ? 10 - P : 7);
            if constexpr (st == 4) wait_vm<(LAST ? NLO : 2 * NLO) + nx4>();
            else if constexpr (LAST) wait_vm<g1_younger(7, 9, NLO)>();
            else wait_vm<nx7>();
            barrier();
        }
        u32x2 nx[4];
        const char* rbase = smem + w1slot<LAST>(st + 1) + KG * 1024 + lane * 16;
        constexpr int NV = WR + 4, V0 = KG == 0 ? 0 : NF - NV;
        static_for<0, NF>([&](auto I) __attribute__((always_inline)) {
            constexpr int i = decltype(I)::value;
            __builtin_amdgcn_sched_barrier(0);
            acc[i][0] = mfma16(wa[st & 1][i], xfrag(st, 0), acc[i][0]);
            if constexpr (st + 1 < 10) wa[(st + 1) & 1][i] = *(const bf16x8*)(rbase + i * 2048);
            acc[i][1] = mfma16(wa[st & 1][i], xfrag(st, 1), acc[i][1]);
            if constexpr (i < 2) {
                u32x4 xv = __builtin_bit_cast(u32x4, xfrag(st, i));
                if constexpr (st == 9 && KG == 1) { if (g == 0) xv[0] &= 0x0000ffffu; }
                gr[i] = mfma16(asbf(xv), asbf(xv), gr[i]);
            }
            __builtin_amdgcn_sched_barrier(0);
            constexpr int v = i - V0;
            if constexpr (v >= 0 && v < WR) { if constexpr (LAST && st + PW >= 8 && st + PW < 10) w1_piece<LAST, v>(srdW, lds0, w, vo, u0, st + PW); }
            else if constexpr (v >= WR && v < NV) { if constexpr (st + P < 10) nx[v - WR] = xload(st + P, v - WR); }
            if constexpr (st >= 6) {
                constexpr int sl = i - (KG == 0 ? 0 : NF - 4), e = 4 * (st - 6) + sl;
                if constexpr (sl >= 0 && sl < 4 && e < (LAST ? 9 : 15)) w2_issue1<e % 3>(srd2, lds0, w, vo, ct2, (LAST ? 2 : 0) + e / 3);
            }
        });
        __builtin_amdgcn_sched_barrier(0);
        if constexpr (st + P < 10) { xh[st % P][0] = nx[0]; xh[st % P][1] = nx[1]; xh[st % P][2] = nx[2]; xh[st % P][3] = nx[3]; }
        if (st == 4) STAMP(KID, 2);
    });
    STAMP(KID, 3);
    u32x2 bv[NF];
#pragma unroll
    for (int i = 0; i < NF; ++i) bv[i] = *(const u32x2*)(a.b1 + min(16 * (u0 + i), F_ - 16) + 4 * g);
    __builtin_amdgcn_sched_barrier(0);
    wait_lgkm();
    barrier();
    float* sx = (float*)(smem + 8 * NF * 512);
#pragma unroll
    for (int i = 0; i < NF; ++i) {
        const f32x4 t = acc[i][1 - KG];
        u32x2 pv = {pk2(t[0], t[1]), pk2(t[2], t[3])};
        *(u32x2*)(smem + ((w * NF + i) * 64 + lane) * 8) = pv;
    }
    float st2[2];
    {
        const int n = lane & 15;
#pragma unroll
        for (int pu = 0; pu < 2; ++pu) {
            const float d01 = (n & 1) ? gr[pu][1] : gr[pu][0], d23 = (n & 1) ? gr[pu][3] : gr[pu][2];
            float d = (n & 2) ? d23 : d01;
            d = (g == (n >> 2)) ? d : 0.f;
            d += __shfl_xor(d, 16); d += __shfl_xor(d, 32);
            st2[pu] = d;
        }
    }
    if (lane < 16) sx[w * 16 + lane] = st2[1 - KG];
    wait_lgkm();
    barrier();
    STAMP(KID, 5);
    const float v = (st2[KG] + sx[(w ^ 4) * 16 + (lane & 15)]) * (1.0f / 640.0f);
    const float r1 = __builtin_amdgcn_rsqf(v + EPS);
    const float rr = r1 * __builtin_amdgcn_rsqf(v * r1 * r1 + EPS);
    const int punit = 8 * pt + 2 * pq + KG;
    u32x2 ov[NF], pv[NF];
#pragma unroll
    for (int i = 0; i < NF; ++i) pv[i] = *(const u32x2*)(smem + (((w ^ 4) * NF + i) * 64 + lane) * 8);
    static_for<0, NF>([&](auto I) __attribute__((always_inline)) {
        constexpr int i = decltype(I)::value;
        if constexpr (LAST ? i < 2 : i == 0) {
            __builtin_amdgcn_sched_barrier(0);
            w2_issue(srd2, lds0, w, vo, ct2, LAST ? i : 5);
            __builtin_amdgcn_sched_barrier(0);
        }
        const f32x2 o0 = {acc[i][KG][0] + bflo(pv[i][0]), acc[i][KG][1] + bfhi(pv[i][0])}, o1 = {acc[i][KG][2] + bflo(pv[i][1]), acc[i][KG][3] + bfhi(pv[i][1])};
        const f32x2 rr2 = {rr, rr};
        const f32x2 b0 = {bflo(bv[i][0]), bfhi(bv[i][0])}, b1 = {bflo(bv[i][1]), bfhi(bv[i][1])};
        f32x2 h0 = gelu_tanh2(o0 * rr2 + b0), h1 = gelu_tanh2(o1 * rr2 + b1);
        if (LAST) {
            const bool lin = u0 + i >= 108;
            h0[0] = lin ? o0[0] : h0[0]; h0[1] = lin ? o0[1] : h0[1]; h1[0] = lin ? o1[0] : h1[0]; h1[1] = lin ? o1[1] : h1[1];
        }
        ov[i] = u32x2{pk2(h0[0], h0[1]), pk2(h1[0], h1[1])};
    });
    __builtin_amdgcn_sched_barrier(0);
    const rsrc_t rh = mk_rsrc(ws + (LAST ? OFF_H2 : OFF_H), H_BYTES), r3 = mk_rsrc(ws + OFF_X3, X3_BYTES);
    const int hb = punit * 54 * 1024;
    auto st1 = [&](int i) __attribute__((always_inline)) {
        const int u = u0 + i;
        if (i < nu) {
            if (!LAST || u < 108) __builtin_amdgcn_raw_buffer_store_b64(ov[i], rh, lane * 16 + (u & 1) * 8, hb + (u >> 1) * 1024, SC1);
            else __builtin_amdgcn_raw_buffer_store_b64(ov[i], r3, lane * 8, (punit * 40 + (u - 108)) * 512, SC1);
        }
    };
    auto st2p = [&](int i) __attribute__((always_inline)) {
        const int u = u0 + i;
        if (i + 1 < nu && (!LAST || u + 1 < 108)) {
            u32x4 q4 = {ov[i][0], ov[i][1], ov[i + 1][0], ov[i + 1][1]};
            __builtin_amdgcn_raw_buffer_store_b128(q4, rh, lane * 16, hb + (u >> 1) * 1024, SC1);
        } else { st1(i); st1(i + 1); }
    };
    if (u0 & 1) {
        st1(0);
#pragma unroll
        for (int i = 1; i + 1 < NF; i += 2) st2p(i);
        if ((NF & 1) == 0) st1(NF - 1);
    } else {
#pragma unroll
        for (int i = 0; i + 1 < NF; i += 2) st2p(i);
        if (NF & 1) st1(NF - 1);
    }
    STAMP(KID, 4);
}

template <bool LAST>
DI void g2_phase(const MainArgs& a, char* smem, int w, int lane, int pt1, int pt, int ct) {
    constexpr int PH = G2_PH, KID = LAST ? 6 : 3;
    char* ws = a.p.ws;
    u32* cnt = (u32*)(ws + OFF_CNT + (LAST ? 8192 : 0));
    const int pg = w & 1, kq = w >> 1, n = lane & 15, q = lane >> 4;
    const int kb = kq < 2 ? 14 * kq : 28 + 13 * (kq - 2), nk = kq < 2 ? 14 : 13;
    const u32x4 srd2 = mk_srd(ws + (LAST ? OFF_W2O : OFF_W2F), W2F_BYTES);
    const u32 lds0 = (u32)(size_t)(LAS char*)smem;
    const rsrc_t rh = mk_rsrc(ws + (LAST ? OFF_H2 : OFF_H), H_BYTES), rw = mk_rsrc(ws + (LAST ? OFF_W2O : OFF_W2F), W2F_BYTES);
    const int vo = lane * 16, hs = ((4 * pt + 2 * pg) * 54 + kb) * 1024;
    bf16x8 wr[6][5];
    asm volatile("" ::: "memory");
    __builtin_amdgcn_sched_barrier(0);
#pragma unroll
    for (int t = 0; t < 3; ++t)
#pragma unroll
        for (int i = 0; i < 5; ++i) wr[t][i] = asbf(__builtin_amdgcn_raw_buffer_load_b128(rw, vo, ((5 * ct + i) * 54 + kb + 8 + t) * 1024, SC1));
    __builtin_amdgcn_sched_barrier(0);
    wait_lgkm();
    wait_vm<15>();
    barrier();
    if (threadIdx.x == 0) add_agent(cnt + 64 * pt1);
    STAMP(LAST ? 5 : 2, 15);
#pragma unroll
    for (int t = LAST ? 5 : 6; t < 8; ++t) w2_issue(srd2, lds0, w, vo, ct, t);
    __builtin_amdgcn_sched_barrier(0);
#pragma unroll
    for (int t = 3; t < 6; ++t)
#pragma unroll
        for (int i = 0; i < 5; ++i) wr[t][i] = asbf(__builtin_amdgcn_raw_buffer_load_b128(rw, vo, ((5 * ct + i) * 54 + kb + 8 + t) * 1024, SC1));
    __builtin_amdgcn_sched_barrier(0);
    if (w == 0) poll_until(cnt + 64 * (pt >> 1), 16u, ws, lane);
    wait_vm<0>();
    barrier();
    STAMP(KID, 5);
    bf16x8 hb[PH][2];
    auto hload = [&](int t, int pu) __attribute__((always_inline)) {
        const int k = t < 13 ? t : nk - 1;
        return asbf(__builtin_amdgcn_raw_buffer_load_b128(rh, vo, hs + (pu * 54 + k) * 1024, SC1));
    };
#pragma unroll
    for (int t = 0; t < PH; ++t) { hb[t][0] = hload(t, 0); hb[t][1] = hload(t, 1); __builtin_amdgcn_sched_barrier(0); }
    u32x2 xv[3], bv[3]; float bb[3];
    f32x4 acc[2][5];
#pragma unroll
    for (int i = 0; i < 5; ++i) { acc[0][i] = f32x4{0, 0, 0, 0}; acc[1][i] = f32x4{0, 0, 0, 0}; }
    bf16x8 wa[2][5];
    const char* wbase = smem + kq * 5120 + lane * 16;
#pragma unroll
    for (int i = 0; i < 5; ++i) wa[0][i] = *(const bf16x8*)(wbase + w2slot(0) + i * 1024);
    static_for<0, 14>([&](auto T) __attribute__((always_inline)) {
        constexpr int t = decltype(T)::value;
        if constexpr (t == 10) {
            __builtin_amdgcn_sched_barrier(0);
#pragma unroll
            for (int j = 0; j < 3; ++j) {
                const int tt = min(kq + 4 * j, 9), pu = 2 * pg + tt / 5, cu = tt % 5;
                if (!LAST) {
                    const int c = 80 * ct + 16 * cu + n;
                    const int off = ((((4 * pt + pu) * 20 + (c >> 5)) * 2 + ((c >> 2) & 1)) * 64 + ((c & 31) >> 3) * 16 + 4 * q) * 8 + (c & 3) * 2;
                    const rsrc_t r1 = mk_rsrc(ws + OFF_XF1, XT_BYTES);
                    const u32 e0 = __builtin_amdgcn_raw_buffer_load_b16(r1, off, 0, SC1), e1 = __builtin_amdgcn_raw_buffer_load_b16(r1, off + 8, 0, SC1);
                    const u32 e2 = __builtin_amdgcn_raw_buffer_load_b16(r1, off + 16, 0, SC1), e3 = __builtin_amdgcn_raw_buffer_load_b16(r1, off + 24, 0, SC1);
                    xv[j] = u32x2{e0 | (e1 << 16), e2 | (e3 << 16)};
                    bb[j] = ((const float*)(ws + OFF_B2P))[c];
                } else {
                    xv[j] = __builtin_amdgcn_raw_buffer_load_b64(mk_rsrc(ws + OFF_X3, X3_BYTES), lane * 8, ((4 * pt + pu) * 40 + 5 * ct + cu) * 512, SC1);
                    bv[j] = *(const u32x2*)(a.p.b2 + 80 * ct + 16 * cu + 4 * q);
                }
            }
        }
        bf16x8 nh[2];
        auto step = [&]() __attribute__((always_inline)) {
            static_for<0, 5>([&](auto I) __attribute__((always_inline)) {
                constexpr int i = decltype(I)::value;
                __builtin_amdgcn_sched_barrier(0);
                bf16x8 wv;
                if constexpr (t < 8) wv = wa[t & 1][i]; else wv = wr[t - 8][i];
                acc[0][i] = LAST ? mfma16(wv, hb[t % PH][0], acc[0][i]) : mfma16(hb[t % PH][0], wv, acc[0][i]);
                if constexpr (t + 1 < 8) wa[(t + 1) & 1][i] = *(const bf16x8*)(wbase + w2slot(t + 1) + i * 1024);
                acc[1][i] = LAST ? mfma16(wv, hb[t % PH][1], acc[1][i]) : mfma16(hb[t % PH][1], wv, acc[1][i]);
                __builtin_amdgcn_sched_barrier(0);
                if constexpr (t + PH < 14 && (i == 1 || i == 3)) nh[i >> 1] = hload(t + PH, i >> 1);
            });
        };
        if constexpr (t < 13) step(); else { if (nk == 14) step(); }
        __builtin_amdgcn_sched_barrier(0);
        if constexpr (t + PH < 14) { hb[t % PH][0] = nh[0]; hb[t % PH][1] = nh[1]; }
        if (t == 0) STAMP(KID, 1);
        if (t == 7) STAMP(KID, 2);
    });
    STAMP(KID, 3);
    wait_lgkm();
    barrier();
#pragma unroll
    for (int t = 0; t < 10; ++t)
        if ((t & 3) != kq) *(f32x4*)(smem + ((w * 10 + t) * 64 + lane) * 16) = acc[t / 5][t % 5];
    wait_lgkm();
    barrier();
    float ssp[2][4];
#pragma unroll
    for (int r = 0; r < 4; ++r) { ssp[0][r] = 0.f; ssp[1][r] = 0.f; }
    const rsrc_t ro = mk_rsrc(ws + OFF_XT2, XT_BYTES);
    auto finish = [&](auto KQ) __attribute__((always_inline)) {
        constexpr int kqc = decltype(KQ)::value;
        static_for<0, 3>([&](auto J) __attribute__((always_inline)) {
            constexpr int j = decltype(J)::value, t = kqc + 4 * j;
            if constexpr (t < 10) {
                constexpr int pl = t / 5, cu = t % 5;
                f32x4 o = acc[pl][cu];
#pragma unroll
                for (int s = 1; s < 4; ++s) o += *(const f32x4*)(smem + (((pg + 2 * ((kqc + s) & 3)) * 10 + t) * 64 + lane) * 16);
                const int pu = 2 * pg + pl;
                if (!LAST) {
                    const int c = 80 * ct + 16 * cu + n, p = 64 * pt + 16 * pu + 4 * q;
                    const u32 o0 = pk2(bflo(xv[j][0]) + o[0] + bb[j], bfhi(xv[j][0]) + o[1] + bb[j]);
                    const u32 o1 = pk2(bflo(xv[j][1]) + o[2] + bb[j], bfhi(xv[j][1]) + o[3] + bb[j]);
                    u32x2 ov = {o0, o1};
                    __builtin_amdgcn_raw_buffer_store_b64(ov, ro, (c * S_ + p) * 2, 0, SC1);
                    const float m = c == 609 ? 0.f : 1.f;
                    ssp[pl][0] += m * bflo(o0) * bflo(o0); ssp[pl][1] += m * bfhi(o0) * bfhi(o0);
                    ssp[pl][2] += m * bflo(o1) * bflo(o1); ssp[pl][3] += m * bfhi(o1) * bfhi(o1);
                } else {
                    const int p = 16 * (4 * pt + pu) + n, d = 80 * ct + 16 * cu + 4 * q;
                    u32x2 ov = {pk2(bflo(xv[j][0]) + o[0] + bflo(bv[j][0]), bfhi(xv[j][0]) + o[1] + bfhi(bv[j][0])),
                                pk2(bflo(xv[j][1]) + o[2] + bflo(bv[j][1]), bfhi(xv[j][1]) + o[3] + bfhi(bv[j][1]))};
                    *(u32x2*)(a.out + (size_t)tok_of_p(p) * D_ + d) = ov;
                }
            }
        });
    };
    if (kq == 0) finish(std::integral_constant<int, 0>{});
    else if (kq == 1) finish(std::integral_constant<int, 1>{});
    else if (kq == 2) finish(std::integral_constant<int, 2>{});
    else finish(std::integral_constant<int, 3>{});
    if (!LAST) {
        float* sp = (float*)smem;
#pragma unroll
        for (int pl = 0; pl < 2; ++pl)
#pragma unroll
            for (int r = 0; r < 4; ++r) {
                const float v = row_sum15(ssp[pl][r]);
                if (n == 15) sp[w * 32 + 16 * pl + 4 * q + r] = v;
            }
        wait_lgkm();
        barrier();
        if (w == 0) {
            const int g2 = lane >> 5, r = lane & 31;
            const float v = (sp[g2 * 32 + r] + sp[(g2 + 2) * 32 + r]) + (sp[(g2 + 4) * 32 + r] + sp[(g2 + 6) * 32 + r]);
            __hip_atomic_fetch_add((__attribute__((address_space(1))) u64*)(ws + OFF_SS2) + 64 * pt + lane, ss_from_float(v), __ATOMIC_RELAXED, __HIP_MEMORY_SCOPE_AGENT);
        }
        wait_lgkm();
        wait_vm<0>();
        barrier();
        signal_all(ws, 1);
    }
}

template <bool LAST>
DI void mlp(const MainArgs& a, char* smem, int w, int lane, int u0) {
    constexpr int NF = LAST ? 10 : 7;
    const int x = blockIdx.x & 7, ib = blockIdx.x >> 3;
    const int pt = 4 * (x >> 1) + (ib & 3), ft = 8 * (x & 1) + (ib >> 2);
    const int pt2 = 8 * (x >> 1) + (ib & 7), ct2 = 4 * (x & 1) + (ib >> 3);
    const int nu = LAST ? (ft < 4 ? 10 : 9) : (ft < 12 ? 7 : 6);
    if (LAST) poll_all(a.p.ws, 2, 20u, w, lane);
    else if (w == 0) {
        const int sh = lane & 7, wh = (lane >> 3) & 1;
        poll_until((const u32*)(a.p.ws + OFF_CNT + 16384 + (wh ? 3 : 0) * 8192) + ((blockIdx.x >> 3) & 7) * 256 + sh * 32, wh ? (sh < 6 ? 12u : 11u) : 20u, a.p.ws, lane);
    }
    wait_vm<(LAST ? 6 : 5)>();
    barrier();
    STAMP(LAST ? 5 : 2, 1);
    if (w >= 4) __builtin_amdgcn_s_setprio(1);
    if (w < 4) g1_phase<NF, LAST, 0>(a, smem, w, lane, pt, u0, nu, ct2);
    else g1_phase<NF, LAST, 1>(a, smem, w, lane, pt, u0, nu, ct2);
    __builtin_amdgcn_s_setprio(0);
    g2_phase<LAST>(a, smem, w, lane, pt, pt2, ct2);
}

__global__ __launch_bounds__(512, 2) void k_main(MainArgs a) {
    extern __shared__ __attribute__((aligned(16))) char smem[];
    const int lane = threadIdx.x & 63, b = blockIdx.x;
    const int w = __builtin_amdgcn_readfirstlane(threadIdx.x >> 6);
    const int ft = 8 * (b & 1) + (b >> 5);
    const int u0a = ft < 12 ? 7 * ft : 84 + 6 * (ft - 12), u0b = ft < 4 ? 10 * ft : 40 + 9 * (ft - 4);
    if (b < 160) col_task<0>(a.p.ws, b, smem, u0a);
    else {
        if (b < 254) {
            if (b < 214) task_w2(a.p, b - 160, smem); else task_Q(a.p, b - 214);
            wait_lgkm();
            wait_vm<0>();
            barrier();
            signal_all(a.p.ws, 3);
        }
        w1_prefetch<false>(a.p.ws, smem, w, lane, u0a);
    }
    mlp<false>(a, smem, w, lane, u0a);
    END_STAMP(3);
    if (b < 160) col_task<1>(a.p.ws, b, smem, u0b); else w1_prefetch<true>(a.p.ws, smem, w, lane, u0b);
    mlp<true>(a, smem, w, lane, u0b);
    END_STAMP(6);
}

extern "C" void kernel_launch(void* const* d_in, const int* in_sizes, int n_in, void* d_out, int out_size, void* d_ws,
                              size_t ws_size, hipStream_t stream) {
    char* ws = (char*)d_ws;
    constexpr int MAIN_LDS = 163840;
    (void)hipFuncSetAttribute((const void*)k_main, hipFuncAttributeMaxDynamicSharedMemorySize, MAIN_LDS);
    PrepArgs pa{(const u16*)d_in[0], (const u16*)d_in[1], (const u16*)d_in[2], (const u16*)d_in[4], (const u16*)d_in[6], (const u16*)d_in[7], ws};
    k0_prep<<<NB_K0, 512, PREP_LDS, stream>>>(pa);
    MainArgs ma{pa, (const u16*)d_in[5], (u16*)d_out};
    k_main<<<256, 512, MAIN_LDS, stream>>>(ma);
}
```

```cpp
#include <hip/hip_runtime.h>
#include <hip/hip_bf16.h>
#include <stdint.h>
#include <type_traits>

typedef unsigned short u16;
typedef unsigned int u32;
typedef unsigned long long u64;
typedef short s16x4 __attribute__((ext_vector_type(4)));
typedef __bf16 bf16x8 __attribute__((ext_vector_type(8)));
typedef __bf16 bf16x2 __attribute__((ext_vector_type(2)));
typedef float f32x4 __attribute__((ext_vector_type(4)));
typedef float f32x2 __attribute__((ext_vector_type(2)));
typedef u32 u32x4 __attribute__((ext_vector_type(4)));
typedef u32 u32x2 __attribute__((ext_vector_type(2)));
typedef __amdgpu_buffer_rsrc_t rsrc_t;
#define LAS __attribute__((address_space(3)))
#define DI __device__ __forceinline__

constexpr int S_ = 2048, D_ = 640, F_ = 1728, DF_ = 321;
constexpr float EPS = 1e-5f;
constexpr int SC1 = 16;
constexpr float SS_SCALE = 1048576.0f;

constexpr size_t XT_BYTES = (size_t)D_ * S_ * 2;
constexpr size_t OFF_XT0 = 0;
constexpr size_t OFF_XT1 = OFF_XT0 + XT_BYTES;
constexpr size_t OFF_XT2 = OFF_XT1 + XT_BYTES;
constexpr size_t OFF_XT3 = OFF_XT2 + XT_BYTES;
constexpr size_t OFF_WT = OFF_XT3 + XT_BYTES;
constexpr size_t OFF_W1F = OFF_WT + (size_t)320 * 2048 * 4;
constexpr size_t W1F_BYTES = (size_t)148 * 20 * 1024;
constexpr size_t OFF_W2F = OFF_W1F + W1F_BYTES;
constexpr size_t W2F_BYTES = (size_t)40 * 54 * 1024;
constexpr size_t OFF_W2O = OFF_W2F + W2F_BYTES;
constexpr size_t OFF_H = OFF_W2O + W2F_BYTES;
constexpr size_t H_BYTES = (size_t)128 * 54 * 1024;
constexpr size_t OFF_H2 = OFF_H;
constexpr size_t OFF_X3 = OFF_H + 2 * H_BYTES;
constexpr size_t OFF_SS0 = OFF_X3 + (size_t)128 * 40 * 512;
constexpr size_t OFF_SS2 = OFF_SS0 + 2048 * 8;
constexpr size_t OFF_B2P = OFF_SS2 + 2048 * 8;
constexpr size_t OFF_XF1 = OFF_B2P + 4096;
constexpr size_t OFF_XF3 = OFF_XF1;
constexpr size_t X3_BYTES = (size_t)128 * 40 * 512;
constexpr size_t OFF_CNT = OFF_XF1 + 2 * XT_BYTES;
constexpr size_t WS_END = OFF_CNT + 65536;

#define STAMP(k, i)
#define CSTAMP(k, i)
#define END_STAMP(k)

DI f32x4 mfma16(bf16x8 a, bf16x8 b, f32x4 c) { return __builtin_amdgcn_mfma_f32_16x16x32_bf16(a, b, c, 0, 0, 0); }
DI u32 pk2(float a, float b) { f32x2 v = {a, b}; const bf16x2 r = __builtin_convertvector(v, bf16x2); return __builtin_bit_cast(u32, r); }
DI bf16x8 pk8(float a0, float a1, float a2, float a3, float a4, float a5, float a6, float a7) {
    u32x4 r = {pk2(a0, a1), pk2(a2, a3), pk2(a4, a5), pk2(a6, a7)};
    return __builtin_bit_cast(bf16x8, r);
}
DI bf16x8 pk8(const float (&a)[8]) { return pk8(a[0], a[1], a[2], a[3], a[4], a[5], a[6], a[7]); }
DI bf16x8 asbf(u32x4 r) { return __builtin_bit_cast(bf16x8, r); }
DI bf16x8 neg8(bf16x8 a) { u32x4 r = __builtin_bit_cast(u32x4, a); r ^= 0x80008000u; return __builtin_bit_cast(bf16x8, r); }
DI float bflo(u32 v) { return __builtin_bit_cast(float, v << 16); }
DI float bfhi(u32 v) { return __builtin_bit_cast(float, v & 0xffff0000u); }
DI float cosr(float rev) { return __builtin_amdgcn_cosf(rev); }
DI float sinr(float rev) { return __builtin_amdgcn_sinf(rev); }
DI int slot32(int q, int j) { return j < 4 ? 4 * q + j : 16 + 4 * q + (j - 4); }
DI s16x4 tr_read(const char* p) { return __builtin_amdgcn_ds_read_tr16_b64_v4i16((LAS s16x4*)p); }
DI bf16x8 cat8(s16x4 lo, s16x4 hi) { return __builtin_bit_cast(bf16x8, __builtin_shufflevector(lo, hi, 0, 1, 2, 3, 4, 5, 6, 7)); }
DI int tok_of_p(int p) { return 32 * (p & 63) + (p >> 6); }
DI rsrc_t mk_rsrc(const void* p, size_t bytes) { return __builtin_amdgcn_make_buffer_rsrc((void*)p, 0, (int)bytes, 0x00020000); }
DI u32x4 bload16(rsrc_t r, int voff, int soff) { return __builtin_amdgcn_raw_buffer_load_b128(r, voff, soff, 0); }
DI float ss_to_float(u64 v) { return (float)(u32)(v >> 32) * 4294967296.0f + (float)(u32)v; }
DI u64 ss_from_float(float s) { return (u64)(fminf(s * SS_SCALE, 1.0e18f) + 0.5f); }
DI void pin(float& x) { asm volatile("" : "+v"(x)); }
DI void pin(bf16x8& x) { asm volatile("" : "+v"(x)); }

struct F32c { bf16x8 c[2], sp[2]; };
DI void f32c_init(F32c& k, int lane) {
    const float m = (float)(lane & 15), q4 = (float)(4 * (lane >> 4));
    float c[4], s[4];
#pragma unroll
    for (int j = 0; j < 4; ++j) { const float a = __builtin_amdgcn_fractf(m * (q4 + (float)j) * (1.0f / 32.0f)); c[j] = cosr(a); s[j] = sinr(a); }
    const u32 sg = (lane & 1) ? 0x80008000u : 0u;
    const u32 c0 = pk2(c[0], c[1]), c1 = pk2(c[2], c[3]), s0 = pk2(s[0], s[1]), s1 = pk2(s[2], s[3]);
    const u32x4 cv = {c0, c1, c0 ^ sg, c1 ^ sg}, sv = {s0, s1, s0 ^ sg, s1 ^ sg};
    k.c[0] = asbf(cv); k.sp[0] = asbf(sv);
    k.c[1] = asbf(cv ^ 0x80000000u); k.sp[1] = asbf(sv ^ 0x80000000u);
}

struct RowC { bf16x8 c[2], sp[2], sm[2], c20, s20m; f32x4 twr[2], twi[2]; int off0, off1; };
constexpr int ROWC_LDS = 10 * 1024;
DI void rowc_build(char* tab, int w, int lane) {
    const int n = lane & 15, q = lane >> 4;
    const float nf = (float)n;
    u32x4* t = (u32x4*)tab + lane;
    if (w == 0) {
        F32c f; f32c_init(f, lane);
        t[0] = __builtin_bit_cast(u32x4, f.c[0]); t[64] = __builtin_bit_cast(u32x4, f.c[1]);
        t[128] = __builtin_bit_cast(u32x4, f.sp[0]); t[192] = __builtin_bit_cast(u32x4, f.sp[1]);
    } else if (w <= 2) {
        float c[8];
#pragma unroll
        for (int j = 0; j < 8; ++j) {
            const int d1 = 8 * q + j;
            const float a = __builtin_amdgcn_fractf((float)d1 * nf * 0.05f);
            const float v = w == 1 ? cosr(a) : -sinr(a);
            c[j] = (d1 < 20 && n <= 10) ? v : 0.f;
        }
        t[(3 + w) * 64] = __builtin_bit_cast(u32x4, pk8(c));
    } else if (w <= 4) {
        const float sc = 0.05590169943749474f;
        f32x4 cr, ci;
#pragma unroll
        for (int i = 0; i < 4; ++i) {
            const float a = (float)(16 * (w - 3) + 4 * q + i) * nf * (1.0f / 640.0f);
            cr[i] = cosr(a) * sc; ci[i] = -sinr(a) * sc;
        }
        t[(6 + w - 3) * 64] = __builtin_bit_cast(u32x4, cr); t[(8 + w - 3) * 64] = __builtin_bit_cast(u32x4, ci);
    }
}
DI void rowc_load(RowC& k, const char* tab, int lane) {
    const u32x4* tb = (const u32x4*)tab + lane;
    k.c[0] = asbf(tb[0]); k.c[1] = asbf(tb[64]); k.sp[0] = asbf(tb[128]); k.sp[1] = asbf(tb[192]);
    k.c20 = asbf(tb[256]); k.s20m = asbf(tb[320]);
    k.twr[0] = __builtin_bit_cast(f32x4, tb[384]); k.twr[1] = __builtin_bit_cast(f32x4, tb[448]);
    k.twi[0] = __builtin_bit_cast(f32x4, tb[512]); k.twi[1] = __builtin_bit_cast(f32x4, tb[576]);
    k.sm[0] = neg8(k.sp[0]); k.sm[1] = neg8(k.sp[1]);
    const int q = lane >> 4, qq = (lane & 15) >> 2, pp = lane & 3;
    const int r0 = min(8 * q + qq, 19), r1 = min(8 * q + 4 + qq, 19);
    k.off0 = r0 * 64 + pp * 8; k.off1 = r1 * 64 + pp * 8;
}
DI float wave_sum63(float v) {
    v += __builtin_bit_cast(float, __builtin_amdgcn_update_dpp(0, __builtin_bit_cast(int, v), 0x111, 0xf, 0xf, true));
    v += __builtin_bit_cast(float, __builtin_amdgcn_update_dpp(0, __builtin_bit_cast(int, v), 0x112, 0xf, 0xf, true));
    v += __builtin_bit_cast(float, __builtin_amdgcn_update_dpp(0, __builtin_bit_cast(int, v), 0x114, 0xf, 0xf, true));
    v += __builtin_bit_cast(float, __builtin_amdgcn_update_dpp(0, __builtin_bit_cast(int, v), 0x118, 0xf, 0xf, true));
    v += __builtin_bit_cast(float, __builtin_amdgcn_update_dpp(0, __builtin_bit_cast(int, v), 0x142, 0xa, 0xf, true));
    v += __builtin_bit_cast(float, __builtin_amdgcn_update_dpp(0, __builtin_bit_cast(int, v), 0x143, 0xc, 0xf, true));
    return v;
}
DI void rowfft(const RowC& k, const char* row, float (&A)[8], float (&B)[8], int lane) {
    f32x4 z = {0.f, 0.f, 0.f, 0.f};
    f32x4 yr[2], yi[2];
#pragma unroll
    for (int mt = 0; mt < 2; ++mt) {
        const bf16x8 a = cat8(tr_read(row + k.off0 + mt * 32), tr_read(row + k.off1 + mt * 32));
        yr[mt] = mfma16(a, k.c20, z); yi[mt] = mfma16(a, k.s20m, z);
    }
    float tr[8], ti[8];
#pragma unroll
    for (int i = 0; i < 8; ++i) {
        const float a = yr[i >> 2][i & 3], b = yi[i >> 2][i & 3], cr = k.twr[i >> 2][i & 3], ci = k.twi[i >> 2][i & 3];
        tr[i] = a * cr - b * ci; ti[i] = a * ci + b * cr;
    }
    const bf16x8 Tr = pk8(tr), Ti = pk8(ti);
    f32x4 ore[2], oim[2];
#pragma unroll
    for (int t = 0; t < 2; ++t) {
        ore[t] = mfma16(k.c[t], Tr, z); ore[t] = mfma16(k.sp[t], Ti, ore[t]);
        oim[t] = mfma16(k.sm[t], Tr, z); oim[t] = mfma16(k.c[t], Ti, oim[t]);
    }
#pragma unroll
    for (int r = 0; r < 4; ++r) { A[2 * r] = ore[0][r]; A[2 * r + 1] = oim[0][r]; B[2 * r] = ore[1][r]; B[2 * r + 1] = oim[1][r]; }
    if (lane == 0) { const float h = 0.7071067811865476f; const float dc = A[0] * h; A[0] = B[0] * h; A[1] = dc; }
}
DI int sw_off(int c) { const int ch = c >> 3; return ((ch ^ ((ch >> 3) & 7)) << 4) + (c & 7) * 2; }
DI int chunk_c0(int n, int q) { return (n == 0 ? 608 : n == 10 ? 576 : 64 * (n - 1)) + 8 * q; }

constexpr int ROWST = 1344;
constexpr int PREP_TAB = 640 * 32 + 16 * ROWST;
constexpr int PREP_LDS = PREP_TAB + ROWC_LDS;

struct PrepArgs { const u16 *x, *wr, *wi, *w1, *w2, *b2; char* ws; };

DI void task_x(const PrepArgs& a, int b, char* smem) {
    const int tid = threadIdx.x, lane = tid & 63, w = tid >> 6;
    const int p0 = 16 * ((b & 7) * 16 + (b >> 3));
    u32x4 v[3];
#pragma unroll
    for (int i = 0; i < 3; ++i) {
        const int id = min(tid + 512 * i, 1279), r = id / 80, ch = id % 80;
        v[i] = *(const u32x4*)(a.x + (size_t)tok_of_p(p0 + r) * D_ + ch * 8);
    }
    __builtin_amdgcn_sched_barrier(0);
    rowc_build(smem + PREP_TAB, w, lane);
    __builtin_amdgcn_sched_barrier(0);
    STAMP(0, 1);
    if (tid < 16) ((u64*)(a.ws + OFF_SS2))[p0 + tid] = 0ull;
    if (b < 32) ((u32*)(a.ws + OFF_CNT))[b * 512 + tid] = 0u;
#pragma unroll
    for (int i = 0; i < 3; ++i) {
        const int id = min(tid + 512 * i, 1279), r = id / 80, ch = id % 80;
        *(u32x4*)(smem + r * ROWST + ch * 16) = v[i];
    }
    STAMP(0, 2);
    __syncthreads();
    STAMP(0, 3);
    RowC k; rowc_load(k, smem + PREP_TAB, lane);
    const int n = lane & 15, q = lane >> 4, c0 = chunk_c0(n, q);
    float A[2][8], B[2][8];
#pragma unroll
    for (int i = 0; i < 2; ++i) rowfft(k, smem + (2 * w + i) * ROWST, A[i], B[i], lane);
#pragma unroll
    for (int i = 0; i < 2; ++i) {
        const int r = 2 * w + i;
        float ss = 0.f;
        if (n <= 10) {
#pragma unroll
            for (int e = 0; e < 8; ++e) ss += (lane == 0 && e == 1) ? 0.f : A[i][e] * A[i][e];
            *(bf16x8*)(smem + r * ROWST + sw_off(c0)) = pk8(A[i]);
        }
        if (n >= 1 && n <= 9) {
#pragma unroll
            for (int e = 0; e < 8; ++e) ss += B[i][e] * B[i][e];
            *(bf16x8*)(smem + r * ROWST + sw_off(c0 + 32)) = pk8(B[i]);
        }
        ss = wave_sum63(ss);
        if (lane == 63) ((u64*)(a.ws + OFF_SS0))[p0 + r] = ss_from_float(ss);
    }
    STAMP(0, 4);
    __syncthreads();
    STAMP(0, 5);
    u16* xt = (u16*)(a.ws + OFF_XT0);
    const int g = lane >> 4, i16 = lane & 15, qq = i16 >> 2, pp = i16 & 3;
#pragma unroll
    for (int i = 0; i < 3; ++i) {
        const int cbk = w + 8 * i;
        if (cbk < 20) {
            const int cb = 32 * cbk + 16 * (g & 1), ph = g >> 1;
            const char* base = smem + (8 * ph + qq) * ROWST + sw_off(cb + 4 * pp);
            const bf16x8 o = cat8(tr_read(base), tr_read(base + 4 * ROWST));
            *(bf16x8*)(xt + (size_t)(cb + i16) * S_ + p0 + 8 * ph) = o;
        }
    }
}

DI void task_W(const PrepArgs& a, int b) {
    const int lane = threadIdx.x & 63, wv = threadIdx.x >> 6, wt = b * 2 + (wv >> 2), kq = wv & 3;
    const int k1 = wt / 5, cg = wt % 5, kp = 1 + 64 * cg + lane;
    const int r = kp % 20, t = kp / 20;
    int j; bool mir = false;
    if (kp == 320) j = 304;
    else if (r == 0) j = 304 + t;
    else if (r < 10) j = 32 * (r - 1) + t;
    else if (r == 10) j = 288 + t;
    else { j = 32 * (19 - r) + 31 - t; mir = true; }
    const rsrc_t rr = mk_rsrc(a.wr, (size_t)S_ * DF_ * 2), ri = mk_rsrc(a.wi, (size_t)S_ * DF_ * 2);
    const int st = (mir ? -64 : 64) * DF_ * 2, off0 = ((mir ? 2048 - k1 : k1) * DF_ + kp) * 2 + 8 * kq * st;
    u32 re[8], im[8];
#pragma unroll
    for (int k2 = 0; k2 < 8; ++k2) {
        int off = off0 + k2 * st;
        if (k2 == 0 && mir && k1 == 0 && kq == 0) off = kp * 2;
        re[k2] = __builtin_amdgcn_raw_buffer_load_b16(rr, off, 0, 0); im[k2] = __builtin_amdgcn_raw_buffer_load_b16(ri, off, 0, 0);
    }
    const u32 sg = mir ? 0x80000000u : 0u;
    u32x4* dst = (u32x4*)(a.ws + OFF_WT + ((size_t)j * 64 + k1) * 128 + kq * 32);
#pragma unroll
    for (int i = 0; i < 2; ++i) {
        u32x4 o;
#pragma unroll
        for (int e = 0; e < 4; ++e) o[e] = (re[4 * i + e] | (im[4 * i + e] << 16)) ^ sg;
        dst[i] = o;
    }
}

DI void task_w1(const PrepArgs& a, int b, char* smem) {
    const int tid = threadIdx.x, lane = tid & 63, w = tid >> 6;
    char* raw = smem;
    char* tr = smem + 640 * 32;
    char* out = raw;
    u32x4 v[3];
#pragma unroll
    for (int i = 0; i < 3; ++i) {
        const int id = min(tid + 512 * i, 1279), d = id >> 1, h = id & 1;
        v[i] = *(const u32x4*)(a.w1 + (size_t)d * F_ + 16 * b + 8 * h);
    }
    __builtin_amdgcn_sched_barrier(0);
    rowc_build(smem + PREP_TAB, w, lane);
    __builtin_amdgcn_sched_barrier(0);
#pragma unroll
    for (int i = 0; i < 3; ++i) *(u32x4*)(raw + min(tid + 512 * i, 1279) * 16) = v[i];
    __syncthreads();
    const int g = lane >> 4, i16 = lane & 15, qq = i16 >> 2, pp = i16 & 3;
#pragma unroll
    for (int i = 0; i < 5; ++i) {
        const int d0 = 16 * (w + 8 * i) + 4 * g;
        const s16x4 t = tr_read(raw + (d0 + qq) * 32 + pp * 8);
        *(s16x4*)(tr + i16 * ROWST + d0 * 2) = t;
    }
    RowC k; rowc_load(k, smem + PREP_TAB, lane);
    __syncthreads();
    const int n = lane & 15, q = lane >> 4, c0 = chunk_c0(n, q);
    float A[2][8], B[2][8];
#pragma unroll
    for (int i = 0; i < 2; ++i) rowfft(k, tr + (2 * w + i) * ROWST, A[i], B[i], lane);
#pragma unroll
    for (int i = 0; i < 2; ++i) {
        const int f = 2 * w + i;
        if (lane == 0) A[i][1] = 0.f;
        if (n <= 10) *(bf16x8*)(out + ((c0 >> 5) * 64 + q * 16 + f) * 16) = pk8(A[i]);
        if (n >= 1 && n <= 9) *(bf16x8*)(out + (((c0 >> 5) + 1) * 64 + q * 16 + f) * 16) = pk8(B[i]);
    }
    __syncthreads();
    u32x4* dst = (u32x4*)(a.ws + OFF_W1F + (size_t)b * 20 * 1024);
#pragma unroll
    for (int i = 0; i < 3; ++i) { const int id = tid + 512 * i; if (id < 1280) dst[id] = *(const u32x4*)(out + id * 16); }
}

template <bool SW> DI void w2_frags(const char* img, char* dstbase, int b, int lane, int w) {
    const int g = lane >> 4, i16 = lane & 15, qq = i16 >> 2, pp = i16 & 3;
    const rsrc_t rd = mk_rsrc(dstbase, W2F_BYTES);
#pragma unroll
    for (int i = 0; i < 5; ++i) {
        const int cu = w + 8 * i, c = 16 * cu + 4 * pp;
        const char* base = img + (4 * g + qq) * ROWST + (SW ? sw_off(c) : c * 2);
        const bf16x8 v = cat8(tr_read(base), tr_read(base + 16 * ROWST));
        __builtin_amdgcn_raw_buffer_store_b128(__builtin_bit_cast(u32x4, v), rd, lane * 16, (cu * 54 + b) * 1024, SC1);
    }
}
constexpr int W2_TAB = 32 * ROWST;
DI void task_w2(const PrepArgs& a, int b, char* smem) {
    const int tid = threadIdx.x, lane = tid & 63, w = tid >> 6;
    u32x4 v[5];
#pragma unroll
    for (int i = 0; i < 5; ++i) {
        const int id = tid + 512 * i, r = id / 80, ch = id % 80;
        v[i] = *(const u32x4*)(a.w2 + (size_t)(32 * b + r) * D_ + ch * 8);
    }
    __builtin_amdgcn_sched_barrier(0);
    rowc_build(smem + W2_TAB, w, lane);
    __builtin_amdgcn_sched_barrier(0);
#pragma unroll
    for (int i = 0; i < 5; ++i) { const int id = tid + 512 * i, r = id / 80, ch = id % 80; *(u32x4*)(smem + r * ROWST + ch * 16) = v[i]; }
    __syncthreads();
    RowC k; rowc_load(k, smem + W2_TAB, lane);
    w2_frags<false>(smem, a.ws + OFF_W2O, b, lane, w);
    const int n = lane & 15, q = lane >> 4, c0 = chunk_c0(n, q);
    u32x4 A8[4], B8[4];
#pragma unroll
    for (int i = 0; i < 4; ++i) {
        float A[8], B[8];
        rowfft(k, smem + (4 * w + i) * ROWST, A, B, lane);
        A8[i] = __builtin_bit_cast(u32x4, pk8(A)); B8[i] = __builtin_bit_cast(u32x4, pk8(B));
    }
    __syncthreads();
#pragma unroll
    for (int i = 0; i < 4; ++i) {
        const int r = 4 * w + i;
        if (n <= 10) *(u32x4*)(smem + r * ROWST + sw_off(c0)) = A8[i];
        if (n >= 1 && n <= 9) *(u32x4*)(smem + r * ROWST + sw_off(c0 + 32)) = B8[i];
    }
    __syncthreads();
    w2_frags<true>(smem, a.ws + OFF_W2F, b, lane, w);
}

DI int kcol_of(int jc) { return jc < 288 ? (jc >> 5) + 1 + 20 * (jc & 31) : jc < 304 ? 10 + 20 * (jc - 288) : 20 * (jc - 304); }
DI void task_Q(const PrepArgs& a, int u) {
    const int tid = threadIdx.x;
#pragma unroll
    for (int ii = 0; ii < 3; ++ii) {
        const int i = tid + 512 * ii;
        if (i < 1280) {
            const int ks = i >> 6, l = i & 63, d = 16 * u + (l & 15), q = l >> 4;
            float v[8];
#pragma unroll
            for (int j = 0; j < 8; j += 2) {
                const int jc = 16 * ks + 4 * q + (j >> 1), kk = kcol_of(jc);
                const float ang = (float)((kk * d) % 640) * (1.0f / 640.0f);
                float c = cosr(ang) * 0.05590169943749474f, s = -sinr(ang) * 0.05590169943749474f;
                if (jc == 304) { c = ((d & 1) ? -1.f : 1.f) * 0.03952847075210474f; s = 0.03952847075210474f; }
                v[j] = c; v[j + 1] = s;
            }
            __builtin_amdgcn_raw_buffer_store_b128(__builtin_bit_cast(u32x4, pk8(v)), mk_rsrc(a.ws + OFF_W1F, W1F_BYTES), i * 16, (108 + u) * 20480, SC1);
        }
    }
}
DI void task_b(const PrepArgs& a, char* smem) {
    const int tid = threadIdx.x, lane = tid & 63;
    if (tid < 80) *(u32x4*)(smem + tid * 16) = *(const u32x4*)(a.b2 + tid * 8);
    rowc_build(smem + PREP_TAB, tid >> 6, lane);
    __syncthreads();
    RowC k; rowc_load(k, smem + PREP_TAB, lane);
    float A[8], B[8];
    rowfft(k, smem, A, B, lane);
    if (tid < 64) {
        const int n = lane & 15, q = lane >> 4, c0 = chunk_c0(n, q);
        float* o = (float*)(a.ws + OFF_B2P);
        if (n <= 10) for (int e = 0; e < 8; ++e) o[c0 + e] = A[e];
        if (n >= 1 && n <= 9) for (int e = 0; e < 8; ++e) o[c0 + 32 + e] = B[e];
    }
}

constexpr int NB_K0 = 128 + 108 + 160 + 1;
constexpr int MAIN_CODE_PREFETCH = 61440;
DI void prefetch_main_code(int slice, int lane) {
    u32 lo, hi;
    asm volatile("s_getpc_b64 s[40:41]\n\ts_add_u32 s40, s40, _Z6k_main8MainArgs@rel32@lo+4\n\ts_addc_u32 s41, s41, _Z6k_main8MainArgs@rel32@hi+12\n\t"
                 "s_mov_b32 %0, s40\n\ts_mov_b32 %1, s41" : "=s"(lo), "=s"(hi) :: "s40", "s41", "scc");
    const u64 base = ((u64)hi << 32) | lo;
    const char* p = (const char*)base + slice * 4096 + lane * 16;
    u32x4 t0, t1, t2, t3;
    asm volatile("global_load_dwordx4 %0, %4, off\n\tglobal_load_dwordx4 %1, %4, off offset:1024\n\tglobal_load_dwordx4 %2, %4, off offset:2048\n\t"
                 "global_load_dwordx4 %3, %4, off offset:3072\n\ts_waitcnt vmcnt(0)"
                 : "=&v"(t0), "=&v"(t1), "=&v"(t2), "=&v"(t3) : "v"(p) : "memory");
}
__global__ __launch_bounds__(512, 4) void k0_prep(PrepArgs a) {
    extern __shared__ __attribute__((aligned(16))) char smem[];
    const int b = blockIdx.x;
    STAMP(0, 0);
    if (b >= 236 && b < 236 + 8 * (MAIN_CODE_PREFETCH / 4096) && (threadIdx.x >> 6) == 7) prefetch_main_code((b - 236) >> 3, threadIdx.x & 63);
    if (b < 128) task_x(a, b, smem);
    else if (b < 236) task_w1(a, b - 128, smem);
    else if (b < 396) task_W(a, b - 236);
    else task_b(a, smem);
    END_STAMP(0);
}

template <int N> DI void wait_vm() { asm volatile("s_waitcnt vmcnt(%0)" ::"n"(N) : "memory"); }
DI void wait_lgkm() { asm volatile("s_waitcnt lgkmcnt(0)" ::: "memory"); }
DI void barrier() { __builtin_amdgcn_s_barrier(); }
template <bool SC = false> DI void dma16(u32x4 srd, int voff, int soff, u32 ldsaddr) {
    if (SC) asm volatile("s_mov_b32 m0, %0\n\ts_nop 0\n\tbuffer_load_dwordx4 %1, %2, %3 offen sc1 lds" ::"s"(ldsaddr), "v"(voff), "s"(srd), "s"(soff) : "memory", "m0");
    else asm volatile("s_mov_b32 m0, %0\n\ts_nop 0\n\tbuffer_load_dwordx4 %1, %2, %3 offen lds" ::"s"(ldsaddr), "v"(voff), "s"(srd), "s"(soff) : "memory", "m0");
}
template <int LIM, bool SC = false> DI void dma16_lim(u32x4 srd, int voff, int soff, u32 ldsaddr, int w) {
    if (SC) asm volatile("s_cmp_lt_i32 %4, %5\n\ts_cbranch_scc0 .Lskip%=\n\ts_mov_b32 m0, %0\n\ts_nop 0\n\tbuffer_load_dwordx4 %1, %2, %3 offen sc1 lds\n.Lskip%=:"
                 ::"s"(ldsaddr), "v"(voff), "s"(srd), "s"(soff), "s"(w), "n"(LIM) : "memory", "m0", "scc");
    else asm volatile("s_cmp_lt_i32 %4, %5\n\ts_cbranch_scc0 .Lskip%=\n\ts_mov_b32 m0, %0\n\ts_nop 0\n\tbuffer_load_dwordx4 %1, %2, %3 offen lds\n.Lskip%=:"
                 ::"s"(ldsaddr), "v"(voff), "s"(srd), "s"(soff), "s"(w), "n"(LIM) : "memory", "m0", "scc");
}
DI u32x4 mk_srd(const void* p, size_t bytes) {
    const u64 a = (u64)p;
    u32x4 r = {(u32)a, (u32)(a >> 32) & 0xffffu, (u32)bytes, 0x00020000u};
    return r;
}
DI float row_sum15(float v) {
    v += __builtin_bit_cast(float, __builtin_amdgcn_update_dpp(0, __builtin_bit_cast(int, v), 0x111, 0xf, 0xf, true));
    v += __builtin_bit_cast(float, __builtin_amdgcn_update_dpp(0, __builtin_bit_cast(int, v), 0x112, 0xf, 0xf, true));
    v += __builtin_bit_cast(float, __builtin_amdgcn_update_dpp(0, __builtin_bit_cast(int, v), 0x114, 0xf, 0xf, true));
    v += __builtin_bit_cast(float, __builtin_amdgcn_update_dpp(0, __builtin_bit_cast(int, v), 0x118, 0xf, 0xf, true));
    return v;
}
template <int I, int N, class F> DI void static_for(F&& f) {
    if constexpr (I < N) { f(std::integral_constant<int, I>{}); static_for<I + 1, N>(f); }
}

#define G1_P_ 3
constexpr int G1_P = G1_P_, G1_PW = 5;
typedef __attribute__((address_space(1))) u32 gu32;
DI u32 ld_agent(const u32* p) { return __hip_atomic_load((gu32*)p, __ATOMIC_RELAXED, __HIP_MEMORY_SCOPE_AGENT); }
DI void add_agent(u32* p) { __hip_atomic_fetch_add((gu32*)p, 1u, __ATOMIC_RELAXED, __HIP_MEMORY_SCOPE_AGENT); }
DI void signal_all(char* ws, int which) {
    if (threadIdx.x == 0) {
        u32* c = (u32*)(ws + OFF_CNT + 16384 + which * 8192) + (blockIdx.x & 7) * 32;
#pragma unroll
        for (int r = 0; r < 8; ++r) add_agent(c + r * 256);
    }
}
#define POLL_SLEEP 16
DI void poll_until(const u32* p, u32 want, char* ws, int lane) {
    for (u32 spins = 0;; ++spins) {
        if (__all(ld_agent(p) >= want)) break;
        __builtin_amdgcn_s_sleep(POLL_SLEEP);
        if (spins > (1u << 18)) { if (lane == 0) __hip_atomic_store((gu32*)(ws + OFF_CNT + 49152), 1u, __ATOMIC_RELAXED, __HIP_MEMORY_SCOPE_AGENT); break; }
    }
}
DI void poll_all(char* ws, int which, u32 per_shard, int w, int lane) {
    if (w == 0) poll_until((const u32*)(ws + OFF_CNT + 16384 + which * 8192) + ((blockIdx.x >> 3) & 7) * 256 + (lane & 7) * 32, per_shard, ws, lane);
}

struct MainArgs { PrepArgs p; const u16* b1; u16* out; };
DI int img128(int row, int ch) { return row * 128 + ((ch ^ ((row >> 1) & 7)) << 4); }
template <bool LAST> DI void w1_prefetch(char* ws, char* smem, int w, int lane, int u0);

template <int L>
DI void col_task(char* ws, int b, char* smem, int u0) {
    const int tid = threadIdx.x & 255, lane = tid & 63, w = tid >> 6, half = threadIdx.x >> 8;
    const int j = 2 * b + half;
    char* Ure = smem + half * 24576; char* Uim = Ure + 4096; char* Ere = Ure + 8192; char* Eim = Ure + 12288;
    char* Rre = Ure + 16384; char* Rim = Ure + 20480;
    const int n = lane & 15, q = lane >> 4;
    const int wv = __builtin_amdgcn_readfirstlane(threadIdx.x >> 6);
    const rsrc_t rx = mk_rsrc(ws + (L == 0 ? OFF_XT0 : OFF_XT2), XT_BYTES), rs = mk_rsrc(ws + (L == 0 ? OFF_SS0 : OFF_SS2), 2048 * 8);
    u32x4 vr, vi, sv[4];
    auto load_in = [&]() __attribute__((always_inline)) {
        vr = __builtin_amdgcn_raw_buffer_load_b128(rx, (2 * j * S_ + tid * 8) * 2, 0, L == 0 ? 0 : SC1);
        vi = __builtin_amdgcn_raw_buffer_load_b128(rx, ((2 * j + 1) * S_ + tid * 8) * 2, 0, L == 0 ? 0 : SC1);
#pragma unroll
        for (int i = 0; i < 4; ++i) sv[i] = __builtin_amdgcn_raw_buffer_load_b128(rs, tid * 64 + 16 * i, 0, L == 0 ? 0 : SC1);
    };
    if (L == 0) load_in();
    u32x4 wf[2];
#pragma unroll
    for (int t = 0; t < 2; ++t) wf[t] = *(const u32x4*)((const u32*)(ws + OFF_WT) + ((size_t)j * 64 + 16 * w + n) * 32 + 16 * t + 4 * q);
    __builtin_amdgcn_sched_barrier(0);
    F32c f; f32c_init(f, lane);
    bf16x8 c32[2] = {f.c[0], f.c[1]}, s32p[2] = {f.sp[0], f.sp[1]}, s32m[2];
    pin(c32[0]); pin(c32[1]); pin(s32p[0]); pin(s32p[1]);
    bf16x8 c64[2], s64p[2], s64m[2];
    const float k1f = (float)(16 * w + n);
    {
        float c[8], s[8];
#pragma unroll
        for (int e = 0; e < 8; ++e) {
            const float ang = __builtin_amdgcn_fractf((float)(8 * q + e) * k1f * (1.0f / 64.0f));
            c[e] = cosr(ang); s[e] = sinr(ang);
        }
        c64[0] = pk8(c); s64p[0] = pk8(s);
        const u32 sg = (lane & 1) ? 0x80008000u : 0u;
        c64[1] = asbf(__builtin_bit_cast(u32x4, c64[0]) ^ sg); s64p[1] = asbf(__builtin_bit_cast(u32x4, s64p[0]) ^ sg);
        pin(c64[0]); pin(c64[1]); pin(s64p[0]); pin(s64p[1]);
    }
    float twr[8], twi[8], ivr[8], ivi[8];
#pragma unroll
    for (int i = 0; i < 8; ++i) {
        const float af = (float)(16 * (i >> 2) + 4 * q + (i & 3)) * k1f * (1.0f / 2048.0f);
        twr[i] = cosr(af); twi[i] = -sinr(af);
        const float ai = (float)(16 * w + 4 * q + (i & 3)) * (float)(16 * (i >> 2) + n) * (1.0f / 2048.0f);
        ivr[i] = cosr(ai) * (1.0f / 2048.0f); ivi[i] = sinr(ai) * (1.0f / 2048.0f);
        pin(twr[i]); pin(twi[i]); pin(ivr[i]); pin(ivi[i]);
    }
    __builtin_amdgcn_sched_barrier(0);
    STAMP(L ? 4 : 1, 1);
    if (L == 1) {
        poll_all(ws, 1, 32u, wv, lane);
        barrier();
        STAMP(4, 5);
        load_in();
        __builtin_amdgcn_sched_barrier(0);
    }
    s64m[0] = neg8(s64p[0]); s64m[1] = neg8(s64p[1]);
    {
        float r[8];
#pragma unroll
        for (int i = 0; i < 8; ++i) {
            const float s = (float)sv[i >> 1][2 * (i & 1) + 1] * 4294967296.0f + (float)sv[i >> 1][2 * (i & 1)];
            r[i] = __builtin_amdgcn_rsqf(s * (1.0f / (SS_SCALE * 640.0f)) + EPS);
        }
        const bf16x8 ur = pk8(bflo(vr[0]) * r[0], bfhi(vr[0]) * r[1], bflo(vr[1]) * r[2], bfhi(vr[1]) * r[3],
                              bflo(vr[2]) * r[4], bfhi(vr[2]) * r[5], bflo(vr[3]) * r[6], bfhi(vr[3]) * r[7]);
        bf16x8 ui = pk8(bflo(vi[0]) * r[0], bfhi(vi[0]) * r[1], bflo(vi[1]) * r[2], bfhi(vi[1]) * r[3],
                        bflo(vi[2]) * r[4], bfhi(vi[2]) * r[5], bflo(vi[3]) * r[6], bfhi(vi[3]) * r[7]);
        if (j == 304) ui = asbf(u32x4{0u, 0u, 0u, 0u});
        const int o = img128(tid >> 3, tid & 7);
        *(bf16x8*)(Ure + o) = ur; *(bf16x8*)(Uim + o) = ui;
        *(u32x4*)(Rre + o) = vr; *(u32x4*)(Rim + o) = vi;
    }
    __syncthreads();
    STAMP(L ? 4 : 1, 2);
    const f32x4 z = {0.f, 0.f, 0.f, 0.f};
    f32x4 dr[2], di[2];
#pragma unroll
    for (int mt = 0; mt < 2; ++mt) {
        dr[mt] = z; di[mt] = z;
#pragma unroll
        for (int ks = 0; ks < 2; ++ks) {
            const int o = img128(16 * mt + n, 4 * ks + q);
            const bf16x8 ar = *(const bf16x8*)(Ure + o), ai = *(const bf16x8*)(Uim + o);
            dr[mt] = mfma16(ar, c64[ks], dr[mt]); dr[mt] = mfma16(ai, s64p[ks], dr[mt]);
            di[mt] = mfma16(ar, s64m[ks], di[mt]); di[mt] = mfma16(ai, c64[ks], di[mt]);
        }
    }
    float tr[8], ti[8];
#pragma unroll
    for (int i = 0; i < 8; ++i) {
        const float x = dr[i >> 2][i & 3], y = di[i >> 2][i & 3];
        tr[i] = x * twr[i] - y * twi[i]; ti[i] = x * twi[i] + y * twr[i];
    }
    bf16x8 Tr = pk8(tr), Ti = pk8(ti);
    s32m[0] = neg8(s32p[0]); s32m[1] = neg8(s32p[1]);
    f32x4 orr[2], oi[2];
#pragma unroll
    for (int t = 0; t < 2; ++t) {
        orr[t] = mfma16(c32[t], Tr, z); orr[t] = mfma16(s32p[t], Ti, orr[t]);
        oi[t] = mfma16(s32m[t], Tr, z); oi[t] = mfma16(c32[t], Ti, oi[t]);
    }
#pragma unroll
    for (int i = 0; i < 8; ++i) {
        const float x = orr[i >> 2][i & 3], y = oi[i >> 2][i & 3];
        const u32 wv = wf[i >> 2][i & 3];
        const float wre = bflo(wv), wim = bfhi(wv);
        tr[i] = x * wre - y * wim; ti[i] = x * wim + y * wre;
    }
    Tr = pk8(tr); Ti = pk8(ti);
#pragma unroll
    for (int nt = 0; nt < 2; ++nt) {
        f32x4 zr = mfma16(Tr, c32[nt], z); zr = mfma16(Ti, s32m[nt], zr);
        f32x4 zi = mfma16(Tr, s32p[nt], z); zi = mfma16(Ti, c32[nt], zi);
        float er[4], ei[4];
#pragma unroll
        for (int r = 0; r < 4; ++r) {
            const float cr = ivr[4 * nt + r], ci = ivi[4 * nt + r];
            er[r] = zr[r] * cr - zi[r] * ci; ei[r] = zr[r] * ci + zi[r] * cr;
        }
        const int o = img128(16 * nt + n, 2 * w + (q >> 1)) + (q & 1) * 8;
        u32x2 pr = {pk2(er[0], er[1]), pk2(er[2], er[3])}, pi = {pk2(ei[0], ei[1]), pk2(ei[2], ei[3])};
        *(u32x2*)(Ere + o) = pr; *(u32x2*)(Eim + o) = pi;
    }
    __syncthreads();
    STAMP(L ? 4 : 1, 3);
    const rsrc_t rf = mk_rsrc(ws + (L == 0 ? OFF_XF1 : OFF_XF3), XT_BYTES);
    char* O = Ure;
#pragma unroll
    for (int nt = 0; nt < 2; ++nt) {
        f32x4 yr = z, yi = z;
#pragma unroll
        for (int ks = 0; ks < 2; ++ks) {
            const int o = img128(16 * nt + n, 4 * ks + q);
            const bf16x8 br = *(const bf16x8*)(Ere + o), bi = *(const bf16x8*)(Eim + o);
            yr = mfma16(c64[ks], br, yr); yr = mfma16(s64m[ks], bi, yr);
            yi = mfma16(s64p[ks], br, yi); yi = mfma16(c64[ks], bi, yi);
        }
        if (j == 304) yi = z;
        const int p = (16 * nt + n) * 64 + 16 * w + 4 * q;
        u32x2 rr[2], ri[2];
        { const int o = img128(16 * nt + n, 2 * w + (q >> 1)) + (q & 1) * 8; rr[nt] = *(const u32x2*)(Rre + o); ri[nt] = *(const u32x2*)(Rim + o); }
        const float fr[4] = {bflo(rr[nt][0]) + yr[0], bfhi(rr[nt][0]) + yr[1], bflo(rr[nt][1]) + yr[2], bfhi(rr[nt][1]) + yr[3]};
        const float fi[4] = {bflo(ri[nt][0]) + yi[0], bfhi(ri[nt][0]) + yi[1], bflo(ri[nt][1]) + yi[2], bfhi(ri[nt][1]) + yi[3]};
        u32x4 ov = {pk2(fr[0], fi[0]), pk2(fr[1], fi[1]), pk2(fr[2], fi[2]), pk2(fr[3], fi[3])};
        *(u32x4*)(O + p * 4) = ov;
    }
    __syncthreads();
    {
        const int c0 = 4 * b, dst = (((c0 >> 5) * 2 + ((c0 >> 2) & 1)) * 64 + ((c0 & 31) >> 3) * 16) * 8;
        const u32* O0 = (const u32*)smem; const u32* O1 = (const u32*)(smem + 24576);
#pragma unroll
        for (int i = 0; i < 4; ++i) {
            const int p = threadIdx.x + 512 * i;
            u32x2 v = {O0[p], O1[p]};
            __builtin_amdgcn_raw_buffer_store_b64(v, rf, (p >> 4) * 20480 + (p & 15) * 8, dst, SC1);
        }
    }
    STAMP(L ? 4 : 1, 4);
    wait_lgkm();
    wait_vm<0>();
    barrier();
    signal_all(ws, L == 0 ? 0 : 2);
    w1_prefetch<L == 1>(ws, smem, wv, lane, u0);
    STAMP(L ? 4 : 1, 15);
}

DI f32x2 gelu_tanh2(f32x2 x) {
    const float k1 = -2.302208198f, k2 = -0.1029432397f;
    const f32x2 c1 = {k1, k1}, c2 = {k2, k2}, one = {1.0f, 1.0f};
    const f32x2 t = x * (x * x * c2 + c1);
    f32x2 e = {__builtin_amdgcn_exp2f(t[0]), __builtin_amdgcn_exp2f(t[1])};
    e += one;
    const f32x2 r = {__builtin_amdgcn_rcpf(e[0]), __builtin_amdgcn_rcpf(e[1])};
    return x * r;
}
#define G2_PH_ 3
constexpr int G2_PH = G2_PH_;
constexpr int W2STG = 20480;
DI u32 w2slot(int t) { return 163840 - ((t & 7) + 1) * W2STG; }
template <bool LAST> constexpr int w1slot(int st) { return LAST ? 163840 - (st % 8 + 1) * 20480 : 163840 - (st + 1) * 14336; }

template <bool LAST, int I> DI void w1_piece(u32x4 srd, u32 lds0, int w, int vo, int u0, int st) {
    constexpr int WP = LAST ? 20 : 14;
    const int pc = w + 8 * I;
    const u32 dst = lds0 + w1slot<LAST>(st) + pc * 1024;
    const int so = (u0 + (pc >> 1)) * 20480 + st * 2048 + (pc & 1) * 1024;
    if (8 * I + 7 < WP) dma16<LAST>(srd, vo, so, dst); else dma16_lim<(WP & 7), LAST>(srd, vo, so, dst, w);
}
template <bool LAST> DI void w1_prefetch(char* ws, char* smem, int w, int lane, int u0) {
    const u32x4 srd = mk_srd(ws + OFF_W1F, W1F_BYTES);
    const u32 lds0 = (u32)(size_t)(LAS char*)smem;
#pragma unroll
    for (int st = 0; st < (LAST ? 8 : 10); ++st) {
        w1_piece<LAST, 0>(srd, lds0, w, lane * 16, u0, st);
        w1_piece<LAST, 1>(srd, lds0, w, lane * 16, u0, st);
        if (LAST) w1_piece<LAST, 2>(srd, lds0, w, lane * 16, u0, st);
    }
}

constexpr int g1_younger(int ssync, int t, int nlo) {
    int cnt = 0; bool seen = false;
    for (int s = 0; s < ssync; ++s) {
        if (s + G1_PW < 10 && s + G1_PW >= 8) { if (seen) cnt += nlo; if (s + G1_PW == t) seen = true; }
        if (s + G1_P < 10 && seen) cnt += 4;
    }
    return cnt;
}

template <int I> DI void w2_issue1(u32x4 srd, u32 lds0, int w, int vo, int ct, int t) {
    const int pc = w + 8 * I, q = pc / 5, cu = pc - 5 * q;
    const int so = ((5 * ct + cu) * 54 + (q < 2 ? 14 * q : 28 + 13 * (q - 2)) + t) * 1024;
    if (I < 2) dma16<true>(srd, vo, so, lds0 + w2slot(t) + pc * 1024); else dma16_lim<4, true>(srd, vo, so, lds0 + w2slot(t) + pc * 1024, w);
}
DI void w2_issue(u32x4 srd, u32 lds0, int w, int vo, int ct, int t) {
    w2_issue1<0>(srd, lds0, w, vo, ct, t); w2_issue1<1>(srd, lds0, w, vo, ct, t); w2_issue1<2>(srd, lds0, w, vo, ct, t);
}

template <int NF, bool LAST, int KG>
DI void g1_phase(const MainArgs& a, char* smem, int w, int lane, int pt, int u0, int nu, int ct2) {
    constexpr int P = G1_P, PW = G1_PW;
    constexpr int WP = 2 * NF, WR = (WP + 7) / 8, NLO = WP / 8, KID = LAST ? 5 : 2;
    char* ws = a.p.ws;
    const int pq = w & 3, g = lane >> 4;
    const u32x4 srdW = mk_srd(ws + OFF_W1F, W1F_BYTES), srd2 = mk_srd(ws + (LAST ? OFF_W2O : OFF_W2F), W2F_BYTES);
    const u32 lds0 = (u32)(size_t)(LAS char*)smem;
    const rsrc_t rx = mk_rsrc(ws + (LAST ? OFF_XF3 : OFF_XF1), XT_BYTES);
    const int xs = (8 * pt + 2 * pq) * 20480 + KG * 1024, vo = lane * 16;
    auto xload = [&](int st, int v) __attribute__((always_inline)) {
        return __builtin_amdgcn_raw_buffer_load_b64(rx, lane * 8, xs + (v >> 1) * 20480 + st * 2048 + (v & 1) * 512, SC1);
    };
    u32x2 xh[P][4];
#pragma unroll
    for (int st = 0; st < P; ++st) {
#pragma unroll
        for (int v = 0; v < 4; ++v) xh[st][v] = xload(st, v);
        __builtin_amdgcn_sched_barrier(0);
    }
    auto xfrag = [&](int st, int pu) __attribute__((always_inline)) { return asbf(u32x4{xh[st % P][2 * pu][0], xh[st % P][2 * pu][1], xh[st % P][2 * pu + 1][0], xh[st % P][2 * pu + 1][1]}); };
    f32x4 acc[NF][2];
#pragma unroll
    for (int i = 0; i < NF; ++i) { acc[i][0] = f32x4{0, 0, 0, 0}; acc[i][1] = f32x4{0, 0, 0, 0}; }
    f32x4 gr[2] = {f32x4{0, 0, 0, 0}, f32x4{0, 0, 0, 0}};
    bf16x8 wa[2][NF];
#pragma unroll
    for (int i = 0; i < NF; ++i) wa[0][i] = *(const bf16x8*)(smem + w1slot<LAST>(0) + KG * 1024 + lane * 16 + i * 2048);
    static_for<0, 10>([&](auto ST) __attribute__((always_inline)) {
        constexpr int st = decltype(ST)::value;
        if constexpr (LAST && st == 2) { wait_lgkm(); barrier(); }
        if constexpr (st == 4 || st == 7) {
            wait_lgkm();
            constexpr int nx4 = 4 * P + 4 * 4, nx7 = 4 * P + 4 * (10 - P < 7 ? 10 - P : 7);
            if constexpr (st == 4) wait_vm<(LAST ? NLO : 2 * NLO) + nx4>();
            else if constexpr (LAST) wait_vm<g1_younger(7, 9, NLO)>();
            else wait_vm<nx7>();
            barrier();
        }
        u32x2 nx[4];
        const char* rbase = smem + w1slot<LAST>(st + 1) + KG * 1024 + lane * 16;
        constexpr int NV = WR + 4, V0 = KG == 0 ? 0 : NF - NV;
        static_for<0, NF>([&](auto I) __attribute__((always_inline)) {
            constexpr int i = decltype(I)::value;
            __builtin_amdgcn_sched_barrier(0);
            acc[i][0] = mfma16(wa[st & 1][i], xfrag(st, 0), acc[i][0]);
            if constexpr (st + 1 < 10) wa[(st + 1) & 1][i] = *(const bf16x8*)(rbase + i * 2048);
            acc[i][1] = mfma16(wa[st & 1][i], xfrag(st, 1), acc[i][1]);
            if constexpr (i < 2) {
                u32x4 xv = __builtin_bit_cast(u32x4, xfrag(st, i));
                if constexpr (st == 9 && KG == 1) { if (g == 0) xv[0] &= 0x0000ffffu; }
                gr[i] = mfma16(asbf(xv), asbf(xv), gr[i]);
            }
            __builtin_amdgcn_sched_barrier(0);
            constexpr int v = i - V0;
            if constexpr (v >= 0 && v < WR) { if constexpr (LAST && st + PW >= 8 && st + PW < 10) w1_piece<LAST, v>(srdW, lds0, w, vo, u0, st + PW); }
            else if constexpr (v >= WR && v < NV) { if constexpr (st + P < 10) nx[v - WR] = xload(st + P, v - WR); }
            if constexpr (st >= 6) {
                constexpr int sl = i - (KG == 0 ? 0 : NF - 4), e = 4 * (st - 6) + sl;
                if constexpr (sl >= 0 && sl < 4 && e < (LAST ? 9 : 15)) w2_issue1<e % 3>(srd2, lds0, w, vo, ct2, (LAST ? 2 : 0) + e / 3);
            }
        });
        __builtin_amdgcn_sched_barrier(0);
        if constexpr (st + P < 10) { xh[st % P][0] = nx[0]; xh[st % P][1] = nx[1]; xh[st % P][2] = nx[2]; xh[st % P][3] = nx[3]; }
        if (st == 4) STAMP(KID, 2);
    });
    STAMP(KID, 3);
    u32x2 bv[NF];
#pragma unroll
    for (int i = 0; i < NF; ++i) bv[i] = *(const u32x2*)(a.b1 + min(16 * (u0 + i), F_ - 16) + 4 * g);
    __builtin_amdgcn_sched_barrier(0);
    wait_lgkm();
    barrier();
    float* sx = (float*)(smem + 8 * NF * 512);
#pragma unroll
    for (int i = 0; i < NF; ++i) {
        const f32x4 t = acc[i][1 - KG];
        u32x2 pv = {pk2(t[0], t[1]), pk2(t[2], t[3])};
        *(u32x2*)(smem + ((w * NF + i) * 64 + lane) * 8) = pv;
    }
    float st2[2];
    {
        const int n = lane & 15;
#pragma unroll
        for (int pu = 0; pu < 2; ++pu) {
            const float d01 = (n & 1) ? gr[pu][1] : gr[pu][0], d23 = (n & 1) ? gr[pu][3] : gr[pu][2];
            float d = (n & 2) ? d23 : d01;
            d = (g == (n >> 2)) ? d : 0.f;
            d += __shfl_xor(d, 16); d += __shfl_xor(d, 32);
            st2[pu] = d;
        }
    }
    if (lane < 16) sx[w * 16 + lane] = st2[1 - KG];
    wait_lgkm();
    barrier();
    STAMP(KID, 5);
    const float v = (st2[KG] + sx[(w ^ 4) * 16 + (lane & 15)]) * (1.0f / 640.0f);
    const float r1 = __builtin_amdgcn_rsqf(v + EPS);
    const float rr = r1 * __builtin_amdgcn_rsqf(v * r1 * r1 + EPS);
    const int punit = 8 * pt + 2 * pq + KG;
    u32x2 ov[NF], pv[NF];
#pragma unroll
    for (int i = 0; i < NF; ++i) pv[i] = *(const u32x2*)(smem + (((w ^ 4) * NF + i) * 64 + lane) * 8);
    static_for<0, NF>([&](auto I) __attribute__((always_inline)) {
        constexpr int i = decltype(I)::value;
        if constexpr (LAST ? i < 2 : i == 0) {
            __builtin_amdgcn_sched_barrier(0);
            w2_issue(srd2, lds0, w, vo, ct2, LAST ? i : 5);
            __builtin_amdgcn_sched_barrier(0);
        }
        const f32x2 o0 = {acc[i][KG][0] + bflo(pv[i][0]), acc[i][KG][1] + bfhi(pv[i][0])}, o1 = {acc[i][KG][2] + bflo(pv[i][1]), acc[i][KG][3] + bfhi(pv[i][1])};
        const f32x2 rr2 = {rr, rr};
        const f32x2 b0 = {bflo(bv[i][0]), bfhi(bv[i][0])}, b1 = {bflo(bv[i][1]), bfhi(bv[i][1])};
        f32x2 h0 = gelu_tanh2(o0 * rr2 + b0), h1 = gelu_tanh2(o1 * rr2 + b1);
        if (LAST) {
            const bool lin = u0 + i >= 108;
            h0[0] = lin ? o0[0] : h0[0]; h0[1] = lin ? o0[1] : h0[1]; h1[0] = lin ? o1[0] : h1[0]; h1[1] = lin ? o1[1] : h1[1];
        }
        ov[i] = u32x2{pk2(h0[0], h0[1]), pk2(h1[0], h1[1])};
    });
    __builtin_amdgcn_sched_barrier(0);
    const rsrc_t rh = mk_rsrc(ws + (LAST ? OFF_H2 : OFF_H), H_BYTES), r3 = mk_rsrc(ws + OFF_X3, X3_BYTES);
    const int hb = punit * 54 * 1024;
    auto st1 = [&](int i) __attribute__((always_inline)) {
        const int u = u0 + i;
        if (i < nu) {
            if (!LAST || u < 108) __builtin_amdgcn_raw_buffer_store_b64(ov[i], rh, lane * 16 + (u & 1) * 8, hb + (u >> 1) * 1024, SC1);
            else __builtin_amdgcn_raw_buffer_store_b64(ov[i], r3, lane * 8, (punit * 40 + (u - 108)) * 512, SC1);
        }
    };
    auto st2p = [&](int i) __attribute__((always_inline)) {
        const int u = u0 + i;
        if (i + 1 < nu && (!LAST || u + 1 < 108)) {
            u32x4 q4 = {ov[i][0], ov[i][1], ov[i + 1][0], ov[i + 1][1]};
            __builtin_amdgcn_raw_buffer_store_b128(q4, rh, lane * 16, hb + (u >> 1) * 1024, SC1);
        } else { st1(i); st1(i + 1); }
    };
    if (u0 & 1) {
        st1(0);
#pragma unroll
        for (int i = 1; i + 1 < NF; i += 2) st2p(i);
        if ((NF & 1) == 0) st1(NF - 1);
    } else {
#pragma unroll
        for (int i = 0; i + 1 < NF; i += 2) st2p(i);
        if (NF & 1) st1(NF - 1);
    }
    STAMP(KID, 4);
}

template <bool LAST>
DI void g2_phase(const MainArgs& a, char* smem, int w, int lane, int pt1, int pt, int ct) {
    constexpr int PH = G2_PH, KID = LAST ? 6 : 3;
    char* ws = a.p.ws;
    u32* cnt = (u32*)(ws + OFF_CNT + (LAST ? 8192 : 0));
    const int pg = w & 1, kq = w >> 1, n = lane & 15, q = lane >> 4;
    const int kb = kq < 2 ? 14 * kq : 28 + 13 * (kq - 2), nk = kq < 2 ? 14 : 13;
    const u32x4 srd2 = mk_srd(ws + (LAST ? OFF_W2O : OFF_W2F), W2F_BYTES);
    const u32 lds0 = (u32)(size_t)(LAS char*)smem;
    const rsrc_t rh = mk_rsrc(ws + (LAST ? OFF_H2 : OFF_H), H_BYTES), rw = mk_rsrc(ws + (LAST ? OFF_W2O : OFF_W2F), W2F_BYTES);
    const int vo = lane * 16, hs = ((4 * pt + 2 * pg) * 54 + kb) * 1024;
    bf16x8 wr[6][5];
    asm volatile("" ::: "memory");
    __builtin_amdgcn_sched_barrier(0);
#pragma unroll
    for (int t = 0; t < 3; ++t)
#pragma unroll
        for (int i = 0; i < 5; ++i) wr[t][i] = asbf(__builtin_amdgcn_raw_buffer_load_b128(rw, vo, ((5 * ct + i) * 54 + kb + 8 + t) * 1024, SC1));
    __builtin_amdgcn_sched_barrier(0);
    wait_lgkm();
    wait_vm<15>();
    barrier();
    if (threadIdx.x == 0) add_agent(cnt + 64 * pt1);
    STAMP(LAST ? 5 : 2, 15);
#pragma unroll
    for (int t = LAST ? 5 : 6; t < 8; ++t) w2_issue(srd2, lds0, w, vo, ct, t);
    __builtin_amdgcn_sched_barrier(0);
#pragma unroll
    for (int t = 3; t < 6; ++t)
#pragma unroll
        for (int i = 0; i < 5; ++i) wr[t][i] = asbf(__builtin_amdgcn_raw_buffer_load_b128(rw, vo, ((5 * ct + i) * 54 + kb + 8 + t) * 1024, SC1));
    __builtin_amdgcn_sched_barrier(0);
    if (w == 0) poll_until(cnt + 64 * (pt >> 1), 16u, ws, lane);
    wait_vm<0>();
    barrier();
    STAMP(KID, 5);
    bf16x8 hb[PH][2];
    auto hload = [&](int t, int pu) __attribute__((always_inline)) {
        const int k = t < 13 ? t : nk - 1;
        return asbf(__builtin_amdgcn_raw_buffer_load_b128(rh, vo, hs + (pu * 54 + k) * 1024, SC1));
    };
#pragma unroll
    for (int t = 0; t < PH; ++t) { hb[t][0] = hload(t, 0); hb[t][1] = hload(t, 1); __builtin_amdgcn_sched_barrier(0); }
    u32x2 xv[3], bv[3]; float bb[3];
    f32x4 acc[2][5];
#pragma unroll
    for (int i = 0; i < 5; ++i) { acc[0][i] = f32x4{0, 0, 0, 0}; acc[1][i] = f32x4{0, 0, 0, 0}; }
    bf16x8 wa[2][5];
    const char* wbase = smem + kq * 5120 + lane * 16;
#pragma unroll
    for (int i = 0; i < 5; ++i) wa[0][i] = *(const bf16x8*)(wbase + w2slot(0) + i * 1024);
    static_for<0, 14>([&](auto T) __attribute__((always_inline)) {
        constexpr int t = decltype(T)::value;
        if constexpr (t == 10) {
            __builtin_amdgcn_sched_barrier(0);
#pragma unroll
            for (int j = 0; j < 3; ++j) {
                const int tt = min(kq + 4 * j, 9), pu = 2 * pg + tt / 5, cu = tt % 5;
                if (!LAST) {
                    const int c = 80 * ct + 16 * cu + n;
                    const int off = ((((4 * pt + pu) * 20 + (c >> 5)) * 2 + ((c >> 2) & 1)) * 64 + ((c & 31) >> 3) * 16 + 4 * q) * 8 + (c & 3) * 2;
                    const rsrc_t r1 = mk_rsrc(ws + OFF_XF1, XT_BYTES);
                    const u32 e0 = __builtin_amdgcn_raw_buffer_load_b16(r1, off, 0, SC1), e1 = __builtin_amdgcn_raw_buffer_load_b16(r1, off + 8, 0, SC1);
                    const u32 e2 = __builtin_amdgcn_raw_buffer_load_b16(r1, off + 16, 0, SC1), e3 = __builtin_amdgcn_raw_buffer_load_b16(r1, off + 24, 0, SC1);
                    xv[j] = u32x2{e0 | (e1 << 16), e2 | (e3 << 16)};
                    bb[j] = ((const float*)(ws + OFF_B2P))[c];
                } else {
                    xv[j] = __builtin_amdgcn_raw_buffer_load_b64(mk_rsrc(ws + OFF_X3, X3_BYTES), lane * 8, ((4 * pt + pu) * 40 + 5 * ct + cu) * 512, SC1);
                    bv[j] = *(const u32x2*)(a.p.b2 + 80 * ct + 16 * cu + 4 * q);
                }
            }
        }
        bf16x8 nh[2];
        auto step = [&]() __attribute__((always_inline)) {
            static_for<0, 5>([&](auto I) __attribute__((always_inline)) {
                constexpr int i = decltype(I)::value;
                __builtin_amdgcn_sched_barrier(0);
                bf16x8 wv;
                if constexpr (t < 8) wv = wa[t & 1][i]; else wv = wr[t - 8][i];
                acc[0][i] = LAST ? mfma16(wv, hb[t % PH][0], acc[0][i]) : mfma16(hb[t % PH][0], wv, acc[0][i]);
                if constexpr (t + 1 < 8) wa[(t + 1) & 1][i] = *(const bf16x8*)(wbase + w2slot(t + 1) + i * 1024);
                acc[1][i] = LAST ? mfma16(wv, hb[t % PH][1], acc[1][i]) : mfma16(hb[t % PH][1], wv, acc[1][i]);
                __builtin_amdgcn_sched_barrier(0);
                if constexpr (t + PH < 14 && (i == 1 || i == 3)) nh[i >> 1] = hload(t + PH, i >> 1);
            });
        };
        if constexpr (t < 13) step(); else { if (nk == 14) step(); }
        __builtin_amdgcn_sched_barrier(0);
        if constexpr (t + PH < 14) { hb[t % PH][0] = nh[0]; hb[t % PH][1] = nh[1]; }
        if (t == 0) STAMP(KID, 1);
        if (t == 7) STAMP(KID, 2);
    });
    STAMP(KID, 3);
    wait_lgkm();
    barrier();
#pragma unroll
    for (int t = 0; t < 10; ++t)
        if ((t & 3) != kq) *(f32x4*)(smem + ((w * 10 + t) * 64 + lane) * 16) = acc[t / 5][t % 5];
    wait_lgkm();
    barrier();
    float ssp[2][4];
#pragma unroll
    for (int r = 0; r < 4; ++r) { ssp[0][r] = 0.f; ssp[1][r] = 0.f; }
    const rsrc_t ro = mk_rsrc(ws + OFF_XT2, XT_BYTES);
    auto finish = [&](auto KQ) __attribute__((always_inline)) {
        constexpr int kqc = decltype(KQ)::value;
        static_for<0, 3>([&](auto J) __attribute__((always_inline)) {
            constexpr int j = decltype(J)::value, t = kqc + 4 * j;
            if constexpr (t < 10) {
                constexpr int pl = t / 5, cu = t % 5;
                f32x4 o = acc[pl][cu];
#pragma unroll
                for (int s = 1; s < 4; ++s) o += *(const f32x4*)(smem + (((pg + 2 * ((kqc + s) & 3)) * 10 + t) * 64 + lane) * 16);
                const int pu = 2 * pg + pl;
                if (!LAST) {
                    const int c = 80 * ct + 16 * cu + n, p = 64 * pt + 16 * pu + 4 * q;
                    const u32 o0 = pk2(bflo(xv[j][0]) + o[0] + bb[j], bfhi(xv[j][0]) + o[1] + bb[j]);
                    const u32 o1 = pk2(bflo(xv[j][1]) + o[2] + bb[j], bfhi(xv[j][1]) + o[3] + bb[j]);
                    u32x2 ov = {o0, o1};
                    __builtin_amdgcn_raw_buffer_store_b64(ov, ro, (c * S_ + p) * 2, 0, SC1);
                    const float m = c == 609 ? 0.f : 1.f;
                    ssp[pl][0] += m * bflo(o0) * bflo(o0); ssp[pl][1] += m * bfhi(o0) * bfhi(o0);
                    ssp[pl][2] += m * bflo(o1) * bflo(o1); ssp[pl][3] += m * bfhi(o1) * bfhi(o1);
                } else {
                    const int p = 16 * (4 * pt + pu) + n, d = 80 * ct + 16 * cu + 4 * q;
                    u32x2 ov = {pk2(bflo(xv[j][0]) + o[0] + bflo(bv[j][0]), bfhi(xv[j][0]) + o[1] + bfhi(bv[j][0])),
                                pk2(bflo(xv[j][1]) + o[2] + bflo(bv[j][1]), bfhi(xv[j][1]) + o[3] + bfhi(bv[j][1]))};
                    *(u32x2*)(a.out + (size_t)tok_of_p(p) * D_ + d) = ov;
                }
            }
        });
    };
    if (kq == 0) finish(std::integral_constant<int, 0>{});
    else if (kq == 1) finish(std::integral_constant<int, 1>{});
    else if (kq == 2) finish(std::integral_constant<int, 2>{});
    else finish(std::integral_constant<int, 3>{});
    if (!LAST) {
        float* sp = (float*)smem;
#pragma unroll
        for (int pl = 0; pl < 2; ++pl)
#pragma unroll
            for (int r = 0; r < 4; ++r) {
                const float v = row_sum15(ssp[pl][r]);
                if (n == 15) sp[w * 32 + 16 * pl + 4 * q + r] = v;
            }
        wait_lgkm();
        barrier();
        if (w == 0) {
            const int g2 = lane >> 5, r = lane & 31;
            const float v = (sp[g2 * 32 + r] + sp[(g2 + 2) * 32 + r]) + (sp[(g2 + 4) * 32 + r] + sp[(g2 + 6) * 32 + r]);
            __hip_atomic_fetch_add((__attribute__((address_space(1))) u64*)(ws + OFF_SS2) + 64 * pt + lane, ss_from_float(v), __ATOMIC_RELAXED, __HIP_MEMORY_SCOPE_AGENT);
        }
        wait_lgkm();
        wait_vm<0>();
        barrier();
        signal_all(ws, 1);
    }
}

template <bool LAST>
DI void mlp(const MainArgs& a, char* smem, int w, int lane, int u0) {
    constexpr int NF = LAST ? 10 : 7;
    const int x = blockIdx.x & 7, ib = blockIdx.x >> 3;
    const int pt = 4 * (x >> 1) + (ib & 3), ft = 8 * (x & 1) + (ib >> 2);
    const int pt2 = 8 * (x >> 1) + (ib & 7), ct2 = 4 * (x & 1) + (ib >> 3);
    const int nu = LAST ? (ft < 4 ? 10 : 9) : (ft < 12 ? 7 : 6);
    if (LAST) poll_all(a.p.ws, 2, 20u, w, lane);
    else if (w == 0) {
        const int sh = lane & 7, wh = (lane >> 3) & 1;
        poll_until((const u32*)(a.p.ws + OFF_CNT + 16384 + (wh ? 3 : 0) * 8192) + ((blockIdx.x >> 3) & 7) * 256 + sh * 32, wh ? (sh < 6 ? 12u : 11u) : 20u, a.p.ws, lane);
    }
    wait_vm<(LAST ? 6 : 5)>();
    barrier();
    STAMP(LAST ? 5 : 2, 1);
    if (w >= 4) __builtin_amdgcn_s_setprio(1);
    if (w < 4) g1_phase<NF, LAST, 0>(a, smem, w, lane, pt, u0, nu, ct2);
    else g1_phase<NF, LAST, 1>(a, smem, w, lane, pt, u0, nu, ct2);
    __builtin_amdgcn_s_setprio(0);
    g2_phase<LAST>(a, smem, w, lane, pt, pt2, ct2);
}

__global__ __launch_bounds__(512, 2) void k_main(MainArgs a) {
    extern __shared__ __attribute__((aligned(16))) char smem[];
    const int lane = threadIdx.x & 63, b = blockIdx.x;
    const int w = __builtin_amdgcn_readfirstlane(threadIdx.x >> 6);
    const int ft = 8 * (b & 1) + (b >> 5);
    const int u0a = ft < 12 ? 7 * ft : 84 + 6 * (ft - 12), u0b = ft < 4 ? 10 * ft : 40 + 9 * (ft - 4);
    if (w == 7 && b < 254) {
        const u32x4 sb1 = mk_srd(a.b1, F_ * 2), sb2 = mk_srd(a.p.b2, D_ * 2);
        const u32 dst = (u32)(size_t)(LAS char*)smem + 53248;
#pragma unroll
        for (int i = 0; i < 4; ++i) dma16(sb1, lane * 16, i * 1024, dst + i * 1024);
        dma16(sb2, lane * 16, 0, dst + 4096); dma16(sb2, lane * 16, 1024, dst + 5120);
    }
    if (b < 160) col_task<0>(a.p.ws, b, smem, u0a);
    else {
        if (b < 254) {
            if (b < 214) task_w2(a.p, b - 160, smem); else task_Q(a.p, b - 214);
            wait_lgkm();
            wait_vm<0>();
            barrier();
            signal_all(a.p.ws, 3);
        }
        w1_prefetch<false>(a.p.ws, smem, w, lane, u0a);
    }
    mlp<false>(a, smem, w, lane, u0a);
    END_STAMP(3);
    if (b < 160) col_task<1>(a.p.ws, b, smem, u0b); else w1_prefetch<true>(a.p.ws, smem, w, lane, u0b);
    mlp<true>(a, smem, w, lane, u0b);
    END_STAMP(6);
}

extern "C" void kernel_launch(void* const* d_in, const int* in_sizes, int n_in, void* d_out, int out_size, void* d_ws,
                              size_t ws_size, hipStream_t stream) {
    char* ws = (char*)d_ws;
    constexpr int MAIN_LDS = 163840;
    (void)hipFuncSetAttribute((const void*)k_main, hipFuncAttributeMaxDynamicSharedMemorySize, MAIN_LDS);
    PrepArgs pa{(const u16*)d_in[0], (const u16*)d_in[1], (const u16*)d_in[2], (const u16*)d_in[4], (const u16*)d_in[6], (const u16*)d_in[7], ws};
    k0_prep<<<NB_K0, 512, PREP_LDS, stream>>>(pa);
    MainArgs ma{pa, (const u16*)d_in[5], (u16*)d_out};
    k_main<<<256, 512, MAIN_LDS, stream>>>(ma);
}
```

```cpp
#include <hip/hip_runtime.h>
#include <hip/hip_bf16.h>
#include <stdint.h>
#include <type_traits>

typedef unsigned short u16;
typedef unsigned int u32;
typedef unsigned long long u64;
typedef short s16x4 __attribute__((ext_vector_type(4)));
typedef __bf16 bf16x8 __attribute__((ext_vector_type(8)));
typedef __bf16 bf16x2 __attribute__((ext_vector_type(2)));
typedef float f32x4 __attribute__((ext_vector_type(4)));
typedef float f32x2 __attribute__((ext_vector_type(2)));
typedef u32 u32x4 __attribute__((ext_vector_type(4)));
typedef u32 u32x2 __attribute__((ext_vector_type(2)));
typedef __amdgpu_buffer_rsrc_t rsrc_t;
#define LAS __attribute__((address_space(3)))
#define DI __device__ __forceinline__

constexpr int S_ = 2048, D_ = 640, F_ = 1728, DF_ = 321;
constexpr float EPS = 1e-5f;
constexpr int SC1 = 16;
constexpr float SS_SCALE = 1048576.0f;

constexpr size_t XT_BYTES = (size_t)D_ * S_ * 2;
constexpr size_t OFF_XT0 = 0;
constexpr size_t OFF_XT2 = OFF_XT0 + XT_BYTES;
constexpr size_t OFF_WT = OFF_XT2 + XT_BYTES;
constexpr size_t OFF_W1F = OFF_WT + (size_t)320 * 2048 * 4;
constexpr size_t W1F_BYTES = (size_t)148 * 20 * 1024;
constexpr size_t OFF_W2F = OFF_W1F + W1F_BYTES;
constexpr size_t W2F_BYTES = (size_t)40 * 54 * 1024;
constexpr size_t OFF_W2O = OFF_W2F + W2F_BYTES;
constexpr size_t OFF_H = OFF_W2O + W2F_BYTES;
constexpr size_t H_BYTES = (size_t)128 * 54 * 1024;
constexpr size_t OFF_H2 = OFF_H;
constexpr size_t OFF_X3 = OFF_H + H_BYTES;
constexpr size_t OFF_SS0 = OFF_X3 + (size_t)128 * 40 * 512;
constexpr size_t OFF_SS2 = OFF_SS0 + 2048 * 8;
constexpr size_t OFF_B2P = OFF_SS2 + 2048 * 8;
constexpr size_t OFF_XF1 = OFF_B2P + 4096;
constexpr size_t OFF_XF3 = OFF_XF1;
constexpr size_t X3_BYTES = (size_t)128 * 40 * 512;
constexpr size_t OFF_CNT = OFF_XF1 + XT_BYTES;
constexpr size_t WS_END = OFF_CNT + 65536;

DI f32x4 mfma16(bf16x8 a, bf16x8 b, f32x4 c) { return __builtin_amdgcn_mfma_f32_16x16x32_bf16(a, b, c, 0, 0, 0); }
DI u32 pk2(float a, float b) { f32x2 v = {a, b}; const bf16x2 r = __builtin_convertvector(v, bf16x2); return __builtin_bit_cast(u32, r); }
DI bf16x8 pk8(float a0, float a1, float a2, float a3, float a4, float a5, float a6, float a7) {
    u32x4 r = {pk2(a0, a1), pk2(a2, a3), pk2(a4, a5), pk2(a6, a7)};
    return __builtin_bit_cast(bf16x8, r);
}
DI bf16x8 pk8(const float (&a)[8]) { return pk8(a[0], a[1], a[2], a[3], a[4], a[5], a[6], a[7]); }
DI bf16x8 asbf(u32x4 r) { return __builtin_bit_cast(bf16x8, r); }
DI bf16x8 neg8(bf16x8 a) { u32x4 r = __builtin_bit_cast(u32x4, a); r ^= 0x80008000u; return __builtin_bit_cast(bf16x8, r); }
DI float bflo(u32 v) { return __builtin_bit_cast(float, v << 16); }
DI float bfhi(u32 v) { return __builtin_bit_cast(float, v & 0xffff0000u); }
DI float cosr(float rev) { return __builtin_amdgcn_cosf(rev); }
DI float sinr(float rev) { return __builtin_amdgcn_sinf(rev); }
DI s16x4 tr_read(const char* p) { return __builtin_amdgcn_ds_read_tr16_b64_v4i16((LAS s16x4*)p); }
DI bf16x8 cat8(s16x4 lo, s16x4 hi) { return __builtin_bit_cast(bf16x8, __builtin_shufflevector(lo, hi, 0, 1, 2, 3, 4, 5, 6, 7)); }
DI int tok_of_p(int p) { return 32 * (p & 63) + (p >> 6); }
DI rsrc_t mk_rsrc(const void* p, size_t bytes) { return __builtin_amdgcn_make_buffer_rsrc((void*)p, 0, (int)bytes, 0x00020000); }
DI u32x4 bload16(rsrc_t r, int voff, int soff) { return __builtin_amdgcn_raw_buffer_load_b128(r, voff, soff, 0); }
DI u64 ss_from_float(float s) { return (u64)(fminf(s * SS_SCALE, 1.0e18f) + 0.5f); }
DI void pin(float& x) { asm volatile("" : "+v"(x)); }
DI void pin(bf16x8& x) { asm volatile("" : "+v"(x)); }

struct F32c { bf16x8 c[2], sp[2]; };
DI void f32c_init(F32c& k, int lane) {
    const float m = (float)(lane & 15), q4 = (float)(4 * (lane >> 4));
    float c[4], s[4];
#pragma unroll
    for (int j = 0; j < 4; ++j) { const float a = __builtin_amdgcn_fractf(m * (q4 + (float)j) * (1.0f / 32.0f)); c[j] = cosr(a); s[j] = sinr(a); }
    const u32 sg = (lane & 1) ? 0x80008000u : 0u;
    const u32 c0 = pk2(c[0], c[1]), c1 = pk2(c[2], c[3]), s0 = pk2(s[0], s[1]), s1 = pk2(s[2], s[3]);
    const u32x4 cv = {c0, c1, c0 ^ sg, c1 ^ sg}, sv = {s0, s1, s0 ^ sg, s1 ^ sg};
    k.c[0] = asbf(cv); k.sp[0] = asbf(sv);
    k.c[1] = asbf(cv ^ 0x80000000u); k.sp[1] = asbf(sv ^ 0x80000000u);
}

struct RowC { bf16x8 c[2], sp[2], sm[2], c20, s20m; f32x4 twr[2], twi[2]; int off0, off1; };
constexpr int ROWC_LDS = 10 * 1024;
DI void rowc_build(char* tab, int w, int lane) {
    const int n = lane & 15, q = lane >> 4;
    const float nf = (float)n;
    u32x4* t = (u32x4*)tab + lane;
    if (w == 0) {
        F32c f; f32c_init(f, lane);
        t[0] = __builtin_bit_cast(u32x4, f.c[0]); t[64] = __builtin_bit_cast(u32x4, f.c[1]);
        t[128] = __builtin_bit_cast(u32x4, f.sp[0]); t[192] = __builtin_bit_cast(u32x4, f.sp[1]);
    } else if (w <= 2) {
        float c[8];
#pragma unroll
        for (int j = 0; j < 8; ++j) {
            const int d1 = 8 * q + j;
            const float a = __builtin_amdgcn_fractf((float)d1 * nf * 0.05f);
            const float v = w == 1 ? cosr(a) : -sinr(a);
            c[j] = (d1 < 20 && n <= 10) ? v : 0.f;
        }
        t[(3 + w) * 64] = __builtin_bit_cast(u32x4, pk8(c));
    } else if (w <= 4) {
        const float sc = 0.05590169943749474f;
        f32x4 cr, ci;
#pragma unroll
        for (int i = 0; i < 4; ++i) {
            const float a = (float)(16 * (w - 3) + 4 * q + i) * nf * (1.0f / 640.0f);
            cr[i] = cosr(a) * sc; ci[i] = -sinr(a) * sc;
        }
        t[(6 + w - 3) * 64] = __builtin_bit_cast(u32x4, cr); t[(8 + w - 3) * 64] = __builtin_bit_cast(u32x4, ci);
    }
}
DI void rowc_load(RowC& k, const char* tab, int lane) {
    const u32x4* tb = (const u32x4*)tab + lane;
    k.c[0] = asbf(tb[0]); k.c[1] = asbf(tb[64]); k.sp[0] = asbf(tb[128]); k.sp[1] = asbf(tb[192]);
    k.c20 = asbf(tb[256]); k.s20m = asbf(tb[320]);
    k.twr[0] = __builtin_bit_cast(f32x4, tb[384]); k.twr[1] = __builtin_bit_cast(f32x4, tb[448]);
    k.twi[0] = __builtin_bit_cast(f32x4, tb[512]); k.twi[1] = __builtin_bit_cast(f32x4, tb[576]);
    k.sm[0] = neg8(k.sp[0]); k.sm[1] = neg8(k.sp[1]);
    const int q = lane >> 4, qq = (lane & 15) >> 2, pp = lane & 3;
    const int r0 = min(8 * q + qq, 19), r1 = min(8 * q + 4 + qq, 19);
    k.off0 = r0 * 64 + pp * 8; k.off1 = r1 * 64 + pp * 8;
}
DI float wave_sum63(float v) {
    v += __builtin_bit_cast(float, __builtin_amdgcn_update_dpp(0, __builtin_bit_cast(int, v), 0x111, 0xf, 0xf, true));
    v += __builtin_bit_cast(float, __builtin_amdgcn_update_dpp(0, __builtin_bit_cast(int, v), 0x112, 0xf, 0xf, true));
    v += __builtin_bit_cast(float, __builtin_amdgcn_update_dpp(0, __builtin_bit_cast(int, v), 0x114, 0xf, 0xf, true));
    v += __builtin_bit_cast(float, __builtin_amdgcn_update_dpp(0, __builtin_bit_cast(int, v), 0x118, 0xf, 0xf, true));
    v += __builtin_bit_cast(float, __builtin_amdgcn_update_dpp(0, __builtin_bit_cast(int, v), 0x142, 0xa, 0xf, true));
    v += __builtin_bit_cast(float, __builtin_amdgcn_update_dpp(0, __builtin_bit_cast(int, v), 0x143, 0xc, 0xf, true));
    return v;
}
DI void rowfft(const RowC& k, const char* row, float (&A)[8], float (&B)[8], int lane) {
    f32x4 z = {0.f, 0.f, 0.f, 0.f};
    f32x4 yr[2], yi[2];
#pragma unroll
    for (int mt = 0; mt < 2; ++mt) {
        const bf16x8 a = cat8(tr_read(row + k.off0 + mt * 32), tr_read(row + k.off1 + mt * 32));
        yr[mt] = mfma16(a, k.c20, z); yi[mt] = mfma16(a, k.s20m, z);
    }
    float tr[8], ti[8];
#pragma unroll
    for (int i = 0; i < 8; ++i) {
        const float a = yr[i >> 2][i & 3], b = yi[i >> 2][i & 3], cr = k.twr[i >> 2][i & 3], ci = k.twi[i >> 2][i & 3];
        tr[i] = a * cr - b * ci; ti[i] = a * ci + b * cr;
    }
    const bf16x8 Tr = pk8(tr), Ti = pk8(ti);
    f32x4 ore[2], oim[2];
#pragma unroll
    for (int t = 0; t < 2; ++t) {
        ore[t] = mfma16(k.c[t], Tr, z); ore[t] = mfma16(k.sp[t], Ti, ore[t]);
        oim[t] = mfma16(k.sm[t], Tr, z); oim[t] = mfma16(k.c[t], Ti, oim[t]);
    }
#pragma unroll
    for (int r = 0; r < 4; ++r) { A[2 * r] = ore[0][r]; A[2 * r + 1] = oim[0][r]; B[2 * r] = ore[1][r]; B[2 * r + 1] = oim[1][r]; }
    if (lane == 0) { const float h = 0.7071067811865476f; const float dc = A[0] * h; A[0] = B[0] * h; A[1] = dc; }
}
DI int sw_off(int c) { const int ch = c >> 3; return ((ch ^ ((ch >> 3) & 7)) << 4) + (c & 7) * 2; }
DI int chunk_c0(int n, int q) { return (n == 0 ? 608 : n == 10 ? 576 : 64 * (n - 1)) + 8 * q; }

constexpr int ROWST = 1344;
constexpr int PREP_TAB = 640 * 32 + 16 * ROWST;
constexpr int PREP_LDS = PREP_TAB + ROWC_LDS;

struct PrepArgs { const u16 *x, *wr, *wi, *w1, *w2, *b2; char* ws; };

DI void task_x(const PrepArgs& a, int b, char* smem) {
    const int tid = threadIdx.x, lane = tid & 63, w = tid >> 6;
    const int p0 = 16 * ((b & 7) * 16 + (b >> 3));
    u32x4 v[3];
#pragma unroll
    for (int i = 0; i < 3; ++i) {
        const int id = min(tid + 512 * i, 1279), r = id / 80, ch = id % 80;
        v[i] = *(const u32x4*)(a.x + (size_t)tok_of_p(p0 + r) * D_ + ch * 8);
    }
    __builtin_amdgcn_sched_barrier(0);
    rowc_build(smem + PREP_TAB, w, lane);
    __builtin_amdgcn_sched_barrier(0);
    if (tid < 16) ((u64*)(a.ws + OFF_SS2))[p0 + tid] = 0ull;
    if (b < 32) ((u32*)(a.ws + OFF_CNT))[b * 512 + tid] = 0u;
#pragma unroll
    for (int i = 0; i < 3; ++i) {
        const int id = min(tid + 512 * i, 1279), r = id / 80, ch = id % 80;
        *(u32x4*)(smem + r * ROWST + ch * 16) = v[i];
    }
    __syncthreads();
    RowC k; rowc_load(k, smem + PREP_TAB, lane);
    const int n = lane & 15, q = lane >> 4, c0 = chunk_c0(n, q);
    float A[2][8], B[2][8];
#pragma unroll
    for (int i = 0; i < 2; ++i) rowfft(k, smem + (2 * w + i) * ROWST, A[i], B[i], lane);
#pragma unroll
    for (int i = 0; i < 2; ++i) {
        const int r = 2 * w + i;
        float ss = 0.f;
        if (n <= 10) {
#pragma unroll
            for (int e = 0; e < 8; ++e) ss += (lane == 0 && e == 1) ? 0.f : A[i][e] * A[i][e];
            *(bf16x8*)(smem + r * ROWST + sw_off(c0)) = pk8(A[i]);
        }
        if (n >= 1 && n <= 9) {
#pragma unroll
            for (int e = 0; e < 8; ++e) ss += B[i][e] * B[i][e];
            *(bf16x8*)(smem + r * ROWST + sw_off(c0 + 32)) = pk8(B[i]);
        }
        ss = wave_sum63(ss);
        if (lane == 63) ((u64*)(a.ws + OFF_SS0))[p0 + r] = ss_from_float(ss);
    }
    __syncthreads();
    u16* xt = (u16*)(a.ws + OFF_XT0);
    const int g = lane >> 4, i16 = lane & 15, qq = i16 >> 2, pp = i16 & 3;
#pragma unroll
    for (int i = 0; i < 3; ++i) {
        const int cbk = w + 8 * i;
        if (cbk < 20) {
            const int cb = 32 * cbk + 16 * (g & 1), ph = g >> 1;
            const char* base = smem + (8 * ph + qq) * ROWST + sw_off(cb + 4 * pp);
            const bf16x8 o = cat8(tr_read(base), tr_read(base + 4 * ROWST));
            *(bf16x8*)(xt + (size_t)(cb + i16) * S_ + p0 + 8 * ph) = o;
        }
    }
}

DI void task_W(const PrepArgs& a, int b) {
    const int lane = threadIdx.x & 63, wv = threadIdx.x >> 6, wt = b * 2 + (wv >> 2), kq = wv & 3;
    const int k1 = wt / 5, cg = wt % 5, kp = 1 + 64 * cg + lane;
    const int r = kp % 20, t = kp / 20;
    int j; bool mir = false;
    if (kp == 320) j = 304;
    else if (r == 0) j = 304 + t;
    else if (r < 10) j = 32 * (r - 1) + t;
    else if (r == 10) j = 288 + t;
    else { j = 32 * (19 - r) + 31 - t; mir = true; }
    const rsrc_t rr = mk_rsrc(a.wr, (size_t)S_ * DF_ * 2), ri = mk_rsrc(a.wi, (size_t)S_ * DF_ * 2);
    const int st = (mir ? -64 : 64) * DF_ * 2, off0 = ((mir ? 2048 - k1 : k1) * DF_ + kp) * 2 + 8 * kq * st;
    u32 re[8], im[8];
#pragma unroll
    for (int k2 = 0; k2 < 8; ++k2) {
        int off = off0 + k2 * st;
        if (k2 == 0 && mir && k1 == 0 && kq == 0) off = kp * 2;
        re[k2] = __builtin_amdgcn_raw_buffer_load_b16(rr, off, 0, 0); im[k2] = __builtin_amdgcn_raw_buffer_load_b16(ri, off, 0, 0);
    }
    const u32 sg = mir ? 0x80000000u : 0u;
    u32x4* dst = (u32x4*)(a.ws + OFF_WT + ((size_t)j * 64 + k1) * 128 + kq * 32);
#pragma unroll
    for (int i = 0; i < 2; ++i) {
        u32x4 o;
#pragma unroll
        for (int e = 0; e < 4; ++e) o[e] = (re[4 * i + e] | (im[4 * i + e] << 16)) ^ sg;
        dst[i] = o;
    }
}

DI void task_w1(const PrepArgs& a, int b, char* smem) {
    const int tid = threadIdx.x, lane = tid & 63, w = tid >> 6;
    char* raw = smem;
    char* tr = smem + 640 * 32;
    char* out = raw;
    u32x4 v[3];
#pragma unroll
    for (int i = 0; i < 3; ++i) {
        const int id = min(tid + 512 * i, 1279), d = id >> 1, h = id & 1;
        v[i] = *(const u32x4*)(a.w1 + (size_t)d * F_ + 16 * b + 8 * h);
    }
    __builtin_amdgcn_sched_barrier(0);
    rowc_build(smem + PREP_TAB, w, lane);
    __builtin_amdgcn_sched_barrier(0);
#pragma unroll
    for (int i = 0; i < 3; ++i) *(u32x4*)(raw + min(tid + 512 * i, 1279) * 16) = v[i];
    __syncthreads();
    const int g = lane >> 4, i16 = lane & 15, qq = i16 >> 2, pp = i16 & 3;
#pragma unroll
    for (int i = 0; i < 5; ++i) {
        const int d0 = 16 * (w + 8 * i) + 4 * g;
        const s16x4 t = tr_read(raw + (d0 + qq) * 32 + pp * 8);
        *(s16x4*)(tr + i16 * ROWST + d0 * 2) = t;
    }
    RowC k; rowc_load(k, smem + PREP_TAB, lane);
    __syncthreads();
    const int n = lane & 15, q = lane >> 4, c0 = chunk_c0(n, q);
    float A[2][8], B[2][8];
#pragma unroll
    for (int i = 0; i < 2; ++i) rowfft(k, tr + (2 * w + i) * ROWST, A[i], B[i], lane);
#pragma unroll
    for (int i = 0; i < 2; ++i) {
        const int f = 2 * w + i;
        if (lane == 0) A[i][1] = 0.f;
        if (n <= 10) *(bf16x8*)(out + ((c0 >> 5) * 64 + q * 16 + f) * 16) = pk8(A[i]);
        if (n >= 1 && n <= 9) *(bf16x8*)(out + (((c0 >> 5) + 1) * 64 + q * 16 + f) * 16) = pk8(B[i]);
    }
    __syncthreads();
    u32x4* dst = (u32x4*)(a.ws + OFF_W1F + (size_t)b * 20 * 1024);
#pragma unroll
    for (int i = 0; i < 3; ++i) { const int id = tid + 512 * i; if (id < 1280) dst[id] = *(const u32x4*)(out + id * 16); }
}

template <bool SW> DI void w2_frags(const char* img, char* dstbase, int b, int lane, int w) {
    const int g = lane >> 4, i16 = lane & 15, qq = i16 >> 2, pp = i16 & 3;
    const rsrc_t rd = mk_rsrc(dstbase, W2F_BYTES);
#pragma unroll
    for (int i = 0; i < 5; ++i) {
        const int cu = w + 8 * i, c = 16 * cu + 4 * pp;
        const char* base = img + (4 * g + qq) * ROWST + (SW ? sw_off(c) : c * 2);
        const bf16x8 v = cat8(tr_read(base), tr_read(base + 16 * ROWST));
        __builtin_amdgcn_raw_buffer_store_b128(__builtin_bit_cast(u32x4, v), rd, lane * 16, (cu * 54 + b) * 1024, SC1);
    }
}
constexpr int W2_TAB = 32 * ROWST;
DI void task_w2(const PrepArgs& a, int b, char* smem) {
    const int tid = threadIdx.x, lane = tid & 63, w = tid >> 6;
    u32x4 v[5];
#pragma unroll
    for (int i = 0; i < 5; ++i) {
        const int id = tid + 512 * i, r = id / 80, ch = id % 80;
        v[i] = *(const u32x4*)(a.w2 + (size_t)(32 * b + r) * D_ + ch * 8);
    }
    __builtin_amdgcn_sched_barrier(0);
    rowc_build(smem + W2_TAB, w, lane);
    __builtin_amdgcn_sched_barrier(0);
#pragma unroll
    for (int i = 0; i < 5; ++i) { const int id = tid + 512 * i, r = id / 80, ch = id % 80; *(u32x4*)(smem + r * ROWST + ch * 16) = v[i]; }
    __syncthreads();
    RowC k; rowc_load(k, smem + W2_TAB, lane);
    w2_frags<false>(smem, a.ws + OFF_W2O, b, lane, w);
    const int n = lane & 15, q = lane >> 4, c0 = chunk_c0(n, q);
    u32x4 A8[4], B8[4];
#pragma unroll
    for (int i = 0; i < 4; ++i) {
        float A[8], B[8];
        rowfft(k, smem + (4 * w + i) * ROWST, A, B, lane);
        A8[i] = __builtin_bit_cast(u32x4, pk8(A)); B8[i] = __builtin_bit_cast(u32x4, pk8(B));
    }
    __syncthreads();
#pragma unroll
    for (int i = 0; i < 4; ++i) {
        const int r = 4 * w + i;
        if (n <= 10) *(u32x4*)(smem + r * ROWST + sw_off(c0)) = A8[i];
        if (n >= 1 && n <= 9) *(u32x4*)(smem + r * ROWST + sw_off(c0 + 32)) = B8[i];
    }
    __syncthreads();
    w2_frags<true>(smem, a.ws + OFF_W2F, b, lane, w);
}

DI int kcol_of(int jc) { return jc < 288 ? (jc >> 5) + 1 + 20 * (jc & 31) : jc < 304 ? 10 + 20 * (jc - 288) : 20 * (jc - 304); }
DI void task_Q(const PrepArgs& a, int u) {
    const int tid = threadIdx.x;
#pragma unroll
    for (int ii = 0; ii < 3; ++ii) {
        const int i = tid + 512 * ii;
        if (i < 1280) {
            const int ks = i >> 6, l = i & 63, d = 16 * u + (l & 15), q = l >> 4;
            float v[8];
#pragma unroll
            for (int j = 0; j < 8; j += 2) {
                const int jc = 16 * ks + 4 * q + (j >> 1), kk = kcol_of(jc);
                const float ang = (float)((kk * d) % 640) * (1.0f / 640.0f);
                float c = cosr(ang) * 0.05590169943749474f, s = -sinr(ang) * 0.05590169943749474f;
                if (jc == 304) { c = ((d & 1) ? -1.f : 1.f) * 0.03952847075210474f; s = 0.03952847075210474f; }
                v[j] = c; v[j + 1] = s;
            }
            __builtin_amdgcn_raw_buffer_store_b128(__builtin_bit_cast(u32x4, pk8(v)), mk_rsrc(a.ws + OFF_W1F, W1F_BYTES), i * 16, (108 + u) * 20480, SC1);
        }
    }
}
DI void task_b(const PrepArgs& a, char* smem) {
    const int tid = threadIdx.x, lane = tid & 63;
    if (tid < 80) *(u32x4*)(smem + tid * 16) = *(const u32x4*)(a.b2 + tid * 8);
    rowc_build(smem + PREP_TAB, tid >> 6, lane);
    __syncthreads();
    RowC k; rowc_load(k, smem + PREP_TAB, lane);
    float A[8], B[8];
    rowfft(k, smem, A, B, lane);
    if (tid < 64) {
        const int n = lane & 15, q = lane >> 4, c0 = chunk_c0(n, q);
        float* o = (float*)(a.ws + OFF_B2P);
        if (n <= 10) for (int e = 0; e < 8; ++e) o[c0 + e] = A[e];
        if (n >= 1 && n <= 9) for (int e = 0; e < 8; ++e) o[c0 + 32 + e] = B[e];
    }
}

constexpr int NB_K0 = 128 + 108 + 160 + 1;
constexpr int MAIN_CODE_PREFETCH = 69632;
DI void prefetch_main_code(int slice, int lane) {
    u32 lo, hi;
    asm volatile("s_getpc_b64 s[40:41]\n\ts_add_u32 s40, s40, _Z6k_main8MainArgs@rel32@lo+4\n\ts_addc_u32 s41, s41, _Z6k_main8MainArgs@rel32@hi+12\n\t"
                 "s_mov_b32 %0, s40\n\ts_mov_b32 %1, s41" : "=s"(lo), "=s"(hi) :: "s40", "s41", "scc");
    const u64 base = ((u64)hi << 32) | lo;
    const char* p = (const char*)base + slice * 4096 + lane * 16;
    u32x4 t0, t1, t2, t3;
    asm volatile("global_load_dwordx4 %0, %4, off\n\tglobal_load_dwordx4 %1, %4, off offset:1024\n\tglobal_load_dwordx4 %2, %4, off offset:2048\n\t"
                 "global_load_dwordx4 %3, %4, off offset:3072\n\ts_waitcnt vmcnt(0)"
                 : "=&v"(t0), "=&v"(t1), "=&v"(t2), "=&v"(t3) : "v"(p) : "memory");
}
__global__ __launch_bounds__(512, 4) void k0_prep(PrepArgs a) {
    extern __shared__ __attribute__((aligned(16))) char smem[];
    const int b = blockIdx.x;
    if (b >= 236 && b < 236 + 8 * (MAIN_CODE_PREFETCH / 4096) && (threadIdx.x >> 6) == 7) prefetch_main_code((b - 236) >> 3, threadIdx.x & 63);
    if (b < 128) task_x(a, b, smem);
    else if (b < 236) task_w1(a, b - 128, smem);
    else if (b < 396) task_W(a, b - 236);
    else task_b(a, smem);
}

template <int N> DI void wait_vm() { asm volatile("s_waitcnt vmcnt(%0)" ::"n"(N) : "memory"); }
DI void wait_lgkm() { asm volatile("s_waitcnt lgkmcnt(0)" ::: "memory"); }
DI void barrier() { __builtin_amdgcn_s_barrier(); }
template <bool SC = false> DI void dma16(u32x4 srd, int voff, int soff, u32 ldsaddr) {
    if (SC) asm volatile("s_mov_b32 m0, %0\n\ts_nop 0\n\tbuffer_load_dwordx4 %1, %2, %3 offen sc1 lds" ::"s"(ldsaddr), "v"(voff), "s"(srd), "s"(soff) : "memory", "m0");
    else asm volatile("s_mov_b32 m0, %0\n\ts_nop 0\n\tbuffer_load_dwordx4 %1, %2, %3 offen lds" ::"s"(ldsaddr), "v"(voff), "s"(srd), "s"(soff) : "memory", "m0");
}
template <int LIM, bool SC = false> DI void dma16_lim(u32x4 srd, int voff, int soff, u32 ldsaddr, int w) {
    if (SC) asm volatile("s_cmp_lt_i32 %4, %5\n\ts_cbranch_scc0 .Lskip%=\n\ts_mov_b32 m0, %0\n\ts_nop 0\n\tbuffer_load_dwordx4 %1, %2, %3 offen sc1 lds\n.Lskip%=:"
                 ::"s"(ldsaddr), "v"(voff), "s"(srd), "s"(soff), "s"(w), "n"(LIM) : "memory", "m0", "scc");
    else asm volatile("s_cmp_lt_i32 %4, %5\n\ts_cbranch_scc0 .Lskip%=\n\ts_mov_b32 m0, %0\n\ts_nop 0\n\tbuffer_load_dwordx4 %1, %2, %3 offen lds\n.Lskip%=:"
                 ::"s"(ldsaddr), "v"(voff), "s"(srd), "s"(soff), "s"(w), "n"(LIM) : "memory", "m0", "scc");
}
DI u32x4 mk_srd(const void* p, size_t bytes) {
    const u64 a = (u64)p;
    u32x4 r = {(u32)a, (u32)(a >> 32) & 0xffffu, (u32)bytes, 0x00020000u};
    return r;
}
DI float row_sum15(float v) {
    v += __builtin_bit_cast(float, __builtin_amdgcn_update_dpp(0, __builtin_bit_cast(int, v), 0x111, 0xf, 0xf, true));
    v += __builtin_bit_cast(float, __builtin_amdgcn_update_dpp(0, __builtin_bit_cast(int, v), 0x112, 0xf, 0xf, true));
    v += __builtin_bit_cast(float, __builtin_amdgcn_update_dpp(0, __builtin_bit_cast(int, v), 0x114, 0xf, 0xf, true));
    v += __builtin_bit_cast(float, __builtin_amdgcn_update_dpp(0, __builtin_bit_cast(int, v), 0x118, 0xf, 0xf, true));
    return v;
}
template <int I, int N, class F> DI void static_for(F&& f) {
    if constexpr (I < N) { f(std::integral_constant<int, I>{}); static_for<I + 1, N>(f); }
}

constexpr int G1_P = 3, G1_PW = 5;
typedef __attribute__((address_space(1))) u32 gu32;
DI u32 ld_agent(const u32* p) { return __hip_atomic_load((gu32*)p, __ATOMIC_RELAXED, __HIP_MEMORY_SCOPE_AGENT); }
DI void add_agent(u32* p) { __hip_atomic_fetch_add((gu32*)p, 1u, __ATOMIC_RELAXED, __HIP_MEMORY_SCOPE_AGENT); }
DI void signal_all(char* ws, int which) {
    if (threadIdx.x == 0) {
        u32* c = (u32*)(ws + OFF_CNT + 16384 + which * 8192) + (blockIdx.x & 7) * 32;
#pragma unroll
        for (int r = 0; r < 8; ++r) add_agent(c + r * 256);
    }
}
DI void poll_until(const u32* p, u32 want, char* ws, int lane) {
    for (u32 spins = 0;; ++spins) {
        if (__all(ld_agent(p) >= want)) break;
        __builtin_amdgcn_s_sleep(16);
        if (spins > (1u << 18)) { if (lane == 0) __hip_atomic_store((gu32*)(ws + OFF_CNT + 49152), 1u, __ATOMIC_RELAXED, __HIP_MEMORY_SCOPE_AGENT); break; }
    }
}
DI void poll_all(char* ws, int which, u32 per_shard, int w, int lane) {
    if (w == 0) poll_until((const u32*)(ws + OFF_CNT + 16384 + which * 8192) + ((blockIdx.x >> 3) & 7) * 256 + (lane & 7) * 32, per_shard, ws, lane);
}

struct MainArgs { PrepArgs p; const u16* b1; u16* out; };
DI int img128(int row, int ch) { return row * 128 + ((ch ^ ((row >> 1) & 7)) << 4); }
template <bool LAST> DI void w1_prefetch(char* ws, char* smem, int w, int lane, int u0);

template <int L>
DI void col_task(char* ws, int b, char* smem, int u0) {
    const int tid = threadIdx.x & 255, lane = tid & 63, w = tid >> 6, half = threadIdx.x >> 8;
    const int j = 2 * b + half;
    char* Ure = smem + half * 24576; char* Uim = Ure + 4096; char* Ere = Ure + 8192; char* Eim = Ure + 12288;
    char* Rre = Ure + 16384; char* Rim = Ure + 20480;
    const int n = lane & 15, q = lane >> 4;
    const int wv = __builtin_amdgcn_readfirstlane(threadIdx.x >> 6);
    const rsrc_t rx = mk_rsrc(ws + (L == 0 ? OFF_XT0 : OFF_XT2), XT_BYTES), rs = mk_rsrc(ws + (L == 0 ? OFF_SS0 : OFF_SS2), 2048 * 8);
    u32x4 vr, vi, sv[4];
    auto load_in = [&]() __attribute__((always_inline)) {
        vr = __builtin_amdgcn_raw_buffer_load_b128(rx, (2 * j * S_ + tid * 8) * 2, 0, L == 0 ? 0 : SC1);
        vi = __builtin_amdgcn_raw_buffer_load_b128(rx, ((2 * j + 1) * S_ + tid * 8) * 2, 0, L == 0 ? 0 : SC1);
#pragma unroll
        for (int i = 0; i < 4; ++i) sv[i] = __builtin_amdgcn_raw_buffer_load_b128(rs, tid * 64 + 16 * i, 0, L == 0 ? 0 : SC1);
    };
    if (L == 0) load_in();
    u32x4 wf[2];
#pragma unroll
    for (int t = 0; t < 2; ++t) wf[t] = *(const u32x4*)((const u32*)(ws + OFF_WT) + ((size_t)j * 64 + 16 * w + n) * 32 + 16 * t + 4 * q);
    __builtin_amdgcn_sched_barrier(0);
    F32c f; f32c_init(f, lane);
    bf16x8 c32[2] = {f.c[0], f.c[1]}, s32p[2] = {f.sp[0], f.sp[1]}, s32m[2];
    pin(c32[0]); pin(c32[1]); pin(s32p[0]); pin(s32p[1]);
    bf16x8 c64[2], s64p[2], s64m[2];
    const float k1f = (float)(16 * w + n);
    {
        float c[8], s[8];
#pragma unroll
        for (int e = 0; e < 8; ++e) {
            const float ang = __builtin_amdgcn_fractf((float)(8 * q + e) * k1f * (1.0f / 64.0f));
            c[e] = cosr(ang); s[e] = sinr(ang);
        }
        c64[0] = pk8(c); s64p[0] = pk8(s);
        const u32 sg = (lane & 1) ? 0x80008000u : 0u;
        c64[1] = asbf(__builtin_bit_cast(u32x4, c64[0]) ^ sg); s64p[1] = asbf(__builtin_bit_cast(u32x4, s64p[0]) ^ sg);
        pin(c64[0]); pin(c64[1]); pin(s64p[0]); pin(s64p[1]);
    }
    float twr[8], twi[8], ivr[8], ivi[8];
#pragma unroll
    for (int i = 0; i < 8; ++i) {
        const float af = (float)(16 * (i >> 2) + 4 * q + (i & 3)) * k1f * (1.0f / 2048.0f);
        twr[i] = cosr(af); twi[i] = -sinr(af);
        const float ai = (float)(16 * w + 4 * q + (i & 3)) * (float)(16 * (i >> 2) + n) * (1.0f / 2048.0f);
        ivr[i] = cosr(ai) * (1.0f / 2048.0f); ivi[i] = sinr(ai) * (1.0f / 2048.0f);
        pin(twr[i]); pin(twi[i]); pin(ivr[i]); pin(ivi[i]);
    }
    __builtin_amdgcn_sched_barrier(0);
    if (L == 1) {
        poll_all(ws, 1, 32u, wv, lane);
        barrier();
        load_in();
        __builtin_amdgcn_sched_barrier(0);
    }
    s64m[0] = neg8(s64p[0]); s64m[1] = neg8(s64p[1]);
    {
        float r[8];
#pragma unroll
        for (int i = 0; i < 8; ++i) {
            const float s = (float)sv[i >> 1][2 * (i & 1) + 1] * 4294967296.0f + (float)sv[i >> 1][2 * (i & 1)];
            r[i] = __builtin_amdgcn_rsqf(s * (1.0f / (SS_SCALE * 640.0f)) + EPS);
        }
        const bf16x8 ur = pk8(bflo(vr[0]) * r[0], bfhi(vr[0]) * r[1], bflo(vr[1]) * r[2], bfhi(vr[1]) * r[3],
                              bflo(vr[2]) * r[4], bfhi(vr[2]) * r[5], bflo(vr[3]) * r[6], bfhi(vr[3]) * r[7]);
        bf16x8 ui = pk8(bflo(vi[0]) * r[0], bfhi(vi[0]) * r[1], bflo(vi[1]) * r[2], bfhi(vi[1]) * r[3],
                        bflo(vi[2]) * r[4], bfhi(vi[2]) * r[5], bflo(vi[3]) * r[6], bfhi(vi[3]) * r[7]);
        if (j == 304) ui = asbf(u32x4{0u, 0u, 0u, 0u});
        const int o = img128(tid >> 3, tid & 7);
        *(bf16x8*)(Ure + o) = ur; *(bf16x8*)(Uim + o) = ui;
        *(u32x4*)(Rre + o) = vr; *(u32x4*)(Rim + o) = vi;
    }
    __syncthreads();
    const f32x4 z = {0.f, 0.f, 0.f, 0.f};
    f32x4 dr[2], di[2];
#pragma unroll
    for (int mt = 0; mt < 2; ++mt) {
        dr[mt] = z; di[mt] = z;
#pragma unroll
        for (int ks = 0; ks < 2; ++ks) {
            const int o = img128(16 * mt + n, 4 * ks + q);
            const bf16x8 ar = *(const bf16x8*)(Ure + o), ai = *(const bf16x8*)(Uim + o);
            dr[mt] = mfma16(ar, c64[ks], dr[mt]); dr[mt] = mfma16(ai, s64p[ks], dr[mt]);
            di[mt] = mfma16(ar, s64m[ks], di[mt]); di[mt] = mfma16(ai, c64[ks], di[mt]);
        }
    }
    float tr[8], ti[8];
#pragma unroll
    for (int i = 0; i < 8; ++i) {
        const float x = dr[i >> 2][i & 3], y = di[i >> 2][i & 3];
        tr[i] = x * twr[i] - y * twi[i]; ti[i] = x * twi[i] + y * twr[i];
    }
    bf16x8 Tr = pk8(tr), Ti = pk8(ti);
    s32m[0] = neg8(s32p[0]); s32m[1] = neg8(s32p[1]);
    f32x4 orr[2], oi[2];
#pragma unroll
    for (int t = 0; t < 2; ++t) {
        orr[t] = mfma16(c32[t], Tr, z); orr[t] = mfma16(s32p[t], Ti, orr[t]);
        oi[t] = mfma16(s32m[t], Tr, z); oi[t] = mfma16(c32[t], Ti, oi[t]);
    }
#pragma unroll
    for (int i = 0; i < 8; ++i) {
        const float x = orr[i >> 2][i & 3], y = oi[i >> 2][i & 3];
        const u32 wv = wf[i >> 2][i & 3];
        const float wre = bflo(wv), wim = bfhi(wv);
        tr[i] = x * wre - y * wim; ti[i] = x * wim + y * wre;
    }
    Tr = pk8(tr); Ti = pk8(ti);
#pragma unroll
    for (int nt = 0; nt < 2; ++nt) {
        f32x4 zr = mfma16(Tr, c32[nt], z); zr = mfma16(Ti, s32m[nt], zr);
        f32x4 zi = mfma16(Tr, s32p[nt], z); zi = mfma16(Ti, c32[nt], zi);
        float er[4], ei[4];
#pragma unroll
        for (int r = 0; r < 4; ++r) {
            const float cr = ivr[4 * nt + r], ci = ivi[4 * nt + r];
            er[r] = zr[r] * cr - zi[r] * ci; ei[r] = zr[r] * ci + zi[r] * cr;
        }
        const int o = img128(16 * nt + n, 2 * w + (q >> 1)) + (q & 1) * 8;
        u32x2 pr = {pk2(er[0], er[1]), pk2(er[2], er[3])}, pi = {pk2(ei[0], ei[1]), pk2(ei[2], ei[3])};
        *(u32x2*)(Ere + o) = pr; *(u32x2*)(Eim + o) = pi;
    }
    __syncthreads();
    const rsrc_t rf = mk_rsrc(ws + (L == 0 ? OFF_XF1 : OFF_XF3), XT_BYTES);
    char* O = Ure;
#pragma unroll
    for (int nt = 0; nt < 2; ++nt) {
        f32x4 yr = z, yi = z;
#pragma unroll
        for (int ks = 0; ks < 2; ++ks) {
            const int o = img128(16 * nt + n, 4 * ks + q);
            const bf16x8 br = *(const bf16x8*)(Ere + o), bi = *(const bf16x8*)(Eim + o);
            yr = mfma16(c64[ks], br, yr); yr = mfma16(s64m[ks], bi, yr);
            yi = mfma16(s64p[ks], br, yi); yi = mfma16(c64[ks], bi, yi);
        }
        if (j == 304) yi = z;
        const int p = (16 * nt + n) * 64 + 16 * w + 4 * q;
        u32x2 rr[2], ri[2];
        { const int o = img128(16 * nt + n, 2 * w + (q >> 1)) + (q & 1) * 8; rr[nt] = *(const u32x2*)(Rre + o); ri[nt] = *(const u32x2*)(Rim + o); }
        const float fr[4] = {bflo(rr[nt][0]) + yr[0], bfhi(rr[nt][0]) + yr[1], bflo(rr[nt][1]) + yr[2], bfhi(rr[nt][1]) + yr[3]};
        const float fi[4] = {bflo(ri[nt][0]) + yi[0], bfhi(ri[nt][0]) + yi[1], bflo(ri[nt][1]) + yi[2], bfhi(ri[nt][1]) + yi[3]};
        u32x4 ov = {pk2(fr[0], fi[0]), pk2(fr[1], fi[1]), pk2(fr[2], fi[2]), pk2(fr[3], fi[3])};
        *(u32x4*)(O + p * 4) = ov;
    }
    __syncthreads();
    {
        const int c0 = 4 * b, dst = (((c0 >> 5) * 2 + ((c0 >> 2) & 1)) * 64 + ((c0 & 31) >> 3) * 16) * 8;
        const u32* O0 = (const u32*)smem; const u32* O1 = (const u32*)(smem + 24576);
#pragma unroll
        for (int i = 0; i < 4; ++i) {
            const int p = threadIdx.x + 512 * i;
            u32x2 v = {O0[p], O1[p]};
            __builtin_amdgcn_raw_buffer_store_b64(v, rf, (p >> 4) * 20480 + (p & 15) * 8, dst, SC1);
        }
    }
    wait_lgkm();
    wait_vm<0>();
    barrier();
    signal_all(ws, L == 0 ? 0 : 2);
    w1_prefetch<L == 1>(ws, smem, wv, lane, u0);
}

DI f32x2 gelu_tanh2(f32x2 x) {
    const float k1 = -2.302208198f, k2 = -0.1029432397f;
    const f32x2 c1 = {k1, k1}, c2 = {k2, k2}, one = {1.0f, 1.0f};
    const f32x2 t = x * (x * x * c2 + c1);
    f32x2 e = {__builtin_amdgcn_exp2f(t[0]), __builtin_amdgcn_exp2f(t[1])};
    e += one;
    const f32x2 r = {__builtin_amdgcn_rcpf(e[0]), __builtin_amdgcn_rcpf(e[1])};
    return x * r;
}
constexpr int G2_PH = 3;
constexpr int W2STG = 20480;
DI u32 w2slot(int t) { return 163840 - ((t & 7) + 1) * W2STG; }
template <bool LAST> constexpr int w1slot(int st) { return LAST ? 163840 - (st % 8 + 1) * 20480 : 163840 - (st + 1) * 14336; }

template <bool LAST, int I> DI void w1_piece(u32x4 srd, u32 lds0, int w, int vo, int u0, int st) {
    constexpr int WP = LAST ? 20 : 14;
    const int pc = w + 8 * I;
    const u32 dst = lds0 + w1slot<LAST>(st) + pc * 1024;
    const int so = min(u0 + (pc >> 1), LAST ? 147 : 107) * 20480 + st * 2048 + (pc & 1) * 1024;
    if (8 * I + 7 < WP) dma16<LAST>(srd, vo, so, dst); else dma16_lim<(WP & 7), LAST>(srd, vo, so, dst, w);
}
template <bool LAST> DI void w1_prefetch(char* ws, char* smem, int w, int lane, int u0) {
    const u32x4 srd = mk_srd(ws + OFF_W1F, W1F_BYTES);
    const u32 lds0 = (u32)(size_t)(LAS char*)smem;
#pragma unroll
    for (int st = 0; st < (LAST ? 8 : 10); ++st) {
        w1_piece<LAST, 0>(srd, lds0, w, lane * 16, u0, st);
        w1_piece<LAST, 1>(srd, lds0, w, lane * 16, u0, st);
        if (LAST) w1_piece<LAST, 2>(srd, lds0, w, lane * 16, u0, st);
    }
}

constexpr int g1_younger(int ssync, int t, int nlo) {
    int cnt = 0; bool seen = false;
    for (int s = 0; s < ssync; ++s) {
        if (s + G1_PW < 10 && s + G1_PW >= 8) { if (seen) cnt += nlo; if (s + G1_PW == t) seen = true; }
        if (s + G1_P < 10 && seen) cnt += 4;
    }
    return cnt;
}

template <int I> DI void w2_issue1(u32x4 srd, u32 lds0, int w, int vo, int ct, int t) {
    const int pc = w + 8 * I, q = pc / 5, cu = pc - 5 * q;
    const int so = ((5 * ct + cu) * 54 + (q < 2 ? 14 * q : 28 + 13 * (q - 2)) + t) * 1024;
    if (I < 2) dma16<true>(srd, vo, so, lds0 + w2slot(t) + pc * 1024); else dma16_lim<4, true>(srd, vo, so, lds0 + w2slot(t) + pc * 1024, w);
}
DI void w2_issue(u32x4 srd, u32 lds0, int w, int vo, int ct, int t) {
    w2_issue1<0>(srd, lds0, w, vo, ct, t); w2_issue1<1>(srd, lds0, w, vo, ct, t); w2_issue1<2>(srd, lds0, w, vo, ct, t);
}

template <int NF, bool LAST, int KG>
DI void g1_phase(const MainArgs& a, char* smem, int w, int lane, int pt, int u0, int nu, int ct2) {
    constexpr int P = G1_P, PW = G1_PW;
    constexpr int WP = 2 * NF, WR = (WP + 7) / 8, NLO = WP / 8, KID = LAST ? 5 : 2;
    char* ws = a.p.ws;
    const int pq = w & 3, g = lane >> 4;
    const u32x4 srdW = mk_srd(ws + OFF_W1F, W1F_BYTES), srd2 = mk_srd(ws + (LAST ? OFF_W2O : OFF_W2F), W2F_BYTES);
    const u32 lds0 = (u32)(size_t)(LAS char*)smem;
    const rsrc_t rx = mk_rsrc(ws + (LAST ? OFF_XF3 : OFF_XF1), XT_BYTES);
    const int xs = (8 * pt + 2 * pq) * 20480 + KG * 1024, vo = lane * 16;
    auto xload = [&](int st, int v) __attribute__((always_inline)) {
        return __builtin_amdgcn_raw_buffer_load_b64(rx, lane * 8, xs + (v >> 1) * 20480 + st * 2048 + (v & 1) * 512, SC1);
    };
    u32x2 xh[P][4];
#pragma unroll
    for (int st = 0; st < P; ++st) {
#pragma unroll
        for (int v = 0; v < 4; ++v) xh[st][v] = xload(st, v);
        __builtin_amdgcn_sched_barrier(0);
    }
    auto xfrag = [&](int st, int pu) __attribute__((always_inline)) { return asbf(u32x4{xh[st % P][2 * pu][0], xh[st % P][2 * pu][1], xh[st % P][2 * pu + 1][0], xh[st % P][2 * pu + 1][1]}); };
    f32x4 acc[NF][2];
#pragma unroll
    for (int i = 0; i < NF; ++i) { acc[i][0] = f32x4{0, 0, 0, 0}; acc[i][1] = f32x4{0, 0, 0, 0}; }
    f32x4 gr[2] = {f32x4{0, 0, 0, 0}, f32x4{0, 0, 0, 0}};
    bf16x8 wa[2][NF];
#pragma unroll
    for (int i = 0; i < NF; ++i) wa[0][i] = *(const bf16x8*)(smem + w1slot<LAST>(0) + KG * 1024 + lane * 16 + i * 2048);
    static_for<0, 10>([&](auto ST) __attribute__((always_inline)) {
        constexpr int st = decltype(ST)::value;
        if constexpr (LAST && st == 2) { wait_lgkm(); barrier(); }
        if constexpr (st == 4 || st == 7) {
            wait_lgkm();
            constexpr int nx4 = 4 * P + 4 * 4, nx7 = 4 * P + 4 * (10 - P < 7 ? 10 - P : 7);
            if constexpr (st == 4) wait_vm<(LAST ? NLO : 2 * NLO) + nx4>();
            else if constexpr (LAST) wait_vm<g1_younger(7, 9, NLO)>();
            else wait_vm<nx7>();
            barrier();
        }
        u32x2 nx[4];
        const char* rbase = smem + w1slot<LAST>(st + 1) + KG * 1024 + lane * 16;
        constexpr int NV = WR + 4, V0 = KG == 0 ? 0 : NF - NV;
        static_for<0, NF>([&](auto I) __attribute__((always_inline)) {
            constexpr int i = decltype(I)::value;
            __builtin_amdgcn_sched_barrier(0);
            acc[i][0] = mfma16(wa[st & 1][i], xfrag(st, 0), acc[i][0]);
            if constexpr (st + 1 < 10) wa[(st + 1) & 1][i] = *(const bf16x8*)(rbase + i * 2048);
            acc[i][1] = mfma16(wa[st & 1][i], xfrag(st, 1), acc[i][1]);
            if constexpr (i < 2) {
                u32x4 xv = __builtin_bit_cast(u32x4, xfrag(st, i));
                if constexpr (st == 9 && KG == 1) { if (g == 0) xv[0] &= 0x0000ffffu; }
                gr[i] = mfma16(asbf(xv), asbf(xv), gr[i]);
            }
            __builtin_amdgcn_sched_barrier(0);
            constexpr int v = i - V0;
            if constexpr (v >= 0 && v < WR) { if constexpr (LAST && st + PW >= 8 && st + PW < 10) w1_piece<LAST, v>(srdW, lds0, w, vo, u0, st + PW); }
            else if constexpr (v >= WR && v < NV) { if constexpr (st + P < 10) nx[v - WR] = xload(st + P, v - WR); }
            if constexpr (st >= 6) {
                constexpr int sl = i - (KG == 0 ? 0 : NF - 4), e = 4 * (st - 6) + sl;
                if constexpr (sl >= 0 && sl < 4 && e < (LAST ? 9 : 15)) w2_issue1<e % 3>(srd2, lds0, w, vo, ct2, (LAST ? 2 : 0) + e / 3);
            }
        });
        __builtin_amdgcn_sched_barrier(0);
        if constexpr (st + P < 10) { xh[st % P][0] = nx[0]; xh[st % P][1] = nx[1]; xh[st % P][2] = nx[2]; xh[st % P][3] = nx[3]; }
    });
    u32x2 bv[NF];
#pragma unroll
    for (int i = 0; i < NF; ++i) bv[i] = *(const u32x2*)(a.b1 + min(16 * (u0 + i), F_ - 16) + 4 * g);
    __builtin_amdgcn_sched_barrier(0);
    wait_lgkm();
    barrier();
    float* sx = (float*)(smem + 8 * NF * 512);
#pragma unroll
    for (int i = 0; i < NF; ++i) {
        const f32x4 t = acc[i][1 - KG];
        u32x2 pv = {pk2(t[0], t[1]), pk2(t[2], t[3])};
        *(u32x2*)(smem + ((w * NF + i) * 64 + lane) * 8) = pv;
    }
    float st2[2];
    {
        const int n = lane & 15;
#pragma unroll
        for (int pu = 0; pu < 2; ++pu) {
            const float d01 = (n & 1) ? gr[pu][1] : gr[pu][0], d23 = (n & 1) ? gr[pu][3] : gr[pu][2];
            float d = (n & 2) ? d23 : d01;
            d = (g == (n >> 2)) ? d : 0.f;
            d += __shfl_xor(d, 16); d += __shfl_xor(d, 32);
            st2[pu] = d;
        }
    }
    if (lane < 16) sx[w * 16 + lane] = st2[1 - KG];
    wait_lgkm();
    barrier();
    const float v = (st2[KG] + sx[(w ^ 4) * 16 + (lane & 15)]) * (1.0f / 640.0f);
    const float r1 = __builtin_amdgcn_rsqf(v + EPS);
    const float rr = r1 * __builtin_amdgcn_rsqf(v * r1 * r1 + EPS);
    const int punit = 8 * pt + 2 * pq + KG;
    u32x2 ov[NF], pv[NF];
#pragma unroll
    for (int i = 0; i < NF; ++i) pv[i] = *(const u32x2*)(smem + (((w ^ 4) * NF + i) * 64 + lane) * 8);
    static_for<0, NF>([&](auto I) __attribute__((always_inline)) {
        constexpr int i = decltype(I)::value;
        if constexpr (LAST ? i < 2 : i == 0) {
            __builtin_amdgcn_sched_barrier(0);
            w2_issue(srd2, lds0, w, vo, ct2, LAST ? i : 5);
            __builtin_amdgcn_sched_barrier(0);
        }
        const f32x2 o0 = {acc[i][KG][0] + bflo(pv[i][0]), acc[i][KG][1] + bfhi(pv[i][0])}, o1 = {acc[i][KG][2] + bflo(pv[i][1]), acc[i][KG][3] + bfhi(pv[i][1])};
        const f32x2 rr2 = {rr, rr};
        const f32x2 b0 = {bflo(bv[i][0]), bfhi(bv[i][0])}, b1 = {bflo(bv[i][1]), bfhi(bv[i][1])};
        f32x2 h0 = gelu_tanh2(o0 * rr2 + b0), h1 = gelu_tanh2(o1 * rr2 + b1);
        if (LAST) {
            const bool lin = u0 + i >= 108;
            h0[0] = lin ? o0[0] : h0[0]; h0[1] = lin ? o0[1] : h0[1]; h1[0] = lin ? o1[0] : h1[0]; h1[1] = lin ? o1[1] : h1[1];
        }
        ov[i] = u32x2{pk2(h0[0], h0[1]), pk2(h1[0], h1[1])};
    });
    __builtin_amdgcn_sched_barrier(0);
    const rsrc_t rh = mk_rsrc(ws + (LAST ? OFF_H2 : OFF_H), H_BYTES), r3 = mk_rsrc(ws + OFF_X3, X3_BYTES);
    const int hb = punit * 54 * 1024;
    auto st1 = [&](int i) __attribute__((always_inline)) {
        const int u = u0 + i;
        if (i < nu) {
            if (!LAST || u < 108) __builtin_amdgcn_raw_buffer_store_b64(ov[i], rh, lane * 16 + (u & 1) * 8, hb + (u >> 1) * 1024, SC1);
            else __builtin_amdgcn_raw_buffer_store_b64(ov[i], r3, lane * 8, (punit * 40 + (u - 108)) * 512, SC1);
        }
    };
    auto st2p = [&](int i) __attribute__((always_inline)) {
        const int u = u0 + i;
        if (i + 1 < nu && (!LAST || u + 1 < 108)) {
            u32x4 q4 = {ov[i][0], ov[i][1], ov[i + 1][0], ov[i + 1][1]};
            __builtin_amdgcn_raw_buffer_store_b128(q4, rh, lane * 16, hb + (u >> 1) * 1024, SC1);
        } else { st1(i); st1(i + 1); }
    };
    if (u0 & 1) {
        st1(0);
#pragma unroll
        for (int i = 1; i + 1 < NF; i += 2) st2p(i);
        if ((NF & 1) == 0) st1(NF - 1);
    } else {
#pragma unroll
        for (int i = 0; i + 1 < NF; i += 2) st2p(i);
        if (NF & 1) st1(NF - 1);
    }
}

template <bool LAST>
DI void g2_phase(const MainArgs& a, char* smem, int w, int lane, int pt1, int pt, int ct) {
    constexpr int PH = G2_PH, KID = LAST ? 6 : 3;
    char* ws = a.p.ws;
    u32* cnt = (u32*)(ws + OFF_CNT + (LAST ? 8192 : 0));
    const int pg = w & 1, kq = w >> 1, n = lane & 15, q = lane >> 4;
    const int kb = kq < 2 ? 14 * kq : 28 + 13 * (kq - 2), nk = kq < 2 ? 14 : 13;
    const u32x4 srd2 = mk_srd(ws + (LAST ? OFF_W2O : OFF_W2F), W2F_BYTES);
    const u32 lds0 = (u32)(size_t)(LAS char*)smem;
    const rsrc_t rh = mk_rsrc(ws + (LAST ? OFF_H2 : OFF_H), H_BYTES), rw = mk_rsrc(ws + (LAST ? OFF_W2O : OFF_W2F), W2F_BYTES);
    const int vo = lane * 16, hs = ((4 * pt + 2 * pg) * 54 + kb) * 1024;
    bf16x8 wr[6][5];
    asm volatile("" ::: "memory");
    __builtin_amdgcn_sched_barrier(0);
#pragma unroll
    for (int t = 0; t < 3; ++t)
#pragma unroll
        for (int i = 0; i < 5; ++i) wr[t][i] = asbf(__builtin_amdgcn_raw_buffer_load_b128(rw, vo, ((5 * ct + i) * 54 + kb + 8 + t) * 1024, SC1));
    __builtin_amdgcn_sched_barrier(0);
    wait_lgkm();
    wait_vm<15>();
    barrier();
    if (threadIdx.x == 0) add_agent(cnt + 64 * pt1);
#pragma unroll
    for (int t = LAST ? 5 : 6; t < 8; ++t) w2_issue(srd2, lds0, w, vo, ct, t);
    __builtin_amdgcn_sched_barrier(0);
#pragma unroll
    for (int t = 3; t < 6; ++t)
#pragma unroll
        for (int i = 0; i < 5; ++i) wr[t][i] = asbf(__builtin_amdgcn_raw_buffer_load_b128(rw, vo, ((5 * ct + i) * 54 + kb + 8 + t) * 1024, SC1));
    __builtin_amdgcn_sched_barrier(0);
    if (w == 0) poll_until(cnt + 64 * (pt >> 1), 16u, ws, lane);
    wait_vm<0>();
    barrier();
    bf16x8 hb[PH][2];
    auto hload = [&](int t, int pu) __attribute__((always_inline)) {
        const int k = t < 13 ? t : nk - 1;
        return asbf(__builtin_amdgcn_raw_buffer_load_b128(rh, vo, hs + (pu * 54 + k) * 1024, SC1));
    };
#pragma unroll
    for (int t = 0; t < PH; ++t) { hb[t][0] = hload(t, 0); hb[t][1] = hload(t, 1); __builtin_amdgcn_sched_barrier(0); }
    u32x2 xv[3], bv[3]; float bb[3];
    f32x4 acc[2][5];
#pragma unroll
    for (int i = 0; i < 5; ++i) { acc[0][i] = f32x4{0, 0, 0, 0}; acc[1][i] = f32x4{0, 0, 0, 0}; }
    bf16x8 wa[2][5];
    const char* wbase = smem + kq * 5120 + lane * 16;
#pragma unroll
    for (int i = 0; i < 5; ++i) wa[0][i] = *(const bf16x8*)(wbase + w2slot(0) + i * 1024);
    static_for<0, 14>([&](auto T) __attribute__((always_inline)) {
        constexpr int t = decltype(T)::value;
        if constexpr (t == 10) {
            __builtin_amdgcn_sched_barrier(0);
#pragma unroll
            for (int j = 0; j < 3; ++j) {
                const int tt = min(kq + 4 * j, 9), pu = 2 * pg + tt / 5, cu = tt % 5;
                if (!LAST) {
                    const int c = 80 * ct + 16 * cu + n;
                    const int off = ((((4 * pt + pu) * 20 + (c >> 5)) * 2 + ((c >> 2) & 1)) * 64 + ((c & 31) >> 3) * 16 + 4 * q) * 8 + (c & 3) * 2;
                    const rsrc_t r1 = mk_rsrc(ws + OFF_XF1, XT_BYTES);
                    const u32 e0 = __builtin_amdgcn_raw_buffer_load_b16(r1, off, 0, SC1), e1 = __builtin_amdgcn_raw_buffer_load_b16(r1, off + 8, 0, SC1);
                    const u32 e2 = __builtin_amdgcn_raw_buffer_load_b16(r1, off + 16, 0, SC1), e3 = __builtin_amdgcn_raw_buffer_load_b16(r1, off + 24, 0, SC1);
                    xv[j] = u32x2{e0 | (e1 << 16), e2 | (e3 << 16)};
                    bb[j] = ((const float*)(ws + OFF_B2P))[c];
                } else {
                    xv[j] = __builtin_amdgcn_raw_buffer_load_b64(mk_rsrc(ws + OFF_X3, X3_BYTES), lane * 8, ((4 * pt + pu) * 40 + 5 * ct + cu) * 512, SC1);
                    bv[j] = *(const u32x2*)(a.p.b2 + 80 * ct + 16 * cu + 4 * q);
                }
            }
        }
        bf16x8 nh[2];
        auto step = [&]() __attribute__((always_inline)) {
            static_for<0, 5>([&](auto I) __attribute__((always_inline)) {
                constexpr int i = decltype(I)::value;
                __builtin_amdgcn_sched_barrier(0);
                bf16x8 wv;
                if constexpr (t < 8) wv = wa[t & 1][i]; else wv = wr[t - 8][i];
                acc[0][i] = LAST ? mfma16(wv, hb[t % PH][0], acc[0][i]) : mfma16(hb[t % PH][0], wv, acc[0][i]);
                if constexpr (t + 1 < 8) wa[(t + 1) & 1][i] = *(const bf16x8*)(wbase + w2slot(t + 1) + i * 1024);
                acc[1][i] = LAST ? mfma16(wv, hb[t % PH][1], acc[1][i]) : mfma16(hb[t % PH][1], wv, acc[1][i]);
                __builtin_amdgcn_sched_barrier(0);
                if constexpr (t + PH < 14 && (i == 1 || i == 3)) nh[i >> 1] = hload(t + PH, i >> 1);
            });
        };
        if constexpr (t < 13) step(); else { if (nk == 14) step(); }
        __builtin_amdgcn_sched_barrier(0);
        if constexpr (t + PH < 14) { hb[t % PH][0] = nh[0]; hb[t % PH][1] = nh[1]; }
    });
    wait_lgkm();
    barrier();
#pragma unroll
    for (int t = 0; t < 10; ++t)
        if ((t & 3) != kq) *(f32x4*)(smem + ((w * 10 + t) * 64 + lane) * 16) = acc[t / 5][t % 5];
    wait_lgkm();
    barrier();
    float ssp[2][4];
#pragma unroll
    for (int r = 0; r < 4; ++r) { ssp[0][r] = 0.f; ssp[1][r] = 0.f; }
    const rsrc_t ro = mk_rsrc(ws + OFF_XT2, XT_BYTES);
    auto finish = [&](auto KQ) __attribute__((always_inline)) {
        constexpr int kqc = decltype(KQ)::value;
        static_for<0, 3>([&](auto J) __attribute__((always_inline)) {
            constexpr int j = decltype(J)::value, t = kqc + 4 * j;
            if constexpr (t < 10) {
                constexpr int pl = t / 5, cu = t % 5;
                f32x4 o = acc[pl][cu];
#pragma unroll
                for (int s = 1; s < 4; ++s) o += *(const f32x4*)(smem + (((pg + 2 * ((kqc + s) & 3)) * 10 + t) * 64 + lane) * 16);
                const int pu = 2 * pg + pl;
                if (!LAST) {
                    const int c = 80 * ct + 16 * cu + n, p = 64 * pt + 16 * pu + 4 * q;
                    const u32 o0 = pk2(bflo(xv[j][0]) + o[0] + bb[j], bfhi(xv[j][0]) + o[1] + bb[j]);
                    const u32 o1 = pk2(bflo(xv[j][1]) + o[2] + bb[j], bfhi(xv[j][1]) + o[3] + bb[j]);
                    u32x2 ov = {o0, o1};
                    __builtin_amdgcn_raw_buffer_store_b64(ov, ro, (c * S_ + p) * 2, 0, SC1);
                    const float m = c == 609 ? 0.f : 1.f;
                    ssp[pl][0] += m * bflo(o0) * bflo(o0); ssp[pl][1] += m * bfhi(o0) * bfhi(o0);
                    ssp[pl][2] += m * bflo(o1) * bflo(o1); ssp[pl][3] += m * bfhi(o1) * bfhi(o1);
                } else {
                    const int p = 16 * (4 * pt + pu) + n, d = 80 * ct + 16 * cu + 4 * q;
                    u32x2 ov = {pk2(bflo(xv[j][0]) + o[0] + bflo(bv[j][0]), bfhi(xv[j][0]) + o[1] + bfhi(bv[j][0])),
                                pk2(bflo(xv[j][1]) + o[2] + bflo(bv[j][1]), bfhi(xv[j][1]) + o[3] + bfhi(bv[j][1]))};
                    *(u32x2*)(a.out + (size_t)tok_of_p(p) * D_ + d) = ov;
                }
            }
        });
    };
    if (kq == 0) finish(std::integral_constant<int, 0>{});
    else if (kq == 1) finish(std::integral_constant<int, 1>{});
    else if (kq == 2) finish(std::integral_constant<int, 2>{});
    else finish(std::integral_constant<int, 3>{});
    if (!LAST) {
        float* sp = (float*)smem;
#pragma unroll
        for (int pl = 0; pl < 2; ++pl)
#pragma unroll
            for (int r = 0; r < 4; ++r) {
                const float v = row_sum15(ssp[pl][r]);
                if (n == 15) sp[w * 32 + 16 * pl + 4 * q + r] = v;
            }
        wait_lgkm();
        barrier();
        if (w == 0) {
            const int g2 = lane >> 5, r = lane & 31;
            const float v = (sp[g2 * 32 + r] + sp[(g2 + 2) * 32 + r]) + (sp[(g2 + 4) * 32 + r] + sp[(g2 + 6) * 32 + r]);
            __hip_atomic_fetch_add((__attribute__((address_space(1))) u64*)(ws + OFF_SS2) + 64 * pt + lane, ss_from_float(v), __ATOMIC_RELAXED, __HIP_MEMORY_SCOPE_AGENT);
        }
        wait_lgkm();
        wait_vm<0>();
        barrier();
        signal_all(ws, 1);
    }
}

template <bool LAST>
DI void mlp(const MainArgs& a, char* smem, int w, int lane, int u0) {
    constexpr int NF = LAST ? 10 : 7;
    const int x = blockIdx.x & 7, ib = blockIdx.x >> 3;
    const int pt = 4 * (x >> 1) + (ib & 3), ft = 8 * (x & 1) + (ib >> 2);
    const int pt2 = 8 * (x >> 1) + (ib & 7), ct2 = 4 * (x & 1) + (ib >> 3);
    const int nu = LAST ? (ft < 4 ? 10 : 9) : (ft < 12 ? 7 : 6);
    if (LAST) poll_all(a.p.ws, 2, 20u, w, lane);
    else if (w == 0) {
        const int sh = lane & 7, wh = (lane >> 3) & 1;
        poll_until((const u32*)(a.p.ws + OFF_CNT + 16384 + (wh ? 3 : 0) * 8192) + ((blockIdx.x >> 3) & 7) * 256 + sh * 32, wh ? (sh < 6 ? 12u : 11u) : 20u, a.p.ws, lane);
    }
    wait_vm<(LAST ? 6 : 5)>();
    barrier();
    if (w >= 4) __builtin_amdgcn_s_setprio(1);
    if (w < 4) g1_phase<NF, LAST, 0>(a, smem, w, lane, pt, u0, nu, ct2);
    else g1_phase<NF, LAST, 1>(a, smem, w, lane, pt, u0, nu, ct2);
    __builtin_amdgcn_s_setprio(0);
    g2_phase<LAST>(a, smem, w, lane, pt, pt2, ct2);
}

__global__ __launch_bounds__(512, 2) void k_main(MainArgs a) {
    extern __shared__ __attribute__((aligned(16))) char smem[];
    const int lane = threadIdx.x & 63, b = blockIdx.x;
    const int w = __builtin_amdgcn_readfirstlane(threadIdx.x >> 6);
    const int ft = 8 * (b & 1) + (b >> 5);
    const int u0a = ft < 12 ? 7 * ft : 84 + 6 * (ft - 12), u0b = ft < 4 ? 10 * ft : 40 + 9 * (ft - 4);
    if (w == 7 && b < 254) {
        const u32x4 sb1 = mk_srd(a.b1, F_ * 2), sb2 = mk_srd(a.p.b2, D_ * 2);
        const u32 dst = (u32)(size_t)(LAS char*)smem + 53248;
#pragma unroll
        for (int i = 0; i < 4; ++i) dma16(sb1, lane * 16, i * 1024, dst + i * 1024);
        dma16(sb2, lane * 16, 0, dst + 4096); dma16(sb2, lane * 16, 1024, dst + 5120);
    }
    if (b < 160) col_task<0>(a.p.ws, b, smem, u0a);
    else {
        if (b < 254) {
            if (b < 214) task_w2(a.p, b - 160, smem); else task_Q(a.p, b - 214);
            wait_lgkm();
            wait_vm<0>();
            barrier();
            signal_all(a.p.ws, 3);
        }
        w1_prefetch<false>(a.p.ws, smem, w, lane, u0a);
    }
    mlp<false>(a, smem, w, lane, u0a);
    if (b < 160) col_task<1>(a.p.ws, b, smem, u0b); else w1_prefetch<true>(a.p.ws, smem, w, lane, u0b);
    mlp<true>(a, smem, w, lane, u0b);
}

extern "C" void kernel_launch(void* const* d_in, const int* in_sizes, int n_in, void* d_out, int out_size, void* d_ws,
                              size_t ws_size, hipStream_t stream) {
    char* ws = (char*)d_ws;
    constexpr int MAIN_LDS = 163840;
    (void)hipFuncSetAttribute((const void*)k_main, hipFuncAttributeMaxDynamicSharedMemorySize, MAIN_LDS);
    PrepArgs pa{(const u16*)d_in[0], (const u16*)d_in[1], (const u16*)d_in[2], (const u16*)d_in[4], (const u16*)d_in[6], (const u16*)d_in[7], ws};
    k0_prep<<<NB_K0, 512, PREP_LDS, stream>>>(pa);
    MainArgs ma{pa, (const u16*)d_in[5], (u16*)d_out};
    k_main<<<256, 512, MAIN_LDS, stream>>>(ma);
}
```
